```python
import torch

def _softmax(x, axis=-1):
    return torch.softmax(x, dim=axis)
def _gelu(x):
    return torch.nn.functional.gelu(x, approximate="tanh")
def _silu(x):
    return torch.nn.functional.silu(x)
def _sigmoid(x):
    return torch.sigmoid(x)
def _layernorm(x, axis=-1, eps=1e-5):
    m = x.mean(dim=axis, keepdim=True)
    v = x.var(dim=axis, keepdim=True, unbiased=False)
    return (x - m) / torch.sqrt(v + eps)
def _scatter_add(tgt, idx, src, axis=0):
    return tgt.clone().index_add_(axis, idx, src)
def _segment_sum(src, seg, n):
    out = torch.zeros((n,) + src.shape[1:], dtype=src.dtype)
    return out.index_add_(0, seg, src)
def _pad(x, pw):
    flat = tuple(v for pair in reversed(pw) for v in pair)
    return torch.nn.functional.pad(x, flat)

kernel_name = 'standalone__kv_cache_quant_int4_int2_kv_with_per_channel_or_per_token_scales__prompt'

def setup_inputs():

    _w = (lambda a: (lambda t: t.to(torch.bfloat16) if t.is_floating_point() else t)(torch.as_tensor(a)))
    B = 1
    Sq = 2048
    Skv = 2048
    Hq = 32
    Hkv = 32
    Dh = 192
    bits = 3
    G = 32
    if G <= 0 or G >= Skv:
        G = max(1, Skv // 2)
    Skv = (Skv // G) * G
    ngk = Skv // G
    qmax = (1 << bits) - 1
    return {
        "q": _w((lambda *s: torch.randn(*s, dtype=torch.bfloat16) * 1.6)(B, Hq, Sq, Dh)),
        "qk": _w(torch.randint(0, qmax + 1, (B, Hkv, Skv, Dh)).to(torch.int32)),
        "qv": _w(torch.randint(0, qmax + 1, (B, Hkv, Skv, Dh)).to(torch.int32)),
        "k_scales": _w(torch.abs((lambda *s: torch.randn(*s, dtype=torch.bfloat16) * 1.6)(B, Hkv, ngk, Dh)) * 0.05 + 0.02),
        "k_zeros": _w((lambda *s: torch.randn(*s, dtype=torch.bfloat16) * 1.6)(B, Hkv, ngk, Dh) + float(qmax) / 2.0),
        "v_scales": _w(torch.abs((lambda *s: torch.randn(*s, dtype=torch.bfloat16) * 0.05)(B, Hkv, Skv, 1)) * 0.05 + 0.02),
        "v_zeros": _w((lambda *s: torch.randn(*s, dtype=torch.bfloat16) * 0.05)(B, Hkv, Skv, 1) + float(qmax) / 2.0),
    }

def reference(q, qk, qv, k_scales, k_zeros, v_scales, v_zeros):
    B, Hq, Sq, Dh = q.shape
    Hkv, Skv = qk.shape[1], qk.shape[2]
    ngk = k_scales.shape[2]
    G = Skv // ngk
    ks = (lambda x, n, axis: torch.repeat_interleave(x, n, dim=axis))(k_scales, G, axis=2)
    kz = (lambda x, n, axis: torch.repeat_interleave(x, n, dim=axis))(k_zeros, G, axis=2)
    k = ((lambda x, dt: x.to(dt))(qk, q.dtype) - kz) * ks
    v = ((lambda x, dt: x.to(dt))(qv, q.dtype) - v_zeros) * v_scales
    rep = Hq // Hkv
    if rep > 1:
        k = (lambda x, n, axis: torch.repeat_interleave(x, n, dim=axis))(k, rep, axis=1)
        v = (lambda x, n, axis: torch.repeat_interleave(x, n, dim=axis))(v, rep, axis=1)
    scale = float(Dh) ** -0.5
    logits = torch.einsum("bhqd,bhkd->bhqk", q, k) * scale
    p = _softmax(logits, axis=-1)
    o = torch.einsum("bhqk,bhkd->bhqd", p, v)
    return o
```

```cpp
#include <hip/hip_runtime.h>
#include <hip/hip_bf16.h>

typedef float    f32x16 __attribute__((ext_vector_type(16)));
typedef float    f32x2  __attribute__((ext_vector_type(2)));
typedef __bf16   bf16x8 __attribute__((ext_vector_type(8)));
typedef __bf16   bf16x2 __attribute__((ext_vector_type(2)));
typedef unsigned u32x4  __attribute__((ext_vector_type(4)));
typedef unsigned u32x2  __attribute__((ext_vector_type(2)));
typedef int      i32x4  __attribute__((ext_vector_type(4)));

constexpr int H = 32, S = 2048, D = 192, G = 32, NG = S / G;

__device__ __forceinline__ unsigned cvtpk(float lo, float hi) {
  f32x2 v = {lo, hi}; bf16x2 b = __builtin_convertvector(v, bf16x2); return __builtin_bit_cast(unsigned, b);
}
__device__ __forceinline__ float bflo(unsigned x) { return __uint_as_float(x << 16); }
__device__ __forceinline__ float bfhi(unsigned x) { return __uint_as_float(x & 0xffff0000u); }

__global__ __launch_bounds__(256) void dequant_kernel(const int* __restrict__ qk, const int* __restrict__ qv,
    const unsigned short* __restrict__ ks, const unsigned short* __restrict__ kz,
    const unsigned short* __restrict__ vs, const unsigned short* __restrict__ vz,
    unsigned short* __restrict__ K, unsigned short* __restrict__ V) {
  const float C = 0.07216878364870322f * 1.4426950408889634f;
  long i = (long)blockIdx.x * 256 + threadIdx.x;
  if (i >= (long)H * S * 24) return;
  int ch = i % 24; long tok = i / 24; int h = tok / S, t = tok % S;
  i32x4 a = *(const i32x4*)(qk + i * 8), b = *(const i32x4*)(qk + i * 8 + 4);
  i32x4 c = *(const i32x4*)(qv + i * 8), d = *(const i32x4*)(qv + i * 8 + 4);
  long po = ((long)h * NG + t / G) * D + ch * 8;
  u32x4 s = *(const u32x4*)(ks + po), z = *(const u32x4*)(kz + po);
  u32x4 o;
  o[0] = cvtpk(((float)a[0] - bflo(z[0])) * (bflo(s[0]) * C), ((float)a[1] - bfhi(z[0])) * (bfhi(s[0]) * C));
  o[1] = cvtpk(((float)a[2] - bflo(z[1])) * (bflo(s[1]) * C), ((float)a[3] - bfhi(z[1])) * (bfhi(s[1]) * C));
  o[2] = cvtpk(((float)b[0] - bflo(z[2])) * (bflo(s[2]) * C), ((float)b[1] - bfhi(z[2])) * (bfhi(s[2]) * C));
  o[3] = cvtpk(((float)b[2] - bflo(z[3])) * (bflo(s[3]) * C), ((float)b[3] - bfhi(z[3])) * (bfhi(s[3]) * C));
  *(u32x4*)(K + i * 8) = o;
  float sv = bflo(vs[tok]), zv = bflo(vz[tok]);
  o[0] = cvtpk(((float)c[0] - zv) * sv, ((float)c[1] - zv) * sv);
  o[1] = cvtpk(((float)c[2] - zv) * sv, ((float)c[3] - zv) * sv);
  o[2] = cvtpk(((float)d[0] - zv) * sv, ((float)d[1] - zv) * sv);
  o[3] = cvtpk(((float)d[2] - zv) * sv, ((float)d[3] - zv) * sv);
  *(u32x4*)(V + i * 8) = o;
}

__global__ __launch_bounds__(256, 2) void attn_kernel(const unsigned short* __restrict__ Q, const unsigned short* __restrict__ K,
    const unsigned short* __restrict__ V, unsigned short* __restrict__ O) {
  __shared__ __attribute__((aligned(16))) unsigned short Ks[32 * 200];
  __shared__ __attribute__((aligned(16))) unsigned short Vt[192 * 40];
  const int tid = threadIdx.x, wid = tid >> 6, lane = tid & 63, r32 = lane & 31, hi = lane >> 5;
  const int h = blockIdx.y, q0 = blockIdx.x * 128 + wid * 32;
  bf16x8 qr[12];
  const unsigned short* Qw = Q + ((long)h * S + q0 + r32) * D + hi * 8;
#pragma unroll
  for (int s = 0; s < 12; ++s) qr[s] = *(const bf16x8*)(Qw + s * 16);
  f32x16 o[6] = {};
  float m = -1e30f, l = 0.f;
  for (int t0 = 0; t0 < S; t0 += 32) {
    __syncthreads();
    for (int i = tid; i < 32 * 24; i += 256) {
      int r = i / 24, c = i % 24;
      *(u32x4*)(Ks + r * 200 + c * 8) = *(const u32x4*)(K + ((long)h * S + t0 + r) * D + c * 8);
      u32x4 v = *(const u32x4*)(V + ((long)h * S + t0 + r) * D + c * 8);
      int ks = r >> 4, rr = r & 15, j = ((rr >> 3) << 2) | (rr & 3), hh = (rr >> 2) & 1;
      int pos = ks * 16 + hh * 8 + j;
      for (int e = 0; e < 4; ++e) { Vt[(c * 8 + 2 * e) * 40 + pos] = v[e] & 0xffff; Vt[(c * 8 + 2 * e + 1) * 40 + pos] = v[e] >> 16; }
    }
    __syncthreads();
    f32x16 p = {};
#pragma unroll
    for (int s = 0; s < 12; ++s) {
      bf16x8 kf = *(const bf16x8*)(Ks + r32 * 200 + s * 16 + hi * 8);
      p = __builtin_amdgcn_mfma_f32_32x32x16_bf16(kf, qr[s], p, 0, 0, 0);
    }
    float pm = p[0];
#pragma unroll
    for (int r = 1; r < 16; ++r) pm = fmaxf(pm, p[r]);
    pm = fmaxf(pm, __shfl_xor(pm, 32));
    float mn = fmaxf(m, pm), al = __builtin_amdgcn_exp2f(m - mn); m = mn;
    float rs = 0;
#pragma unroll
    for (int r = 0; r < 16; ++r) { p[r] = __builtin_amdgcn_exp2f(p[r] - mn); rs += p[r]; }
    l = l * al + rs;
#pragma unroll
    for (int d = 0; d < 6; ++d) o[d] *= al;
    u32x4 pk[2];
#pragma unroll
    for (int s = 0; s < 2; ++s)
#pragma unroll
      for (int e = 0; e < 4; ++e) pk[s][e] = cvtpk(p[8 * s + 2 * e], p[8 * s + 2 * e + 1]);
#pragma unroll
    for (int d = 0; d < 6; ++d)
#pragma unroll
      for (int s = 0; s < 2; ++s) {
        bf16x8 vf = *(const bf16x8*)(Vt + (d * 32 + r32) * 40 + s * 16 + hi * 8);
        o[d] = __builtin_amdgcn_mfma_f32_32x32x16_bf16(vf, __builtin_bit_cast(bf16x8, pk[s]), o[d], 0, 0, 0);
      }
  }
  l += __shfl_xor(l, 32);
  float il = 1.f / l;
  unsigned short* Ow = O + ((long)h * S + q0 + r32) * D;
#pragma unroll
  for (int d = 0; d < 6; ++d)
#pragma unroll
    for (int g = 0; g < 4; ++g) {
      u32x2 w = {cvtpk(o[d][4 * g] * il, o[d][4 * g + 1] * il), cvtpk(o[d][4 * g + 2] * il, o[d][4 * g + 3] * il)};
      *(u32x2*)(Ow + d * 32 + 8 * g + 4 * hi) = w;
    }
}

extern "C" void kernel_launch(void* const* d_in, const int* in_sizes, int n_in,
                              void* d_out, int out_size, void* d_ws, size_t ws_size,
                              hipStream_t stream) {
  unsigned short* K = (unsigned short*)d_ws;
  unsigned short* V = K + (size_t)H * S * D;
  dequant_kernel<<<H * S * 24 / 256, 256, 0, stream>>>((const int*)d_in[1], (const int*)d_in[2],
      (const unsigned short*)d_in[3], (const unsigned short*)d_in[4], (const unsigned short*)d_in[5], (const unsigned short*)d_in[6], K, V);
  attn_kernel<<<dim3(S / 128, H), 256, 0, stream>>>((const unsigned short*)d_in[0], K, V, (unsigned short*)d_out);
}
```

```cpp
#include <hip/hip_runtime.h>
#include <hip/hip_bf16.h>

constexpr int S = 2048, NT = 64, TILE = 24576;
constexpr size_t FLAG_OFF = 50331648;

__global__ __launch_bounds__(256, 1) void attn_kernel(const char* __restrict__ q, const char* __restrict__ qk, const char* __restrict__ qv,
    const char* __restrict__ ks, const char* __restrict__ kz, const char* __restrict__ vs, const char* __restrict__ vz,
    char* __restrict__ ws, char* __restrict__ out) {
  {
    const unsigned ci = ((blockIdx.x >> 3) * 4 + __builtin_amdgcn_readfirstlane(threadIdx.x >> 6)) & 63;
    asm volatile("s_getpc_b64 s[40:41]\n\tL_pcc:\n\ts_lshl_b32 s42, %0, 10\n\ts_sub_u32 s43, s42, L_cend-L_pcc-1024\n\ts_min_u32 s42, s42, s43\n\ts_min_u32 s42, s42, L_cend-L_pcc-1024\n\t"
                 "v_mbcnt_lo_u32_b32 v124, -1, 0\n\tv_and_b32 v124, 7, v124\n\tv_lshl_add_u32 v124, v124, 7, s42\n\t"
                 "global_load_dword v124, v124, s[40:41]" :: "s"(ci) : "s40", "s41", "s42", "s43", "v124", "scc");
  }
  __shared__ __attribute__((aligned(16))) char lds[159744];
  const unsigned w = __builtin_amdgcn_readfirstlane(threadIdx.x >> 6);
  const unsigned b = blockIdx.x, h = (b & 7) * 4 + (b >> 6), j = (b >> 3) & 7;
  const size_t row0 = (size_t)h * S + j * 256 + w * 64;
  const size_t key0 = (size_t)h * S + j * 32 + w * 8;
  asm volatile("" :: "s"((unsigned)(size_t)lds));
  asm volatile(
    "v_mbcnt_lo_u32_b32 v112, -1, 0\n\t"
    "v_mbcnt_hi_u32_b32 v112, -1, v112\n\t"
    "s_mov_b64 s[58:59], %11\n\t"
    "s_mov_b32 s95, %3\n\t"
    "s_lshl_b32 s96, s95, 6\n\t"
    "s_add_u32 s96, s96, 256\n\t"
    "s_add_u32 s90, s58, s96\n\t"
    "s_addc_u32 s91, s59, 0\n\t"
    "v_and_b32 v181, 7, v112\n\t"
    "v_lshlrev_b32 v184, 6, v181\n\t"
    "v_lshlrev_b32 v182, 3, v181\n\t"
    "v_lshlrev_b32 v181, 2, v181\n\t"
    "global_load_dword v183, v181, s[58:59] sc1\n\t"
    "global_load_dword v202, v182, s[90:91] sc1\n\t"
    "s_mov_b64 s[78:79], %5\n\t"
    "s_mov_b64 s[80:81], %6\n\t"
    "s_mov_b64 s[82:83], %7\n\t"
    "s_mov_b64 s[84:85], %8\n\t"
    "s_mov_b64 s[86:87], %9\n\t"
    "s_mov_b64 s[88:89], %10\n\t"
    "v_mov_b32 v186, 0\n\t"
    "v_subrev_u32 v115, 48, v112\n\t"
    "v_min_u32 v116, v115, v112\n\t"
    "v_subrev_u32 v115, 24, v116\n\t"
    "v_min_u32 v117, v115, v116\n\t"
    "v_lshlrev_b32 v198, 4, v117\n\t"
    "v_lshlrev_b32 v196, 5, v116\n\t"
    "v_lshlrev_b32 v197, 4, v116\n\t"
    "global_load_dwordx4 v[252:255], v198, s[82:83]\n\t"
    "global_load_dwordx4 v[188:191], v198, s[84:85]\n\t"
    "global_load_dwordx4 v[220:223], v196, s[78:79] offset:-2048\n\t"
    "global_load_dwordx4 v[224:227], v196, s[78:79] offset:-2032\n\t"
    "global_load_dwordx4 v[228:231], v196, s[78:79] offset:-512\n\t"
    "global_load_dwordx4 v[232:235], v196, s[78:79] offset:-496\n\t"
    "global_load_dwordx4 v[236:239], v196, s[78:79] offset:1024\n\t"
    "global_load_dwordx4 v[240:243], v196, s[78:79] offset:1040\n\t"
    "global_load_dwordx4 v[160:163], v196, s[78:79] offset:2560\n\t"
    "global_load_dwordx4 v[164:167], v196, s[78:79] offset:2576\n\t"
    "global_load_dwordx4 v[96:99], v186, s[86:87]\n\t"
    "global_load_dwordx4 v[100:103], v186, s[88:89]\n\t"
    "global_load_dwordx4 v[32:35], v197, s[80:81] offset:-2048\n\t"
    "global_load_dwordx4 v[36:39], v197, s[80:81] offset:-1280\n\t"
    "global_load_dwordx4 v[40:43], v197, s[80:81] offset:-512\n\t"
    "global_load_dwordx4 v[44:47], v197, s[80:81] offset:256\n\t"
    "global_load_dwordx4 v[48:51], v197, s[80:81] offset:1024\n\t"
    "global_load_dwordx4 v[52:55], v197, s[80:81] offset:1792\n\t"
    "global_load_dwordx4 v[56:59], v197, s[80:81] offset:2560\n\t"
    "global_load_dwordx4 v[60:63], v197, s[80:81] offset:3328\n\t"
    "s_mov_b64 s[52:53], %0\n\t"
    "s_mov_b64 s[40:41], %1\n\t"
    "s_mul_i32 s60, %4, 3072\n\t"
    "s_mov_b64 s[48:49], s[40:41]\n\t"
    "s_add_u32 s44, s40, 12288\n\t"
    "s_addc_u32 s45, s41, 0\n\t"
    "s_and_b32 s53, s53, 0xffff\n\t"
    "s_mov_b32 s54, 24576\n\t"
    "s_mov_b32 s55, 0x20000\n\t"
    "s_and_b32 s41, s41, 0xffff\n\t"
    "s_mov_b32 s42, 2097152\n\t"
    "s_mov_b32 s43, 0x20000\n\t"
    "s_and_b32 s45, s45, 0xffff\n\t"
    "s_mov_b32 s46, 2097152\n\t"
    "s_mov_b32 s47, 0x20000\n\t"
    "s_and_b32 s49, s49, 0xffff\n\t"
    "s_mov_b32 s50, 1572864\n\t"
    "s_mov_b32 s51, 0x20000\n\t"
    "v_and_b32 v113, 15, v112\n\t"
    "v_lshrrev_b32 v114, 4, v112\n\t"
    "v_lshlrev_b32 v195, 4, v112\n\t"
    "s_mov_b32 s97, 2731\n\t"
    "v_add_u32 v104, 0, v112\n\t"
    "v_mul_u32_u24 v104, s97, v104\n\t"
    "v_bfe_u32 v104, v104, 17, 3\n\t"
    "v_xor_b32 v104, v104, v112\n\t"
    "v_lshlrev_b32 v104, 4, v104\n\t"
    "v_add_u32 v105, 64, v112\n\t"
    "v_mul_u32_u24 v105, s97, v105\n\t"
    "v_bfe_u32 v105, v105, 17, 3\n\t"
    "v_xor_b32 v105, v105, v112\n\t"
    "v_lshlrev_b32 v105, 4, v105\n\t"
    "v_add_u32 v106, 128, v112\n\t"
    "v_mul_u32_u24 v106, s97, v106\n\t"
    "v_bfe_u32 v106, v106, 17, 3\n\t"
    "v_xor_b32 v106, v106, v112\n\t"
    "v_lshlrev_b32 v106, 4, v106\n\t"
    "v_add_u32 v107, 192, v112\n\t"
    "v_mul_u32_u24 v107, s97, v107\n\t"
    "v_bfe_u32 v107, v107, 17, 3\n\t"
    "v_xor_b32 v107, v107, v112\n\t"
    "v_lshlrev_b32 v107, 4, v107\n\t"
    "v_add_u32 v108, 256, v112\n\t"
    "v_mul_u32_u24 v108, s97, v108\n\t"
    "v_bfe_u32 v108, v108, 17, 3\n\t"
    "v_xor_b32 v108, v108, v112\n\t"
    "v_lshlrev_b32 v108, 4, v108\n\t"
    "v_add_u32 v109, 320, v112\n\t"
    "v_mul_u32_u24 v109, s97, v109\n\t"
    "v_bfe_u32 v109, v109, 17, 3\n\t"
    "v_xor_b32 v109, v109, v112\n\t"
    "v_lshlrev_b32 v109, 4, v109\n\t"
    "s_lshl_b32 s37, s60, 3\n\t"
    "s_add_u32 s37, s37, 61440\n\t"
    "v_lshrrev_b32 v115, 1, v113\n\t"
    "v_xor_b32 v115, v115, v114\n\t"
    "v_mul_u32_u24 v110, 384, v113\n\t"
    "v_lshl_add_u32 v110, v115, 4, v110\n\t"
    "v_add_u32 v110, s37, v110\n\t"
    "v_xor_b32 v111, 64, v110\n\t"
    "s_mov_b32 s96, 0\n\t"
    "s_add_u32 m0, s37, 0\n\t"
    "s_nop 0\n\t"
    "buffer_load_dwordx4 v104, s[52:55], s96 offen offset:0 lds\n\t"
    "buffer_load_dwordx4 v105, s[52:55], s96 offen offset:1024 lds\n\t"
    "buffer_load_dwordx4 v106, s[52:55], s96 offen offset:2048 lds\n\t"
    "buffer_load_dwordx4 v107, s[52:55], s96 offen offset:3072 lds\n\t"
    "s_mov_b32 s96, 4096\n\t"
    "s_add_u32 m0, s37, 4096\n\t"
    "s_nop 0\n\t"
    "buffer_load_dwordx4 v108, s[52:55], s96 offen offset:0 lds\n\t"
    "buffer_load_dwordx4 v109, s[52:55], s96 offen offset:1024 lds\n\t"
    "s_add_u32 s78, s78, 196608\n\t"
    "s_addc_u32 s79, s79, 0\n\t"
    "s_add_u32 s80, s80, 196608\n\t"
    "s_addc_u32 s81, s81, 0\n\t"
    "s_add_u32 s82, s82, 3072\n\t"
    "s_addc_u32 s83, s83, 0\n\t"
    "s_add_u32 s84, s84, 3072\n\t"
    "s_addc_u32 s85, s85, 0\n\t"
    "s_add_u32 s86, s86, 512\n\t"
    "s_addc_u32 s87, s87, 0\n\t"
    "s_add_u32 s88, s88, 512\n\t"
    "s_addc_u32 s89, s89, 0\n\t"
    "s_add_u32 s97, s95, 0\n\t"
    "s_and_b32 s97, s97, 7\n\t"
    "s_mul_i32 s97, s97, 24576\n\t"
    "s_add_u32 s64, s97, s60\n\t"
    "s_add_u32 s97, s95, 1\n\t"
    "s_and_b32 s97, s97, 7\n\t"
    "s_mul_i32 s97, s97, 24576\n\t"
    "s_add_u32 s65, s97, s60\n\t"
    "s_add_u32 s97, s95, 2\n\t"
    "s_and_b32 s97, s97, 7\n\t"
    "s_mul_i32 s97, s97, 24576\n\t"
    "s_add_u32 s66, s97, s60\n\t"
    "s_add_u32 s97, s95, 3\n\t"
    "s_and_b32 s97, s97, 7\n\t"
    "s_mul_i32 s97, s97, 24576\n\t"
    "s_add_u32 s67, s97, s60\n\t"
    "s_add_u32 s97, s95, 4\n\t"
    "s_and_b32 s97, s97, 7\n\t"
    "s_mul_i32 s97, s97, 24576\n\t"
    "s_add_u32 s68, s97, s60\n\t"
    "s_add_u32 s97, s95, 5\n\t"
    "s_and_b32 s97, s97, 7\n\t"
    "s_mul_i32 s97, s97, 24576\n\t"
    "s_add_u32 s69, s97, s60\n\t"
    "s_add_u32 s97, s95, 6\n\t"
    "s_and_b32 s97, s97, 7\n\t"
    "s_mul_i32 s97, s97, 24576\n\t"
    "s_add_u32 s70, s97, s60\n\t"
    "s_add_u32 s97, s95, 7\n\t"
    "s_and_b32 s97, s97, 7\n\t"
    "s_mul_i32 s97, s97, 24576\n\t"
    "s_add_u32 s71, s97, s60\n\t"
    "s_mul_i32 s92, s95, 24576\n\t"
    "s_mov_b32 s93, 0x3dd53b94\n\t"
    "s_mov_b32 s62, 0x47000000\n\t"
    "s_mov_b32 s61, 0\n\t"
    "s_mov_b32 s94, 1\n\t"
    "s_lshl_b32 s97, s95, 3\n\t"
    "s_add_u32 s97, s97, 256\n\t"
    "s_add_u32 s56, s58, s97\n\t"
    "s_addc_u32 s57, s59, 0\n\t"
    "v_and_b32 v119, 12, v113\n\t"
    "v_add_u32 v119, v119, v113\n\t"
    "v_mul_u32_u24 v192, 384, v119\n\t"
    "v_lshl_add_u32 v192, v115, 4, v192\n\t"
    "v_add_u32 v192, 24576, v192\n\t"
    "v_xor_b32 v193, 64, v192\n\t"
    "v_mul_u32_u24 v194, 3072, v114\n\t"
    "v_lshl_add_u32 v194, v113, 4, v194\n\t"
    "s_lshl_b32 s97, s60, 1\n\t"
    "s_add_u32 s97, s97, 98304\n\t"
    "v_add_u32 v185, s97, v195\n\t"
    "s_lshl_b32 s97, %4, 1\n\t"
    "v_xor_b32 v115, s97, v117\n\t"
    "v_lshlrev_b32 v115, 4, v115\n\t"
    "v_sub_u32 v199, v116, v117\n\t"
    "v_lshl_add_u32 v199, v199, 4, v115\n\t"
    "v_add_u32 v199, s60, v199\n\t"
    "v_cmp_lt_u32 vcc, 47, v112\n\t"
    "v_mov_b32 v115, 0x40000000\n\t"
    "s_nop 1\n\t"
    "v_cndmask_b32 v199, v199, v115, vcc\n\t"
    "v_xor_b32 v200, 16, v199\n\t"
    "v_and_b32 v119, 1, v112\n\t"
    "v_lshlrev_b32 v119, 10, v119\n\t"
    "v_lshrrev_b32 v201, 1, v112\n\t"
    "v_lshl_add_u32 v201, v201, 4, v119\n\t"
    "v_add_u32 v119, 1536, v195\n\t"
    "v_cndmask_b32 v119, v119, v115, vcc\n\t"
    "v_cmp_lt_u32 vcc, 31, v112\n\t"
    "s_add_u32 s97, s60, 12288\n\t"
    "s_nop 0\n\t"
    "v_cndmask_b32 v201, v201, v119, vcc\n\t"
    "v_add_u32 v201, s97, v201\n\t"
    "s_getpc_b64 s[72:73]\n\t"
    "L_pc0:\n\t"
    "s_add_u32 s74, s72, L_slow_1-L_pc0\n\t"
    "s_addc_u32 s75, s73, 0\n\t"
    "s_add_u32 s72, s72, L_slow_0-L_pc0\n\t"
    "s_addc_u32 s73, s73, 0\n\t"
    "s_waitcnt vmcnt(26)\n\t"
    "v_readlane_b32 s99, v183, s95\n\t"
    "s_nop 3\n\t"
    "L_nonce:\n\t"
    "s_add_u32 s99, s99, 16\n\t"
    "v_subrev_u32 v115, s99, v202\n\t"
    "v_cmp_gt_u32 vcc, 16, v115\n\t"
    "s_cbranch_vccnz L_nonce\n\t"
    "s_mov_b32 s63, s99\n\t"
    "v_mov_b32 v203, s99\n\t"
    "s_lshl_b32 s97, s95, 2\n\t"
    "v_mov_b32 v119, s97\n\t"
    "global_store_dword v119, v203, s[58:59] sc1\n\t"
    "s_waitcnt vmcnt(25)\n\t"
    "v_subrev_u32 v115, 12288, v201\n\t"
    "s_mov_b32 exec_hi, 0xffff\n\t"
    "v_lshlrev_b32 v204, 16, v252\n\t"
    "v_and_b32 v205, 0xffff0000, v252\n\t"
    "v_lshlrev_b32 v212, 16, v188\n\t"
    "v_and_b32 v213, 0xffff0000, v188\n\t"
    "v_lshlrev_b32 v206, 16, v253\n\t"
    "v_and_b32 v207, 0xffff0000, v253\n\t"
    "v_lshlrev_b32 v214, 16, v189\n\t"
    "v_and_b32 v215, 0xffff0000, v189\n\t"
    "v_lshlrev_b32 v208, 16, v254\n\t"
    "v_and_b32 v209, 0xffff0000, v254\n\t"
    "v_lshlrev_b32 v216, 16, v190\n\t"
    "v_and_b32 v217, 0xffff0000, v190\n\t"
    "v_lshlrev_b32 v210, 16, v255\n\t"
    "v_and_b32 v211, 0xffff0000, v255\n\t"
    "v_lshlrev_b32 v218, 16, v191\n\t"
    "v_and_b32 v219, 0xffff0000, v191\n\t"
    "v_mul_f32 v204, s93, v204\n\t"
    "v_mul_f32 v205, s93, v205\n\t"
    "v_mul_f32 v206, s93, v206\n\t"
    "v_mul_f32 v207, s93, v207\n\t"
    "v_mul_f32 v208, s93, v208\n\t"
    "v_mul_f32 v209, s93, v209\n\t"
    "v_mul_f32 v210, s93, v210\n\t"
    "v_mul_f32 v211, s93, v211\n\t"
    "v_mul_f32_e64 v212, -v212, v204\n\t"
    "v_mul_f32_e64 v213, -v213, v205\n\t"
    "v_mul_f32_e64 v214, -v214, v206\n\t"
    "v_mul_f32_e64 v215, -v215, v207\n\t"
    "v_mul_f32_e64 v216, -v216, v208\n\t"
    "v_mul_f32_e64 v217, -v217, v209\n\t"
    "v_mul_f32_e64 v218, -v218, v210\n\t"
    "v_mul_f32_e64 v219, -v219, v211\n\t"
    "v_mul_f32 v204, 0x4b800000, v204\n\t"
    "v_mul_f32 v205, 0x4b800000, v205\n\t"
    "v_mul_f32 v206, 0x4b800000, v206\n\t"
    "v_mul_f32 v207, 0x4b800000, v207\n\t"
    "v_mul_f32 v208, 0x4b800000, v208\n\t"
    "v_mul_f32 v209, 0x4b800000, v209\n\t"
    "v_mul_f32 v210, 0x4b800000, v210\n\t"
    "v_mul_f32 v211, 0x4b800000, v211\n\t"
    "s_waitcnt vmcnt(23)\n\t"
    "v_fma_mix_f32 v220, v220, v204, v212 op_sel_hi:[1,0,0]\n\t"
    "v_fma_mix_f32 v221, v221, v205, v213 op_sel_hi:[1,0,0]\n\t"
    "v_fma_mix_f32 v222, v222, v206, v214 op_sel_hi:[1,0,0]\n\t"
    "v_fma_mix_f32 v223, v223, v207, v215 op_sel_hi:[1,0,0]\n\t"
    "v_fma_mix_f32 v224, v224, v208, v216 op_sel_hi:[1,0,0]\n\t"
    "v_fma_mix_f32 v225, v225, v209, v217 op_sel_hi:[1,0,0]\n\t"
    "v_fma_mix_f32 v226, v226, v210, v218 op_sel_hi:[1,0,0]\n\t"
    "v_fma_mix_f32 v227, v227, v211, v219 op_sel_hi:[1,0,0]\n\t"
    "v_cvt_pk_bf16_f32 v244, v220, v221\n\t"
    "v_cvt_pk_bf16_f32 v245, v222, v223\n\t"
    "v_cvt_pk_bf16_f32 v246, v224, v225\n\t"
    "v_cvt_pk_bf16_f32 v247, v226, v227\n\t"
    "buffer_store_dwordx4 v[244:247], v199, s[48:51], s92 offen offset:0 sc1\n\t"
    "ds_write_b128 v199, v[244:247] offset:24576\n\t"
    "s_waitcnt vmcnt(22)\n\t"
    "v_fma_mix_f32 v228, v228, v204, v212 op_sel_hi:[1,0,0]\n\t"
    "v_fma_mix_f32 v229, v229, v205, v213 op_sel_hi:[1,0,0]\n\t"
    "v_fma_mix_f32 v230, v230, v206, v214 op_sel_hi:[1,0,0]\n\t"
    "v_fma_mix_f32 v231, v231, v207, v215 op_sel_hi:[1,0,0]\n\t"
    "v_fma_mix_f32 v232, v232, v208, v216 op_sel_hi:[1,0,0]\n\t"
    "v_fma_mix_f32 v233, v233, v209, v217 op_sel_hi:[1,0,0]\n\t"
    "v_fma_mix_f32 v234, v234, v210, v218 op_sel_hi:[1,0,0]\n\t"
    "v_fma_mix_f32 v235, v235, v211, v219 op_sel_hi:[1,0,0]\n\t"
    "v_cvt_pk_bf16_f32 v248, v228, v229\n\t"
    "v_cvt_pk_bf16_f32 v249, v230, v231\n\t"
    "v_cvt_pk_bf16_f32 v250, v232, v233\n\t"
    "v_cvt_pk_bf16_f32 v251, v234, v235\n\t"
    "buffer_store_dwordx4 v[248:251], v200, s[48:51], s92 offen offset:768 sc1\n\t"
    "ds_write_b128 v200, v[248:251] offset:25344\n\t"
    "s_waitcnt vmcnt(21)\n\t"
    "v_fma_mix_f32 v236, v236, v204, v212 op_sel_hi:[1,0,0]\n\t"
    "v_fma_mix_f32 v237, v237, v205, v213 op_sel_hi:[1,0,0]\n\t"
    "v_fma_mix_f32 v238, v238, v206, v214 op_sel_hi:[1,0,0]\n\t"
    "v_fma_mix_f32 v239, v239, v207, v215 op_sel_hi:[1,0,0]\n\t"
    "v_fma_mix_f32 v240, v240, v208, v216 op_sel_hi:[1,0,0]\n\t"
    "v_fma_mix_f32 v241, v241, v209, v217 op_sel_hi:[1,0,0]\n\t"
    "v_fma_mix_f32 v242, v242, v210, v218 op_sel_hi:[1,0,0]\n\t"
    "v_fma_mix_f32 v243, v243, v211, v219 op_sel_hi:[1,0,0]\n\t"
    "v_cvt_pk_bf16_f32 v252, v236, v237\n\t"
    "v_cvt_pk_bf16_f32 v253, v238, v239\n\t"
    "v_cvt_pk_bf16_f32 v254, v240, v241\n\t"
    "v_cvt_pk_bf16_f32 v255, v242, v243\n\t"
    "buffer_store_dwordx4 v[252:255], v199, s[48:51], s92 offen offset:1536 sc1\n\t"
    "ds_write_b128 v199, v[252:255] offset:26112\n\t"
    "s_waitcnt vmcnt(20)\n\t"
    "v_fma_mix_f32 v160, v160, v204, v212 op_sel_hi:[1,0,0]\n\t"
    "v_fma_mix_f32 v161, v161, v205, v213 op_sel_hi:[1,0,0]\n\t"
    "v_fma_mix_f32 v162, v162, v206, v214 op_sel_hi:[1,0,0]\n\t"
    "v_fma_mix_f32 v163, v163, v207, v215 op_sel_hi:[1,0,0]\n\t"
    "v_fma_mix_f32 v164, v164, v208, v216 op_sel_hi:[1,0,0]\n\t"
    "v_fma_mix_f32 v165, v165, v209, v217 op_sel_hi:[1,0,0]\n\t"
    "v_fma_mix_f32 v166, v166, v210, v218 op_sel_hi:[1,0,0]\n\t"
    "v_fma_mix_f32 v167, v167, v211, v219 op_sel_hi:[1,0,0]\n\t"
    "v_cvt_pk_bf16_f32 v188, v160, v161\n\t"
    "v_cvt_pk_bf16_f32 v189, v162, v163\n\t"
    "v_cvt_pk_bf16_f32 v190, v164, v165\n\t"
    "v_cvt_pk_bf16_f32 v191, v166, v167\n\t"
    "buffer_store_dwordx4 v[188:191], v200, s[48:51], s92 offen offset:2304 sc1\n\t"
    "ds_write_b128 v200, v[188:191] offset:26880\n\t"
    "s_waitcnt vmcnt(19)\n\t"
    "v_lshlrev_b32 v204, 16, v96\n\t"
    "v_and_b32 v205, 0xffff0000, v96\n\t"
    "v_lshlrev_b32 v212, 16, v100\n\t"
    "v_and_b32 v213, 0xffff0000, v100\n\t"
    "v_lshlrev_b32 v206, 16, v97\n\t"
    "v_and_b32 v207, 0xffff0000, v97\n\t"
    "v_lshlrev_b32 v214, 16, v101\n\t"
    "v_and_b32 v215, 0xffff0000, v101\n\t"
    "v_lshlrev_b32 v208, 16, v98\n\t"
    "v_and_b32 v209, 0xffff0000, v98\n\t"
    "v_lshlrev_b32 v216, 16, v102\n\t"
    "v_and_b32 v217, 0xffff0000, v102\n\t"
    "v_lshlrev_b32 v210, 16, v99\n\t"
    "v_and_b32 v211, 0xffff0000, v99\n\t"
    "v_lshlrev_b32 v218, 16, v103\n\t"
    "v_and_b32 v219, 0xffff0000, v103\n\t"
    "v_mul_f32_e64 v212, -v212, v204\n\t"
    "v_mul_f32_e64 v213, -v213, v205\n\t"
    "v_mul_f32_e64 v214, -v214, v206\n\t"
    "v_mul_f32_e64 v215, -v215, v207\n\t"
    "v_mul_f32_e64 v216, -v216, v208\n\t"
    "v_mul_f32_e64 v217, -v217, v209\n\t"
    "v_mul_f32_e64 v218, -v218, v210\n\t"
    "v_mul_f32_e64 v219, -v219, v211\n\t"
    "v_mul_f32 v204, 0x4b800000, v204\n\t"
    "v_mul_f32 v205, 0x4b800000, v205\n\t"
    "v_mul_f32 v206, 0x4b800000, v206\n\t"
    "v_mul_f32 v207, 0x4b800000, v207\n\t"
    "v_mul_f32 v208, 0x4b800000, v208\n\t"
    "v_mul_f32 v209, 0x4b800000, v209\n\t"
    "v_mul_f32 v210, 0x4b800000, v210\n\t"
    "v_mul_f32 v211, 0x4b800000, v211\n\t"
    "s_waitcnt vmcnt(17)\n\t"
    "v_fma_mix_f32 v32, v32, v204, v212 op_sel_hi:[1,0,0]\n\t"
    "v_fma_mix_f32 v33, v33, v204, v212 op_sel_hi:[1,0,0]\n\t"
    "v_fma_mix_f32 v34, v34, v204, v212 op_sel_hi:[1,0,0]\n\t"
    "v_fma_mix_f32 v35, v35, v204, v212 op_sel_hi:[1,0,0]\n\t"
    "v_fma_mix_f32 v36, v36, v205, v213 op_sel_hi:[1,0,0]\n\t"
    "v_fma_mix_f32 v37, v37, v205, v213 op_sel_hi:[1,0,0]\n\t"
    "v_fma_mix_f32 v38, v38, v205, v213 op_sel_hi:[1,0,0]\n\t"
    "v_fma_mix_f32 v39, v39, v205, v213 op_sel_hi:[1,0,0]\n\t"
    "v_cvt_pk_bf16_f32 v244, v32, v36\n\t"
    "v_cvt_pk_bf16_f32 v248, v33, v37\n\t"
    "v_cvt_pk_bf16_f32 v252, v34, v38\n\t"
    "v_cvt_pk_bf16_f32 v188, v35, v39\n\t"
    "s_waitcnt vmcnt(15)\n\t"
    "v_fma_mix_f32 v40, v40, v206, v214 op_sel_hi:[1,0,0]\n\t"
    "v_fma_mix_f32 v41, v41, v206, v214 op_sel_hi:[1,0,0]\n\t"
    "v_fma_mix_f32 v42, v42, v206, v214 op_sel_hi:[1,0,0]\n\t"
    "v_fma_mix_f32 v43, v43, v206, v214 op_sel_hi:[1,0,0]\n\t"
    "v_fma_mix_f32 v44, v44, v207, v215 op_sel_hi:[1,0,0]\n\t"
    "v_fma_mix_f32 v45, v45, v207, v215 op_sel_hi:[1,0,0]\n\t"
    "v_fma_mix_f32 v46, v46, v207, v215 op_sel_hi:[1,0,0]\n\t"
    "v_fma_mix_f32 v47, v47, v207, v215 op_sel_hi:[1,0,0]\n\t"
    "v_cvt_pk_bf16_f32 v245, v40, v44\n\t"
    "v_cvt_pk_bf16_f32 v249, v41, v45\n\t"
    "v_cvt_pk_bf16_f32 v253, v42, v46\n\t"
    "v_cvt_pk_bf16_f32 v189, v43, v47\n\t"
    "s_waitcnt vmcnt(13)\n\t"
    "v_fma_mix_f32 v48, v48, v208, v216 op_sel_hi:[1,0,0]\n\t"
    "v_fma_mix_f32 v49, v49, v208, v216 op_sel_hi:[1,0,0]\n\t"
    "v_fma_mix_f32 v50, v50, v208, v216 op_sel_hi:[1,0,0]\n\t"
    "v_fma_mix_f32 v51, v51, v208, v216 op_sel_hi:[1,0,0]\n\t"
    "v_fma_mix_f32 v52, v52, v209, v217 op_sel_hi:[1,0,0]\n\t"
    "v_fma_mix_f32 v53, v53, v209, v217 op_sel_hi:[1,0,0]\n\t"
    "v_fma_mix_f32 v54, v54, v209, v217 op_sel_hi:[1,0,0]\n\t"
    "v_fma_mix_f32 v55, v55, v209, v217 op_sel_hi:[1,0,0]\n\t"
    "v_cvt_pk_bf16_f32 v246, v48, v52\n\t"
    "v_cvt_pk_bf16_f32 v250, v49, v53\n\t"
    "v_cvt_pk_bf16_f32 v254, v50, v54\n\t"
    "v_cvt_pk_bf16_f32 v190, v51, v55\n\t"
    "s_waitcnt vmcnt(11)\n\t"
    "v_fma_mix_f32 v56, v56, v210, v218 op_sel_hi:[1,0,0]\n\t"
    "v_fma_mix_f32 v57, v57, v210, v218 op_sel_hi:[1,0,0]\n\t"
    "v_fma_mix_f32 v58, v58, v210, v218 op_sel_hi:[1,0,0]\n\t"
    "v_fma_mix_f32 v59, v59, v210, v218 op_sel_hi:[1,0,0]\n\t"
    "v_fma_mix_f32 v60, v60, v211, v219 op_sel_hi:[1,0,0]\n\t"
    "v_fma_mix_f32 v61, v61, v211, v219 op_sel_hi:[1,0,0]\n\t"
    "v_fma_mix_f32 v62, v62, v211, v219 op_sel_hi:[1,0,0]\n\t"
    "v_fma_mix_f32 v63, v63, v211, v219 op_sel_hi:[1,0,0]\n\t"
    "v_cvt_pk_bf16_f32 v247, v56, v60\n\t"
    "v_cvt_pk_bf16_f32 v251, v57, v61\n\t"
    "v_cvt_pk_bf16_f32 v255, v58, v62\n\t"
    "v_cvt_pk_bf16_f32 v191, v59, v63\n\t"
    "buffer_store_dwordx4 v[244:247], v201, s[48:51], s92 offen offset:0 sc1\n\t"
    "buffer_store_dwordx4 v[248:251], v201, s[48:51], s92 offen offset:256 sc1\n\t"
    "buffer_store_dwordx4 v[252:255], v201, s[48:51], s92 offen offset:512 sc1\n\t"
    "buffer_store_dwordx4 v[188:191], v201, s[48:51], s92 offen offset:768 sc1\n\t"
    "ds_write_b128 v115, v[244:247] offset:0\n\t"
    "ds_write_b128 v115, v[248:251] offset:256\n\t"
    "ds_write_b128 v115, v[252:255] offset:512\n\t"
    "ds_write_b128 v115, v[188:191] offset:768\n\t"
    "s_mov_b32 exec_hi, -1\n\t"
    "s_add_u32 s92, s92, 196608\n\t"
    "global_load_dword v202, v181, s[58:59] sc1\n\t"
    "s_mov_b32 s96, 4096\n\t"
    "s_add_u32 m0, s37, 4096\n\t"
    "s_nop 0\n\t"
    "buffer_load_dwordx4 v104, s[52:55], s96 offen offset:2048 lds\n\t"
    "buffer_load_dwordx4 v105, s[52:55], s96 offen offset:3072 lds\n\t"
    "s_mov_b32 s96, 8192\n\t"
    "s_add_u32 m0, s37, 8192\n\t"
    "s_nop 0\n\t"
    "buffer_load_dwordx4 v106, s[52:55], s96 offen offset:0 lds\n\t"
    "buffer_load_dwordx4 v107, s[52:55], s96 offen offset:1024 lds\n\t"
    "buffer_load_dwordx4 v108, s[52:55], s96 offen offset:2048 lds\n\t"
    "buffer_load_dwordx4 v109, s[52:55], s96 offen offset:3072 lds\n\t"
    "v_accvgpr_write_b32 a0, 0\n\t"
    "v_accvgpr_write_b32 a1, 0\n\t"
    "v_accvgpr_write_b32 a2, 0\n\t"
    "v_accvgpr_write_b32 a3, 0\n\t"
    "v_accvgpr_write_b32 a4, 0\n\t"
    "v_accvgpr_write_b32 a5, 0\n\t"
    "v_accvgpr_write_b32 a6, 0\n\t"
    "v_accvgpr_write_b32 a7, 0\n\t"
    "v_accvgpr_write_b32 a8, 0\n\t"
    "v_accvgpr_write_b32 a9, 0\n\t"
    "v_accvgpr_write_b32 a10, 0\n\t"
    "v_accvgpr_write_b32 a11, 0\n\t"
    "v_accvgpr_write_b32 a12, 0\n\t"
    "v_accvgpr_write_b32 a13, 0\n\t"
    "v_accvgpr_write_b32 a14, 0\n\t"
    "v_accvgpr_write_b32 a15, 0\n\t"
    "v_accvgpr_write_b32 a16, 0\n\t"
    "v_accvgpr_write_b32 a17, 0\n\t"
    "v_accvgpr_write_b32 a18, 0\n\t"
    "v_accvgpr_write_b32 a19, 0\n\t"
    "v_accvgpr_write_b32 a20, 0\n\t"
    "v_accvgpr_write_b32 a21, 0\n\t"
    "v_accvgpr_write_b32 a22, 0\n\t"
    "v_accvgpr_write_b32 a23, 0\n\t"
    "v_accvgpr_write_b32 a24, 0\n\t"
    "v_accvgpr_write_b32 a25, 0\n\t"
    "v_accvgpr_write_b32 a26, 0\n\t"
    "v_accvgpr_write_b32 a27, 0\n\t"
    "v_accvgpr_write_b32 a28, 0\n\t"
    "v_accvgpr_write_b32 a29, 0\n\t"
    "v_accvgpr_write_b32 a30, 0\n\t"
    "v_accvgpr_write_b32 a31, 0\n\t"
    "v_accvgpr_write_b32 a32, 0\n\t"
    "v_accvgpr_write_b32 a33, 0\n\t"
    "v_accvgpr_write_b32 a34, 0\n\t"
    "v_accvgpr_write_b32 a35, 0\n\t"
    "v_accvgpr_write_b32 a36, 0\n\t"
    "v_accvgpr_write_b32 a37, 0\n\t"
    "v_accvgpr_write_b32 a38, 0\n\t"
    "v_accvgpr_write_b32 a39, 0\n\t"
    "v_accvgpr_write_b32 a40, 0\n\t"
    "v_accvgpr_write_b32 a41, 0\n\t"
    "v_accvgpr_write_b32 a42, 0\n\t"
    "v_accvgpr_write_b32 a43, 0\n\t"
    "v_accvgpr_write_b32 a44, 0\n\t"
    "v_accvgpr_write_b32 a45, 0\n\t"
    "v_accvgpr_write_b32 a46, 0\n\t"
    "v_accvgpr_write_b32 a47, 0\n\t"
    "v_accvgpr_write_b32 a48, 0\n\t"
    "v_accvgpr_write_b32 a49, 0\n\t"
    "v_accvgpr_write_b32 a50, 0\n\t"
    "v_accvgpr_write_b32 a51, 0\n\t"
    "v_accvgpr_write_b32 a52, 0\n\t"
    "v_accvgpr_write_b32 a53, 0\n\t"
    "v_accvgpr_write_b32 a54, 0\n\t"
    "v_accvgpr_write_b32 a55, 0\n\t"
    "v_accvgpr_write_b32 a56, 0\n\t"
    "v_accvgpr_write_b32 a57, 0\n\t"
    "v_accvgpr_write_b32 a58, 0\n\t"
    "v_accvgpr_write_b32 a59, 0\n\t"
    "v_accvgpr_write_b32 a60, 0\n\t"
    "v_accvgpr_write_b32 a61, 0\n\t"
    "v_accvgpr_write_b32 a62, 0\n\t"
    "v_accvgpr_write_b32 a63, 0\n\t"
    "v_accvgpr_write_b32 a64, 0\n\t"
    "v_accvgpr_write_b32 a65, 0\n\t"
    "v_accvgpr_write_b32 a66, 0\n\t"
    "v_accvgpr_write_b32 a67, 0\n\t"
    "v_accvgpr_write_b32 a68, 0\n\t"
    "v_accvgpr_write_b32 a69, 0\n\t"
    "v_accvgpr_write_b32 a70, 0\n\t"
    "v_accvgpr_write_b32 a71, 0\n\t"
    "v_accvgpr_write_b32 a72, 0\n\t"
    "v_accvgpr_write_b32 a73, 0\n\t"
    "v_accvgpr_write_b32 a74, 0\n\t"
    "v_accvgpr_write_b32 a75, 0\n\t"
    "v_accvgpr_write_b32 a76, 0\n\t"
    "v_accvgpr_write_b32 a77, 0\n\t"
    "v_accvgpr_write_b32 a78, 0\n\t"
    "v_accvgpr_write_b32 a79, 0\n\t"
    "v_accvgpr_write_b32 a80, 0\n\t"
    "v_accvgpr_write_b32 a81, 0\n\t"
    "v_accvgpr_write_b32 a82, 0\n\t"
    "v_accvgpr_write_b32 a83, 0\n\t"
    "v_accvgpr_write_b32 a84, 0\n\t"
    "v_accvgpr_write_b32 a85, 0\n\t"
    "v_accvgpr_write_b32 a86, 0\n\t"
    "v_accvgpr_write_b32 a87, 0\n\t"
    "v_accvgpr_write_b32 a88, 0\n\t"
    "v_accvgpr_write_b32 a89, 0\n\t"
    "v_accvgpr_write_b32 a90, 0\n\t"
    "v_accvgpr_write_b32 a91, 0\n\t"
    "v_accvgpr_write_b32 a92, 0\n\t"
    "v_accvgpr_write_b32 a93, 0\n\t"
    "v_accvgpr_write_b32 a94, 0\n\t"
    "v_accvgpr_write_b32 a95, 0\n\t"
    "v_accvgpr_write_b32 a96, 0\n\t"
    "v_accvgpr_write_b32 a97, 0\n\t"
    "v_accvgpr_write_b32 a98, 0\n\t"
    "v_accvgpr_write_b32 a99, 0\n\t"
    "v_accvgpr_write_b32 a100, 0\n\t"
    "v_accvgpr_write_b32 a101, 0\n\t"
    "v_accvgpr_write_b32 a102, 0\n\t"
    "v_accvgpr_write_b32 a103, 0\n\t"
    "v_accvgpr_write_b32 a104, 0\n\t"
    "v_accvgpr_write_b32 a105, 0\n\t"
    "v_accvgpr_write_b32 a106, 0\n\t"
    "v_accvgpr_write_b32 a107, 0\n\t"
    "v_accvgpr_write_b32 a108, 0\n\t"
    "v_accvgpr_write_b32 a109, 0\n\t"
    "v_accvgpr_write_b32 a110, 0\n\t"
    "v_accvgpr_write_b32 a111, 0\n\t"
    "v_accvgpr_write_b32 a112, 0\n\t"
    "v_accvgpr_write_b32 a113, 0\n\t"
    "v_accvgpr_write_b32 a114, 0\n\t"
    "v_accvgpr_write_b32 a115, 0\n\t"
    "v_accvgpr_write_b32 a116, 0\n\t"
    "v_accvgpr_write_b32 a117, 0\n\t"
    "v_accvgpr_write_b32 a118, 0\n\t"
    "v_accvgpr_write_b32 a119, 0\n\t"
    "v_accvgpr_write_b32 a120, 0\n\t"
    "v_accvgpr_write_b32 a121, 0\n\t"
    "v_accvgpr_write_b32 a122, 0\n\t"
    "v_accvgpr_write_b32 a123, 0\n\t"
    "v_accvgpr_write_b32 a124, 0\n\t"
    "v_accvgpr_write_b32 a125, 0\n\t"
    "v_accvgpr_write_b32 a126, 0\n\t"
    "v_accvgpr_write_b32 a127, 0\n\t"
    "v_accvgpr_write_b32 a128, 0\n\t"
    "v_accvgpr_write_b32 a129, 0\n\t"
    "v_accvgpr_write_b32 a130, 0\n\t"
    "v_accvgpr_write_b32 a131, 0\n\t"
    "v_accvgpr_write_b32 a132, 0\n\t"
    "v_accvgpr_write_b32 a133, 0\n\t"
    "v_accvgpr_write_b32 a134, 0\n\t"
    "v_accvgpr_write_b32 a135, 0\n\t"
    "v_accvgpr_write_b32 a136, 0\n\t"
    "v_accvgpr_write_b32 a137, 0\n\t"
    "v_accvgpr_write_b32 a138, 0\n\t"
    "v_accvgpr_write_b32 a139, 0\n\t"
    "v_accvgpr_write_b32 a140, 0\n\t"
    "v_accvgpr_write_b32 a141, 0\n\t"
    "v_accvgpr_write_b32 a142, 0\n\t"
    "v_accvgpr_write_b32 a143, 0\n\t"
    "v_accvgpr_write_b32 a144, 0\n\t"
    "v_accvgpr_write_b32 a145, 0\n\t"
    "v_accvgpr_write_b32 a146, 0\n\t"
    "v_accvgpr_write_b32 a147, 0\n\t"
    "v_accvgpr_write_b32 a148, 0\n\t"
    "v_accvgpr_write_b32 a149, 0\n\t"
    "v_accvgpr_write_b32 a150, 0\n\t"
    "v_accvgpr_write_b32 a151, 0\n\t"
    "v_accvgpr_write_b32 a152, 0\n\t"
    "v_accvgpr_write_b32 a153, 0\n\t"
    "v_accvgpr_write_b32 a154, 0\n\t"
    "v_accvgpr_write_b32 a155, 0\n\t"
    "v_accvgpr_write_b32 a156, 0\n\t"
    "v_accvgpr_write_b32 a157, 0\n\t"
    "v_accvgpr_write_b32 a158, 0\n\t"
    "v_accvgpr_write_b32 a159, 0\n\t"
    "v_accvgpr_write_b32 a160, 0\n\t"
    "v_accvgpr_write_b32 a161, 0\n\t"
    "v_accvgpr_write_b32 a162, 0\n\t"
    "v_accvgpr_write_b32 a163, 0\n\t"
    "v_accvgpr_write_b32 a164, 0\n\t"
    "v_accvgpr_write_b32 a165, 0\n\t"
    "v_accvgpr_write_b32 a166, 0\n\t"
    "v_accvgpr_write_b32 a167, 0\n\t"
    "v_accvgpr_write_b32 a168, 0\n\t"
    "v_accvgpr_write_b32 a169, 0\n\t"
    "v_accvgpr_write_b32 a170, 0\n\t"
    "v_accvgpr_write_b32 a171, 0\n\t"
    "v_accvgpr_write_b32 a172, 0\n\t"
    "v_accvgpr_write_b32 a173, 0\n\t"
    "v_accvgpr_write_b32 a174, 0\n\t"
    "v_accvgpr_write_b32 a175, 0\n\t"
    "v_accvgpr_write_b32 a176, 0\n\t"
    "v_accvgpr_write_b32 a177, 0\n\t"
    "v_accvgpr_write_b32 a178, 0\n\t"
    "v_accvgpr_write_b32 a179, 0\n\t"
    "v_accvgpr_write_b32 a180, 0\n\t"
    "v_accvgpr_write_b32 a181, 0\n\t"
    "v_accvgpr_write_b32 a182, 0\n\t"
    "v_accvgpr_write_b32 a183, 0\n\t"
    "v_accvgpr_write_b32 a184, 0\n\t"
    "v_accvgpr_write_b32 a185, 0\n\t"
    "v_accvgpr_write_b32 a186, 0\n\t"
    "v_accvgpr_write_b32 a187, 0\n\t"
    "v_accvgpr_write_b32 a188, 0\n\t"
    "v_accvgpr_write_b32 a189, 0\n\t"
    "v_accvgpr_write_b32 a190, 0\n\t"
    "v_accvgpr_write_b32 a191, 0\n\t"
    "v_mov_b32 v168, 0\n\t"
    "v_mov_b32 v169, 0\n\t"
    "v_mov_b32 v170, 0\n\t"
    "v_mov_b32 v171, 0\n\t"
    "s_waitcnt vmcnt(6)\n\t"
    "global_store_dwordx2 v184, v[202:203], s[56:57] sc1\n\t"
    "s_sleep 30\n\t"
    "global_load_dwordx2 v[120:121], v182, s[90:91] sc1\n\t"
    "s_mov_b32 s96, 12288\n\t"
    "s_add_u32 m0, s37, 12288\n\t"
    "s_nop 0\n\t"
    "buffer_load_dwordx4 v104, s[52:55], s96 offen offset:0 lds\n\t"
    "buffer_load_dwordx4 v105, s[52:55], s96 offen offset:1024 lds\n\t"
    "buffer_load_dwordx4 v106, s[52:55], s96 offen offset:2048 lds\n\t"
    "buffer_load_dwordx4 v107, s[52:55], s96 offen offset:3072 lds\n\t"
    "s_mov_b32 s96, 16384\n\t"
    "s_add_u32 m0, s37, 16384\n\t"
    "s_nop 0\n\t"
    "buffer_load_dwordx4 v108, s[52:55], s96 offen offset:0 lds\n\t"
    "buffer_load_dwordx4 v109, s[52:55], s96 offen offset:1024 lds\n\t"
    "buffer_load_dwordx4 v104, s[52:55], s96 offen offset:2048 lds\n\t"
    "buffer_load_dwordx4 v105, s[52:55], s96 offen offset:3072 lds\n\t"
    "s_mov_b32 s96, 20480\n\t"
    "s_add_u32 m0, s37, 20480\n\t"
    "s_nop 0\n\t"
    "buffer_load_dwordx4 v106, s[52:55], s96 offen offset:0 lds\n\t"
    "buffer_load_dwordx4 v107, s[52:55], s96 offen offset:1024 lds\n\t"
    "buffer_load_dwordx4 v108, s[52:55], s96 offen offset:2048 lds\n\t"
    "buffer_load_dwordx4 v109, s[52:55], s96 offen offset:3072 lds\n\t"
    "s_sub_u32 s52, s80, 2048\n\t"
    "s_subb_u32 s53, s81, 0\n\t"
    "s_and_b32 s53, s53, 0xffff\n\t"
    "s_mov_b32 s54, 6144\n\t"
    "s_add_u32 s80, s80, 196608\n\t"
    "s_addc_u32 s81, s81, 0\n\t"
    "s_waitcnt vmcnt(12)\n\t"
    "v_subrev_u32 v120, s63, v120\n\t"
    "v_cmp_lt_u32 vcc, 7, v120\n\t"
    "s_cbranch_vccnz L_spin_p\n\t"
    "L_spinret_p:\n\t"
    "v_mov_b32 v183, v121\n\t"
    "v_mov_b32 v202, v121\n\t"
    "global_store_dwordx2 v184, v[202:203], s[56:57] sc1\n\t"
    "s_add_u32 s63, s63, 1\n\t"
    "s_add_u32 m0, s60, 36864\n\t"
    "s_nop 0\n\t"
    "buffer_load_dwordx4 v195, s[40:43], s65 offen offset:0 sc1 lds\n\t"
    "buffer_load_dwordx4 v195, s[40:43], s65 offen offset:1024 sc1 lds\n\t"
    "buffer_load_dwordx4 v195, s[40:43], s65 offen offset:2048 sc1 lds\n\t"
    "v_add_u32 v181, 73728, v194\n\t"
    "s_waitcnt lgkmcnt(0)\n\t"
    "s_barrier\n\t"
    "ds_read_b128 v[64:67], v192 offset:0\n\t"
    "ds_read_b128 v[68:71], v193 offset:0\n\t"
    "ds_read_b128 v[72:75], v192 offset:128\n\t"
    "ds_read_b128 v[76:79], v193 offset:128\n\t"
    "ds_read_b128 v[80:83], v192 offset:256\n\t"
    "ds_read_b128 v[84:87], v193 offset:256\n\t"
    "ds_read_b128 v[88:91], v192 offset:1536\n\t"
    "ds_read_b128 v[92:95], v193 offset:1536\n\t"
    "ds_read_b128 v[204:207], v192 offset:1664\n\t"
    "ds_read_b128 v[208:211], v193 offset:1664\n\t"
    "ds_read_b128 v[212:215], v192 offset:1792\n\t"
    "ds_read_b128 v[216:219], v193 offset:1792\n\t"
    "s_waitcnt vmcnt(34)\n\t"
    "ds_read_b128 a[192:195], v110 offset:0\n\t"
    "ds_read_b128 a[196:199], v111 offset:0\n\t"
    "ds_read_b128 a[200:203], v110 offset:128\n\t"
    "ds_read_b128 a[204:207], v111 offset:128\n\t"
    "ds_read_b128 v[0:3], v110 offset:256\n\t"
    "ds_read_b128 v[4:7], v111 offset:256\n\t"
    "s_waitcnt lgkmcnt(5)\n\t"
    "v_mfma_f32_16x16x32_bf16 v[32:35], v[64:67], a[192:195], 0\n\t"
    "v_mfma_f32_16x16x32_bf16 v[48:51], v[88:91], a[192:195], 0\n\t"
    "s_waitcnt lgkmcnt(4)\n\t"
    "v_mfma_f32_16x16x32_bf16 v[32:35], v[68:71], a[196:199], v[32:35]\n\t"
    "v_mfma_f32_16x16x32_bf16 v[48:51], v[92:95], a[196:199], v[48:51]\n\t"
    "s_waitcnt lgkmcnt(3)\n\t"
    "v_mfma_f32_16x16x32_bf16 v[32:35], v[72:75], a[200:203], v[32:35]\n\t"
    "v_mfma_f32_16x16x32_bf16 v[48:51], v[204:207], a[200:203], v[48:51]\n\t"
    "s_waitcnt lgkmcnt(2)\n\t"
    "v_mfma_f32_16x16x32_bf16 v[32:35], v[76:79], a[204:207], v[32:35]\n\t"
    "v_mfma_f32_16x16x32_bf16 v[48:51], v[208:211], a[204:207], v[48:51]\n\t"
    "s_waitcnt lgkmcnt(1)\n\t"
    "v_mfma_f32_16x16x32_bf16 v[32:35], v[80:83], v[0:3], v[32:35]\n\t"
    "v_mfma_f32_16x16x32_bf16 v[48:51], v[212:215], v[0:3], v[48:51]\n\t"
    "s_waitcnt lgkmcnt(0)\n\t"
    "v_mfma_f32_16x16x32_bf16 v[32:35], v[84:87], v[4:7], v[32:35]\n\t"
    "v_mfma_f32_16x16x32_bf16 v[48:51], v[216:219], v[4:7], v[48:51]\n\t"
    "s_add_u32 m0, s60, 49152\n\t"
    "s_nop 0\n\t"
    "buffer_load_dwordx4 v195, s[40:43], s66 offen offset:0 sc1 lds\n\t"
    "buffer_load_dwordx4 v195, s[40:43], s66 offen offset:1024 sc1 lds\n\t"
    "buffer_load_dwordx4 v195, s[40:43], s66 offen offset:2048 sc1 lds\n\t"
    "global_load_dwordx4 v[252:255], v198, s[82:83]\n\t"
    "global_load_dwordx4 v[188:191], v198, s[84:85]\n\t"
    "global_load_dwordx4 v[220:223], v196, s[78:79] offset:-2048\n\t"
    "global_load_dwordx4 v[224:227], v196, s[78:79] offset:-2032\n\t"
    "s_waitcnt vmcnt(25)\n\t"
    "ds_read_b128 a[208:211], v110 offset:6144\n\t"
    "ds_read_b128 a[212:215], v111 offset:6144\n\t"
    "ds_read_b128 a[216:219], v110 offset:6272\n\t"
    "ds_read_b128 a[220:223], v111 offset:6272\n\t"
    "ds_read_b128 v[8:11], v110 offset:6400\n\t"
    "ds_read_b128 v[12:15], v111 offset:6400\n\t"
    "s_nop 1\n\t"
    "v_max3_f32 v122, v32, v33, v34\n\t"
    "v_max3_f32 v122, v122, v35, v48\n\t"
    "v_max3_f32 v122, v122, v49, v50\n\t"
    "v_max_f32 v122, v122, v51\n\t"
    "v_mov_b32 v123, v122\n\t"
    "s_nop 1\n\t"
    "v_permlane32_swap_b32 v122, v123\n\t"
    "s_nop 1\n\t"
    "v_max_f32 v122, v122, v123\n\t"
    "v_mov_b32 v123, v122\n\t"
    "s_nop 1\n\t"
    "v_permlane16_swap_b32 v122, v123\n\t"
    "s_nop 1\n\t"
    "v_max_f32 v122, v122, v123\n\t"
    "v_sub_f32 v128, 0, v122\n\t"
    "v_sub_f32 v129, 0, v122\n\t"
    "v_sub_f32 v130, 0, v122\n\t"
    "v_sub_f32 v131, 0, v122\n\t"
    "v_sub_f32 v32, v32, v122\n\t"
    "v_sub_f32 v33, v33, v122\n\t"
    "v_sub_f32 v34, v34, v122\n\t"
    "v_sub_f32 v35, v35, v122\n\t"
    "v_sub_f32 v48, v48, v122\n\t"
    "v_sub_f32 v49, v49, v122\n\t"
    "v_sub_f32 v50, v50, v122\n\t"
    "v_sub_f32 v51, v51, v122\n\t"
    "v_exp_f32 v176, v32\n\t"
    "v_exp_f32 v177, v33\n\t"
    "s_nop 0\n\t"
    "v_add_f32 v172, v176, v177\n\t"
    "v_cvt_pk_bf16_f32 v96, v176, v177\n\t"
    "v_exp_f32 v178, v34\n\t"
    "v_exp_f32 v179, v35\n\t"
    "s_nop 0\n\t"
    "v_add_f32 v172, v172, v178\n\t"
    "v_add_f32 v172, v172, v179\n\t"
    "v_cvt_pk_bf16_f32 v97, v178, v179\n\t"
    "v_exp_f32 v176, v48\n\t"
    "v_exp_f32 v177, v49\n\t"
    "s_nop 0\n\t"
    "v_add_f32 v172, v172, v176\n\t"
    "v_add_f32 v172, v172, v177\n\t"
    "v_cvt_pk_bf16_f32 v98, v176, v177\n\t"
    "v_exp_f32 v178, v50\n\t"
    "v_exp_f32 v179, v51\n\t"
    "s_nop 0\n\t"
    "v_add_f32 v172, v172, v178\n\t"
    "v_add_f32 v172, v172, v179\n\t"
    "v_cvt_pk_bf16_f32 v99, v178, v179\n\t"
    "s_waitcnt lgkmcnt(5)\n\t"
    "v_mfma_f32_16x16x32_bf16 v[36:39], v[64:67], a[208:211], 0\n\t"
    "v_mfma_f32_16x16x32_bf16 v[52:55], v[88:91], a[208:211], 0\n\t"
    "s_waitcnt lgkmcnt(4)\n\t"
    "v_mfma_f32_16x16x32_bf16 v[36:39], v[68:71], a[212:215], v[36:39]\n\t"
    "v_mfma_f32_16x16x32_bf16 v[52:55], v[92:95], a[212:215], v[52:55]\n\t"
    "s_waitcnt lgkmcnt(3)\n\t"
    "v_mfma_f32_16x16x32_bf16 v[36:39], v[72:75], a[216:219], v[36:39]\n\t"
    "v_mfma_f32_16x16x32_bf16 v[52:55], v[204:207], a[216:219], v[52:55]\n\t"
    "s_waitcnt lgkmcnt(2)\n\t"
    "v_mfma_f32_16x16x32_bf16 v[36:39], v[76:79], a[220:223], v[36:39]\n\t"
    "v_mfma_f32_16x16x32_bf16 v[52:55], v[208:211], a[220:223], v[52:55]\n\t"
    "s_waitcnt lgkmcnt(1)\n\t"
    "v_mfma_f32_16x16x32_bf16 v[36:39], v[80:83], v[8:11], v[36:39]\n\t"
    "v_mfma_f32_16x16x32_bf16 v[52:55], v[212:215], v[8:11], v[52:55]\n\t"
    "s_waitcnt lgkmcnt(0)\n\t"
    "v_mfma_f32_16x16x32_bf16 v[36:39], v[84:87], v[12:15], v[36:39]\n\t"
    "v_mfma_f32_16x16x32_bf16 v[52:55], v[216:219], v[12:15], v[52:55]\n\t"
    "s_add_u32 m0, s60, 12288\n\t"
    "s_nop 0\n\t"
    "buffer_load_dwordx4 v195, s[44:47], s65 offen offset:0 sc1 lds\n\t"
    "buffer_load_dwordx4 v195, s[44:47], s65 offen offset:1024 sc1 lds\n\t"
    "buffer_load_dwordx4 v195, s[44:47], s65 offen offset:2048 sc1 lds\n\t"
    "global_load_dwordx4 v[228:231], v196, s[78:79] offset:-512\n\t"
    "global_load_dwordx4 v[232:235], v196, s[78:79] offset:-496\n\t"
    "s_waitcnt vmcnt(22)\n\t"
    "ds_read_b128 a[224:227], v110 offset:12288\n\t"
    "ds_read_b128 a[228:231], v111 offset:12288\n\t"
    "ds_read_b128 a[232:235], v110 offset:12416\n\t"
    "ds_read_b128 a[236:239], v111 offset:12416\n\t"
    "ds_read_b128 v[16:19], v110 offset:12544\n\t"
    "ds_read_b128 v[20:23], v111 offset:12544\n\t"
    "s_nop 1\n\t"
    "v_max3_f32 v122, v36, v37, v38\n\t"
    "v_max3_f32 v122, v122, v39, v52\n\t"
    "v_max3_f32 v122, v122, v53, v54\n\t"
    "v_max_f32 v122, v122, v55\n\t"
    "v_mov_b32 v123, v122\n\t"
    "s_nop 1\n\t"
    "v_permlane32_swap_b32 v122, v123\n\t"
    "s_nop 1\n\t"
    "v_max_f32 v122, v122, v123\n\t"
    "v_mov_b32 v123, v122\n\t"
    "s_nop 1\n\t"
    "v_permlane16_swap_b32 v122, v123\n\t"
    "s_nop 1\n\t"
    "v_max_f32 v122, v122, v123\n\t"
    "v_sub_f32 v132, 0, v122\n\t"
    "v_sub_f32 v133, 0, v122\n\t"
    "v_sub_f32 v134, 0, v122\n\t"
    "v_sub_f32 v135, 0, v122\n\t"
    "v_sub_f32 v36, v36, v122\n\t"
    "v_sub_f32 v37, v37, v122\n\t"
    "v_sub_f32 v38, v38, v122\n\t"
    "v_sub_f32 v39, v39, v122\n\t"
    "v_sub_f32 v52, v52, v122\n\t"
    "v_sub_f32 v53, v53, v122\n\t"
    "v_sub_f32 v54, v54, v122\n\t"
    "v_sub_f32 v55, v55, v122\n\t"
    "v_exp_f32 v176, v36\n\t"
    "v_exp_f32 v177, v37\n\t"
    "s_nop 0\n\t"
    "v_add_f32 v173, v176, v177\n\t"
    "v_cvt_pk_bf16_f32 v100, v176, v177\n\t"
    "v_exp_f32 v178, v38\n\t"
    "v_exp_f32 v179, v39\n\t"
    "s_nop 0\n\t"
    "v_add_f32 v173, v173, v178\n\t"
    "v_add_f32 v173, v173, v179\n\t"
    "v_cvt_pk_bf16_f32 v101, v178, v179\n\t"
    "v_exp_f32 v176, v52\n\t"
    "v_exp_f32 v177, v53\n\t"
    "s_nop 0\n\t"
    "v_add_f32 v173, v173, v176\n\t"
    "v_add_f32 v173, v173, v177\n\t"
    "v_cvt_pk_bf16_f32 v102, v176, v177\n\t"
    "v_exp_f32 v178, v54\n\t"
    "v_exp_f32 v179, v55\n\t"
    "s_nop 0\n\t"
    "v_add_f32 v173, v173, v178\n\t"
    "v_add_f32 v173, v173, v179\n\t"
    "v_cvt_pk_bf16_f32 v103, v178, v179\n\t"
    "s_waitcnt lgkmcnt(5)\n\t"
    "v_mfma_f32_16x16x32_bf16 v[40:43], v[64:67], a[224:227], 0\n\t"
    "v_mfma_f32_16x16x32_bf16 v[56:59], v[88:91], a[224:227], 0\n\t"
    "s_waitcnt lgkmcnt(4)\n\t"
    "v_mfma_f32_16x16x32_bf16 v[40:43], v[68:71], a[228:231], v[40:43]\n\t"
    "v_mfma_f32_16x16x32_bf16 v[56:59], v[92:95], a[228:231], v[56:59]\n\t"
    "s_waitcnt lgkmcnt(3)\n\t"
    "v_mfma_f32_16x16x32_bf16 v[40:43], v[72:75], a[232:235], v[40:43]\n\t"
    "v_mfma_f32_16x16x32_bf16 v[56:59], v[204:207], a[232:235], v[56:59]\n\t"
    "s_waitcnt lgkmcnt(2)\n\t"
    "v_mfma_f32_16x16x32_bf16 v[40:43], v[76:79], a[236:239], v[40:43]\n\t"
    "v_mfma_f32_16x16x32_bf16 v[56:59], v[208:211], a[236:239], v[56:59]\n\t"
    "s_waitcnt lgkmcnt(1)\n\t"
    "v_mfma_f32_16x16x32_bf16 v[40:43], v[80:83], v[16:19], v[40:43]\n\t"
    "v_mfma_f32_16x16x32_bf16 v[56:59], v[212:215], v[16:19], v[56:59]\n\t"
    "s_waitcnt lgkmcnt(0)\n\t"
    "v_mfma_f32_16x16x32_bf16 v[40:43], v[84:87], v[20:23], v[40:43]\n\t"
    "v_mfma_f32_16x16x32_bf16 v[56:59], v[216:219], v[20:23], v[56:59]\n\t"
    "global_load_dwordx4 v[236:239], v196, s[78:79] offset:1024\n\t"
    "global_load_dwordx4 v[240:243], v196, s[78:79] offset:1040\n\t"
    "s_waitcnt vmcnt(18)\n\t"
    "ds_read_b128 a[240:243], v110 offset:18432\n\t"
    "ds_read_b128 a[244:247], v111 offset:18432\n\t"
    "ds_read_b128 a[248:251], v110 offset:18560\n\t"
    "ds_read_b128 a[252:255], v111 offset:18560\n\t"
    "ds_read_b128 v[24:27], v110 offset:18688\n\t"
    "ds_read_b128 v[28:31], v111 offset:18688\n\t"
    "s_nop 1\n\t"
    "v_max3_f32 v122, v40, v41, v42\n\t"
    "v_max3_f32 v122, v122, v43, v56\n\t"
    "v_max3_f32 v122, v122, v57, v58\n\t"
    "v_max_f32 v122, v122, v59\n\t"
    "v_mov_b32 v123, v122\n\t"
    "s_nop 1\n\t"
    "v_permlane32_swap_b32 v122, v123\n\t"
    "s_nop 1\n\t"
    "v_max_f32 v122, v122, v123\n\t"
    "v_mov_b32 v123, v122\n\t"
    "s_nop 1\n\t"
    "v_permlane16_swap_b32 v122, v123\n\t"
    "s_nop 1\n\t"
    "v_max_f32 v122, v122, v123\n\t"
    "v_sub_f32 v136, 0, v122\n\t"
    "v_sub_f32 v137, 0, v122\n\t"
    "v_sub_f32 v138, 0, v122\n\t"
    "v_sub_f32 v139, 0, v122\n\t"
    "v_sub_f32 v40, v40, v122\n\t"
    "v_sub_f32 v41, v41, v122\n\t"
    "v_sub_f32 v42, v42, v122\n\t"
    "v_sub_f32 v43, v43, v122\n\t"
    "v_sub_f32 v56, v56, v122\n\t"
    "v_sub_f32 v57, v57, v122\n\t"
    "v_sub_f32 v58, v58, v122\n\t"
    "v_sub_f32 v59, v59, v122\n\t"
    "s_waitcnt lgkmcnt(5)\n\t"
    "v_mfma_f32_16x16x32_bf16 v[44:47], v[64:67], a[240:243], 0\n\t"
    "v_mfma_f32_16x16x32_bf16 v[60:63], v[88:91], a[240:243], 0\n\t"
    "s_waitcnt lgkmcnt(4)\n\t"
    "v_mfma_f32_16x16x32_bf16 v[44:47], v[68:71], a[244:247], v[44:47]\n\t"
    "v_mfma_f32_16x16x32_bf16 v[60:63], v[92:95], a[244:247], v[60:63]\n\t"
    "s_waitcnt lgkmcnt(3)\n\t"
    "v_mfma_f32_16x16x32_bf16 v[44:47], v[72:75], a[248:251], v[44:47]\n\t"
    "v_mfma_f32_16x16x32_bf16 v[60:63], v[204:207], a[248:251], v[60:63]\n\t"
    "s_waitcnt lgkmcnt(2)\n\t"
    "v_mfma_f32_16x16x32_bf16 v[44:47], v[76:79], a[252:255], v[44:47]\n\t"
    "v_mfma_f32_16x16x32_bf16 v[60:63], v[208:211], a[252:255], v[60:63]\n\t"
    "s_waitcnt lgkmcnt(1)\n\t"
    "v_mfma_f32_16x16x32_bf16 v[44:47], v[80:83], v[24:27], v[44:47]\n\t"
    "v_mfma_f32_16x16x32_bf16 v[60:63], v[212:215], v[24:27], v[60:63]\n\t"
    "s_waitcnt lgkmcnt(0)\n\t"
    "v_mfma_f32_16x16x32_bf16 v[44:47], v[84:87], v[28:31], v[44:47]\n\t"
    "v_mfma_f32_16x16x32_bf16 v[60:63], v[216:219], v[28:31], v[60:63]\n\t"
    "global_load_dwordx4 v[160:163], v196, s[78:79] offset:2560\n\t"
    "global_load_dwordx4 v[164:167], v196, s[78:79] offset:2576\n\t"
    "s_add_u32 s78, s78, 196608\n\t"
    "s_addc_u32 s79, s79, 0\n\t"
    "s_add_u32 s82, s82, 3072\n\t"
    "s_addc_u32 s83, s83, 0\n\t"
    "s_add_u32 s84, s84, 3072\n\t"
    "s_addc_u32 s85, s85, 0\n\t"
    "s_waitcnt vmcnt(16)\n\t"
    "s_barrier\n\t"
    "ds_read_b128 v[144:147], v192 offset:12288\n\t"
    "ds_read_b128 v[148:151], v192 offset:13824\n\t"
    "s_add_u32 m0, s60, 61440\n\t"
    "s_nop 0\n\t"
    "buffer_load_dwordx4 v195, s[40:43], s67 offen offset:0 sc1 lds\n\t"
    "buffer_load_dwordx4 v195, s[40:43], s67 offen offset:1024 sc1 lds\n\t"
    "s_lshl_b32 s36, s60, 1\n\t"
    "s_add_u32 s36, s36, 98304\n\t"
    "v_max3_f32 v122, v44, v45, v46\n\t"
    "v_max3_f32 v122, v122, v47, v60\n\t"
    "v_max3_f32 v122, v122, v61, v62\n\t"
    "v_max_f32 v122, v122, v63\n\t"
    "v_mov_b32 v123, v122\n\t"
    "s_nop 1\n\t"
    "v_permlane32_swap_b32 v122, v123\n\t"
    "s_nop 1\n\t"
    "v_max_f32 v122, v122, v123\n\t"
    "v_mov_b32 v123, v122\n\t"
    "s_nop 1\n\t"
    "v_permlane16_swap_b32 v122, v123\n\t"
    "s_nop 1\n\t"
    "v_max_f32 v122, v122, v123\n\t"
    "v_sub_f32 v140, 0, v122\n\t"
    "v_sub_f32 v141, 0, v122\n\t"
    "v_sub_f32 v142, 0, v122\n\t"
    "v_sub_f32 v143, 0, v122\n\t"
    "v_sub_f32 v44, v44, v122\n\t"
    "v_sub_f32 v45, v45, v122\n\t"
    "v_sub_f32 v46, v46, v122\n\t"
    "v_sub_f32 v47, v47, v122\n\t"
    "v_sub_f32 v60, v60, v122\n\t"
    "v_sub_f32 v61, v61, v122\n\t"
    "v_sub_f32 v62, v62, v122\n\t"
    "v_sub_f32 v63, v63, v122\n\t"
    "s_waitcnt lgkmcnt(1)\n\t"
    "v_mfma_f32_16x16x32_bf16 v[64:67], v[144:147], a[192:195], v[128:131]\n\t"
    "s_waitcnt lgkmcnt(0)\n\t"
    "ds_read_b128 v[152:155], v193 offset:12288\n\t"
    "v_mfma_f32_16x16x32_bf16 v[80:83], v[148:151], a[192:195], v[128:131]\n\t"
    "ds_read_b128 v[156:159], v193 offset:13824\n\t"
    "v_mfma_f32_16x16x32_bf16 v[84:87], v[148:151], a[208:211], v[132:135]\n\t"
    "v_exp_f32 v176, v40\n\t"
    "v_mfma_f32_16x16x32_bf16 v[68:71], v[144:147], a[208:211], v[132:135]\n\t"
    "v_exp_f32 v177, v41\n\t"
    "v_mfma_f32_16x16x32_bf16 v[72:75], v[144:147], a[224:227], v[136:139]\n\t"
    "buffer_load_dwordx4 v195, s[40:43], s67 offen offset:2048 sc1 lds\n\t"
    "v_mfma_f32_16x16x32_bf16 v[88:91], v[148:151], a[224:227], v[136:139]\n\t"
    "s_mov_b32 s96, 0\n\t"
    "s_add_u32 m0, s36, 0\n\t"
    "s_nop 0\n\t"
    "buffer_load_dwordx4 v195, s[52:55], s96 offen lds\n\t"
    "v_exp_f32 v178, v42\n\t"
    "v_mfma_f32_16x16x32_bf16 v[92:95], v[148:151], a[240:243], v[140:143]\n\t"
    "s_mov_b32 s97, 1024\n\t"
    "s_add_u32 m0, s36, 1024\n\t"
    "s_nop 0\n\t"
    "buffer_load_dwordx4 v195, s[52:55], s97 offen lds\n\t"
    "v_add_f32 v174, v176, v177\n\t"
    "v_cvt_pk_bf16_f32 v104, v176, v177\n\t"
    "v_mfma_f32_16x16x32_bf16 v[76:79], v[144:147], a[240:243], v[140:143]\n\t"
    "s_waitcnt lgkmcnt(1)\n\t"
    "s_mov_b32 s96, 2048\n\t"
    "s_add_u32 m0, s36, 2048\n\t"
    "s_nop 0\n\t"
    "buffer_load_dwordx4 v195, s[52:55], s96 offen lds\n\t"
    "v_exp_f32 v179, v43\n\t"
    "v_mfma_f32_16x16x32_bf16 v[64:67], v[152:155], a[196:199], v[64:67]\n\t"
    "s_waitcnt lgkmcnt(0)\n\t"
    "ds_read_b128 v[144:147], v192 offset:12416\n\t"
    "v_add_f32 v174, v174, v178\n\t"
    "v_mfma_f32_16x16x32_bf16 v[80:83], v[156:159], a[196:199], v[80:83]\n\t"
    "ds_read_b128 v[148:151], v192 offset:13952\n\t"
    "v_add_f32 v174, v174, v179\n\t"
    "v_mfma_f32_16x16x32_bf16 v[84:87], v[156:159], a[212:215], v[84:87]\n\t"
    "s_mov_b32 s97, 3072\n\t"
    "s_add_u32 m0, s36, 3072\n\t"
    "s_nop 0\n\t"
    "buffer_load_dwordx4 v195, s[52:55], s97 offen lds\n\t"
    "v_cvt_pk_bf16_f32 v105, v178, v179\n\t"
    "v_mfma_f32_16x16x32_bf16 v[68:71], v[152:155], a[212:215], v[68:71]\n\t"
    "s_mov_b32 s96, 4096\n\t"
    "s_add_u32 m0, s36, 4096\n\t"
    "s_nop 0\n\t"
    "buffer_load_dwordx4 v195, s[52:55], s96 offen lds\n\t"
    "v_exp_f32 v176, v56\n\t"
    "v_mfma_f32_16x16x32_bf16 v[72:75], v[152:155], a[228:231], v[72:75]\n\t"
    "s_mov_b32 s97, 5120\n\t"
    "s_add_u32 m0, s36, 5120\n\t"
    "s_nop 0\n\t"
    "buffer_load_dwordx4 v195, s[52:55], s97 offen lds\n\t"
    "v_mfma_f32_16x16x32_bf16 v[88:91], v[156:159], a[228:231], v[88:91]\n\t"
    "v_exp_f32 v177, v57\n\t"
    "v_mfma_f32_16x16x32_bf16 v[92:95], v[156:159], a[244:247], v[92:95]\n\t"
    "v_add_f32 v174, v174, v176\n\t"
    "v_add_f32 v174, v174, v177\n\t"
    "v_mfma_f32_16x16x32_bf16 v[76:79], v[152:155], a[244:247], v[76:79]\n\t"
    "s_waitcnt lgkmcnt(1)\n\t"
    "v_exp_f32 v178, v58\n\t"
    "v_mfma_f32_16x16x32_bf16 v[64:67], v[144:147], a[200:203], v[64:67]\n\t"
    "s_waitcnt lgkmcnt(0)\n\t"
    "ds_read_b128 v[152:155], v193 offset:12416\n\t"
    "v_cvt_pk_bf16_f32 v106, v176, v177\n\t"
    "v_mfma_f32_16x16x32_bf16 v[80:83], v[148:151], a[200:203], v[80:83]\n\t"
    "s_waitcnt vmcnt(20)\n\t"
    "ds_read_b128 v[156:159], v193 offset:13952\n\t"
    "v_lshlrev_b32 v204, 16, v252\n\t"
    "v_and_b32 v205, 0xffff0000, v252\n\t"
    "v_mfma_f32_16x16x32_bf16 v[84:87], v[148:151], a[216:219], v[84:87]\n\t"
    "v_lshlrev_b32 v212, 16, v188\n\t"
    "v_and_b32 v213, 0xffff0000, v188\n\t"
    "v_lshlrev_b32 v206, 16, v253\n\t"
    "v_mfma_f32_16x16x32_bf16 v[68:71], v[144:147], a[216:219], v[68:71]\n\t"
    "s_add_u32 m0, s60, 73728\n\t"
    "v_exp_f32 v179, v59\n\t"
    "v_and_b32 v207, 0xffff0000, v253\n\t"
    "v_mfma_f32_16x16x32_bf16 v[72:75], v[144:147], a[232:235], v[72:75]\n\t"
    "buffer_load_dwordx4 v195, s[44:47], s66 offen offset:0 sc1 lds\n\t"
    "v_lshlrev_b32 v214, 16, v189\n\t"
    "v_mfma_f32_16x16x32_bf16 v[88:91], v[148:151], a[232:235], v[88:91]\n\t"
    "v_exp_f32 v176, v44\n\t"
    "v_and_b32 v215, 0xffff0000, v189\n\t"
    "v_mfma_f32_16x16x32_bf16 v[92:95], v[148:151], a[248:251], v[92:95]\n\t"
    "v_add_f32 v174, v174, v178\n\t"
    "v_add_f32 v174, v174, v179\n\t"
    "v_lshlrev_b32 v208, 16, v254\n\t"
    "v_mfma_f32_16x16x32_bf16 v[76:79], v[144:147], a[248:251], v[76:79]\n\t"
    "s_waitcnt lgkmcnt(1)\n\t"
    "v_exp_f32 v177, v45\n\t"
    "v_and_b32 v209, 0xffff0000, v254\n\t"
    "v_mfma_f32_16x16x32_bf16 v[64:67], v[152:155], a[204:207], v[64:67]\n\t"
    "s_waitcnt lgkmcnt(0)\n\t"
    "ds_read_b128 v[144:147], v192 offset:12544\n\t"
    "v_cvt_pk_bf16_f32 v107, v178, v179\n\t"
    "v_lshlrev_b32 v216, 16, v190\n\t"
    "v_mfma_f32_16x16x32_bf16 v[80:83], v[156:159], a[204:207], v[80:83]\n\t"
    "ds_read_b128 v[148:151], v192 offset:14080\n\t"
    "v_add_f32 v175, v176, v177\n\t"
    "v_and_b32 v217, 0xffff0000, v190\n\t"
    "v_mfma_f32_16x16x32_bf16 v[84:87], v[156:159], a[220:223], v[84:87]\n\t"
    "v_cvt_pk_bf16_f32 v108, v176, v177\n\t"
    "v_lshlrev_b32 v210, 16, v255\n\t"
    "v_mfma_f32_16x16x32_bf16 v[68:71], v[152:155], a[220:223], v[68:71]\n\t"
    "v_exp_f32 v178, v46\n\t"
    "v_and_b32 v211, 0xffff0000, v255\n\t"
    "v_mfma_f32_16x16x32_bf16 v[72:75], v[152:155], a[236:239], v[72:75]\n\t"
    "buffer_load_dwordx4 v195, s[44:47], s66 offen offset:1024 sc1 lds\n\t"
    "v_mfma_f32_16x16x32_bf16 v[88:91], v[156:159], a[236:239], v[88:91]\n\t"
    "v_exp_f32 v179, v47\n\t"
    "v_lshlrev_b32 v218, 16, v191\n\t"
    "v_mfma_f32_16x16x32_bf16 v[92:95], v[156:159], a[252:255], v[92:95]\n\t"
    "v_add_f32 v175, v175, v178\n\t"
    "v_add_f32 v175, v175, v179\n\t"
    "v_mfma_f32_16x16x32_bf16 v[76:79], v[152:155], a[252:255], v[76:79]\n\t"
    "s_waitcnt lgkmcnt(1)\n\t"
    "v_exp_f32 v176, v60\n\t"
    "v_mfma_f32_16x16x32_bf16 v[64:67], v[144:147], v[0:3], v[64:67]\n\t"
    "s_waitcnt lgkmcnt(0)\n\t"
    "ds_read_b128 v[152:155], v193 offset:12544\n\t"
    "v_cvt_pk_bf16_f32 v109, v178, v179\n\t"
    "v_and_b32 v219, 0xffff0000, v191\n\t"
    "v_mfma_f32_16x16x32_bf16 v[80:83], v[148:151], v[0:3], v[80:83]\n\t"
    "ds_read_b128 v[156:159], v193 offset:14080\n\t"
    "v_mfma_f32_16x16x32_bf16 v[84:87], v[148:151], v[8:11], v[84:87]\n\t"
    "v_mul_f32 v204, s93, v204\n\t"
    "v_mfma_f32_16x16x32_bf16 v[68:71], v[144:147], v[8:11], v[68:71]\n\t"
    "v_exp_f32 v177, v61\n\t"
    "v_mfma_f32_16x16x32_bf16 v[72:75], v[144:147], v[16:19], v[72:75]\n\t"
    "buffer_load_dwordx4 v195, s[44:47], s66 offen offset:2048 sc1 lds\n\t"
    "v_mul_f32 v205, s93, v205\n\t"
    "v_mfma_f32_16x16x32_bf16 v[88:91], v[148:151], v[16:19], v[88:91]\n\t"
    "v_exp_f32 v178, v62\n\t"
    "v_mfma_f32_16x16x32_bf16 v[92:95], v[148:151], v[24:27], v[92:95]\n\t"
    "v_add_f32 v175, v175, v176\n\t"
    "v_add_f32 v175, v175, v177\n\t"
    "v_mul_f32 v206, s93, v206\n\t"
    "v_mfma_f32_16x16x32_bf16 v[76:79], v[144:147], v[24:27], v[76:79]\n\t"
    "s_waitcnt lgkmcnt(1)\n\t"
    "v_exp_f32 v179, v63\n\t"
    "v_mfma_f32_16x16x32_bf16 v[64:67], v[152:155], v[4:7], v[64:67]\n\t"
    "s_waitcnt lgkmcnt(0)\n\t"
    "ds_read_b128 v[144:147], v194 offset:0\n\t"
    "v_cvt_pk_bf16_f32 v110, v176, v177\n\t"
    "v_mul_f32 v207, s93, v207\n\t"
    "v_mfma_f32_16x16x32_bf16 v[80:83], v[156:159], v[4:7], v[80:83]\n\t"
    "ds_read_b128 v[148:151], v194 offset:256\n\t"
    "v_add_f32 v175, v175, v178\n\t"
    "v_mfma_f32_16x16x32_bf16 v[84:87], v[156:159], v[12:15], v[84:87]\n\t"
    "v_add_f32 v175, v175, v179\n\t"
    "v_cvt_pk_bf16_f32 v111, v178, v179\n\t"
    "v_mul_f32 v208, s93, v208\n\t"
    "v_mfma_f32_16x16x32_bf16 v[68:71], v[152:155], v[12:15], v[68:71]\n\t"
    "v_max3_f32 v180, v172, v173, v174\n\t"
    "v_max_f32 v180, v180, v175\n\t"
    "v_mfma_f32_16x16x32_bf16 v[72:75], v[152:155], v[20:23], v[72:75]\n\t"
    "v_cmp_lt_f32 vcc, s62, v180\n\t"
    "v_mul_f32 v209, s93, v209\n\t"
    "v_mfma_f32_16x16x32_bf16 v[88:91], v[156:159], v[20:23], v[88:91]\n\t"
    "global_load_dwordx4 v[252:255], v186, s[86:87]\n\t"
    "v_mfma_f32_16x16x32_bf16 v[92:95], v[156:159], v[28:31], v[92:95]\n\t"
    "v_mul_f32 v210, s93, v210\n\t"
    "v_mfma_f32_16x16x32_bf16 v[76:79], v[152:155], v[28:31], v[76:79]\n\t"
    "s_waitcnt lgkmcnt(1)\n\t"
    "s_cbranch_vccnz L_stub_s0_0\n\t"
    "L_ret_s0_0:\n\t"
    "v_mfma_f32_16x16x32_bf16 a[0:3], v[96:99], v[144:147], a[0:3]\n\t"
    "s_add_u32 s86, s86, 512\n\t"
    "ds_read_b128 v[152:155], v194 offset:512\n\t"
    "v_add_f32 v168, v168, v172\n\t"
    "v_mfma_f32_16x16x32_bf16 a[4:7], v[100:103], v[144:147], a[4:7]\n\t"
    "s_addc_u32 s87, s87, 0\n\t"
    "v_exp_f32 v176, v64\n\t"
    "v_mul_f32 v211, s93, v211\n\t"
    "v_mfma_f32_16x16x32_bf16 a[8:11], v[104:107], v[144:147], a[8:11]\n\t"
    "v_add_f32 v169, v169, v173\n\t"
    "v_add_f32 v170, v170, v174\n\t"
    "v_mfma_f32_16x16x32_bf16 a[12:15], v[108:111], v[144:147], a[12:15]\n\t"
    "s_waitcnt lgkmcnt(1)\n\t"
    "v_exp_f32 v177, v65\n\t"
    "v_mul_f32_e64 v212, -v212, v204\n\t"
    "v_mfma_f32_16x16x32_bf16 a[28:31], v[108:111], v[148:151], a[28:31]\n\t"
    "ds_read_b128 v[156:159], v194 offset:768\n\t"
    "v_add_f32 v171, v171, v175\n\t"
    "v_mfma_f32_16x16x32_bf16 a[24:27], v[104:107], v[148:151], a[24:27]\n\t"
    "v_add_f32 v172, v176, v177\n\t"
    "v_cvt_pk_bf16_f32 v112, v176, v177\n\t"
    "v_mul_f32_e64 v213, -v213, v205\n\t"
    "v_mfma_f32_16x16x32_bf16 a[20:23], v[100:103], v[148:151], a[20:23]\n\t"
    "global_load_dwordx4 v[188:191], v186, s[88:89]\n\t"
    "v_mfma_f32_16x16x32_bf16 a[16:19], v[96:99], v[148:151], a[16:19]\n\t"
    "s_waitcnt lgkmcnt(1)\n\t"
    "v_exp_f32 v178, v66\n\t"
    "v_mul_f32_e64 v214, -v214, v206\n\t"
    "v_mfma_f32_16x16x32_bf16 a[32:35], v[96:99], v[152:155], a[32:35]\n\t"
    "s_add_u32 s88, s88, 512\n\t"
    "ds_read_b128 v[144:147], v194 offset:1024\n\t"
    "v_mfma_f32_16x16x32_bf16 a[36:39], v[100:103], v[152:155], a[36:39]\n\t"
    "s_addc_u32 s89, s89, 0\n\t"
    "v_exp_f32 v179, v67\n\t"
    "v_mul_f32_e64 v215, -v215, v207\n\t"
    "v_mfma_f32_16x16x32_bf16 a[40:43], v[104:107], v[152:155], a[40:43]\n\t"
    "v_add_f32 v172, v172, v178\n\t"
    "v_add_f32 v172, v172, v179\n\t"
    "v_mfma_f32_16x16x32_bf16 a[44:47], v[108:111], v[152:155], a[44:47]\n\t"
    "s_waitcnt lgkmcnt(1)\n\t"
    "v_cvt_pk_bf16_f32 v113, v178, v179\n\t"
    "v_mul_f32_e64 v216, -v216, v208\n\t"
    "v_mfma_f32_16x16x32_bf16 a[60:63], v[108:111], v[156:159], a[60:63]\n\t"
    "ds_read_b128 v[148:151], v194 offset:1280\n\t"
    "v_mfma_f32_16x16x32_bf16 a[56:59], v[104:107], v[156:159], a[56:59]\n\t"
    "v_exp_f32 v176, v80\n\t"
    "v_mul_f32_e64 v217, -v217, v209\n\t"
    "v_mfma_f32_16x16x32_bf16 a[52:55], v[100:103], v[156:159], a[52:55]\n\t"
    "v_mfma_f32_16x16x32_bf16 a[48:51], v[96:99], v[156:159], a[48:51]\n\t"
    "s_waitcnt lgkmcnt(1)\n\t"
    "v_exp_f32 v177, v81\n\t"
    "v_mfma_f32_16x16x32_bf16 a[64:67], v[96:99], v[144:147], a[64:67]\n\t"
    "ds_read_b128 v[152:155], v194 offset:1536\n\t"
    "v_add_f32 v172, v172, v176\n\t"
    "v_mul_f32_e64 v218, -v218, v210\n\t"
    "v_mfma_f32_16x16x32_bf16 a[68:71], v[100:103], v[144:147], a[68:71]\n\t"
    "v_add_f32 v172, v172, v177\n\t"
    "v_cvt_pk_bf16_f32 v114, v176, v177\n\t"
    "v_mfma_f32_16x16x32_bf16 a[72:75], v[104:107], v[144:147], a[72:75]\n\t"
    "v_mul_f32_e64 v219, -v219, v211\n\t"
    "v_mfma_f32_16x16x32_bf16 a[76:79], v[108:111], v[144:147], a[76:79]\n\t"
    "s_waitcnt lgkmcnt(1)\n\t"
    "v_exp_f32 v178, v82\n\t"
    "v_mfma_f32_16x16x32_bf16 a[92:95], v[108:111], v[148:151], a[92:95]\n\t"
    "ds_read_b128 v[156:159], v194 offset:1792\n\t"
    "v_mul_f32 v204, 0x4b800000, v204\n\t"
    "v_mfma_f32_16x16x32_bf16 a[88:91], v[104:107], v[148:151], a[88:91]\n\t"
    "v_exp_f32 v179, v83\n\t"
    "v_mfma_f32_16x16x32_bf16 a[84:87], v[100:103], v[148:151], a[84:87]\n\t"
    "v_add_f32 v172, v172, v178\n\t"
    "v_add_f32 v172, v172, v179\n\t"
    "v_mul_f32 v205, 0x4b800000, v205\n\t"
    "v_mfma_f32_16x16x32_bf16 a[80:83], v[96:99], v[148:151], a[80:83]\n\t"
    "s_waitcnt lgkmcnt(1)\n\t"
    "v_cvt_pk_bf16_f32 v115, v178, v179\n\t"
    "v_mfma_f32_16x16x32_bf16 a[96:99], v[96:99], v[152:155], a[96:99]\n\t"
    "ds_read_b128 v[144:147], v194 offset:2048\n\t"
    "v_mul_f32 v206, 0x4b800000, v206\n\t"
    "v_mfma_f32_16x16x32_bf16 a[100:103], v[100:103], v[152:155], a[100:103]\n\t"
    "v_exp_f32 v176, v68\n\t"
    "v_mfma_f32_16x16x32_bf16 a[104:107], v[104:107], v[152:155], a[104:107]\n\t"
    "v_mul_f32 v207, 0x4b800000, v207\n\t"
    "v_mfma_f32_16x16x32_bf16 a[108:111], v[108:111], v[152:155], a[108:111]\n\t"
    "s_waitcnt lgkmcnt(1)\n\t"
    "v_exp_f32 v177, v69\n\t"
    "v_mfma_f32_16x16x32_bf16 a[124:127], v[108:111], v[156:159], a[124:127]\n\t"
    "ds_read_b128 v[148:151], v194 offset:2304\n\t"
    "v_add_f32 v173, v176, v177\n\t"
    "v_mul_f32 v208, 0x4b800000, v208\n\t"
    "v_mfma_f32_16x16x32_bf16 a[120:123], v[104:107], v[156:159], a[120:123]\n\t"
    "v_cvt_pk_bf16_f32 v116, v176, v177\n\t"
    "v_mfma_f32_16x16x32_bf16 a[116:119], v[100:103], v[156:159], a[116:119]\n\t"
    "v_mul_f32 v209, 0x4b800000, v209\n\t"
    "v_mfma_f32_16x16x32_bf16 a[112:115], v[96:99], v[156:159], a[112:115]\n\t"
    "s_waitcnt lgkmcnt(1)\n\t"
    "v_exp_f32 v178, v70\n\t"
    "v_mfma_f32_16x16x32_bf16 a[128:131], v[96:99], v[144:147], a[128:131]\n\t"
    "ds_read_b128 v[152:155], v194 offset:2560\n\t"
    "v_mfma_f32_16x16x32_bf16 a[132:135], v[100:103], v[144:147], a[132:135]\n\t"
    "s_waitcnt vmcnt(20)\n\t"
    "v_exp_f32 v179, v71\n\t"
    "v_mul_f32 v210, 0x4b800000, v210\n\t"
    "v_mfma_f32_16x16x32_bf16 a[136:139], v[104:107], v[144:147], a[136:139]\n\t"
    "s_barrier\n\t"
    "v_add_f32 v173, v173, v178\n\t"
    "v_add_f32 v173, v173, v179\n\t"
    "v_mfma_f32_16x16x32_bf16 a[140:143], v[108:111], v[144:147], a[140:143]\n\t"
    "s_waitcnt lgkmcnt(1)\n\t"
    "v_cvt_pk_bf16_f32 v117, v178, v179\n\t"
    "v_mul_f32 v211, 0x4b800000, v211\n\t"
    "v_mfma_f32_16x16x32_bf16 a[156:159], v[108:111], v[148:151], a[156:159]\n\t"
    "s_waitcnt vmcnt(23)\n\t"
    "ds_read_b128 v[156:159], v194 offset:2816\n\t"
    "v_mfma_f32_16x16x32_bf16 a[152:155], v[104:107], v[148:151], a[152:155]\n\t"
    "s_add_u32 m0, s60, 24576\n\t"
    "v_exp_f32 v176, v84\n\t"
    "v_fma_mix_f32 v220, v220, v204, v212 op_sel_hi:[1,0,0]\n\t"
    "v_mfma_f32_16x16x32_bf16 a[148:151], v[100:103], v[148:151], a[148:151]\n\t"
    "buffer_load_dwordx4 v195, s[40:43], s68 offen offset:0 sc1 lds\n\t"
    "v_mfma_f32_16x16x32_bf16 a[144:147], v[96:99], v[148:151], a[144:147]\n\t"
    "s_waitcnt lgkmcnt(1)\n\t"
    "v_exp_f32 v177, v85\n\t"
    "v_fma_mix_f32 v221, v221, v205, v213 op_sel_hi:[1,0,0]\n\t"
    "v_mfma_f32_16x16x32_bf16 a[160:163], v[96:99], v[152:155], a[160:163]\n\t"
    "ds_read_b128 v[144:147], v192 offset:24576\n\t"
    "v_add_f32 v173, v173, v176\n\t"
    "v_mfma_f32_16x16x32_bf16 a[164:167], v[100:103], v[152:155], a[164:167]\n\t"
    "ds_read_b128 v[148:151], v192 offset:26112\n\t"
    "v_add_f32 v173, v173, v177\n\t"
    "v_fma_mix_f32 v222, v222, v206, v214 op_sel_hi:[1,0,0]\n\t"
    "v_mfma_f32_16x16x32_bf16 a[168:171], v[104:107], v[152:155], a[168:171]\n\t"
    "buffer_load_dwordx4 v195, s[40:43], s68 offen offset:1024 sc1 lds\n\t"
    "v_mfma_f32_16x16x32_bf16 a[172:175], v[108:111], v[152:155], a[172:175]\n\t"
    "s_waitcnt lgkmcnt(2)\n\t"
    "v_exp_f32 v178, v86\n\t"
    "v_fma_mix_f32 v223, v223, v207, v215 op_sel_hi:[1,0,0]\n\t"
    "v_mfma_f32_16x16x32_bf16 a[188:191], v[108:111], v[156:159], a[188:191]\n\t"
    "v_cvt_pk_bf16_f32 v118, v176, v177\n\t"
    "v_mfma_f32_16x16x32_bf16 a[184:187], v[104:107], v[156:159], a[184:187]\n\t"
    "v_exp_f32 v179, v87\n\t"
    "v_fma_mix_f32 v224, v224, v208, v216 op_sel_hi:[1,0,0]\n\t"
    "v_mfma_f32_16x16x32_bf16 a[180:183], v[100:103], v[156:159], a[180:183]\n\t"
    "v_add_f32 v173, v173, v178\n\t"
    "v_add_f32 v173, v173, v179\n\t"
    "v_mfma_f32_16x16x32_bf16 a[176:179], v[96:99], v[156:159], a[176:179]\n\t"
    "s_waitcnt lgkmcnt(1)\n\t"
    "v_cvt_pk_bf16_f32 v119, v178, v179\n\t"
    "v_mfma_f32_16x16x32_bf16 v[32:35], v[144:147], a[192:195], v[128:131]\n\t"
    "s_waitcnt lgkmcnt(0)\n\t"
    "ds_read_b128 v[152:155], v193 offset:24576\n\t"
    "v_fma_mix_f32 v225, v225, v209, v217 op_sel_hi:[1,0,0]\n\t"
    "v_mfma_f32_16x16x32_bf16 v[48:51], v[148:151], a[192:195], v[128:131]\n\t"
    "ds_read_b128 v[156:159], v193 offset:26112\n\t"
    "v_mfma_f32_16x16x32_bf16 v[52:55], v[148:151], a[208:211], v[132:135]\n\t"
    "v_exp_f32 v176, v72\n\t"
    "v_fma_mix_f32 v226, v226, v210, v218 op_sel_hi:[1,0,0]\n\t"
    "v_mfma_f32_16x16x32_bf16 v[36:39], v[144:147], a[208:211], v[132:135]\n\t"
    "v_exp_f32 v177, v73\n\t"
    "v_mfma_f32_16x16x32_bf16 v[40:43], v[144:147], a[224:227], v[136:139]\n\t"
    "buffer_load_dwordx4 v195, s[40:43], s68 offen offset:2048 sc1 lds\n\t"
    "v_fma_mix_f32 v227, v227, v211, v219 op_sel_hi:[1,0,0]\n\t"
    "v_mfma_f32_16x16x32_bf16 v[56:59], v[148:151], a[224:227], v[136:139]\n\t"
    "v_exp_f32 v178, v74\n\t"
    "v_mfma_f32_16x16x32_bf16 v[60:63], v[148:151], a[240:243], v[140:143]\n\t"
    "v_add_f32 v174, v176, v177\n\t"
    "v_cvt_pk_bf16_f32 v120, v176, v177\n\t"
    "v_cvt_pk_bf16_f32 v244, v220, v221\n\t"
    "v_mfma_f32_16x16x32_bf16 v[44:47], v[144:147], a[240:243], v[140:143]\n\t"
    "s_waitcnt lgkmcnt(1)\n\t"
    "v_exp_f32 v179, v75\n\t"
    "v_mfma_f32_16x16x32_bf16 v[32:35], v[152:155], a[196:199], v[32:35]\n\t"
    "s_waitcnt lgkmcnt(0)\n\t"
    "ds_read_b128 v[144:147], v192 offset:24704\n\t"
    "v_add_f32 v174, v174, v178\n\t"
    "v_cvt_pk_bf16_f32 v245, v222, v223\n\t"
    "v_mfma_f32_16x16x32_bf16 v[48:51], v[156:159], a[196:199], v[48:51]\n\t"
    "ds_read_b128 v[148:151], v192 offset:26240\n\t"
    "v_add_f32 v174, v174, v179\n\t"
    "v_mfma_f32_16x16x32_bf16 v[52:55], v[156:159], a[212:215], v[52:55]\n\t"
    "v_cvt_pk_bf16_f32 v121, v178, v179\n\t"
    "v_cvt_pk_bf16_f32 v246, v224, v225\n\t"
    "v_mfma_f32_16x16x32_bf16 v[36:39], v[152:155], a[212:215], v[36:39]\n\t"
    "v_exp_f32 v176, v88\n\t"
    "v_mfma_f32_16x16x32_bf16 v[40:43], v[152:155], a[228:231], v[40:43]\n\t"
    "v_cvt_pk_bf16_f32 v247, v226, v227\n\t"
    "v_mfma_f32_16x16x32_bf16 v[56:59], v[156:159], a[228:231], v[56:59]\n\t"
    "s_waitcnt vmcnt(21)\n\t"
    "v_exp_f32 v177, v89\n\t"
    "v_mfma_f32_16x16x32_bf16 v[60:63], v[156:159], a[244:247], v[60:63]\n\t"
    "v_add_f32 v174, v174, v176\n\t"
    "v_add_f32 v174, v174, v177\n\t"
    "v_fma_mix_f32 v228, v228, v204, v212 op_sel_hi:[1,0,0]\n\t"
    "v_mfma_f32_16x16x32_bf16 v[44:47], v[152:155], a[244:247], v[44:47]\n\t"
    "s_waitcnt lgkmcnt(1)\n\t"
    "v_exp_f32 v178, v90\n\t"
    "v_mfma_f32_16x16x32_bf16 v[32:35], v[144:147], a[200:203], v[32:35]\n\t"
    "s_waitcnt lgkmcnt(0)\n\t"
    "ds_read_b128 v[152:155], v193 offset:24704\n\t"
    "v_cvt_pk_bf16_f32 v122, v176, v177\n\t"
    "v_mfma_f32_16x16x32_bf16 v[48:51], v[148:151], a[200:203], v[48:51]\n\t"
    "ds_read_b128 v[156:159], v193 offset:26240\n\t"
    "v_fma_mix_f32 v229, v229, v205, v213 op_sel_hi:[1,0,0]\n\t"
    "v_mfma_f32_16x16x32_bf16 v[52:55], v[148:151], a[216:219], v[52:55]\n\t"
    "buffer_store_dwordx4 v[244:247], v199, s[48:51], s92 offen offset:0 sc1\n\t"
    "v_mfma_f32_16x16x32_bf16 v[36:39], v[144:147], a[216:219], v[36:39]\n\t"
    "s_add_u32 m0, s60, 86016\n\t"
    "v_exp_f32 v179, v91\n\t"
    "v_fma_mix_f32 v230, v230, v206, v214 op_sel_hi:[1,0,0]\n\t"
    "v_mfma_f32_16x16x32_bf16 v[40:43], v[144:147], a[232:235], v[40:43]\n\t"
    "buffer_load_dwordx4 v195, s[44:47], s67 offen offset:0 sc1 lds\n\t"
    "v_mfma_f32_16x16x32_bf16 v[56:59], v[148:151], a[232:235], v[56:59]\n\t"
    "v_exp_f32 v176, v76\n\t"
    "v_fma_mix_f32 v231, v231, v207, v215 op_sel_hi:[1,0,0]\n\t"
    "v_mfma_f32_16x16x32_bf16 v[60:63], v[148:151], a[248:251], v[60:63]\n\t"
    "v_add_f32 v174, v174, v178\n\t"
    "v_add_f32 v174, v174, v179\n\t"
    "v_mfma_f32_16x16x32_bf16 v[44:47], v[144:147], a[248:251], v[44:47]\n\t"
    "s_waitcnt lgkmcnt(1)\n\t"
    "v_exp_f32 v177, v77\n\t"
    "v_fma_mix_f32 v232, v232, v208, v216 op_sel_hi:[1,0,0]\n\t"
    "v_mfma_f32_16x16x32_bf16 v[32:35], v[152:155], a[204:207], v[32:35]\n\t"
    "s_waitcnt lgkmcnt(0)\n\t"
    "ds_read_b128 v[144:147], v192 offset:24832\n\t"
    "v_cvt_pk_bf16_f32 v123, v178, v179\n\t"
    "v_mfma_f32_16x16x32_bf16 v[48:51], v[156:159], a[204:207], v[48:51]\n\t"
    "ds_read_b128 v[148:151], v192 offset:26368\n\t"
    "v_add_f32 v175, v176, v177\n\t"
    "v_fma_mix_f32 v233, v233, v209, v217 op_sel_hi:[1,0,0]\n\t"
    "v_mfma_f32_16x16x32_bf16 v[52:55], v[156:159], a[220:223], v[52:55]\n\t"
    "v_cvt_pk_bf16_f32 v124, v176, v177\n\t"
    "v_mfma_f32_16x16x32_bf16 v[36:39], v[152:155], a[220:223], v[36:39]\n\t"
    "v_exp_f32 v178, v78\n\t"
    "v_fma_mix_f32 v234, v234, v210, v218 op_sel_hi:[1,0,0]\n\t"
    "v_mfma_f32_16x16x32_bf16 v[40:43], v[152:155], a[236:239], v[40:43]\n\t"
    "buffer_load_dwordx4 v195, s[44:47], s67 offen offset:1024 sc1 lds\n\t"
    "v_mfma_f32_16x16x32_bf16 v[56:59], v[156:159], a[236:239], v[56:59]\n\t"
    "v_exp_f32 v179, v79\n\t"
    "v_fma_mix_f32 v235, v235, v211, v219 op_sel_hi:[1,0,0]\n\t"
    "v_mfma_f32_16x16x32_bf16 v[60:63], v[156:159], a[252:255], v[60:63]\n\t"
    "v_add_f32 v175, v175, v178\n\t"
    "v_add_f32 v175, v175, v179\n\t"
    "v_mfma_f32_16x16x32_bf16 v[44:47], v[152:155], a[252:255], v[44:47]\n\t"
    "s_waitcnt lgkmcnt(1)\n\t"
    "v_exp_f32 v176, v92\n\t"
    "v_mfma_f32_16x16x32_bf16 v[32:35], v[144:147], v[0:3], v[32:35]\n\t"
    "s_waitcnt lgkmcnt(0)\n\t"
    "ds_read_b128 v[152:155], v193 offset:24832\n\t"
    "v_cvt_pk_bf16_f32 v125, v178, v179\n\t"
    "v_cvt_pk_bf16_f32 v248, v228, v229\n\t"
    "v_mfma_f32_16x16x32_bf16 v[48:51], v[148:151], v[0:3], v[48:51]\n\t"
    "ds_read_b128 v[156:159], v193 offset:26368\n\t"
    "v_mfma_f32_16x16x32_bf16 v[52:55], v[148:151], v[8:11], v[52:55]\n\t"
    "v_cvt_pk_bf16_f32 v249, v230, v231\n\t"
    "v_mfma_f32_16x16x32_bf16 v[36:39], v[144:147], v[8:11], v[36:39]\n\t"
    "v_exp_f32 v177, v93\n\t"
    "v_mfma_f32_16x16x32_bf16 v[40:43], v[144:147], v[16:19], v[40:43]\n\t"
    "buffer_load_dwordx4 v195, s[44:47], s67 offen offset:2048 sc1 lds\n\t"
    "v_cvt_pk_bf16_f32 v250, v232, v233\n\t"
    "v_mfma_f32_16x16x32_bf16 v[56:59], v[148:151], v[16:19], v[56:59]\n\t"
    "v_exp_f32 v178, v94\n\t"
    "v_mfma_f32_16x16x32_bf16 v[60:63], v[148:151], v[24:27], v[60:63]\n\t"
    "v_add_f32 v175, v175, v176\n\t"
    "v_add_f32 v175, v175, v177\n\t"
    "v_cvt_pk_bf16_f32 v251, v234, v235\n\t"
    "v_mfma_f32_16x16x32_bf16 v[44:47], v[144:147], v[24:27], v[44:47]\n\t"
    "s_waitcnt lgkmcnt(1)\n\t"
    "v_exp_f32 v179, v95\n\t"
    "v_mfma_f32_16x16x32_bf16 v[32:35], v[152:155], v[4:7], v[32:35]\n\t"
    "s_waitcnt lgkmcnt(0)\n\t"
    "ds_read_b128 v[144:147], v194 offset:12288\n\t"
    "v_cvt_pk_bf16_f32 v126, v176, v177\n\t"
    "v_mfma_f32_16x16x32_bf16 v[48:51], v[156:159], v[4:7], v[48:51]\n\t"
    "s_waitcnt vmcnt(23)\n\t"
    "ds_read_b128 v[148:151], v194 offset:12544\n\t"
    "v_add_f32 v175, v175, v178\n\t"
    "v_fma_mix_f32 v236, v236, v204, v212 op_sel_hi:[1,0,0]\n\t"
    "v_mfma_f32_16x16x32_bf16 v[52:55], v[156:159], v[12:15], v[52:55]\n\t"
    "buffer_store_dwordx4 v[248:251], v200, s[48:51], s92 offen offset:768 sc1\n\t"
    "v_add_f32 v175, v175, v179\n\t"
    "v_cvt_pk_bf16_f32 v127, v178, v179\n\t"
    "v_mfma_f32_16x16x32_bf16 v[36:39], v[152:155], v[12:15], v[36:39]\n\t"
    "v_max3_f32 v180, v172, v173, v174\n\t"
    "v_max_f32 v180, v180, v175\n\t"
    "v_fma_mix_f32 v237, v237, v205, v213 op_sel_hi:[1,0,0]\n\t"
    "v_mfma_f32_16x16x32_bf16 v[40:43], v[152:155], v[20:23], v[40:43]\n\t"
    "v_cmp_lt_f32 vcc, s62, v180\n\t"
    "v_fma_mix_f32 v238, v238, v206, v214 op_sel_hi:[1,0,0]\n\t"
    "v_mfma_f32_16x16x32_bf16 v[56:59], v[156:159], v[20:23], v[56:59]\n\t"
    "v_mfma_f32_16x16x32_bf16 v[60:63], v[156:159], v[28:31], v[60:63]\n\t"
    "v_fma_mix_f32 v239, v239, v207, v215 op_sel_hi:[1,0,0]\n\t"
    "v_mfma_f32_16x16x32_bf16 v[44:47], v[152:155], v[28:31], v[44:47]\n\t"
    "s_waitcnt lgkmcnt(1)\n\t"
    "s_cbranch_vccnz L_stub_s1_1\n\t"
    "L_ret_s1_1:\n\t"
    "v_mfma_f32_16x16x32_bf16 a[0:3], v[112:115], v[144:147], a[0:3]\n\t"
    "ds_read_b128 v[152:155], v194 offset:12800\n\t"
    "v_add_f32 v168, v168, v172\n\t"
    "v_mfma_f32_16x16x32_bf16 a[4:7], v[116:119], v[144:147], a[4:7]\n\t"
    "v_exp_f32 v176, v32\n\t"
    "v_fma_mix_f32 v240, v240, v208, v216 op_sel_hi:[1,0,0]\n\t"
    "v_mfma_f32_16x16x32_bf16 a[8:11], v[120:123], v[144:147], a[8:11]\n\t"
    "v_add_f32 v169, v169, v173\n\t"
    "v_add_f32 v170, v170, v174\n\t"
    "v_mfma_f32_16x16x32_bf16 a[12:15], v[124:127], v[144:147], a[12:15]\n\t"
    "s_waitcnt lgkmcnt(1)\n\t"
    "v_exp_f32 v177, v33\n\t"
    "v_fma_mix_f32 v241, v241, v209, v217 op_sel_hi:[1,0,0]\n\t"
    "v_mfma_f32_16x16x32_bf16 a[28:31], v[124:127], v[148:151], a[28:31]\n\t"
    "ds_read_b128 v[156:159], v194 offset:13056\n\t"
    "v_add_f32 v171, v171, v175\n\t"
    "v_mfma_f32_16x16x32_bf16 a[24:27], v[120:123], v[148:151], a[24:27]\n\t"
    "v_add_f32 v172, v176, v177\n\t"
    "v_cvt_pk_bf16_f32 v96, v176, v177\n\t"
    "v_fma_mix_f32 v242, v242, v210, v218 op_sel_hi:[1,0,0]\n\t"
    "v_mfma_f32_16x16x32_bf16 a[20:23], v[116:119], v[148:151], a[20:23]\n\t"
    "v_mfma_f32_16x16x32_bf16 a[16:19], v[112:115], v[148:151], a[16:19]\n\t"
    "s_waitcnt lgkmcnt(1)\n\t"
    "v_exp_f32 v178, v34\n\t"
    "v_fma_mix_f32 v243, v243, v211, v219 op_sel_hi:[1,0,0]\n\t"
    "v_mfma_f32_16x16x32_bf16 a[32:35], v[112:115], v[152:155], a[32:35]\n\t"
    "ds_read_b128 v[144:147], v194 offset:13312\n\t"
    "v_mfma_f32_16x16x32_bf16 a[36:39], v[116:119], v[152:155], a[36:39]\n\t"
    "v_exp_f32 v179, v35\n\t"
    "v_cvt_pk_bf16_f32 v244, v236, v237\n\t"
    "v_mfma_f32_16x16x32_bf16 a[40:43], v[120:123], v[152:155], a[40:43]\n\t"
    "v_add_f32 v172, v172, v178\n\t"
    "v_add_f32 v172, v172, v179\n\t"
    "v_mfma_f32_16x16x32_bf16 a[44:47], v[124:127], v[152:155], a[44:47]\n\t"
    "s_waitcnt lgkmcnt(1)\n\t"
    "v_cvt_pk_bf16_f32 v97, v178, v179\n\t"
    "v_cvt_pk_bf16_f32 v245, v238, v239\n\t"
    "v_mfma_f32_16x16x32_bf16 a[60:63], v[124:127], v[156:159], a[60:63]\n\t"
    "ds_read_b128 v[148:151], v194 offset:13568\n\t"
    "v_mfma_f32_16x16x32_bf16 a[56:59], v[120:123], v[156:159], a[56:59]\n\t"
    "v_exp_f32 v176, v48\n\t"
    "v_cvt_pk_bf16_f32 v246, v240, v241\n\t"
    "v_mfma_f32_16x16x32_bf16 a[52:55], v[116:119], v[156:159], a[52:55]\n\t"
    "v_mfma_f32_16x16x32_bf16 a[48:51], v[112:115], v[156:159], a[48:51]\n\t"
    "s_waitcnt lgkmcnt(1)\n\t"
    "v_exp_f32 v177, v49\n\t"
    "v_mfma_f32_16x16x32_bf16 a[64:67], v[112:115], v[144:147], a[64:67]\n\t"
    "ds_read_b128 v[152:155], v194 offset:13824\n\t"
    "v_add_f32 v172, v172, v176\n\t"
    "v_cvt_pk_bf16_f32 v247, v242, v243\n\t"
    "v_mfma_f32_16x16x32_bf16 a[68:71], v[116:119], v[144:147], a[68:71]\n\t"
    "s_waitcnt vmcnt(22)\n\t"
    "v_add_f32 v172, v172, v177\n\t"
    "v_cvt_pk_bf16_f32 v98, v176, v177\n\t"
    "v_mfma_f32_16x16x32_bf16 a[72:75], v[120:123], v[144:147], a[72:75]\n\t"
    "buffer_store_dwordx4 v[244:247], v199, s[48:51], s92 offen offset:1536 sc1\n\t"
    "v_fma_mix_f32 v160, v160, v204, v212 op_sel_hi:[1,0,0]\n\t"
    "v_mfma_f32_16x16x32_bf16 a[76:79], v[124:127], v[144:147], a[76:79]\n\t"
    "s_waitcnt lgkmcnt(1)\n\t"
    "v_exp_f32 v178, v50\n\t"
    "v_mfma_f32_16x16x32_bf16 a[92:95], v[124:127], v[148:151], a[92:95]\n\t"
    "ds_read_b128 v[156:159], v194 offset:14080\n\t"
    "v_fma_mix_f32 v161, v161, v205, v213 op_sel_hi:[1,0,0]\n\t"
    "v_mfma_f32_16x16x32_bf16 a[88:91], v[120:123], v[148:151], a[88:91]\n\t"
    "v_exp_f32 v179, v51\n\t"
    "v_mfma_f32_16x16x32_bf16 a[84:87], v[116:119], v[148:151], a[84:87]\n\t"
    "v_add_f32 v172, v172, v178\n\t"
    "v_add_f32 v172, v172, v179\n\t"
    "v_fma_mix_f32 v162, v162, v206, v214 op_sel_hi:[1,0,0]\n\t"
    "v_mfma_f32_16x16x32_bf16 a[80:83], v[112:115], v[148:151], a[80:83]\n\t"
    "s_waitcnt lgkmcnt(1)\n\t"
    "v_cvt_pk_bf16_f32 v99, v178, v179\n\t"
    "v_mfma_f32_16x16x32_bf16 a[96:99], v[112:115], v[152:155], a[96:99]\n\t"
    "ds_read_b128 v[144:147], v194 offset:14336\n\t"
    "v_fma_mix_f32 v163, v163, v207, v215 op_sel_hi:[1,0,0]\n\t"
    "v_mfma_f32_16x16x32_bf16 a[100:103], v[116:119], v[152:155], a[100:103]\n\t"
    "v_exp_f32 v176, v36\n\t"
    "v_mfma_f32_16x16x32_bf16 a[104:107], v[120:123], v[152:155], a[104:107]\n\t"
    "v_fma_mix_f32 v164, v164, v208, v216 op_sel_hi:[1,0,0]\n\t"
    "v_mfma_f32_16x16x32_bf16 a[108:111], v[124:127], v[152:155], a[108:111]\n\t"
    "s_waitcnt lgkmcnt(1)\n\t"
    "v_exp_f32 v177, v37\n\t"
    "v_mfma_f32_16x16x32_bf16 a[124:127], v[124:127], v[156:159], a[124:127]\n\t"
    "ds_read_b128 v[148:151], v194 offset:14592\n\t"
    "v_add_f32 v173, v176, v177\n\t"
    "v_fma_mix_f32 v165, v165, v209, v217 op_sel_hi:[1,0,0]\n\t"
    "v_mfma_f32_16x16x32_bf16 a[120:123], v[120:123], v[156:159], a[120:123]\n\t"
    "v_cvt_pk_bf16_f32 v100, v176, v177\n\t"
    "v_mfma_f32_16x16x32_bf16 a[116:119], v[116:119], v[156:159], a[116:119]\n\t"
    "v_fma_mix_f32 v166, v166, v210, v218 op_sel_hi:[1,0,0]\n\t"
    "v_mfma_f32_16x16x32_bf16 a[112:115], v[112:115], v[156:159], a[112:115]\n\t"
    "s_waitcnt lgkmcnt(1)\n\t"
    "v_exp_f32 v178, v38\n\t"
    "v_mfma_f32_16x16x32_bf16 a[128:131], v[112:115], v[144:147], a[128:131]\n\t"
    "ds_read_b128 v[152:155], v194 offset:14848\n\t"
    "v_mfma_f32_16x16x32_bf16 a[132:135], v[116:119], v[144:147], a[132:135]\n\t"
    "s_waitcnt vmcnt(11)\n\t"
    "v_exp_f32 v179, v39\n\t"
    "v_fma_mix_f32 v167, v167, v211, v219 op_sel_hi:[1,0,0]\n\t"
    "v_mfma_f32_16x16x32_bf16 a[136:139], v[120:123], v[144:147], a[136:139]\n\t"
    "s_barrier\n\t"
    "v_add_f32 v173, v173, v178\n\t"
    "v_add_f32 v173, v173, v179\n\t"
    "v_mfma_f32_16x16x32_bf16 a[140:143], v[124:127], v[144:147], a[140:143]\n\t"
    "s_waitcnt lgkmcnt(1)\n\t"
    "v_cvt_pk_bf16_f32 v101, v178, v179\n\t"
    "v_cvt_pk_bf16_f32 v248, v160, v161\n\t"
    "v_mfma_f32_16x16x32_bf16 a[156:159], v[124:127], v[148:151], a[156:159]\n\t"
    "ds_read_b128 v[156:159], v194 offset:15104\n\t"
    "v_mfma_f32_16x16x32_bf16 a[152:155], v[120:123], v[148:151], a[152:155]\n\t"
    "s_add_u32 m0, s60, 36864\n\t"
    "v_exp_f32 v176, v52\n\t"
    "v_cvt_pk_bf16_f32 v249, v162, v163\n\t"
    "v_mfma_f32_16x16x32_bf16 a[148:151], v[116:119], v[148:151], a[148:151]\n\t"
    "buffer_load_dwordx4 v195, s[40:43], s69 offen offset:0 sc1 lds\n\t"
    "v_mfma_f32_16x16x32_bf16 a[144:147], v[112:115], v[148:151], a[144:147]\n\t"
    "s_waitcnt lgkmcnt(1)\n\t"
    "v_exp_f32 v177, v53\n\t"
    "v_cvt_pk_bf16_f32 v250, v164, v165\n\t"
    "v_mfma_f32_16x16x32_bf16 a[160:163], v[112:115], v[152:155], a[160:163]\n\t"
    "ds_read_b128 v[144:147], v192 offset:36864\n\t"
    "v_add_f32 v173, v173, v176\n\t"
    "v_mfma_f32_16x16x32_bf16 a[164:167], v[116:119], v[152:155], a[164:167]\n\t"
    "ds_read_b128 v[148:151], v192 offset:38400\n\t"
    "v_add_f32 v173, v173, v177\n\t"
    "v_cvt_pk_bf16_f32 v251, v166, v167\n\t"
    "v_mfma_f32_16x16x32_bf16 a[168:171], v[120:123], v[152:155], a[168:171]\n\t"
    "s_waitcnt vmcnt(15)\n\t"
    "buffer_load_dwordx4 v195, s[40:43], s69 offen offset:1024 sc1 lds\n\t"
    "v_mfma_f32_16x16x32_bf16 a[172:175], v[124:127], v[152:155], a[172:175]\n\t"
    "s_waitcnt lgkmcnt(2)\n\t"
    "v_exp_f32 v178, v54\n\t"
    "v_mfma_f32_16x16x32_bf16 a[188:191], v[124:127], v[156:159], a[188:191]\n\t"
    "s_waitcnt vmcnt(11)\n\t"
    "v_cvt_pk_bf16_f32 v102, v176, v177\n\t"
    "v_lshlrev_b32 v204, 16, v252\n\t"
    "v_mfma_f32_16x16x32_bf16 a[184:187], v[120:123], v[156:159], a[184:187]\n\t"
    "buffer_store_dwordx4 v[248:251], v200, s[48:51], s92 offen offset:2304 sc1\n\t"
    "v_exp_f32 v179, v55\n\t"
    "v_and_b32 v205, 0xffff0000, v252\n\t"
    "v_mfma_f32_16x16x32_bf16 a[180:183], v[116:119], v[156:159], a[180:183]\n\t"
    "ds_read_b128 v[220:223], v185 offset:0\n\t"
    "v_add_f32 v173, v173, v178\n\t"
    "v_add_f32 v173, v173, v179\n\t"
    "v_mfma_f32_16x16x32_bf16 a[176:179], v[112:115], v[156:159], a[176:179]\n\t"
    "s_waitcnt lgkmcnt(2)\n\t"
    "ds_read_b128 v[224:227], v185 offset:768\n\t"
    "v_cvt_pk_bf16_f32 v103, v178, v179\n\t"
    "v_mfma_f32_16x16x32_bf16 v[64:67], v[144:147], a[192:195], v[128:131]\n\t"
    "s_waitcnt lgkmcnt(2)\n\t"
    "ds_read_b128 v[152:155], v193 offset:36864\n\t"
    "v_lshlrev_b32 v212, 16, v188\n\t"
    "v_mfma_f32_16x16x32_bf16 v[80:83], v[148:151], a[192:195], v[128:131]\n\t"
    "ds_read_b128 v[156:159], v193 offset:38400\n\t"
    "v_mfma_f32_16x16x32_bf16 v[84:87], v[148:151], a[208:211], v[132:135]\n\t"
    "ds_read_b128 v[228:231], v185 offset:1536\n\t"
    "v_exp_f32 v176, v40\n\t"
    "v_and_b32 v213, 0xffff0000, v188\n\t"
    "v_mfma_f32_16x16x32_bf16 v[68:71], v[144:147], a[208:211], v[132:135]\n\t"
    "ds_read_b128 v[232:235], v185 offset:2304\n\t"
    "v_exp_f32 v177, v41\n\t"
    "v_mfma_f32_16x16x32_bf16 v[72:75], v[144:147], a[224:227], v[136:139]\n\t"
    "buffer_load_dwordx4 v195, s[40:43], s69 offen offset:2048 sc1 lds\n\t"
    "v_lshlrev_b32 v206, 16, v253\n\t"
    "v_mfma_f32_16x16x32_bf16 v[88:91], v[148:151], a[224:227], v[136:139]\n\t"
    "ds_read_b128 v[236:239], v185 offset:3072\n\t"
    "v_exp_f32 v178, v42\n\t"
    "v_mfma_f32_16x16x32_bf16 v[92:95], v[148:151], a[240:243], v[140:143]\n\t"
    "ds_read_b128 v[240:243], v185 offset:3840\n\t"
    "v_add_f32 v174, v176, v177\n\t"
    "v_cvt_pk_bf16_f32 v104, v176, v177\n\t"
    "v_mfma_f32_16x16x32_bf16 v[76:79], v[144:147], a[240:243], v[140:143]\n\t"
    "s_waitcnt lgkmcnt(5)\n\t"
    "ds_read_b128 v[160:163], v185 offset:4608\n\t"
    "v_exp_f32 v179, v43\n\t"
    "v_and_b32 v207, 0xffff0000, v253\n\t"
    "v_mfma_f32_16x16x32_bf16 v[64:67], v[152:155], a[196:199], v[64:67]\n\t"
    "s_waitcnt lgkmcnt(5)\n\t"
    "ds_read_b128 v[144:147], v192 offset:36992\n\t"
    "v_add_f32 v174, v174, v178\n\t"
    "v_lshlrev_b32 v214, 16, v189\n\t"
    "v_mfma_f32_16x16x32_bf16 v[80:83], v[156:159], a[196:199], v[80:83]\n\t"
    "ds_read_b128 v[148:151], v192 offset:38528\n\t"
    "v_add_f32 v174, v174, v179\n\t"
    "v_mfma_f32_16x16x32_bf16 v[84:87], v[156:159], a[212:215], v[84:87]\n\t"
    "ds_read_b128 v[164:167], v185 offset:5376\n\t"
    "v_cvt_pk_bf16_f32 v105, v178, v179\n\t"
    "v_and_b32 v215, 0xffff0000, v189\n\t"
    "v_mfma_f32_16x16x32_bf16 v[68:71], v[152:155], a[212:215], v[68:71]\n\t"
    "v_exp_f32 v176, v56\n\t"
    "v_mfma_f32_16x16x32_bf16 v[72:75], v[152:155], a[228:231], v[72:75]\n\t"
    "v_lshlrev_b32 v208, 16, v254\n\t"
    "v_mfma_f32_16x16x32_bf16 v[88:91], v[156:159], a[228:231], v[88:91]\n\t"
    "v_exp_f32 v177, v57\n\t"
    "v_mfma_f32_16x16x32_bf16 v[92:95], v[156:159], a[244:247], v[92:95]\n\t"
    "v_add_f32 v174, v174, v176\n\t"
    "v_add_f32 v174, v174, v177\n\t"
    "v_and_b32 v209, 0xffff0000, v254\n\t"
    "v_mfma_f32_16x16x32_bf16 v[76:79], v[152:155], a[244:247], v[76:79]\n\t"
    "s_waitcnt lgkmcnt(2)\n\t"
    "v_exp_f32 v178, v58\n\t"
    "v_mfma_f32_16x16x32_bf16 v[64:67], v[144:147], a[200:203], v[64:67]\n\t"
    "s_waitcnt lgkmcnt(1)\n\t"
    "ds_read_b128 v[152:155], v193 offset:36992\n\t"
    "v_cvt_pk_bf16_f32 v106, v176, v177\n\t"
    "v_mfma_f32_16x16x32_bf16 v[80:83], v[148:151], a[200:203], v[80:83]\n\t"
    "ds_read_b128 v[156:159], v193 offset:38528\n\t"
    "v_lshlrev_b32 v216, 16, v190\n\t"
    "v_mfma_f32_16x16x32_bf16 v[84:87], v[148:151], a[216:219], v[84:87]\n\t"
    "v_mfma_f32_16x16x32_bf16 v[68:71], v[144:147], a[216:219], v[68:71]\n\t"
    "s_add_u32 m0, s60, 0\n\t"
    "v_exp_f32 v179, v59\n\t"
    "v_and_b32 v217, 0xffff0000, v190\n\t"
    "v_mfma_f32_16x16x32_bf16 v[72:75], v[144:147], a[232:235], v[72:75]\n\t"
    "buffer_load_dwordx4 v195, s[44:47], s68 offen offset:0 sc1 lds\n\t"
    "v_mfma_f32_16x16x32_bf16 v[88:91], v[148:151], a[232:235], v[88:91]\n\t"
    "v_exp_f32 v176, v44\n\t"
    "v_lshlrev_b32 v210, 16, v255\n\t"
    "v_mfma_f32_16x16x32_bf16 v[92:95], v[148:151], a[248:251], v[92:95]\n\t"
    "v_add_f32 v174, v174, v178\n\t"
    "v_add_f32 v174, v174, v179\n\t"
    "v_mfma_f32_16x16x32_bf16 v[76:79], v[144:147], a[248:251], v[76:79]\n\t"
    "s_waitcnt lgkmcnt(1)\n\t"
    "v_exp_f32 v177, v45\n\t"
    "v_and_b32 v211, 0xffff0000, v255\n\t"
    "v_mfma_f32_16x16x32_bf16 v[64:67], v[152:155], a[204:207], v[64:67]\n\t"
    "s_waitcnt lgkmcnt(0)\n\t"
    "ds_read_b128 v[144:147], v192 offset:37120\n\t"
    "v_cvt_pk_bf16_f32 v107, v178, v179\n\t"
    "v_mfma_f32_16x16x32_bf16 v[80:83], v[156:159], a[204:207], v[80:83]\n\t"
    "ds_read_b128 v[148:151], v192 offset:38656\n\t"
    "v_add_f32 v175, v176, v177\n\t"
    "v_lshlrev_b32 v218, 16, v191\n\t"
    "v_mfma_f32_16x16x32_bf16 v[84:87], v[156:159], a[220:223], v[84:87]\n\t"
    "v_cvt_pk_bf16_f32 v108, v176, v177\n\t"
    "v_mfma_f32_16x16x32_bf16 v[68:71], v[152:155], a[220:223], v[68:71]\n\t"
    "v_exp_f32 v178, v46\n\t"
    "v_and_b32 v219, 0xffff0000, v191\n\t"
    "v_mfma_f32_16x16x32_bf16 v[72:75], v[152:155], a[236:239], v[72:75]\n\t"
    "buffer_load_dwordx4 v195, s[44:47], s68 offen offset:1024 sc1 lds\n\t"
    "v_mfma_f32_16x16x32_bf16 v[88:91], v[156:159], a[236:239], v[88:91]\n\t"
    "v_exp_f32 v179, v47\n\t"
    "v_mul_f32_e64 v212, -v212, v204\n\t"
    "v_mfma_f32_16x16x32_bf16 v[92:95], v[156:159], a[252:255], v[92:95]\n\t"
    "v_add_f32 v175, v175, v178\n\t"
    "v_add_f32 v175, v175, v179\n\t"
    "v_mfma_f32_16x16x32_bf16 v[76:79], v[152:155], a[252:255], v[76:79]\n\t"
    "s_waitcnt lgkmcnt(1)\n\t"
    "v_exp_f32 v176, v60\n\t"
    "v_mfma_f32_16x16x32_bf16 v[64:67], v[144:147], v[0:3], v[64:67]\n\t"
    "s_waitcnt lgkmcnt(0)\n\t"
    "ds_read_b128 v[152:155], v193 offset:37120\n\t"
    "v_cvt_pk_bf16_f32 v109, v178, v179\n\t"
    "v_mul_f32_e64 v213, -v213, v205\n\t"
    "v_mfma_f32_16x16x32_bf16 v[80:83], v[148:151], v[0:3], v[80:83]\n\t"
    "ds_read_b128 v[156:159], v193 offset:38656\n\t"
    "v_mfma_f32_16x16x32_bf16 v[84:87], v[148:151], v[8:11], v[84:87]\n\t"
    "v_mul_f32_e64 v214, -v214, v206\n\t"
    "v_mfma_f32_16x16x32_bf16 v[68:71], v[144:147], v[8:11], v[68:71]\n\t"
    "v_exp_f32 v177, v61\n\t"
    "v_mfma_f32_16x16x32_bf16 v[72:75], v[144:147], v[16:19], v[72:75]\n\t"
    "buffer_load_dwordx4 v195, s[44:47], s68 offen offset:2048 sc1 lds\n\t"
    "v_mul_f32_e64 v215, -v215, v207\n\t"
    "v_mfma_f32_16x16x32_bf16 v[88:91], v[148:151], v[16:19], v[88:91]\n\t"
    "v_exp_f32 v178, v62\n\t"
    "v_mfma_f32_16x16x32_bf16 v[92:95], v[148:151], v[24:27], v[92:95]\n\t"
    "v_add_f32 v175, v175, v176\n\t"
    "v_add_f32 v175, v175, v177\n\t"
    "v_mul_f32_e64 v216, -v216, v208\n\t"
    "v_mfma_f32_16x16x32_bf16 v[76:79], v[144:147], v[24:27], v[76:79]\n\t"
    "s_waitcnt lgkmcnt(1)\n\t"
    "v_exp_f32 v179, v63\n\t"
    "v_mfma_f32_16x16x32_bf16 v[64:67], v[152:155], v[4:7], v[64:67]\n\t"
    "s_waitcnt lgkmcnt(0)\n\t"
    "ds_read_b128 v[144:147], v181 offset:0\n\t"
    "v_cvt_pk_bf16_f32 v110, v176, v177\n\t"
    "v_mul_f32_e64 v217, -v217, v209\n\t"
    "v_mfma_f32_16x16x32_bf16 v[80:83], v[156:159], v[4:7], v[80:83]\n\t"
    "ds_read_b128 v[148:151], v181 offset:256\n\t"
    "v_add_f32 v175, v175, v178\n\t"
    "v_mfma_f32_16x16x32_bf16 v[84:87], v[156:159], v[12:15], v[84:87]\n\t"
    "v_add_f32 v175, v175, v179\n\t"
    "v_cvt_pk_bf16_f32 v111, v178, v179\n\t"
    "v_mul_f32_e64 v218, -v218, v210\n\t"
    "v_mfma_f32_16x16x32_bf16 v[68:71], v[152:155], v[12:15], v[68:71]\n\t"
    "v_max3_f32 v180, v172, v173, v174\n\t"
    "v_max_f32 v180, v180, v175\n\t"
    "v_mfma_f32_16x16x32_bf16 v[72:75], v[152:155], v[20:23], v[72:75]\n\t"
    "v_cmp_lt_f32 vcc, s62, v180\n\t"
    "v_mul_f32_e64 v219, -v219, v211\n\t"
    "v_mfma_f32_16x16x32_bf16 v[88:91], v[156:159], v[20:23], v[88:91]\n\t"
    "v_mfma_f32_16x16x32_bf16 v[92:95], v[156:159], v[28:31], v[92:95]\n\t"
    "v_mul_f32 v204, 0x4b800000, v204\n\t"
    "v_mfma_f32_16x16x32_bf16 v[76:79], v[152:155], v[28:31], v[76:79]\n\t"
    "s_waitcnt lgkmcnt(1)\n\t"
    "s_cbranch_vccnz L_stub_s2_0\n\t"
    "L_ret_s2_0:\n\t"
    "v_mfma_f32_16x16x32_bf16 a[0:3], v[96:99], v[144:147], a[0:3]\n\t"
    "ds_read_b128 v[152:155], v181 offset:512\n\t"
    "v_add_f32 v168, v168, v172\n\t"
    "v_mfma_f32_16x16x32_bf16 a[4:7], v[100:103], v[144:147], a[4:7]\n\t"
    "v_exp_f32 v176, v64\n\t"
    "v_mul_f32 v205, 0x4b800000, v205\n\t"
    "v_mfma_f32_16x16x32_bf16 a[8:11], v[104:107], v[144:147], a[8:11]\n\t"
    "v_add_f32 v169, v169, v173\n\t"
    "v_add_f32 v170, v170, v174\n\t"
    "v_mfma_f32_16x16x32_bf16 a[12:15], v[108:111], v[144:147], a[12:15]\n\t"
    "s_waitcnt lgkmcnt(1)\n\t"
    "v_exp_f32 v177, v65\n\t"
    "v_mul_f32 v206, 0x4b800000, v206\n\t"
    "v_mfma_f32_16x16x32_bf16 a[28:31], v[108:111], v[148:151], a[28:31]\n\t"
    "ds_read_b128 v[156:159], v181 offset:768\n\t"
    "v_add_f32 v171, v171, v175\n\t"
    "v_mfma_f32_16x16x32_bf16 a[24:27], v[104:107], v[148:151], a[24:27]\n\t"
    "v_add_f32 v172, v176, v177\n\t"
    "v_cvt_pk_bf16_f32 v112, v176, v177\n\t"
    "v_mul_f32 v207, 0x4b800000, v207\n\t"
    "v_mfma_f32_16x16x32_bf16 a[20:23], v[100:103], v[148:151], a[20:23]\n\t"
    "v_mfma_f32_16x16x32_bf16 a[16:19], v[96:99], v[148:151], a[16:19]\n\t"
    "s_waitcnt lgkmcnt(1)\n\t"
    "v_exp_f32 v178, v66\n\t"
    "v_mul_f32 v208, 0x4b800000, v208\n\t"
    "v_mfma_f32_16x16x32_bf16 a[32:35], v[96:99], v[152:155], a[32:35]\n\t"
    "ds_read_b128 v[144:147], v181 offset:1024\n\t"
    "v_mfma_f32_16x16x32_bf16 a[36:39], v[100:103], v[152:155], a[36:39]\n\t"
    "v_exp_f32 v179, v67\n\t"
    "v_mul_f32 v209, 0x4b800000, v209\n\t"
    "v_mfma_f32_16x16x32_bf16 a[40:43], v[104:107], v[152:155], a[40:43]\n\t"
    "v_add_f32 v172, v172, v178\n\t"
    "v_add_f32 v172, v172, v179\n\t"
    "v_mfma_f32_16x16x32_bf16 a[44:47], v[108:111], v[152:155], a[44:47]\n\t"
    "s_waitcnt lgkmcnt(1)\n\t"
    "v_cvt_pk_bf16_f32 v113, v178, v179\n\t"
    "v_mul_f32 v210, 0x4b800000, v210\n\t"
    "v_mfma_f32_16x16x32_bf16 a[60:63], v[108:111], v[156:159], a[60:63]\n\t"
    "ds_read_b128 v[148:151], v181 offset:1280\n\t"
    "v_mfma_f32_16x16x32_bf16 a[56:59], v[104:107], v[156:159], a[56:59]\n\t"
    "v_exp_f32 v176, v80\n\t"
    "v_mul_f32 v211, 0x4b800000, v211\n\t"
    "v_mfma_f32_16x16x32_bf16 a[52:55], v[100:103], v[156:159], a[52:55]\n\t"
    "s_waitcnt lgkmcnt(15)\n\t"
    "v_mfma_f32_16x16x32_bf16 a[48:51], v[96:99], v[156:159], a[48:51]\n\t"
    "s_waitcnt lgkmcnt(1)\n\t"
    "v_exp_f32 v177, v81\n\t"
    "v_mfma_f32_16x16x32_bf16 a[64:67], v[96:99], v[144:147], a[64:67]\n\t"
    "ds_read_b128 v[152:155], v181 offset:1536\n\t"
    "v_add_f32 v172, v172, v176\n\t"
    "v_fma_mix_f32 v220, v220, v204, v212 op_sel_hi:[1,0,0]\n\t"
    "v_mfma_f32_16x16x32_bf16 a[68:71], v[100:103], v[144:147], a[68:71]\n\t"
    "v_add_f32 v172, v172, v177\n\t"
    "v_cvt_pk_bf16_f32 v114, v176, v177\n\t"
    "v_mfma_f32_16x16x32_bf16 a[72:75], v[104:107], v[144:147], a[72:75]\n\t"
    "v_fma_mix_f32 v221, v221, v204, v212 op_sel_hi:[1,0,0]\n\t"
    "v_mfma_f32_16x16x32_bf16 a[76:79], v[108:111], v[144:147], a[76:79]\n\t"
    "s_waitcnt lgkmcnt(1)\n\t"
    "v_exp_f32 v178, v82\n\t"
    "v_mfma_f32_16x16x32_bf16 a[92:95], v[108:111], v[148:151], a[92:95]\n\t"
    "ds_read_b128 v[156:159], v181 offset:1792\n\t"
    "v_fma_mix_f32 v222, v222, v204, v212 op_sel_hi:[1,0,0]\n\t"
    "v_mfma_f32_16x16x32_bf16 a[88:91], v[104:107], v[148:151], a[88:91]\n\t"
    "v_exp_f32 v179, v83\n\t"
    "v_mfma_f32_16x16x32_bf16 a[84:87], v[100:103], v[148:151], a[84:87]\n\t"
    "v_add_f32 v172, v172, v178\n\t"
    "v_add_f32 v172, v172, v179\n\t"
    "v_fma_mix_f32 v223, v223, v204, v212 op_sel_hi:[1,0,0]\n\t"
    "v_mfma_f32_16x16x32_bf16 a[80:83], v[96:99], v[148:151], a[80:83]\n\t"
    "s_waitcnt lgkmcnt(1)\n\t"
    "v_cvt_pk_bf16_f32 v115, v178, v179\n\t"
    "v_mfma_f32_16x16x32_bf16 a[96:99], v[96:99], v[152:155], a[96:99]\n\t"
    "ds_read_b128 v[144:147], v181 offset:2048\n\t"
    "v_fma_mix_f32 v224, v224, v205, v213 op_sel_hi:[1,0,0]\n\t"
    "v_mfma_f32_16x16x32_bf16 a[100:103], v[100:103], v[152:155], a[100:103]\n\t"
    "v_exp_f32 v176, v68\n\t"
    "v_mfma_f32_16x16x32_bf16 a[104:107], v[104:107], v[152:155], a[104:107]\n\t"
    "v_fma_mix_f32 v225, v225, v205, v213 op_sel_hi:[1,0,0]\n\t"
    "v_mfma_f32_16x16x32_bf16 a[108:111], v[108:111], v[152:155], a[108:111]\n\t"
    "s_waitcnt lgkmcnt(1)\n\t"
    "v_exp_f32 v177, v69\n\t"
    "v_mfma_f32_16x16x32_bf16 a[124:127], v[108:111], v[156:159], a[124:127]\n\t"
    "ds_read_b128 v[148:151], v181 offset:2304\n\t"
    "v_add_f32 v173, v176, v177\n\t"
    "v_fma_mix_f32 v226, v226, v205, v213 op_sel_hi:[1,0,0]\n\t"
    "v_mfma_f32_16x16x32_bf16 a[120:123], v[104:107], v[156:159], a[120:123]\n\t"
    "v_cvt_pk_bf16_f32 v116, v176, v177\n\t"
    "v_mfma_f32_16x16x32_bf16 a[116:119], v[100:103], v[156:159], a[116:119]\n\t"
    "v_fma_mix_f32 v227, v227, v205, v213 op_sel_hi:[1,0,0]\n\t"
    "v_mfma_f32_16x16x32_bf16 a[112:115], v[96:99], v[156:159], a[112:115]\n\t"
    "s_waitcnt lgkmcnt(1)\n\t"
    "v_exp_f32 v178, v70\n\t"
    "v_mfma_f32_16x16x32_bf16 a[128:131], v[96:99], v[144:147], a[128:131]\n\t"
    "ds_read_b128 v[152:155], v181 offset:2560\n\t"
    "v_mfma_f32_16x16x32_bf16 a[132:135], v[100:103], v[144:147], a[132:135]\n\t"
    "s_waitcnt vmcnt(9)\n\t"
    "v_exp_f32 v179, v71\n\t"
    "v_cvt_pk_bf16_f32 v244, v220, v224\n\t"
    "v_mfma_f32_16x16x32_bf16 a[136:139], v[104:107], v[144:147], a[136:139]\n\t"
    "s_barrier\n\t"
    "v_add_f32 v173, v173, v178\n\t"
    "v_add_f32 v173, v173, v179\n\t"
    "v_mfma_f32_16x16x32_bf16 a[140:143], v[108:111], v[144:147], a[140:143]\n\t"
    "s_waitcnt lgkmcnt(1)\n\t"
    "v_cvt_pk_bf16_f32 v117, v178, v179\n\t"
    "v_cvt_pk_bf16_f32 v248, v221, v225\n\t"
    "v_mfma_f32_16x16x32_bf16 a[156:159], v[108:111], v[148:151], a[156:159]\n\t"
    "ds_read_b128 v[156:159], v181 offset:2816\n\t"
    "v_mfma_f32_16x16x32_bf16 a[152:155], v[104:107], v[148:151], a[152:155]\n\t"
    "s_add_u32 m0, s60, 49152\n\t"
    "v_exp_f32 v176, v84\n\t"
    "v_cvt_pk_bf16_f32 v252, v222, v226\n\t"
    "v_mfma_f32_16x16x32_bf16 a[148:151], v[100:103], v[148:151], a[148:151]\n\t"
    "buffer_load_dwordx4 v195, s[40:43], s70 offen offset:0 sc1 lds\n\t"
    "v_mfma_f32_16x16x32_bf16 a[144:147], v[96:99], v[148:151], a[144:147]\n\t"
    "s_waitcnt lgkmcnt(1)\n\t"
    "v_exp_f32 v177, v85\n\t"
    "v_cvt_pk_bf16_f32 v188, v223, v227\n\t"
    "v_mfma_f32_16x16x32_bf16 a[160:163], v[96:99], v[152:155], a[160:163]\n\t"
    "s_waitcnt lgkmcnt(15)\n\t"
    "ds_read_b128 v[144:147], v192 offset:0\n\t"
    "v_add_f32 v173, v173, v176\n\t"
    "v_mfma_f32_16x16x32_bf16 a[164:167], v[100:103], v[152:155], a[164:167]\n\t"
    "ds_read_b128 v[148:151], v192 offset:1536\n\t"
    "v_add_f32 v173, v173, v177\n\t"
    "v_fma_mix_f32 v228, v228, v206, v214 op_sel_hi:[1,0,0]\n\t"
    "v_mfma_f32_16x16x32_bf16 a[168:171], v[104:107], v[152:155], a[168:171]\n\t"
    "buffer_load_dwordx4 v195, s[40:43], s70 offen offset:1024 sc1 lds\n\t"
    "v_mfma_f32_16x16x32_bf16 a[172:175], v[108:111], v[152:155], a[172:175]\n\t"
    "s_waitcnt lgkmcnt(2)\n\t"
    "global_load_dwordx4 v[220:223], v196, s[78:79] offset:-2048\n\t"
    "v_exp_f32 v178, v86\n\t"
    "v_fma_mix_f32 v229, v229, v206, v214 op_sel_hi:[1,0,0]\n\t"
    "v_mfma_f32_16x16x32_bf16 a[188:191], v[108:111], v[156:159], a[188:191]\n\t"
    "global_load_dwordx4 v[224:227], v196, s[78:79] offset:-2032\n\t"
    "v_cvt_pk_bf16_f32 v118, v176, v177\n\t"
    "v_mfma_f32_16x16x32_bf16 a[184:187], v[104:107], v[156:159], a[184:187]\n\t"
    "v_exp_f32 v179, v87\n\t"
    "v_fma_mix_f32 v230, v230, v206, v214 op_sel_hi:[1,0,0]\n\t"
    "v_mfma_f32_16x16x32_bf16 a[180:183], v[100:103], v[156:159], a[180:183]\n\t"
    "v_add_f32 v173, v173, v178\n\t"
    "v_add_f32 v173, v173, v179\n\t"
    "v_mfma_f32_16x16x32_bf16 a[176:179], v[96:99], v[156:159], a[176:179]\n\t"
    "s_waitcnt lgkmcnt(1)\n\t"
    "v_cvt_pk_bf16_f32 v119, v178, v179\n\t"
    "v_mfma_f32_16x16x32_bf16 v[32:35], v[144:147], a[192:195], v[128:131]\n\t"
    "s_waitcnt lgkmcnt(0)\n\t"
    "ds_read_b128 v[152:155], v193 offset:0\n\t"
    "v_fma_mix_f32 v231, v231, v206, v214 op_sel_hi:[1,0,0]\n\t"
    "v_mfma_f32_16x16x32_bf16 v[48:51], v[148:151], a[192:195], v[128:131]\n\t"
    "ds_read_b128 v[156:159], v193 offset:1536\n\t"
    "v_mfma_f32_16x16x32_bf16 v[52:55], v[148:151], a[208:211], v[132:135]\n\t"
    "v_exp_f32 v176, v72\n\t"
    "v_fma_mix_f32 v232, v232, v207, v215 op_sel_hi:[1,0,0]\n\t"
    "v_mfma_f32_16x16x32_bf16 v[36:39], v[144:147], a[208:211], v[132:135]\n\t"
    "v_exp_f32 v177, v73\n\t"
    "v_mfma_f32_16x16x32_bf16 v[40:43], v[144:147], a[224:227], v[136:139]\n\t"
    "buffer_load_dwordx4 v195, s[40:43], s70 offen offset:2048 sc1 lds\n\t"
    "v_fma_mix_f32 v233, v233, v207, v215 op_sel_hi:[1,0,0]\n\t"
    "v_mfma_f32_16x16x32_bf16 v[56:59], v[148:151], a[224:227], v[136:139]\n\t"
    "v_exp_f32 v178, v74\n\t"
    "v_mfma_f32_16x16x32_bf16 v[60:63], v[148:151], a[240:243], v[140:143]\n\t"
    "v_add_f32 v174, v176, v177\n\t"
    "v_cvt_pk_bf16_f32 v120, v176, v177\n\t"
    "v_fma_mix_f32 v234, v234, v207, v215 op_sel_hi:[1,0,0]\n\t"
    "v_mfma_f32_16x16x32_bf16 v[44:47], v[144:147], a[240:243], v[140:143]\n\t"
    "s_waitcnt lgkmcnt(1)\n\t"
    "v_exp_f32 v179, v75\n\t"
    "v_mfma_f32_16x16x32_bf16 v[32:35], v[152:155], a[196:199], v[32:35]\n\t"
    "s_waitcnt lgkmcnt(0)\n\t"
    "ds_read_b128 v[144:147], v192 offset:128\n\t"
    "v_add_f32 v174, v174, v178\n\t"
    "v_fma_mix_f32 v235, v235, v207, v215 op_sel_hi:[1,0,0]\n\t"
    "v_mfma_f32_16x16x32_bf16 v[48:51], v[156:159], a[196:199], v[48:51]\n\t"
    "ds_read_b128 v[148:151], v192 offset:1664\n\t"
    "v_add_f32 v174, v174, v179\n\t"
    "v_mfma_f32_16x16x32_bf16 v[52:55], v[156:159], a[212:215], v[52:55]\n\t"
    "v_cvt_pk_bf16_f32 v121, v178, v179\n\t"
    "v_cvt_pk_bf16_f32 v245, v228, v232\n\t"
    "v_mfma_f32_16x16x32_bf16 v[36:39], v[152:155], a[212:215], v[36:39]\n\t"
    "v_exp_f32 v176, v88\n\t"
    "v_mfma_f32_16x16x32_bf16 v[40:43], v[152:155], a[228:231], v[40:43]\n\t"
    "v_cvt_pk_bf16_f32 v249, v229, v233\n\t"
    "v_mfma_f32_16x16x32_bf16 v[56:59], v[156:159], a[228:231], v[56:59]\n\t"
    "v_exp_f32 v177, v89\n\t"
    "v_mfma_f32_16x16x32_bf16 v[60:63], v[156:159], a[244:247], v[60:63]\n\t"
    "v_add_f32 v174, v174, v176\n\t"
    "v_add_f32 v174, v174, v177\n\t"
    "v_cvt_pk_bf16_f32 v253, v230, v234\n\t"
    "v_mfma_f32_16x16x32_bf16 v[44:47], v[152:155], a[244:247], v[44:47]\n\t"
    "s_waitcnt lgkmcnt(1)\n\t"
    "v_exp_f32 v178, v90\n\t"
    "v_mfma_f32_16x16x32_bf16 v[32:35], v[144:147], a[200:203], v[32:35]\n\t"
    "s_waitcnt lgkmcnt(0)\n\t"
    "ds_read_b128 v[152:155], v193 offset:128\n\t"
    "v_cvt_pk_bf16_f32 v122, v176, v177\n\t"
    "v_mfma_f32_16x16x32_bf16 v[48:51], v[148:151], a[200:203], v[48:51]\n\t"
    "ds_read_b128 v[156:159], v193 offset:1664\n\t"
    "v_cvt_pk_bf16_f32 v189, v231, v235\n\t"
    "v_mfma_f32_16x16x32_bf16 v[52:55], v[148:151], a[216:219], v[52:55]\n\t"
    "s_waitcnt lgkmcnt(15)\n\t"
    "global_load_dwordx4 v[228:231], v196, s[78:79] offset:-512\n\t"
    "v_mfma_f32_16x16x32_bf16 v[36:39], v[144:147], a[216:219], v[36:39]\n\t"
    "s_add_u32 m0, s60, 12288\n\t"
    "v_exp_f32 v179, v91\n\t"
    "v_fma_mix_f32 v236, v236, v208, v216 op_sel_hi:[1,0,0]\n\t"
    "v_mfma_f32_16x16x32_bf16 v[40:43], v[144:147], a[232:235], v[40:43]\n\t"
    "buffer_load_dwordx4 v195, s[44:47], s69 offen offset:0 sc1 lds\n\t"
    "v_mfma_f32_16x16x32_bf16 v[56:59], v[148:151], a[232:235], v[56:59]\n\t"
    "global_load_dwordx4 v[232:235], v196, s[78:79] offset:-496\n\t"
    "v_exp_f32 v176, v76\n\t"
    "v_fma_mix_f32 v237, v237, v208, v216 op_sel_hi:[1,0,0]\n\t"
    "v_mfma_f32_16x16x32_bf16 v[60:63], v[148:151], a[248:251], v[60:63]\n\t"
    "v_add_f32 v174, v174, v178\n\t"
    "v_add_f32 v174, v174, v179\n\t"
    "v_mfma_f32_16x16x32_bf16 v[44:47], v[144:147], a[248:251], v[44:47]\n\t"
    "s_waitcnt lgkmcnt(1)\n\t"
    "v_exp_f32 v177, v77\n\t"
    "v_fma_mix_f32 v238, v238, v208, v216 op_sel_hi:[1,0,0]\n\t"
    "v_mfma_f32_16x16x32_bf16 v[32:35], v[152:155], a[204:207], v[32:35]\n\t"
    "s_waitcnt lgkmcnt(0)\n\t"
    "ds_read_b128 v[144:147], v192 offset:256\n\t"
    "v_cvt_pk_bf16_f32 v123, v178, v179\n\t"
    "v_mfma_f32_16x16x32_bf16 v[48:51], v[156:159], a[204:207], v[48:51]\n\t"
    "ds_read_b128 v[148:151], v192 offset:1792\n\t"
    "v_add_f32 v175, v176, v177\n\t"
    "v_fma_mix_f32 v239, v239, v208, v216 op_sel_hi:[1,0,0]\n\t"
    "v_mfma_f32_16x16x32_bf16 v[52:55], v[156:159], a[220:223], v[52:55]\n\t"
    "v_cvt_pk_bf16_f32 v124, v176, v177\n\t"
    "v_mfma_f32_16x16x32_bf16 v[36:39], v[152:155], a[220:223], v[36:39]\n\t"
    "v_exp_f32 v178, v78\n\t"
    "v_fma_mix_f32 v240, v240, v209, v217 op_sel_hi:[1,0,0]\n\t"
    "v_mfma_f32_16x16x32_bf16 v[40:43], v[152:155], a[236:239], v[40:43]\n\t"
    "buffer_load_dwordx4 v195, s[44:47], s69 offen offset:1024 sc1 lds\n\t"
    "v_mfma_f32_16x16x32_bf16 v[56:59], v[156:159], a[236:239], v[56:59]\n\t"
    "v_exp_f32 v179, v79\n\t"
    "v_fma_mix_f32 v241, v241, v209, v217 op_sel_hi:[1,0,0]\n\t"
    "v_mfma_f32_16x16x32_bf16 v[60:63], v[156:159], a[252:255], v[60:63]\n\t"
    "v_add_f32 v175, v175, v178\n\t"
    "v_add_f32 v175, v175, v179\n\t"
    "v_mfma_f32_16x16x32_bf16 v[44:47], v[152:155], a[252:255], v[44:47]\n\t"
    "s_waitcnt lgkmcnt(1)\n\t"
    "v_exp_f32 v176, v92\n\t"
    "v_mfma_f32_16x16x32_bf16 v[32:35], v[144:147], v[0:3], v[32:35]\n\t"
    "s_waitcnt lgkmcnt(0)\n\t"
    "ds_read_b128 v[152:155], v193 offset:256\n\t"
    "v_cvt_pk_bf16_f32 v125, v178, v179\n\t"
    "v_fma_mix_f32 v242, v242, v209, v217 op_sel_hi:[1,0,0]\n\t"
    "v_mfma_f32_16x16x32_bf16 v[48:51], v[148:151], v[0:3], v[48:51]\n\t"
    "ds_read_b128 v[156:159], v193 offset:1792\n\t"
    "v_mfma_f32_16x16x32_bf16 v[52:55], v[148:151], v[8:11], v[52:55]\n\t"
    "v_fma_mix_f32 v243, v243, v209, v217 op_sel_hi:[1,0,0]\n\t"
    "v_mfma_f32_16x16x32_bf16 v[36:39], v[144:147], v[8:11], v[36:39]\n\t"
    "v_exp_f32 v177, v93\n\t"
    "v_mfma_f32_16x16x32_bf16 v[40:43], v[144:147], v[16:19], v[40:43]\n\t"
    "buffer_load_dwordx4 v195, s[44:47], s69 offen offset:2048 sc1 lds\n\t"
    "v_cvt_pk_bf16_f32 v246, v236, v240\n\t"
    "v_mfma_f32_16x16x32_bf16 v[56:59], v[148:151], v[16:19], v[56:59]\n\t"
    "v_exp_f32 v178, v94\n\t"
    "v_mfma_f32_16x16x32_bf16 v[60:63], v[148:151], v[24:27], v[60:63]\n\t"
    "v_add_f32 v175, v175, v176\n\t"
    "v_add_f32 v175, v175, v177\n\t"
    "v_cvt_pk_bf16_f32 v250, v237, v241\n\t"
    "v_mfma_f32_16x16x32_bf16 v[44:47], v[144:147], v[24:27], v[44:47]\n\t"
    "s_waitcnt lgkmcnt(1)\n\t"
    "v_exp_f32 v179, v95\n\t"
    "v_mfma_f32_16x16x32_bf16 v[32:35], v[152:155], v[4:7], v[32:35]\n\t"
    "s_waitcnt lgkmcnt(0)\n\t"
    "ds_read_b128 v[144:147], v181 offset:12288\n\t"
    "v_cvt_pk_bf16_f32 v126, v176, v177\n\t"
    "v_cvt_pk_bf16_f32 v254, v238, v242\n\t"
    "v_mfma_f32_16x16x32_bf16 v[48:51], v[156:159], v[4:7], v[48:51]\n\t"
    "ds_read_b128 v[148:151], v181 offset:12544\n\t"
    "v_add_f32 v175, v175, v178\n\t"
    "v_mfma_f32_16x16x32_bf16 v[52:55], v[156:159], v[12:15], v[52:55]\n\t"
    "v_add_f32 v175, v175, v179\n\t"
    "v_cvt_pk_bf16_f32 v127, v178, v179\n\t"
    "v_cvt_pk_bf16_f32 v190, v239, v243\n\t"
    "v_mfma_f32_16x16x32_bf16 v[36:39], v[152:155], v[12:15], v[36:39]\n\t"
    "s_waitcnt lgkmcnt(15)\n\t"
    "v_max3_f32 v180, v172, v173, v174\n\t"
    "v_max_f32 v180, v180, v175\n\t"
    "v_mfma_f32_16x16x32_bf16 v[40:43], v[152:155], v[20:23], v[40:43]\n\t"
    "v_cmp_lt_f32 vcc, s62, v180\n\t"
    "v_fma_mix_f32 v160, v160, v210, v218 op_sel_hi:[1,0,0]\n\t"
    "v_mfma_f32_16x16x32_bf16 v[56:59], v[156:159], v[20:23], v[56:59]\n\t"
    "global_load_dwordx4 v[236:239], v196, s[78:79] offset:1024\n\t"
    "v_mfma_f32_16x16x32_bf16 v[60:63], v[156:159], v[28:31], v[60:63]\n\t"
    "global_load_dwordx4 v[240:243], v196, s[78:79] offset:1040\n\t"
    "v_fma_mix_f32 v161, v161, v210, v218 op_sel_hi:[1,0,0]\n\t"
    "v_mfma_f32_16x16x32_bf16 v[44:47], v[152:155], v[28:31], v[44:47]\n\t"
    "s_waitcnt lgkmcnt(1)\n\t"
    "s_cbranch_vccnz L_stub_s3_1\n\t"
    "L_ret_s3_1:\n\t"
    "v_mfma_f32_16x16x32_bf16 a[0:3], v[112:115], v[144:147], a[0:3]\n\t"
    "ds_read_b128 v[152:155], v181 offset:12800\n\t"
    "v_add_f32 v168, v168, v172\n\t"
    "v_mfma_f32_16x16x32_bf16 a[4:7], v[116:119], v[144:147], a[4:7]\n\t"
    "v_exp_f32 v176, v32\n\t"
    "v_fma_mix_f32 v162, v162, v210, v218 op_sel_hi:[1,0,0]\n\t"
    "v_mfma_f32_16x16x32_bf16 a[8:11], v[120:123], v[144:147], a[8:11]\n\t"
    "v_add_f32 v169, v169, v173\n\t"
    "v_add_f32 v170, v170, v174\n\t"
    "v_mfma_f32_16x16x32_bf16 a[12:15], v[124:127], v[144:147], a[12:15]\n\t"
    "s_waitcnt lgkmcnt(1)\n\t"
    "v_exp_f32 v177, v33\n\t"
    "v_fma_mix_f32 v163, v163, v210, v218 op_sel_hi:[1,0,0]\n\t"
    "v_mfma_f32_16x16x32_bf16 a[28:31], v[124:127], v[148:151], a[28:31]\n\t"
    "ds_read_b128 v[156:159], v181 offset:13056\n\t"
    "v_add_f32 v171, v171, v175\n\t"
    "v_mfma_f32_16x16x32_bf16 a[24:27], v[120:123], v[148:151], a[24:27]\n\t"
    "v_add_f32 v172, v176, v177\n\t"
    "v_cvt_pk_bf16_f32 v96, v176, v177\n\t"
    "v_fma_mix_f32 v164, v164, v211, v219 op_sel_hi:[1,0,0]\n\t"
    "v_mfma_f32_16x16x32_bf16 a[20:23], v[116:119], v[148:151], a[20:23]\n\t"
    "v_mfma_f32_16x16x32_bf16 a[16:19], v[112:115], v[148:151], a[16:19]\n\t"
    "s_waitcnt lgkmcnt(1)\n\t"
    "v_exp_f32 v178, v34\n\t"
    "v_fma_mix_f32 v165, v165, v211, v219 op_sel_hi:[1,0,0]\n\t"
    "v_mfma_f32_16x16x32_bf16 a[32:35], v[112:115], v[152:155], a[32:35]\n\t"
    "ds_read_b128 v[144:147], v181 offset:13312\n\t"
    "v_mfma_f32_16x16x32_bf16 a[36:39], v[116:119], v[152:155], a[36:39]\n\t"
    "v_exp_f32 v179, v35\n\t"
    "v_fma_mix_f32 v166, v166, v211, v219 op_sel_hi:[1,0,0]\n\t"
    "v_mfma_f32_16x16x32_bf16 a[40:43], v[120:123], v[152:155], a[40:43]\n\t"
    "v_add_f32 v172, v172, v178\n\t"
    "v_add_f32 v172, v172, v179\n\t"
    "v_mfma_f32_16x16x32_bf16 a[44:47], v[124:127], v[152:155], a[44:47]\n\t"
    "s_waitcnt lgkmcnt(1)\n\t"
    "v_cvt_pk_bf16_f32 v97, v178, v179\n\t"
    "v_fma_mix_f32 v167, v167, v211, v219 op_sel_hi:[1,0,0]\n\t"
    "v_mfma_f32_16x16x32_bf16 a[60:63], v[124:127], v[156:159], a[60:63]\n\t"
    "ds_read_b128 v[148:151], v181 offset:13568\n\t"
    "v_mfma_f32_16x16x32_bf16 a[56:59], v[120:123], v[156:159], a[56:59]\n\t"
    "v_exp_f32 v176, v48\n\t"
    "v_cvt_pk_bf16_f32 v247, v160, v164\n\t"
    "v_mfma_f32_16x16x32_bf16 a[52:55], v[116:119], v[156:159], a[52:55]\n\t"
    "buffer_store_dwordx4 v[244:247], v201, s[48:51], s92 offen offset:0 sc1\n\t"
    "v_mfma_f32_16x16x32_bf16 a[48:51], v[112:115], v[156:159], a[48:51]\n\t"
    "s_waitcnt lgkmcnt(1)\n\t"
    "v_exp_f32 v177, v49\n\t"
    "v_mfma_f32_16x16x32_bf16 a[64:67], v[112:115], v[144:147], a[64:67]\n\t"
    "ds_read_b128 v[152:155], v181 offset:13824\n\t"
    "v_add_f32 v172, v172, v176\n\t"
    "v_cvt_pk_bf16_f32 v251, v161, v165\n\t"
    "v_mfma_f32_16x16x32_bf16 a[68:71], v[116:119], v[144:147], a[68:71]\n\t"
    "v_add_f32 v172, v172, v177\n\t"
    "v_cvt_pk_bf16_f32 v98, v176, v177\n\t"
    "v_mfma_f32_16x16x32_bf16 a[72:75], v[120:123], v[144:147], a[72:75]\n\t"
    "v_cvt_pk_bf16_f32 v255, v162, v166\n\t"
    "v_mfma_f32_16x16x32_bf16 a[76:79], v[124:127], v[144:147], a[76:79]\n\t"
    "s_waitcnt lgkmcnt(1)\n\t"
    "v_exp_f32 v178, v50\n\t"
    "v_mfma_f32_16x16x32_bf16 a[92:95], v[124:127], v[148:151], a[92:95]\n\t"
    "ds_read_b128 v[156:159], v181 offset:14080\n\t"
    "v_cvt_pk_bf16_f32 v191, v163, v167\n\t"
    "v_mfma_f32_16x16x32_bf16 a[88:91], v[120:123], v[148:151], a[88:91]\n\t"
    "v_exp_f32 v179, v51\n\t"
    "v_mfma_f32_16x16x32_bf16 a[84:87], v[116:119], v[148:151], a[84:87]\n\t"
    "v_add_f32 v172, v172, v178\n\t"
    "v_add_f32 v172, v172, v179\n\t"
    "v_mfma_f32_16x16x32_bf16 a[80:83], v[112:115], v[148:151], a[80:83]\n\t"
    "s_waitcnt lgkmcnt(1)\n\t"
    "v_cvt_pk_bf16_f32 v99, v178, v179\n\t"
    "v_mfma_f32_16x16x32_bf16 a[96:99], v[112:115], v[152:155], a[96:99]\n\t"
    "ds_read_b128 v[144:147], v181 offset:14336\n\t"
    "v_mfma_f32_16x16x32_bf16 a[100:103], v[116:119], v[152:155], a[100:103]\n\t"
    "buffer_store_dwordx4 v[248:251], v201, s[48:51], s92 offen offset:256 sc1\n\t"
    "v_exp_f32 v176, v36\n\t"
    "v_mfma_f32_16x16x32_bf16 a[104:107], v[120:123], v[152:155], a[104:107]\n\t"
    "buffer_store_dwordx4 v[252:255], v201, s[48:51], s92 offen offset:512 sc1\n\t"
    "v_mfma_f32_16x16x32_bf16 a[108:111], v[124:127], v[152:155], a[108:111]\n\t"
    "s_waitcnt lgkmcnt(1)\n\t"
    "buffer_store_dwordx4 v[188:191], v201, s[48:51], s92 offen offset:768 sc1\n\t"
    "v_exp_f32 v177, v37\n\t"
    "v_mfma_f32_16x16x32_bf16 a[124:127], v[124:127], v[156:159], a[124:127]\n\t"
    "s_add_u32 s92, s92, 196608\n\t"
    "ds_read_b128 v[148:151], v181 offset:14592\n\t"
    "v_add_f32 v173, v176, v177\n\t"
    "v_mfma_f32_16x16x32_bf16 a[120:123], v[120:123], v[156:159], a[120:123]\n\t"
    "global_load_dwordx4 v[160:163], v196, s[78:79] offset:2560\n\t"
    "v_cvt_pk_bf16_f32 v100, v176, v177\n\t"
    "v_mfma_f32_16x16x32_bf16 a[116:119], v[116:119], v[156:159], a[116:119]\n\t"
    "global_load_dwordx4 v[164:167], v196, s[78:79] offset:2576\n\t"
    "v_mfma_f32_16x16x32_bf16 a[112:115], v[112:115], v[156:159], a[112:115]\n\t"
    "s_waitcnt lgkmcnt(1)\n\t"
    "global_load_dwordx4 v[252:255], v198, s[82:83]\n\t"
    "v_exp_f32 v178, v38\n\t"
    "v_mfma_f32_16x16x32_bf16 a[128:131], v[112:115], v[144:147], a[128:131]\n\t"
    "ds_read_b128 v[152:155], v181 offset:14848\n\t"
    "v_mfma_f32_16x16x32_bf16 a[132:135], v[116:119], v[144:147], a[132:135]\n\t"
    "s_waitcnt vmcnt(19)\n\t"
    "global_load_dwordx4 v[188:191], v198, s[84:85]\n\t"
    "v_exp_f32 v179, v39\n\t"
    "v_mfma_f32_16x16x32_bf16 a[136:139], v[120:123], v[144:147], a[136:139]\n\t"
    "s_barrier\n\t"
    "v_add_f32 v173, v173, v178\n\t"
    "v_add_f32 v173, v173, v179\n\t"
    "v_mfma_f32_16x16x32_bf16 a[140:143], v[124:127], v[144:147], a[140:143]\n\t"
    "s_waitcnt lgkmcnt(1)\n\t"
    "v_cvt_pk_bf16_f32 v101, v178, v179\n\t"
    "v_mfma_f32_16x16x32_bf16 a[156:159], v[124:127], v[148:151], a[156:159]\n\t"
    "ds_read_b128 v[156:159], v181 offset:15104\n\t"
    "v_mfma_f32_16x16x32_bf16 a[152:155], v[120:123], v[148:151], a[152:155]\n\t"
    "s_add_u32 m0, s60, 61440\n\t"
    "v_exp_f32 v176, v52\n\t"
    "v_mfma_f32_16x16x32_bf16 a[148:151], v[116:119], v[148:151], a[148:151]\n\t"
    "buffer_load_dwordx4 v195, s[40:43], s71 offen offset:0 sc1 lds\n\t"
    "v_mfma_f32_16x16x32_bf16 a[144:147], v[112:115], v[148:151], a[144:147]\n\t"
    "s_waitcnt lgkmcnt(1)\n\t"
    "v_exp_f32 v177, v53\n\t"
    "v_mfma_f32_16x16x32_bf16 a[160:163], v[112:115], v[152:155], a[160:163]\n\t"
    "ds_read_b128 v[144:147], v192 offset:12288\n\t"
    "v_add_f32 v173, v173, v176\n\t"
    "v_mfma_f32_16x16x32_bf16 a[164:167], v[116:119], v[152:155], a[164:167]\n\t"
    "ds_read_b128 v[148:151], v192 offset:13824\n\t"
    "v_add_f32 v173, v173, v177\n\t"
    "v_mfma_f32_16x16x32_bf16 a[168:171], v[120:123], v[152:155], a[168:171]\n\t"
    "buffer_load_dwordx4 v195, s[40:43], s71 offen offset:1024 sc1 lds\n\t"
    "v_mfma_f32_16x16x32_bf16 a[172:175], v[124:127], v[152:155], a[172:175]\n\t"
    "s_waitcnt lgkmcnt(2)\n\t"
    "v_exp_f32 v178, v54\n\t"
    "v_mfma_f32_16x16x32_bf16 a[188:191], v[124:127], v[156:159], a[188:191]\n\t"
    "s_cmp_lt_u32 s61, 4\n\t"
    "v_cvt_pk_bf16_f32 v102, v176, v177\n\t"
    "v_mfma_f32_16x16x32_bf16 a[184:187], v[120:123], v[156:159], a[184:187]\n\t"
    "s_cselect_b32 s37, 196608, 0\n\t"
    "v_exp_f32 v179, v55\n\t"
    "v_mfma_f32_16x16x32_bf16 a[180:183], v[116:119], v[156:159], a[180:183]\n\t"
    "s_cselect_b32 s38, 3072, 0\n\t"
    "v_add_f32 v173, v173, v178\n\t"
    "v_add_f32 v173, v173, v179\n\t"
    "v_mfma_f32_16x16x32_bf16 a[176:179], v[112:115], v[156:159], a[176:179]\n\t"
    "s_waitcnt lgkmcnt(1)\n\t"
    "v_cvt_pk_bf16_f32 v103, v178, v179\n\t"
    "v_mfma_f32_16x16x32_bf16 v[64:67], v[144:147], a[192:195], v[128:131]\n\t"
    "s_waitcnt lgkmcnt(0)\n\t"
    "ds_read_b128 v[152:155], v193 offset:12288\n\t"
    "v_mfma_f32_16x16x32_bf16 v[80:83], v[148:151], a[192:195], v[128:131]\n\t"
    "s_add_u32 s78, s78, s37\n\t"
    "ds_read_b128 v[156:159], v193 offset:13824\n\t"
    "v_mfma_f32_16x16x32_bf16 v[84:87], v[148:151], a[208:211], v[132:135]\n\t"
    "s_addc_u32 s79, s79, 0\n\t"
    "v_exp_f32 v176, v40\n\t"
    "v_mfma_f32_16x16x32_bf16 v[68:71], v[144:147], a[208:211], v[132:135]\n\t"
    "v_exp_f32 v177, v41\n\t"
    "v_mfma_f32_16x16x32_bf16 v[72:75], v[144:147], a[224:227], v[136:139]\n\t"
    "buffer_load_dwordx4 v195, s[40:43], s71 offen offset:2048 sc1 lds\n\t"
    "v_mfma_f32_16x16x32_bf16 v[88:91], v[148:151], a[224:227], v[136:139]\n\t"
    "s_add_u32 s82, s82, s38\n\t"
    "v_exp_f32 v178, v42\n\t"
    "v_mfma_f32_16x16x32_bf16 v[92:95], v[148:151], a[240:243], v[140:143]\n\t"
    "s_addc_u32 s83, s83, 0\n\t"
    "v_add_f32 v174, v176, v177\n\t"
    "v_cvt_pk_bf16_f32 v104, v176, v177\n\t"
    "v_mfma_f32_16x16x32_bf16 v[76:79], v[144:147], a[240:243], v[140:143]\n\t"
    "s_waitcnt lgkmcnt(1)\n\t"
    "v_exp_f32 v179, v43\n\t"
    "v_mfma_f32_16x16x32_bf16 v[64:67], v[152:155], a[196:199], v[64:67]\n\t"
    "s_waitcnt lgkmcnt(0)\n\t"
    "ds_read_b128 v[144:147], v192 offset:12416\n\t"
    "v_add_f32 v174, v174, v178\n\t"
    "v_mfma_f32_16x16x32_bf16 v[80:83], v[156:159], a[196:199], v[80:83]\n\t"
    "s_add_u32 s40, s40, 196608\n\t"
    "ds_read_b128 v[148:151], v192 offset:13952\n\t"
    "v_add_f32 v174, v174, v179\n\t"
    "v_mfma_f32_16x16x32_bf16 v[84:87], v[156:159], a[212:215], v[84:87]\n\t"
    "s_addc_u32 s41, s41, 0\n\t"
    "v_cvt_pk_bf16_f32 v105, v178, v179\n\t"
    "v_mfma_f32_16x16x32_bf16 v[68:71], v[152:155], a[212:215], v[68:71]\n\t"
    "s_add_u32 s84, s84, s38\n\t"
    "v_exp_f32 v176, v56\n\t"
    "v_mfma_f32_16x16x32_bf16 v[72:75], v[152:155], a[228:231], v[72:75]\n\t"
    "s_addc_u32 s85, s85, 0\n\t"
    "v_mfma_f32_16x16x32_bf16 v[88:91], v[156:159], a[228:231], v[88:91]\n\t"
    "s_add_u32 s52, s72, L_sfx-L_slow_0\n\t"
    "v_exp_f32 v177, v57\n\t"
    "v_mfma_f32_16x16x32_bf16 v[92:95], v[156:159], a[244:247], v[92:95]\n\t"
    "s_addc_u32 s53, s73, -1\n\t"
    "v_add_f32 v174, v174, v176\n\t"
    "v_add_f32 v174, v174, v177\n\t"
    "v_mfma_f32_16x16x32_bf16 v[76:79], v[152:155], a[244:247], v[76:79]\n\t"
    "s_waitcnt lgkmcnt(1)\n\t"
    "v_exp_f32 v178, v58\n\t"
    "v_mfma_f32_16x16x32_bf16 v[64:67], v[144:147], a[200:203], v[64:67]\n\t"
    "s_waitcnt lgkmcnt(0)\n\t"
    "ds_read_b128 v[152:155], v193 offset:12416\n\t"
    "v_cvt_pk_bf16_f32 v106, v176, v177\n\t"
    "v_mfma_f32_16x16x32_bf16 v[80:83], v[148:151], a[200:203], v[80:83]\n\t"
    "s_and_b32 s54, %[ci], 31\n\t"
    "ds_read_b128 v[156:159], v193 offset:13952\n\t"
    "v_mfma_f32_16x16x32_bf16 v[84:87], v[148:151], a[216:219], v[84:87]\n\t"
    "s_lshl_b32 s54, s54, 10\n\t"
    "v_mfma_f32_16x16x32_bf16 v[68:71], v[144:147], a[216:219], v[68:71]\n\t"
    "s_add_u32 m0, s60, 73728\n\t"
    "v_exp_f32 v179, v59\n\t"
    "v_mfma_f32_16x16x32_bf16 v[72:75], v[144:147], a[232:235], v[72:75]\n\t"
    "buffer_load_dwordx4 v195, s[44:47], s70 offen offset:0 sc1 lds\n\t"
    "v_mfma_f32_16x16x32_bf16 v[88:91], v[148:151], a[232:235], v[88:91]\n\t"
    "s_sub_u32 s55, s54, L_cend-L_sfx-1024\n\t"
    "v_exp_f32 v176, v44\n\t"
    "v_mfma_f32_16x16x32_bf16 v[92:95], v[148:151], a[248:251], v[92:95]\n\t"
    "s_min_u32 s54, s54, s55\n\t"
    "v_add_f32 v174, v174, v178\n\t"
    "v_add_f32 v174, v174, v179\n\t"
    "v_mfma_f32_16x16x32_bf16 v[76:79], v[144:147], a[248:251], v[76:79]\n\t"
    "s_waitcnt lgkmcnt(1)\n\t"
    "v_exp_f32 v177, v45\n\t"
    "v_mfma_f32_16x16x32_bf16 v[64:67], v[152:155], a[204:207], v[64:67]\n\t"
    "s_waitcnt lgkmcnt(0)\n\t"
    "ds_read_b128 v[144:147], v192 offset:12544\n\t"
    "v_cvt_pk_bf16_f32 v107, v178, v179\n\t"
    "v_mfma_f32_16x16x32_bf16 v[80:83], v[156:159], a[204:207], v[80:83]\n\t"
    "s_min_u32 s54, s54, L_cend-L_sfx-1024\n\t"
    "ds_read_b128 v[148:151], v192 offset:14080\n\t"
    "v_add_f32 v175, v176, v177\n\t"
    "v_mfma_f32_16x16x32_bf16 v[84:87], v[156:159], a[220:223], v[84:87]\n\t"
    "v_cvt_pk_bf16_f32 v108, v176, v177\n\t"
    "v_mfma_f32_16x16x32_bf16 v[68:71], v[152:155], a[220:223], v[68:71]\n\t"
    "v_exp_f32 v178, v46\n\t"
    "v_mfma_f32_16x16x32_bf16 v[72:75], v[152:155], a[236:239], v[72:75]\n\t"
    "buffer_load_dwordx4 v195, s[44:47], s70 offen offset:1024 sc1 lds\n\t"
    "v_mfma_f32_16x16x32_bf16 v[88:91], v[156:159], a[236:239], v[88:91]\n\t"
    "v_exp_f32 v179, v47\n\t"
    "v_mfma_f32_16x16x32_bf16 v[92:95], v[156:159], a[252:255], v[92:95]\n\t"
    "v_add_f32 v175, v175, v178\n\t"
    "v_add_f32 v175, v175, v179\n\t"
    "v_mfma_f32_16x16x32_bf16 v[76:79], v[152:155], a[252:255], v[76:79]\n\t"
    "s_waitcnt lgkmcnt(1)\n\t"
    "v_exp_f32 v176, v60\n\t"
    "v_mfma_f32_16x16x32_bf16 v[64:67], v[144:147], v[0:3], v[64:67]\n\t"
    "s_waitcnt lgkmcnt(0)\n\t"
    "ds_read_b128 v[152:155], v193 offset:12544\n\t"
    "v_cvt_pk_bf16_f32 v109, v178, v179\n\t"
    "v_mfma_f32_16x16x32_bf16 v[80:83], v[148:151], v[0:3], v[80:83]\n\t"
    "ds_read_b128 v[156:159], v193 offset:14080\n\t"
    "v_mfma_f32_16x16x32_bf16 v[84:87], v[148:151], v[8:11], v[84:87]\n\t"
    "v_mfma_f32_16x16x32_bf16 v[68:71], v[144:147], v[8:11], v[68:71]\n\t"
    "v_exp_f32 v177, v61\n\t"
    "v_mfma_f32_16x16x32_bf16 v[72:75], v[144:147], v[16:19], v[72:75]\n\t"
    "buffer_load_dwordx4 v195, s[44:47], s70 offen offset:2048 sc1 lds\n\t"
    "v_mfma_f32_16x16x32_bf16 v[88:91], v[148:151], v[16:19], v[88:91]\n\t"
    "v_exp_f32 v178, v62\n\t"
    "v_mfma_f32_16x16x32_bf16 v[92:95], v[148:151], v[24:27], v[92:95]\n\t"
    "v_add_f32 v175, v175, v176\n\t"
    "v_add_f32 v175, v175, v177\n\t"
    "v_mfma_f32_16x16x32_bf16 v[76:79], v[144:147], v[24:27], v[76:79]\n\t"
    "s_waitcnt lgkmcnt(1)\n\t"
    "v_exp_f32 v179, v63\n\t"
    "v_mfma_f32_16x16x32_bf16 v[64:67], v[152:155], v[4:7], v[64:67]\n\t"
    "s_waitcnt lgkmcnt(0)\n\t"
    "ds_read_b128 v[144:147], v194 offset:0\n\t"
    "v_cvt_pk_bf16_f32 v110, v176, v177\n\t"
    "v_mfma_f32_16x16x32_bf16 v[80:83], v[156:159], v[4:7], v[80:83]\n\t"
    "ds_read_b128 v[148:151], v194 offset:256\n\t"
    "v_add_f32 v175, v175, v178\n\t"
    "v_mfma_f32_16x16x32_bf16 v[84:87], v[156:159], v[12:15], v[84:87]\n\t"
    "v_add_f32 v175, v175, v179\n\t"
    "v_cvt_pk_bf16_f32 v111, v178, v179\n\t"
    "v_mfma_f32_16x16x32_bf16 v[68:71], v[152:155], v[12:15], v[68:71]\n\t"
    "v_max3_f32 v180, v172, v173, v174\n\t"
    "v_max_f32 v180, v180, v175\n\t"
    "v_mfma_f32_16x16x32_bf16 v[72:75], v[152:155], v[20:23], v[72:75]\n\t"
    "v_cmp_lt_f32 vcc, s62, v180\n\t"
    "v_mfma_f32_16x16x32_bf16 v[88:91], v[156:159], v[20:23], v[88:91]\n\t"
    "v_mfma_f32_16x16x32_bf16 v[92:95], v[156:159], v[28:31], v[92:95]\n\t"
    "v_mfma_f32_16x16x32_bf16 v[76:79], v[152:155], v[28:31], v[76:79]\n\t"
    "s_waitcnt lgkmcnt(1)\n\t"
    "s_cbranch_vccnz L_stub_s4_0\n\t"
    "L_ret_s4_0:\n\t"
    "v_mfma_f32_16x16x32_bf16 a[0:3], v[96:99], v[144:147], a[0:3]\n\t"
    "ds_read_b128 v[152:155], v194 offset:512\n\t"
    "v_add_f32 v168, v168, v172\n\t"
    "v_mfma_f32_16x16x32_bf16 a[4:7], v[100:103], v[144:147], a[4:7]\n\t"
    "v_exp_f32 v176, v64\n\t"
    "v_mfma_f32_16x16x32_bf16 a[8:11], v[104:107], v[144:147], a[8:11]\n\t"
    "v_add_f32 v169, v169, v173\n\t"
    "v_add_f32 v170, v170, v174\n\t"
    "v_mfma_f32_16x16x32_bf16 a[12:15], v[108:111], v[144:147], a[12:15]\n\t"
    "s_waitcnt lgkmcnt(1)\n\t"
    "v_exp_f32 v177, v65\n\t"
    "v_mfma_f32_16x16x32_bf16 a[28:31], v[108:111], v[148:151], a[28:31]\n\t"
    "ds_read_b128 v[156:159], v194 offset:768\n\t"
    "v_add_f32 v171, v171, v175\n\t"
    "v_mfma_f32_16x16x32_bf16 a[24:27], v[104:107], v[148:151], a[24:27]\n\t"
    "v_add_f32 v172, v176, v177\n\t"
    "v_cvt_pk_bf16_f32 v112, v176, v177\n\t"
    "v_mfma_f32_16x16x32_bf16 a[20:23], v[100:103], v[148:151], a[20:23]\n\t"
    "v_mfma_f32_16x16x32_bf16 a[16:19], v[96:99], v[148:151], a[16:19]\n\t"
    "s_waitcnt lgkmcnt(1)\n\t"
    "v_exp_f32 v178, v66\n\t"
    "v_mfma_f32_16x16x32_bf16 a[32:35], v[96:99], v[152:155], a[32:35]\n\t"
    "ds_read_b128 v[144:147], v194 offset:1024\n\t"
    "v_mfma_f32_16x16x32_bf16 a[36:39], v[100:103], v[152:155], a[36:39]\n\t"
    "v_exp_f32 v179, v67\n\t"
    "v_mfma_f32_16x16x32_bf16 a[40:43], v[104:107], v[152:155], a[40:43]\n\t"
    "v_add_f32 v172, v172, v178\n\t"
    "v_add_f32 v172, v172, v179\n\t"
    "v_mfma_f32_16x16x32_bf16 a[44:47], v[108:111], v[152:155], a[44:47]\n\t"
    "s_waitcnt lgkmcnt(1)\n\t"
    "v_cvt_pk_bf16_f32 v113, v178, v179\n\t"
    "v_mfma_f32_16x16x32_bf16 a[60:63], v[108:111], v[156:159], a[60:63]\n\t"
    "s_waitcnt vmcnt(10)\n\t"
    "ds_read_b128 v[148:151], v194 offset:1280\n\t"
    "v_add_u32 v202, s94, v183\n\t"
    "v_mfma_f32_16x16x32_bf16 a[56:59], v[104:107], v[156:159], a[56:59]\n\t"
    "s_add_u32 s94, s94, 1\n\t"
    "v_exp_f32 v176, v80\n\t"
    "v_mfma_f32_16x16x32_bf16 a[52:55], v[100:103], v[156:159], a[52:55]\n\t"
    "global_store_dwordx2 v184, v[202:203], s[56:57] sc1\n\t"
    "v_mfma_f32_16x16x32_bf16 a[48:51], v[96:99], v[156:159], a[48:51]\n\t"
    "s_waitcnt lgkmcnt(1)\n\t"
    "v_exp_f32 v177, v81\n\t"
    "v_mfma_f32_16x16x32_bf16 a[64:67], v[96:99], v[144:147], a[64:67]\n\t"
    "ds_read_b128 v[152:155], v194 offset:1536\n\t"
    "v_add_f32 v172, v172, v176\n\t"
    "v_mfma_f32_16x16x32_bf16 a[68:71], v[100:103], v[144:147], a[68:71]\n\t"
    "v_add_f32 v172, v172, v177\n\t"
    "v_cvt_pk_bf16_f32 v114, v176, v177\n\t"
    "v_mfma_f32_16x16x32_bf16 a[72:75], v[104:107], v[144:147], a[72:75]\n\t"
    "v_mfma_f32_16x16x32_bf16 a[76:79], v[108:111], v[144:147], a[76:79]\n\t"
    "s_waitcnt lgkmcnt(1)\n\t"
    "v_exp_f32 v178, v82\n\t"
    "v_mfma_f32_16x16x32_bf16 a[92:95], v[108:111], v[148:151], a[92:95]\n\t"
    "ds_read_b128 v[156:159], v194 offset:1792\n\t"
    "v_mfma_f32_16x16x32_bf16 a[88:91], v[104:107], v[148:151], a[88:91]\n\t"
    "v_exp_f32 v179, v83\n\t"
    "v_mfma_f32_16x16x32_bf16 a[84:87], v[100:103], v[148:151], a[84:87]\n\t"
    "v_add_f32 v172, v172, v178\n\t"
    "v_add_f32 v172, v172, v179\n\t"
    "v_mfma_f32_16x16x32_bf16 a[80:83], v[96:99], v[148:151], a[80:83]\n\t"
    "s_waitcnt lgkmcnt(1)\n\t"
    "v_cvt_pk_bf16_f32 v115, v178, v179\n\t"
    "v_mfma_f32_16x16x32_bf16 a[96:99], v[96:99], v[152:155], a[96:99]\n\t"
    "ds_read_b128 v[144:147], v194 offset:2048\n\t"
    "v_mfma_f32_16x16x32_bf16 a[100:103], v[100:103], v[152:155], a[100:103]\n\t"
    "v_exp_f32 v176, v68\n\t"
    "v_mfma_f32_16x16x32_bf16 a[104:107], v[104:107], v[152:155], a[104:107]\n\t"
    "v_mfma_f32_16x16x32_bf16 a[108:111], v[108:111], v[152:155], a[108:111]\n\t"
    "s_waitcnt lgkmcnt(1)\n\t"
    "v_exp_f32 v177, v69\n\t"
    "v_mfma_f32_16x16x32_bf16 a[124:127], v[108:111], v[156:159], a[124:127]\n\t"
    "ds_read_b128 v[148:151], v194 offset:2304\n\t"
    "v_add_f32 v173, v176, v177\n\t"
    "v_mfma_f32_16x16x32_bf16 a[120:123], v[104:107], v[156:159], a[120:123]\n\t"
    "v_cvt_pk_bf16_f32 v116, v176, v177\n\t"
    "v_mfma_f32_16x16x32_bf16 a[116:119], v[100:103], v[156:159], a[116:119]\n\t"
    "v_mfma_f32_16x16x32_bf16 a[112:115], v[96:99], v[156:159], a[112:115]\n\t"
    "s_waitcnt lgkmcnt(1)\n\t"
    "v_exp_f32 v178, v70\n\t"
    "v_mfma_f32_16x16x32_bf16 a[128:131], v[96:99], v[144:147], a[128:131]\n\t"
    "ds_read_b128 v[152:155], v194 offset:2560\n\t"
    "v_mfma_f32_16x16x32_bf16 a[132:135], v[100:103], v[144:147], a[132:135]\n\t"
    "s_waitcnt vmcnt(17)\n\t"
    "v_exp_f32 v179, v71\n\t"
    "v_mfma_f32_16x16x32_bf16 a[136:139], v[104:107], v[144:147], a[136:139]\n\t"
    "s_barrier\n\t"
    "v_add_f32 v173, v173, v178\n\t"
    "v_add_f32 v173, v173, v179\n\t"
    "v_mfma_f32_16x16x32_bf16 a[140:143], v[108:111], v[144:147], a[140:143]\n\t"
    "s_waitcnt lgkmcnt(1)\n\t"
    "v_cvt_pk_bf16_f32 v117, v178, v179\n\t"
    "v_mfma_f32_16x16x32_bf16 a[156:159], v[108:111], v[148:151], a[156:159]\n\t"
    "ds_read_b128 v[156:159], v194 offset:2816\n\t"
    "v_mfma_f32_16x16x32_bf16 a[152:155], v[104:107], v[148:151], a[152:155]\n\t"
    "s_add_u32 m0, s60, 24576\n\t"
    "v_exp_f32 v176, v84\n\t"
    "v_mfma_f32_16x16x32_bf16 a[148:151], v[100:103], v[148:151], a[148:151]\n\t"
    "buffer_load_dwordx4 v195, s[40:43], s64 offen offset:0 sc1 lds\n\t"
    "v_mfma_f32_16x16x32_bf16 a[144:147], v[96:99], v[148:151], a[144:147]\n\t"
    "s_waitcnt lgkmcnt(1)\n\t"
    "v_exp_f32 v177, v85\n\t"
    "v_mfma_f32_16x16x32_bf16 a[160:163], v[96:99], v[152:155], a[160:163]\n\t"
    "ds_read_b128 v[144:147], v192 offset:24576\n\t"
    "v_add_f32 v173, v173, v176\n\t"
    "v_mfma_f32_16x16x32_bf16 a[164:167], v[100:103], v[152:155], a[164:167]\n\t"
    "ds_read_b128 v[148:151], v192 offset:26112\n\t"
    "v_add_f32 v173, v173, v177\n\t"
    "v_mfma_f32_16x16x32_bf16 a[168:171], v[104:107], v[152:155], a[168:171]\n\t"
    "buffer_load_dwordx4 v195, s[40:43], s64 offen offset:1024 sc1 lds\n\t"
    "v_mfma_f32_16x16x32_bf16 a[172:175], v[108:111], v[152:155], a[172:175]\n\t"
    "s_waitcnt lgkmcnt(2)\n\t"
    "v_exp_f32 v178, v86\n\t"
    "v_mfma_f32_16x16x32_bf16 a[188:191], v[108:111], v[156:159], a[188:191]\n\t"
    "v_cvt_pk_bf16_f32 v118, v176, v177\n\t"
    "v_mfma_f32_16x16x32_bf16 a[184:187], v[104:107], v[156:159], a[184:187]\n\t"
    "v_exp_f32 v179, v87\n\t"
    "v_mfma_f32_16x16x32_bf16 a[180:183], v[100:103], v[156:159], a[180:183]\n\t"
    "v_add_f32 v173, v173, v178\n\t"
    "v_add_f32 v173, v173, v179\n\t"
    "v_mfma_f32_16x16x32_bf16 a[176:179], v[96:99], v[156:159], a[176:179]\n\t"
    "s_waitcnt lgkmcnt(1)\n\t"
    "v_cvt_pk_bf16_f32 v119, v178, v179\n\t"
    ".p2align 6\n\t"
    "L_loop:\n\t"
    "v_mfma_f32_16x16x32_bf16 v[32:35], v[144:147], a[192:195], v[128:131]\n\t"
    "s_waitcnt lgkmcnt(0)\n\t"
    "ds_read_b128 v[152:155], v193 offset:24576\n\t"
    "v_mfma_f32_16x16x32_bf16 v[48:51], v[148:151], a[192:195], v[128:131]\n\t"
    "s_waitcnt vmcnt(9)\n\t"
    "ds_read_b128 v[156:159], v193 offset:26112\n\t"
    "v_lshlrev_b32 v204, 16, v252\n\t"
    "v_mfma_f32_16x16x32_bf16 v[52:55], v[148:151], a[208:211], v[132:135]\n\t"
    "v_exp_f32 v176, v72\n\t"
    "v_mfma_f32_16x16x32_bf16 v[36:39], v[144:147], a[208:211], v[132:135]\n\t"
    "v_exp_f32 v177, v73\n\t"
    "v_mfma_f32_16x16x32_bf16 v[40:43], v[144:147], a[224:227], v[136:139]\n\t"
    "buffer_load_dwordx4 v195, s[40:43], s64 offen offset:2048 sc1 lds\n\t"
    "v_mfma_f32_16x16x32_bf16 v[56:59], v[148:151], a[224:227], v[136:139]\n\t"
    "v_exp_f32 v178, v74\n\t"
    "v_mfma_f32_16x16x32_bf16 v[60:63], v[148:151], a[240:243], v[140:143]\n\t"
    "v_add_f32 v174, v176, v177\n\t"
    "v_cvt_pk_bf16_f32 v120, v176, v177\n\t"
    "v_mfma_f32_16x16x32_bf16 v[44:47], v[144:147], a[240:243], v[140:143]\n\t"
    "s_waitcnt lgkmcnt(1)\n\t"
    "v_exp_f32 v179, v75\n\t"
    "v_mfma_f32_16x16x32_bf16 v[32:35], v[152:155], a[196:199], v[32:35]\n\t"
    "s_waitcnt lgkmcnt(0)\n\t"
    "ds_read_b128 v[144:147], v192 offset:24704\n\t"
    "v_add_f32 v174, v174, v178\n\t"
    "v_mfma_f32_16x16x32_bf16 v[48:51], v[156:159], a[196:199], v[48:51]\n\t"
    "ds_read_b128 v[148:151], v192 offset:26240\n\t"
    "v_add_f32 v174, v174, v179\n\t"
    "v_mfma_f32_16x16x32_bf16 v[52:55], v[156:159], a[212:215], v[52:55]\n\t"
    "v_cvt_pk_bf16_f32 v121, v178, v179\n\t"
    "v_and_b32 v205, 0xffff0000, v252\n\t"
    "v_mfma_f32_16x16x32_bf16 v[36:39], v[152:155], a[212:215], v[36:39]\n\t"
    "v_exp_f32 v176, v88\n\t"
    "v_mfma_f32_16x16x32_bf16 v[40:43], v[152:155], a[228:231], v[40:43]\n\t"
    "v_lshlrev_b32 v212, 16, v188\n\t"
    "v_and_b32 v213, 0xffff0000, v188\n\t"
    "v_mfma_f32_16x16x32_bf16 v[56:59], v[156:159], a[228:231], v[56:59]\n\t"
    "v_exp_f32 v177, v89\n\t"
    "v_mfma_f32_16x16x32_bf16 v[60:63], v[156:159], a[244:247], v[60:63]\n\t"
    "v_add_f32 v174, v174, v176\n\t"
    "v_add_f32 v174, v174, v177\n\t"
    "v_mfma_f32_16x16x32_bf16 v[44:47], v[152:155], a[244:247], v[44:47]\n\t"
    "s_waitcnt lgkmcnt(1)\n\t"
    "v_exp_f32 v178, v90\n\t"
    "v_mfma_f32_16x16x32_bf16 v[32:35], v[144:147], a[200:203], v[32:35]\n\t"
    "s_waitcnt lgkmcnt(0)\n\t"
    "ds_read_b128 v[152:155], v193 offset:24704\n\t"
    "v_cvt_pk_bf16_f32 v122, v176, v177\n\t"
    "v_mfma_f32_16x16x32_bf16 v[48:51], v[148:151], a[200:203], v[48:51]\n\t"
    "ds_read_b128 v[156:159], v193 offset:26240\n\t"
    "v_lshlrev_b32 v206, 16, v253\n\t"
    "v_mfma_f32_16x16x32_bf16 v[52:55], v[148:151], a[216:219], v[52:55]\n\t"
    "v_and_b32 v207, 0xffff0000, v253\n\t"
    "v_lshlrev_b32 v214, 16, v189\n\t"
    "v_mfma_f32_16x16x32_bf16 v[36:39], v[144:147], a[216:219], v[36:39]\n\t"
    "v_exp_f32 v179, v91\n\t"
    "v_mfma_f32_16x16x32_bf16 v[40:43], v[144:147], a[232:235], v[40:43]\n\t"
    "v_add_f32 v174, v174, v178\n\t"
    "v_add_f32 v174, v174, v179\n\t"
    "v_mfma_f32_16x16x32_bf16 v[56:59], v[148:151], a[232:235], v[56:59]\n\t"
    "v_exp_f32 v176, v76\n\t"
    "v_mfma_f32_16x16x32_bf16 v[60:63], v[148:151], a[248:251], v[60:63]\n\t"
    "v_cvt_pk_bf16_f32 v123, v178, v179\n\t"
    "v_and_b32 v215, 0xffff0000, v189\n\t"
    "v_mfma_f32_16x16x32_bf16 v[44:47], v[144:147], a[248:251], v[44:47]\n\t"
    "s_waitcnt lgkmcnt(1)\n\t"
    "v_exp_f32 v177, v77\n\t"
    "v_mfma_f32_16x16x32_bf16 v[32:35], v[152:155], a[204:207], v[32:35]\n\t"
    "s_waitcnt lgkmcnt(0)\n\t"
    "ds_read_b128 v[144:147], v192 offset:24832\n\t"
    "v_add_f32 v175, v176, v177\n\t"
    "v_mfma_f32_16x16x32_bf16 v[48:51], v[156:159], a[204:207], v[48:51]\n\t"
    "ds_read_b128 v[148:151], v192 offset:26368\n\t"
    "v_cvt_pk_bf16_f32 v124, v176, v177\n\t"
    "v_mfma_f32_16x16x32_bf16 v[52:55], v[156:159], a[220:223], v[52:55]\n\t"
    "v_lshlrev_b32 v208, 16, v254\n\t"
    "v_and_b32 v209, 0xffff0000, v254\n\t"
    "v_mfma_f32_16x16x32_bf16 v[36:39], v[152:155], a[220:223], v[36:39]\n\t"
    "v_exp_f32 v178, v78\n\t"
    "v_mfma_f32_16x16x32_bf16 v[40:43], v[152:155], a[236:239], v[40:43]\n\t"
    "v_lshlrev_b32 v216, 16, v190\n\t"
    "v_and_b32 v217, 0xffff0000, v190\n\t"
    "v_mfma_f32_16x16x32_bf16 v[56:59], v[156:159], a[236:239], v[56:59]\n\t"
    "v_exp_f32 v179, v79\n\t"
    "v_mfma_f32_16x16x32_bf16 v[60:63], v[156:159], a[252:255], v[60:63]\n\t"
    "v_add_f32 v175, v175, v178\n\t"
    "v_add_f32 v175, v175, v179\n\t"
    "v_mfma_f32_16x16x32_bf16 v[44:47], v[152:155], a[252:255], v[44:47]\n\t"
    "s_waitcnt lgkmcnt(1)\n\t"
    "v_exp_f32 v176, v92\n\t"
    "v_mfma_f32_16x16x32_bf16 v[32:35], v[144:147], v[0:3], v[32:35]\n\t"
    "s_waitcnt lgkmcnt(0)\n\t"
    "ds_read_b128 v[152:155], v193 offset:24832\n\t"
    "v_cvt_pk_bf16_f32 v125, v178, v179\n\t"
    "v_mfma_f32_16x16x32_bf16 v[48:51], v[148:151], v[0:3], v[48:51]\n\t"
    "ds_read_b128 v[156:159], v193 offset:26368\n\t"
    "v_lshlrev_b32 v210, 16, v255\n\t"
    "v_mfma_f32_16x16x32_bf16 v[52:55], v[148:151], v[8:11], v[52:55]\n\t"
    "v_and_b32 v211, 0xffff0000, v255\n\t"
    "v_lshlrev_b32 v218, 16, v191\n\t"
    "v_mfma_f32_16x16x32_bf16 v[36:39], v[144:147], v[8:11], v[36:39]\n\t"
    "v_exp_f32 v177, v93\n\t"
    "v_mfma_f32_16x16x32_bf16 v[40:43], v[144:147], v[16:19], v[40:43]\n\t"
    "v_add_f32 v175, v175, v176\n\t"
    "v_add_f32 v175, v175, v177\n\t"
    "v_mfma_f32_16x16x32_bf16 v[56:59], v[148:151], v[16:19], v[56:59]\n\t"
    "v_exp_f32 v178, v94\n\t"
    "v_mfma_f32_16x16x32_bf16 v[60:63], v[148:151], v[24:27], v[60:63]\n\t"
    "v_cvt_pk_bf16_f32 v126, v176, v177\n\t"
    "v_and_b32 v219, 0xffff0000, v191\n\t"
    "v_mfma_f32_16x16x32_bf16 v[44:47], v[144:147], v[24:27], v[44:47]\n\t"
    "s_waitcnt lgkmcnt(1)\n\t"
    "v_exp_f32 v179, v95\n\t"
    "v_mfma_f32_16x16x32_bf16 v[32:35], v[152:155], v[4:7], v[32:35]\n\t"
    "s_waitcnt lgkmcnt(0)\n\t"
    "ds_read_b128 v[144:147], v194 offset:12288\n\t"
    "v_add_f32 v175, v175, v178\n\t"
    "v_mfma_f32_16x16x32_bf16 v[48:51], v[156:159], v[4:7], v[48:51]\n\t"
    "ds_read_b128 v[148:151], v194 offset:12544\n\t"
    "v_add_f32 v175, v175, v179\n\t"
    "v_mfma_f32_16x16x32_bf16 v[52:55], v[156:159], v[12:15], v[52:55]\n\t"
    "v_cvt_pk_bf16_f32 v127, v178, v179\n\t"
    "v_max3_f32 v180, v172, v173, v174\n\t"
    "v_mfma_f32_16x16x32_bf16 v[36:39], v[152:155], v[12:15], v[36:39]\n\t"
    "v_max_f32 v180, v180, v175\n\t"
    "v_cmp_lt_f32 vcc, s62, v180\n\t"
    "v_mfma_f32_16x16x32_bf16 v[40:43], v[152:155], v[20:23], v[40:43]\n\t"
    "global_load_dword v202, v182, s[90:91] sc1\n\t"
    "v_mfma_f32_16x16x32_bf16 v[56:59], v[156:159], v[20:23], v[56:59]\n\t"
    "s_add_u32 m0, s60, 86016\n\t"
    "v_mul_f32 v204, s93, v204\n\t"
    "v_mul_f32 v205, s93, v205\n\t"
    "v_mfma_f32_16x16x32_bf16 v[60:63], v[156:159], v[28:31], v[60:63]\n\t"
    "buffer_load_dwordx4 v195, s[44:47], s71 offen offset:0 sc1 lds\n\t"
    "v_mfma_f32_16x16x32_bf16 v[44:47], v[152:155], v[28:31], v[44:47]\n\t"
    "s_waitcnt lgkmcnt(1)\n\t"
    "s_cbranch_vccnz L_stub_l0_1\n\t"
    "L_ret_l0_1:\n\t"
    "v_mfma_f32_16x16x32_bf16 a[0:3], v[112:115], v[144:147], a[0:3]\n\t"
    "ds_read_b128 v[152:155], v194 offset:12800\n\t"
    "v_add_f32 v168, v168, v172\n\t"
    "v_mfma_f32_16x16x32_bf16 a[4:7], v[116:119], v[144:147], a[4:7]\n\t"
    "v_exp_f32 v176, v32\n\t"
    "v_mfma_f32_16x16x32_bf16 a[8:11], v[120:123], v[144:147], a[8:11]\n\t"
    "v_add_f32 v169, v169, v173\n\t"
    "v_add_f32 v170, v170, v174\n\t"
    "v_mfma_f32_16x16x32_bf16 a[12:15], v[124:127], v[144:147], a[12:15]\n\t"
    "s_waitcnt lgkmcnt(1)\n\t"
    "v_exp_f32 v177, v33\n\t"
    "v_mfma_f32_16x16x32_bf16 a[28:31], v[124:127], v[148:151], a[28:31]\n\t"
    "ds_read_b128 v[156:159], v194 offset:13056\n\t"
    "v_add_f32 v171, v171, v175\n\t"
    "v_mfma_f32_16x16x32_bf16 a[24:27], v[120:123], v[148:151], a[24:27]\n\t"
    "v_add_f32 v172, v176, v177\n\t"
    "v_cvt_pk_bf16_f32 v96, v176, v177\n\t"
    "v_mfma_f32_16x16x32_bf16 a[20:23], v[116:119], v[148:151], a[20:23]\n\t"
    "buffer_load_dwordx4 v195, s[44:47], s71 offen offset:1024 sc1 lds\n\t"
    "v_mfma_f32_16x16x32_bf16 a[16:19], v[112:115], v[148:151], a[16:19]\n\t"
    "s_waitcnt lgkmcnt(1)\n\t"
    "v_exp_f32 v178, v34\n\t"
    "v_mfma_f32_16x16x32_bf16 a[32:35], v[112:115], v[152:155], a[32:35]\n\t"
    "ds_read_b128 v[144:147], v194 offset:13312\n\t"
    "v_mul_f32 v206, s93, v206\n\t"
    "v_mfma_f32_16x16x32_bf16 a[36:39], v[116:119], v[152:155], a[36:39]\n\t"
    "v_exp_f32 v179, v35\n\t"
    "v_mfma_f32_16x16x32_bf16 a[40:43], v[120:123], v[152:155], a[40:43]\n\t"
    "v_add_f32 v172, v172, v178\n\t"
    "v_add_f32 v172, v172, v179\n\t"
    "v_mfma_f32_16x16x32_bf16 a[44:47], v[124:127], v[152:155], a[44:47]\n\t"
    "s_waitcnt lgkmcnt(1)\n\t"
    "v_cvt_pk_bf16_f32 v97, v178, v179\n\t"
    "v_mul_f32 v207, s93, v207\n\t"
    "v_mfma_f32_16x16x32_bf16 a[60:63], v[124:127], v[156:159], a[60:63]\n\t"
    "ds_read_b128 v[148:151], v194 offset:13568\n\t"
    "v_mul_f32 v208, s93, v208\n\t"
    "v_mfma_f32_16x16x32_bf16 a[56:59], v[120:123], v[156:159], a[56:59]\n\t"
    "v_exp_f32 v176, v48\n\t"
    "v_mfma_f32_16x16x32_bf16 a[52:55], v[116:119], v[156:159], a[52:55]\n\t"
    "buffer_load_dwordx4 v195, s[44:47], s71 offen offset:2048 sc1 lds\n\t"
    "v_mfma_f32_16x16x32_bf16 a[48:51], v[112:115], v[156:159], a[48:51]\n\t"
    "s_waitcnt lgkmcnt(1)\n\t"
    "v_exp_f32 v177, v49\n\t"
    "v_mfma_f32_16x16x32_bf16 a[64:67], v[112:115], v[144:147], a[64:67]\n\t"
    "s_add_u32 s44, s44, 196608\n\t"
    "ds_read_b128 v[152:155], v194 offset:13824\n\t"
    "v_add_f32 v172, v172, v176\n\t"
    "v_mfma_f32_16x16x32_bf16 a[68:71], v[116:119], v[144:147], a[68:71]\n\t"
    "s_addc_u32 s45, s45, 0\n\t"
    "v_add_f32 v172, v172, v177\n\t"
    "v_cvt_pk_bf16_f32 v98, v176, v177\n\t"
    "v_mfma_f32_16x16x32_bf16 a[72:75], v[120:123], v[144:147], a[72:75]\n\t"
    "v_mul_f32 v209, s93, v209\n\t"
    "v_mul_f32 v210, s93, v210\n\t"
    "v_mfma_f32_16x16x32_bf16 a[76:79], v[124:127], v[144:147], a[76:79]\n\t"
    "s_waitcnt lgkmcnt(1)\n\t"
    "v_exp_f32 v178, v50\n\t"
    "v_mfma_f32_16x16x32_bf16 a[92:95], v[124:127], v[148:151], a[92:95]\n\t"
    "ds_read_b128 v[156:159], v194 offset:14080\n\t"
    "v_mul_f32 v211, s93, v211\n\t"
    "v_mfma_f32_16x16x32_bf16 a[88:91], v[120:123], v[148:151], a[88:91]\n\t"
    "v_exp_f32 v179, v51\n\t"
    "v_mfma_f32_16x16x32_bf16 a[84:87], v[116:119], v[148:151], a[84:87]\n\t"
    "v_add_f32 v172, v172, v178\n\t"
    "v_add_f32 v172, v172, v179\n\t"
    "v_mfma_f32_16x16x32_bf16 a[80:83], v[112:115], v[148:151], a[80:83]\n\t"
    "s_waitcnt lgkmcnt(1)\n\t"
    "v_cvt_pk_bf16_f32 v99, v178, v179\n\t"
    "v_mul_f32_e64 v212, -v212, v204\n\t"
    "v_mfma_f32_16x16x32_bf16 a[96:99], v[112:115], v[152:155], a[96:99]\n\t"
    "ds_read_b128 v[144:147], v194 offset:14336\n\t"
    "v_mul_f32_e64 v213, -v213, v205\n\t"
    "v_mfma_f32_16x16x32_bf16 a[100:103], v[116:119], v[152:155], a[100:103]\n\t"
    "v_exp_f32 v176, v36\n\t"
    "v_mfma_f32_16x16x32_bf16 a[104:107], v[120:123], v[152:155], a[104:107]\n\t"
    "s_waitcnt vmcnt(3)\n\t"
    "v_subrev_u32 v202, s63, v202\n\t"
    "v_cmp_lt_u32 vcc, 7, v202\n\t"
    "v_mfma_f32_16x16x32_bf16 a[108:111], v[124:127], v[152:155], a[108:111]\n\t"
    "s_waitcnt lgkmcnt(1)\n\t"
    "v_exp_f32 v177, v37\n\t"
    "v_mfma_f32_16x16x32_bf16 a[124:127], v[124:127], v[156:159], a[124:127]\n\t"
    "s_add_u32 s63, s63, 1\n\t"
    "ds_read_b128 v[148:151], v194 offset:14592\n\t"
    "v_add_f32 v173, v176, v177\n\t"
    "v_mfma_f32_16x16x32_bf16 a[120:123], v[120:123], v[156:159], a[120:123]\n\t"
    "v_cvt_pk_bf16_f32 v100, v176, v177\n\t"
    "v_mul_f32_e64 v214, -v214, v206\n\t"
    "v_mfma_f32_16x16x32_bf16 a[116:119], v[116:119], v[156:159], a[116:119]\n\t"
    "s_cbranch_vccnz L_spin_l\n\t"
    "L_spinret_l:\n\t"
    "v_mfma_f32_16x16x32_bf16 a[112:115], v[112:115], v[156:159], a[112:115]\n\t"
    "s_waitcnt lgkmcnt(1)\n\t"
    "v_exp_f32 v178, v38\n\t"
    "v_mfma_f32_16x16x32_bf16 a[128:131], v[112:115], v[144:147], a[128:131]\n\t"
    "ds_read_b128 v[152:155], v194 offset:14848\n\t"
    "v_mul_f32_e64 v215, -v215, v207\n\t"
    "v_mfma_f32_16x16x32_bf16 a[132:135], v[116:119], v[144:147], a[132:135]\n\t"
    "s_waitcnt vmcnt(7)\n\t"
    "v_exp_f32 v179, v39\n\t"
    "v_mfma_f32_16x16x32_bf16 a[136:139], v[120:123], v[144:147], a[136:139]\n\t"
    "s_barrier\n\t"
    "v_add_f32 v173, v173, v178\n\t"
    "v_add_f32 v173, v173, v179\n\t"
    "v_mfma_f32_16x16x32_bf16 a[140:143], v[124:127], v[144:147], a[140:143]\n\t"
    "s_waitcnt lgkmcnt(1)\n\t"
    "v_cvt_pk_bf16_f32 v101, v178, v179\n\t"
    "v_mul_f32_e64 v216, -v216, v208\n\t"
    "v_mfma_f32_16x16x32_bf16 a[156:159], v[124:127], v[148:151], a[156:159]\n\t"
    "ds_read_b128 v[156:159], v194 offset:15104\n\t"
    "v_mul_f32_e64 v217, -v217, v209\n\t"
    "v_mfma_f32_16x16x32_bf16 a[152:155], v[120:123], v[148:151], a[152:155]\n\t"
    "s_add_u32 m0, s60, 36864\n\t"
    "v_exp_f32 v176, v52\n\t"
    "v_mfma_f32_16x16x32_bf16 a[148:151], v[116:119], v[148:151], a[148:151]\n\t"
    "buffer_load_dwordx4 v195, s[40:43], s65 offen offset:0 sc1 lds\n\t"
    "v_mfma_f32_16x16x32_bf16 a[144:147], v[112:115], v[148:151], a[144:147]\n\t"
    "s_waitcnt lgkmcnt(1)\n\t"
    "v_exp_f32 v177, v53\n\t"
    "v_mfma_f32_16x16x32_bf16 a[160:163], v[112:115], v[152:155], a[160:163]\n\t"
    "ds_read_b128 v[144:147], v192 offset:36864\n\t"
    "v_add_f32 v173, v173, v176\n\t"
    "v_mfma_f32_16x16x32_bf16 a[164:167], v[116:119], v[152:155], a[164:167]\n\t"
    "ds_read_b128 v[148:151], v192 offset:38400\n\t"
    "v_add_f32 v173, v173, v177\n\t"
    "v_mfma_f32_16x16x32_bf16 a[168:171], v[120:123], v[152:155], a[168:171]\n\t"
    "buffer_load_dwordx4 v195, s[40:43], s65 offen offset:1024 sc1 lds\n\t"
    "v_mfma_f32_16x16x32_bf16 a[172:175], v[124:127], v[152:155], a[172:175]\n\t"
    "s_waitcnt lgkmcnt(2)\n\t"
    "v_exp_f32 v178, v54\n\t"
    "v_mfma_f32_16x16x32_bf16 a[188:191], v[124:127], v[156:159], a[188:191]\n\t"
    "v_cvt_pk_bf16_f32 v102, v176, v177\n\t"
    "v_mul_f32_e64 v218, -v218, v210\n\t"
    "v_mfma_f32_16x16x32_bf16 a[184:187], v[120:123], v[156:159], a[184:187]\n\t"
    "v_exp_f32 v179, v55\n\t"
    "v_mfma_f32_16x16x32_bf16 a[180:183], v[116:119], v[156:159], a[180:183]\n\t"
    "v_add_f32 v173, v173, v178\n\t"
    "v_add_f32 v173, v173, v179\n\t"
    "v_mfma_f32_16x16x32_bf16 a[176:179], v[112:115], v[156:159], a[176:179]\n\t"
    "s_waitcnt lgkmcnt(1)\n\t"
    "v_cvt_pk_bf16_f32 v103, v178, v179\n\t"
    "v_mul_f32_e64 v219, -v219, v211\n\t"
    "v_mfma_f32_16x16x32_bf16 v[64:67], v[144:147], a[192:195], v[128:131]\n\t"
    "s_waitcnt lgkmcnt(0)\n\t"
    "ds_read_b128 v[152:155], v193 offset:36864\n\t"
    "v_mul_f32 v204, 0x4b800000, v204\n\t"
    "v_mfma_f32_16x16x32_bf16 v[80:83], v[148:151], a[192:195], v[128:131]\n\t"
    "ds_read_b128 v[156:159], v193 offset:38400\n\t"
    "v_mul_f32 v205, 0x4b800000, v205\n\t"
    "v_mfma_f32_16x16x32_bf16 v[84:87], v[148:151], a[208:211], v[132:135]\n\t"
    "v_exp_f32 v176, v40\n\t"
    "v_mfma_f32_16x16x32_bf16 v[68:71], v[144:147], a[208:211], v[132:135]\n\t"
    "v_exp_f32 v177, v41\n\t"
    "v_mfma_f32_16x16x32_bf16 v[72:75], v[144:147], a[224:227], v[136:139]\n\t"
    "buffer_load_dwordx4 v195, s[40:43], s65 offen offset:2048 sc1 lds\n\t"
    "v_mfma_f32_16x16x32_bf16 v[88:91], v[148:151], a[224:227], v[136:139]\n\t"
    "v_exp_f32 v178, v42\n\t"
    "v_mfma_f32_16x16x32_bf16 v[92:95], v[148:151], a[240:243], v[140:143]\n\t"
    "v_add_f32 v174, v176, v177\n\t"
    "v_cvt_pk_bf16_f32 v104, v176, v177\n\t"
    "v_mfma_f32_16x16x32_bf16 v[76:79], v[144:147], a[240:243], v[140:143]\n\t"
    "s_waitcnt lgkmcnt(1)\n\t"
    "v_exp_f32 v179, v43\n\t"
    "v_mfma_f32_16x16x32_bf16 v[64:67], v[152:155], a[196:199], v[64:67]\n\t"
    "s_waitcnt lgkmcnt(0)\n\t"
    "ds_read_b128 v[144:147], v192 offset:36992\n\t"
    "v_add_f32 v174, v174, v178\n\t"
    "v_mfma_f32_16x16x32_bf16 v[80:83], v[156:159], a[196:199], v[80:83]\n\t"
    "ds_read_b128 v[148:151], v192 offset:38528\n\t"
    "v_add_f32 v174, v174, v179\n\t"
    "v_mfma_f32_16x16x32_bf16 v[84:87], v[156:159], a[212:215], v[84:87]\n\t"
    "v_cvt_pk_bf16_f32 v105, v178, v179\n\t"
    "v_mul_f32 v206, 0x4b800000, v206\n\t"
    "v_mfma_f32_16x16x32_bf16 v[68:71], v[152:155], a[212:215], v[68:71]\n\t"
    "v_exp_f32 v176, v56\n\t"
    "v_mfma_f32_16x16x32_bf16 v[72:75], v[152:155], a[228:231], v[72:75]\n\t"
    "v_mul_f32 v207, 0x4b800000, v207\n\t"
    "v_mul_f32 v208, 0x4b800000, v208\n\t"
    "v_mfma_f32_16x16x32_bf16 v[88:91], v[156:159], a[228:231], v[88:91]\n\t"
    "v_exp_f32 v177, v57\n\t"
    "v_mfma_f32_16x16x32_bf16 v[92:95], v[156:159], a[244:247], v[92:95]\n\t"
    "v_add_f32 v174, v174, v176\n\t"
    "v_add_f32 v174, v174, v177\n\t"
    "v_mfma_f32_16x16x32_bf16 v[76:79], v[152:155], a[244:247], v[76:79]\n\t"
    "s_waitcnt lgkmcnt(1)\n\t"
    "v_exp_f32 v178, v58\n\t"
    "v_mfma_f32_16x16x32_bf16 v[64:67], v[144:147], a[200:203], v[64:67]\n\t"
    "s_waitcnt lgkmcnt(0)\n\t"
    "ds_read_b128 v[152:155], v193 offset:36992\n\t"
    "v_cvt_pk_bf16_f32 v106, v176, v177\n\t"
    "v_mfma_f32_16x16x32_bf16 v[80:83], v[148:151], a[200:203], v[80:83]\n\t"
    "ds_read_b128 v[156:159], v193 offset:38528\n\t"
    "v_mul_f32 v209, 0x4b800000, v209\n\t"
    "v_mfma_f32_16x16x32_bf16 v[84:87], v[148:151], a[216:219], v[84:87]\n\t"
    "v_mul_f32 v210, 0x4b800000, v210\n\t"
    "v_mul_f32 v211, 0x4b800000, v211\n\t"
    "v_mfma_f32_16x16x32_bf16 v[68:71], v[144:147], a[216:219], v[68:71]\n\t"
    "s_add_u32 m0, s60, 0\n\t"
    "v_exp_f32 v179, v59\n\t"
    "v_mfma_f32_16x16x32_bf16 v[72:75], v[144:147], a[232:235], v[72:75]\n\t"
    "s_waitcnt vmcnt(33)\n\t"
    "buffer_load_dwordx4 v195, s[44:47], s64 offen offset:0 sc1 lds\n\t"
    "v_mfma_f32_16x16x32_bf16 v[88:91], v[148:151], a[232:235], v[88:91]\n\t"
    "v_exp_f32 v176, v44\n\t"
    "v_mfma_f32_16x16x32_bf16 v[92:95], v[148:151], a[248:251], v[92:95]\n\t"
    "v_add_f32 v174, v174, v178\n\t"
    "v_add_f32 v174, v174, v179\n\t"
    "v_mfma_f32_16x16x32_bf16 v[76:79], v[144:147], a[248:251], v[76:79]\n\t"
    "s_waitcnt lgkmcnt(1)\n\t"
    "v_exp_f32 v177, v45\n\t"
    "v_mfma_f32_16x16x32_bf16 v[64:67], v[152:155], a[204:207], v[64:67]\n\t"
    "s_waitcnt lgkmcnt(0)\n\t"
    "ds_read_b128 v[144:147], v192 offset:37120\n\t"
    "v_cvt_pk_bf16_f32 v107, v178, v179\n\t"
    "v_mfma_f32_16x16x32_bf16 v[80:83], v[156:159], a[204:207], v[80:83]\n\t"
    "ds_read_b128 v[148:151], v192 offset:38656\n\t"
    "v_add_f32 v175, v176, v177\n\t"
    "v_mfma_f32_16x16x32_bf16 v[84:87], v[156:159], a[220:223], v[84:87]\n\t"
    "v_cvt_pk_bf16_f32 v108, v176, v177\n\t"
    "v_fma_mix_f32 v220, v220, v204, v212 op_sel_hi:[1,0,0]\n\t"
    "v_mfma_f32_16x16x32_bf16 v[68:71], v[152:155], a[220:223], v[68:71]\n\t"
    "v_exp_f32 v178, v46\n\t"
    "v_mfma_f32_16x16x32_bf16 v[72:75], v[152:155], a[236:239], v[72:75]\n\t"
    "buffer_load_dwordx4 v195, s[44:47], s64 offen offset:1024 sc1 lds\n\t"
    "v_mfma_f32_16x16x32_bf16 v[88:91], v[156:159], a[236:239], v[88:91]\n\t"
    "v_exp_f32 v179, v47\n\t"
    "v_mfma_f32_16x16x32_bf16 v[92:95], v[156:159], a[252:255], v[92:95]\n\t"
    "v_add_f32 v175, v175, v178\n\t"
    "v_add_f32 v175, v175, v179\n\t"
    "v_mfma_f32_16x16x32_bf16 v[76:79], v[152:155], a[252:255], v[76:79]\n\t"
    "s_waitcnt lgkmcnt(1)\n\t"
    "v_exp_f32 v176, v60\n\t"
    "v_mfma_f32_16x16x32_bf16 v[64:67], v[144:147], v[0:3], v[64:67]\n\t"
    "s_waitcnt lgkmcnt(0)\n\t"
    "ds_read_b128 v[152:155], v193 offset:37120\n\t"
    "v_cvt_pk_bf16_f32 v109, v178, v179\n\t"
    "v_mfma_f32_16x16x32_bf16 v[80:83], v[148:151], v[0:3], v[80:83]\n\t"
    "ds_read_b128 v[156:159], v193 offset:38656\n\t"
    "v_fma_mix_f32 v221, v221, v205, v213 op_sel_hi:[1,0,0]\n\t"
    "v_mfma_f32_16x16x32_bf16 v[84:87], v[148:151], v[8:11], v[84:87]\n\t"
    "global_load_dwordx4 v[252:255], v186, s[86:87]\n\t"
    "v_mfma_f32_16x16x32_bf16 v[68:71], v[144:147], v[8:11], v[68:71]\n\t"
    "v_exp_f32 v177, v61\n\t"
    "v_mfma_f32_16x16x32_bf16 v[72:75], v[144:147], v[16:19], v[72:75]\n\t"
    "buffer_load_dwordx4 v195, s[44:47], s64 offen offset:2048 sc1 lds\n\t"
    "v_mfma_f32_16x16x32_bf16 v[88:91], v[148:151], v[16:19], v[88:91]\n\t"
    "s_add_u32 s86, s86, 512\n\t"
    "v_exp_f32 v178, v62\n\t"
    "v_mfma_f32_16x16x32_bf16 v[92:95], v[148:151], v[24:27], v[92:95]\n\t"
    "s_addc_u32 s87, s87, 0\n\t"
    "v_add_f32 v175, v175, v176\n\t"
    "v_add_f32 v175, v175, v177\n\t"
    "v_mfma_f32_16x16x32_bf16 v[76:79], v[144:147], v[24:27], v[76:79]\n\t"
    "s_waitcnt lgkmcnt(1)\n\t"
    "v_exp_f32 v179, v63\n\t"
    "v_mfma_f32_16x16x32_bf16 v[64:67], v[152:155], v[4:7], v[64:67]\n\t"
    "s_waitcnt lgkmcnt(0)\n\t"
    "ds_read_b128 v[144:147], v181 offset:0\n\t"
    "v_cvt_pk_bf16_f32 v110, v176, v177\n\t"
    "v_mfma_f32_16x16x32_bf16 v[80:83], v[156:159], v[4:7], v[80:83]\n\t"
    "ds_read_b128 v[148:151], v181 offset:256\n\t"
    "v_add_f32 v175, v175, v178\n\t"
    "v_mfma_f32_16x16x32_bf16 v[84:87], v[156:159], v[12:15], v[84:87]\n\t"
    "v_add_f32 v175, v175, v179\n\t"
    "v_cvt_pk_bf16_f32 v111, v178, v179\n\t"
    "v_mfma_f32_16x16x32_bf16 v[68:71], v[152:155], v[12:15], v[68:71]\n\t"
    "v_max3_f32 v180, v172, v173, v174\n\t"
    "v_max_f32 v180, v180, v175\n\t"
    "v_mfma_f32_16x16x32_bf16 v[72:75], v[152:155], v[20:23], v[72:75]\n\t"
    "v_cmp_lt_f32 vcc, s62, v180\n\t"
    "v_fma_mix_f32 v222, v222, v206, v214 op_sel_hi:[1,0,0]\n\t"
    "v_mfma_f32_16x16x32_bf16 v[88:91], v[156:159], v[20:23], v[88:91]\n\t"
    "global_load_dwordx4 v[188:191], v186, s[88:89]\n\t"
    "v_mfma_f32_16x16x32_bf16 v[92:95], v[156:159], v[28:31], v[92:95]\n\t"
    "v_fma_mix_f32 v223, v223, v207, v215 op_sel_hi:[1,0,0]\n\t"
    "v_fma_mix_f32 v224, v224, v208, v216 op_sel_hi:[1,0,0]\n\t"
    "v_mfma_f32_16x16x32_bf16 v[76:79], v[152:155], v[28:31], v[76:79]\n\t"
    "s_waitcnt lgkmcnt(1)\n\t"
    "s_cbranch_vccnz L_stub_l1_0\n\t"
    "L_ret_l1_0:\n\t"
    "v_mfma_f32_16x16x32_bf16 a[0:3], v[96:99], v[144:147], a[0:3]\n\t"
    "s_add_u32 s88, s88, 512\n\t"
    "ds_read_b128 v[152:155], v181 offset:512\n\t"
    "v_add_f32 v168, v168, v172\n\t"
    "v_mfma_f32_16x16x32_bf16 a[4:7], v[100:103], v[144:147], a[4:7]\n\t"
    "s_addc_u32 s89, s89, 0\n\t"
    "v_exp_f32 v176, v64\n\t"
    "v_mfma_f32_16x16x32_bf16 a[8:11], v[104:107], v[144:147], a[8:11]\n\t"
    "v_add_f32 v169, v169, v173\n\t"
    "v_add_f32 v170, v170, v174\n\t"
    "v_mfma_f32_16x16x32_bf16 a[12:15], v[108:111], v[144:147], a[12:15]\n\t"
    "s_waitcnt lgkmcnt(1)\n\t"
    "v_exp_f32 v177, v65\n\t"
    "v_mfma_f32_16x16x32_bf16 a[28:31], v[108:111], v[148:151], a[28:31]\n\t"
    "ds_read_b128 v[156:159], v181 offset:768\n\t"
    "v_add_f32 v171, v171, v175\n\t"
    "v_mfma_f32_16x16x32_bf16 a[24:27], v[104:107], v[148:151], a[24:27]\n\t"
    "v_add_f32 v172, v176, v177\n\t"
    "v_cvt_pk_bf16_f32 v112, v176, v177\n\t"
    "v_mfma_f32_16x16x32_bf16 a[20:23], v[100:103], v[148:151], a[20:23]\n\t"
    "v_fma_mix_f32 v225, v225, v209, v217 op_sel_hi:[1,0,0]\n\t"
    "v_fma_mix_f32 v226, v226, v210, v218 op_sel_hi:[1,0,0]\n\t"
    "v_mfma_f32_16x16x32_bf16 a[16:19], v[96:99], v[148:151], a[16:19]\n\t"
    "s_waitcnt lgkmcnt(1)\n\t"
    "v_exp_f32 v178, v66\n\t"
    "v_mfma_f32_16x16x32_bf16 a[32:35], v[96:99], v[152:155], a[32:35]\n\t"
    "ds_read_b128 v[144:147], v181 offset:1024\n\t"
    "v_fma_mix_f32 v227, v227, v211, v219 op_sel_hi:[1,0,0]\n\t"
    "v_mfma_f32_16x16x32_bf16 a[36:39], v[100:103], v[152:155], a[36:39]\n\t"
    "v_exp_f32 v179, v67\n\t"
    "v_mfma_f32_16x16x32_bf16 a[40:43], v[104:107], v[152:155], a[40:43]\n\t"
    "v_add_f32 v172, v172, v178\n\t"
    "v_add_f32 v172, v172, v179\n\t"
    "v_mfma_f32_16x16x32_bf16 a[44:47], v[108:111], v[152:155], a[44:47]\n\t"
    "s_waitcnt lgkmcnt(1)\n\t"
    "v_cvt_pk_bf16_f32 v113, v178, v179\n\t"
    "v_cvt_pk_bf16_f32 v244, v220, v221\n\t"
    "v_mfma_f32_16x16x32_bf16 a[60:63], v[108:111], v[156:159], a[60:63]\n\t"
    "ds_read_b128 v[148:151], v181 offset:1280\n\t"
    "v_cvt_pk_bf16_f32 v245, v222, v223\n\t"
    "v_mfma_f32_16x16x32_bf16 a[56:59], v[104:107], v[156:159], a[56:59]\n\t"
    "v_exp_f32 v176, v80\n\t"
    "v_mfma_f32_16x16x32_bf16 a[52:55], v[100:103], v[156:159], a[52:55]\n\t"
    "v_cvt_pk_bf16_f32 v246, v224, v225\n\t"
    "v_cvt_pk_bf16_f32 v247, v226, v227\n\t"
    "v_mfma_f32_16x16x32_bf16 a[48:51], v[96:99], v[156:159], a[48:51]\n\t"
    "s_waitcnt lgkmcnt(1)\n\t"
    "v_exp_f32 v177, v81\n\t"
    "v_mfma_f32_16x16x32_bf16 a[64:67], v[96:99], v[144:147], a[64:67]\n\t"
    "s_waitcnt vmcnt(34)\n\t"
    "ds_read_b128 v[152:155], v181 offset:1536\n\t"
    "v_add_f32 v172, v172, v176\n\t"
    "v_mfma_f32_16x16x32_bf16 a[68:71], v[100:103], v[144:147], a[68:71]\n\t"
    "v_add_f32 v172, v172, v177\n\t"
    "v_cvt_pk_bf16_f32 v114, v176, v177\n\t"
    "v_mfma_f32_16x16x32_bf16 a[72:75], v[104:107], v[144:147], a[72:75]\n\t"
    "buffer_store_dwordx4 v[244:247], v199, s[48:51], s92 offen offset:0 sc1\n\t"
    "v_mfma_f32_16x16x32_bf16 a[76:79], v[108:111], v[144:147], a[76:79]\n\t"
    "s_waitcnt lgkmcnt(1)\n\t"
    "v_exp_f32 v178, v82\n\t"
    "v_mfma_f32_16x16x32_bf16 a[92:95], v[108:111], v[148:151], a[92:95]\n\t"
    "ds_read_b128 v[156:159], v181 offset:1792\n\t"
    "v_fma_mix_f32 v228, v228, v204, v212 op_sel_hi:[1,0,0]\n\t"
    "v_mfma_f32_16x16x32_bf16 a[88:91], v[104:107], v[148:151], a[88:91]\n\t"
    "v_exp_f32 v179, v83\n\t"
    "v_mfma_f32_16x16x32_bf16 a[84:87], v[100:103], v[148:151], a[84:87]\n\t"
    "v_add_f32 v172, v172, v178\n\t"
    "v_add_f32 v172, v172, v179\n\t"
    "v_mfma_f32_16x16x32_bf16 a[80:83], v[96:99], v[148:151], a[80:83]\n\t"
    "s_waitcnt lgkmcnt(1)\n\t"
    "v_cvt_pk_bf16_f32 v115, v178, v179\n\t"
    "v_fma_mix_f32 v229, v229, v205, v213 op_sel_hi:[1,0,0]\n\t"
    "v_mfma_f32_16x16x32_bf16 a[96:99], v[96:99], v[152:155], a[96:99]\n\t"
    "ds_read_b128 v[144:147], v181 offset:2048\n\t"
    "v_fma_mix_f32 v230, v230, v206, v214 op_sel_hi:[1,0,0]\n\t"
    "v_mfma_f32_16x16x32_bf16 a[100:103], v[100:103], v[152:155], a[100:103]\n\t"
    "v_exp_f32 v176, v68\n\t"
    "v_mfma_f32_16x16x32_bf16 a[104:107], v[104:107], v[152:155], a[104:107]\n\t"
    "global_load_dwordx4 v[220:223], v197, s[80:81] offset:-2048\n\t"
    "v_mfma_f32_16x16x32_bf16 a[108:111], v[108:111], v[152:155], a[108:111]\n\t"
    "s_waitcnt lgkmcnt(1)\n\t"
    "v_exp_f32 v177, v69\n\t"
    "v_mfma_f32_16x16x32_bf16 a[124:127], v[108:111], v[156:159], a[124:127]\n\t"
    "ds_read_b128 v[148:151], v181 offset:2304\n\t"
    "v_add_f32 v173, v176, v177\n\t"
    "v_mfma_f32_16x16x32_bf16 a[120:123], v[104:107], v[156:159], a[120:123]\n\t"
    "v_cvt_pk_bf16_f32 v116, v176, v177\n\t"
    "v_fma_mix_f32 v231, v231, v207, v215 op_sel_hi:[1,0,0]\n\t"
    "v_mfma_f32_16x16x32_bf16 a[116:119], v[100:103], v[156:159], a[116:119]\n\t"
    "global_load_dwordx4 v[224:227], v197, s[80:81] offset:-1280\n\t"
    "v_mfma_f32_16x16x32_bf16 a[112:115], v[96:99], v[156:159], a[112:115]\n\t"
    "s_waitcnt lgkmcnt(1)\n\t"
    "v_exp_f32 v178, v70\n\t"
    "v_mfma_f32_16x16x32_bf16 a[128:131], v[96:99], v[144:147], a[128:131]\n\t"
    "ds_read_b128 v[152:155], v181 offset:2560\n\t"
    "v_fma_mix_f32 v232, v232, v208, v216 op_sel_hi:[1,0,0]\n\t"
    "v_mfma_f32_16x16x32_bf16 a[132:135], v[100:103], v[144:147], a[132:135]\n\t"
    "s_waitcnt vmcnt(11)\n\t"
    "v_exp_f32 v179, v71\n\t"
    "v_mfma_f32_16x16x32_bf16 a[136:139], v[104:107], v[144:147], a[136:139]\n\t"
    "s_barrier\n\t"
    "v_add_f32 v173, v173, v178\n\t"
    "v_add_f32 v173, v173, v179\n\t"
    "v_mfma_f32_16x16x32_bf16 a[140:143], v[108:111], v[144:147], a[140:143]\n\t"
    "s_waitcnt lgkmcnt(1)\n\t"
    "v_cvt_pk_bf16_f32 v117, v178, v179\n\t"
    "v_fma_mix_f32 v233, v233, v209, v217 op_sel_hi:[1,0,0]\n\t"
    "v_mfma_f32_16x16x32_bf16 a[156:159], v[108:111], v[148:151], a[156:159]\n\t"
    "ds_read_b128 v[156:159], v181 offset:2816\n\t"
    "v_fma_mix_f32 v234, v234, v210, v218 op_sel_hi:[1,0,0]\n\t"
    "v_mfma_f32_16x16x32_bf16 a[152:155], v[104:107], v[148:151], a[152:155]\n\t"
    "s_add_u32 m0, s60, 49152\n\t"
    "v_exp_f32 v176, v84\n\t"
    "v_mfma_f32_16x16x32_bf16 a[148:151], v[100:103], v[148:151], a[148:151]\n\t"
    "buffer_load_dwordx4 v195, s[40:43], s66 offen offset:0 sc1 lds\n\t"
    "v_mfma_f32_16x16x32_bf16 a[144:147], v[96:99], v[148:151], a[144:147]\n\t"
    "s_waitcnt lgkmcnt(1)\n\t"
    "v_exp_f32 v177, v85\n\t"
    "v_mfma_f32_16x16x32_bf16 a[160:163], v[96:99], v[152:155], a[160:163]\n\t"
    "ds_read_b128 v[144:147], v192 offset:0\n\t"
    "v_add_f32 v173, v173, v176\n\t"
    "v_mfma_f32_16x16x32_bf16 a[164:167], v[100:103], v[152:155], a[164:167]\n\t"
    "ds_read_b128 v[148:151], v192 offset:1536\n\t"
    "v_add_f32 v173, v173, v177\n\t"
    "v_mfma_f32_16x16x32_bf16 a[168:171], v[104:107], v[152:155], a[168:171]\n\t"
    "buffer_load_dwordx4 v195, s[40:43], s66 offen offset:1024 sc1 lds\n\t"
    "v_mfma_f32_16x16x32_bf16 a[172:175], v[108:111], v[152:155], a[172:175]\n\t"
    "s_waitcnt lgkmcnt(2)\n\t"
    "v_exp_f32 v178, v86\n\t"
    "v_mfma_f32_16x16x32_bf16 a[188:191], v[108:111], v[156:159], a[188:191]\n\t"
    "v_cvt_pk_bf16_f32 v118, v176, v177\n\t"
    "v_fma_mix_f32 v235, v235, v211, v219 op_sel_hi:[1,0,0]\n\t"
    "v_mfma_f32_16x16x32_bf16 a[184:187], v[104:107], v[156:159], a[184:187]\n\t"
    "v_exp_f32 v179, v87\n\t"
    "v_mfma_f32_16x16x32_bf16 a[180:183], v[100:103], v[156:159], a[180:183]\n\t"
    "v_add_f32 v173, v173, v178\n\t"
    "v_add_f32 v173, v173, v179\n\t"
    "v_mfma_f32_16x16x32_bf16 a[176:179], v[96:99], v[156:159], a[176:179]\n\t"
    "s_waitcnt lgkmcnt(1)\n\t"
    "v_cvt_pk_bf16_f32 v119, v178, v179\n\t"
    "v_cvt_pk_bf16_f32 v248, v228, v229\n\t"
    "v_mfma_f32_16x16x32_bf16 v[32:35], v[144:147], a[192:195], v[128:131]\n\t"
    "s_waitcnt lgkmcnt(0)\n\t"
    "ds_read_b128 v[152:155], v193 offset:0\n\t"
    "v_cvt_pk_bf16_f32 v249, v230, v231\n\t"
    "v_mfma_f32_16x16x32_bf16 v[48:51], v[148:151], a[192:195], v[128:131]\n\t"
    "ds_read_b128 v[156:159], v193 offset:1536\n\t"
    "v_cvt_pk_bf16_f32 v250, v232, v233\n\t"
    "v_mfma_f32_16x16x32_bf16 v[52:55], v[148:151], a[208:211], v[132:135]\n\t"
    "v_exp_f32 v176, v72\n\t"
    "v_mfma_f32_16x16x32_bf16 v[36:39], v[144:147], a[208:211], v[132:135]\n\t"
    "v_exp_f32 v177, v73\n\t"
    "v_mfma_f32_16x16x32_bf16 v[40:43], v[144:147], a[224:227], v[136:139]\n\t"
    "buffer_load_dwordx4 v195, s[40:43], s66 offen offset:2048 sc1 lds\n\t"
    "v_mfma_f32_16x16x32_bf16 v[56:59], v[148:151], a[224:227], v[136:139]\n\t"
    "v_exp_f32 v178, v74\n\t"
    "v_mfma_f32_16x16x32_bf16 v[60:63], v[148:151], a[240:243], v[140:143]\n\t"
    "v_add_f32 v174, v176, v177\n\t"
    "v_cvt_pk_bf16_f32 v120, v176, v177\n\t"
    "v_mfma_f32_16x16x32_bf16 v[44:47], v[144:147], a[240:243], v[140:143]\n\t"
    "s_waitcnt lgkmcnt(1)\n\t"
    "v_exp_f32 v179, v75\n\t"
    "v_mfma_f32_16x16x32_bf16 v[32:35], v[152:155], a[196:199], v[32:35]\n\t"
    "s_waitcnt lgkmcnt(0)\n\t"
    "ds_read_b128 v[144:147], v192 offset:128\n\t"
    "v_add_f32 v174, v174, v178\n\t"
    "v_mfma_f32_16x16x32_bf16 v[48:51], v[156:159], a[196:199], v[48:51]\n\t"
    "ds_read_b128 v[148:151], v192 offset:1664\n\t"
    "v_add_f32 v174, v174, v179\n\t"
    "v_mfma_f32_16x16x32_bf16 v[52:55], v[156:159], a[212:215], v[52:55]\n\t"
    "v_cvt_pk_bf16_f32 v121, v178, v179\n\t"
    "v_cvt_pk_bf16_f32 v251, v234, v235\n\t"
    "v_mfma_f32_16x16x32_bf16 v[36:39], v[152:155], a[212:215], v[36:39]\n\t"
    "s_waitcnt vmcnt(36)\n\t"
    "v_exp_f32 v176, v88\n\t"
    "v_mfma_f32_16x16x32_bf16 v[40:43], v[152:155], a[228:231], v[40:43]\n\t"
    "buffer_store_dwordx4 v[248:251], v200, s[48:51], s92 offen offset:768 sc1\n\t"
    "v_mfma_f32_16x16x32_bf16 v[56:59], v[156:159], a[228:231], v[56:59]\n\t"
    "v_exp_f32 v177, v89\n\t"
    "v_mfma_f32_16x16x32_bf16 v[60:63], v[156:159], a[244:247], v[60:63]\n\t"
    "v_add_f32 v174, v174, v176\n\t"
    "v_add_f32 v174, v174, v177\n\t"
    "v_mfma_f32_16x16x32_bf16 v[44:47], v[152:155], a[244:247], v[44:47]\n\t"
    "s_waitcnt lgkmcnt(1)\n\t"
    "v_exp_f32 v178, v90\n\t"
    "v_mfma_f32_16x16x32_bf16 v[32:35], v[144:147], a[200:203], v[32:35]\n\t"
    "s_waitcnt lgkmcnt(0)\n\t"
    "ds_read_b128 v[152:155], v193 offset:128\n\t"
    "v_cvt_pk_bf16_f32 v122, v176, v177\n\t"
    "v_mfma_f32_16x16x32_bf16 v[48:51], v[148:151], a[200:203], v[48:51]\n\t"
    "ds_read_b128 v[156:159], v193 offset:1664\n\t"
    "v_fma_mix_f32 v236, v236, v204, v212 op_sel_hi:[1,0,0]\n\t"
    "v_mfma_f32_16x16x32_bf16 v[52:55], v[148:151], a[216:219], v[52:55]\n\t"
    "global_load_dwordx4 v[228:231], v197, s[80:81] offset:-512\n\t"
    "v_mfma_f32_16x16x32_bf16 v[36:39], v[144:147], a[216:219], v[36:39]\n\t"
    "s_add_u32 m0, s60, 12288\n\t"
    "v_exp_f32 v179, v91\n\t"
    "v_mfma_f32_16x16x32_bf16 v[40:43], v[144:147], a[232:235], v[40:43]\n\t"
    "buffer_load_dwordx4 v195, s[44:47], s65 offen offset:0 sc1 lds\n\t"
    "v_mfma_f32_16x16x32_bf16 v[56:59], v[148:151], a[232:235], v[56:59]\n\t"
    "v_exp_f32 v176, v76\n\t"
    "v_mfma_f32_16x16x32_bf16 v[60:63], v[148:151], a[248:251], v[60:63]\n\t"
    "v_add_f32 v174, v174, v178\n\t"
    "v_add_f32 v174, v174, v179\n\t"
    "v_mfma_f32_16x16x32_bf16 v[44:47], v[144:147], a[248:251], v[44:47]\n\t"
    "s_waitcnt lgkmcnt(1)\n\t"
    "v_exp_f32 v177, v77\n\t"
    "v_mfma_f32_16x16x32_bf16 v[32:35], v[152:155], a[204:207], v[32:35]\n\t"
    "s_waitcnt lgkmcnt(0)\n\t"
    "ds_read_b128 v[144:147], v192 offset:256\n\t"
    "v_cvt_pk_bf16_f32 v123, v178, v179\n\t"
    "v_mfma_f32_16x16x32_bf16 v[48:51], v[156:159], a[204:207], v[48:51]\n\t"
    "ds_read_b128 v[148:151], v192 offset:1792\n\t"
    "v_add_f32 v175, v176, v177\n\t"
    "v_mfma_f32_16x16x32_bf16 v[52:55], v[156:159], a[220:223], v[52:55]\n\t"
    "v_cvt_pk_bf16_f32 v124, v176, v177\n\t"
    "v_fma_mix_f32 v237, v237, v205, v213 op_sel_hi:[1,0,0]\n\t"
    "v_mfma_f32_16x16x32_bf16 v[36:39], v[152:155], a[220:223], v[36:39]\n\t"
    "v_exp_f32 v178, v78\n\t"
    "v_mfma_f32_16x16x32_bf16 v[40:43], v[152:155], a[236:239], v[40:43]\n\t"
    "buffer_load_dwordx4 v195, s[44:47], s65 offen offset:1024 sc1 lds\n\t"
    "v_mfma_f32_16x16x32_bf16 v[56:59], v[156:159], a[236:239], v[56:59]\n\t"
    "v_exp_f32 v179, v79\n\t"
    "v_mfma_f32_16x16x32_bf16 v[60:63], v[156:159], a[252:255], v[60:63]\n\t"
    "v_add_f32 v175, v175, v178\n\t"
    "v_add_f32 v175, v175, v179\n\t"
    "v_mfma_f32_16x16x32_bf16 v[44:47], v[152:155], a[252:255], v[44:47]\n\t"
    "s_waitcnt lgkmcnt(1)\n\t"
    "v_exp_f32 v176, v92\n\t"
    "v_mfma_f32_16x16x32_bf16 v[32:35], v[144:147], v[0:3], v[32:35]\n\t"
    "s_waitcnt lgkmcnt(0)\n\t"
    "ds_read_b128 v[152:155], v193 offset:256\n\t"
    "v_cvt_pk_bf16_f32 v125, v178, v179\n\t"
    "v_mfma_f32_16x16x32_bf16 v[48:51], v[148:151], v[0:3], v[48:51]\n\t"
    "ds_read_b128 v[156:159], v193 offset:1792\n\t"
    "v_fma_mix_f32 v238, v238, v206, v214 op_sel_hi:[1,0,0]\n\t"
    "v_mfma_f32_16x16x32_bf16 v[52:55], v[148:151], v[8:11], v[52:55]\n\t"
    "global_load_dwordx4 v[232:235], v197, s[80:81] offset:256\n\t"
    "v_mfma_f32_16x16x32_bf16 v[36:39], v[144:147], v[8:11], v[36:39]\n\t"
    "v_exp_f32 v177, v93\n\t"
    "v_mfma_f32_16x16x32_bf16 v[40:43], v[144:147], v[16:19], v[40:43]\n\t"
    "buffer_load_dwordx4 v195, s[44:47], s65 offen offset:2048 sc1 lds\n\t"
    "v_mfma_f32_16x16x32_bf16 v[56:59], v[148:151], v[16:19], v[56:59]\n\t"
    "v_exp_f32 v178, v94\n\t"
    "v_mfma_f32_16x16x32_bf16 v[60:63], v[148:151], v[24:27], v[60:63]\n\t"
    "v_add_f32 v175, v175, v176\n\t"
    "v_add_f32 v175, v175, v177\n\t"
    "v_mfma_f32_16x16x32_bf16 v[44:47], v[144:147], v[24:27], v[44:47]\n\t"
    "s_waitcnt lgkmcnt(1)\n\t"
    "v_exp_f32 v179, v95\n\t"
    "v_mfma_f32_16x16x32_bf16 v[32:35], v[152:155], v[4:7], v[32:35]\n\t"
    "s_waitcnt lgkmcnt(0)\n\t"
    "ds_read_b128 v[144:147], v181 offset:12288\n\t"
    "v_cvt_pk_bf16_f32 v126, v176, v177\n\t"
    "v_mfma_f32_16x16x32_bf16 v[48:51], v[156:159], v[4:7], v[48:51]\n\t"
    "ds_read_b128 v[148:151], v181 offset:12544\n\t"
    "v_add_f32 v175, v175, v178\n\t"
    "v_mfma_f32_16x16x32_bf16 v[52:55], v[156:159], v[12:15], v[52:55]\n\t"
    "v_add_f32 v175, v175, v179\n\t"
    "v_cvt_pk_bf16_f32 v127, v178, v179\n\t"
    "v_mfma_f32_16x16x32_bf16 v[36:39], v[152:155], v[12:15], v[36:39]\n\t"
    "v_max3_f32 v180, v172, v173, v174\n\t"
    "v_max_f32 v180, v180, v175\n\t"
    "v_mfma_f32_16x16x32_bf16 v[40:43], v[152:155], v[20:23], v[40:43]\n\t"
    "v_cmp_lt_f32 vcc, s62, v180\n\t"
    "v_fma_mix_f32 v239, v239, v207, v215 op_sel_hi:[1,0,0]\n\t"
    "v_mfma_f32_16x16x32_bf16 v[56:59], v[156:159], v[20:23], v[56:59]\n\t"
    "v_fma_mix_f32 v240, v240, v208, v216 op_sel_hi:[1,0,0]\n\t"
    "v_fma_mix_f32 v241, v241, v209, v217 op_sel_hi:[1,0,0]\n\t"
    "v_mfma_f32_16x16x32_bf16 v[60:63], v[156:159], v[28:31], v[60:63]\n\t"
    "v_fma_mix_f32 v242, v242, v210, v218 op_sel_hi:[1,0,0]\n\t"
    "v_fma_mix_f32 v243, v243, v211, v219 op_sel_hi:[1,0,0]\n\t"
    "v_mfma_f32_16x16x32_bf16 v[44:47], v[152:155], v[28:31], v[44:47]\n\t"
    "s_waitcnt lgkmcnt(1)\n\t"
    "s_cbranch_vccnz L_stub_l2_1\n\t"
    "L_ret_l2_1:\n\t"
    "v_mfma_f32_16x16x32_bf16 a[0:3], v[112:115], v[144:147], a[0:3]\n\t"
    "ds_read_b128 v[152:155], v181 offset:12800\n\t"
    "v_add_f32 v168, v168, v172\n\t"
    "v_mfma_f32_16x16x32_bf16 a[4:7], v[116:119], v[144:147], a[4:7]\n\t"
    "v_exp_f32 v176, v32\n\t"
    "v_mfma_f32_16x16x32_bf16 a[8:11], v[120:123], v[144:147], a[8:11]\n\t"
    "v_add_f32 v169, v169, v173\n\t"
    "v_add_f32 v170, v170, v174\n\t"
    "v_mfma_f32_16x16x32_bf16 a[12:15], v[124:127], v[144:147], a[12:15]\n\t"
    "s_waitcnt lgkmcnt(1)\n\t"
    "v_exp_f32 v177, v33\n\t"
    "v_mfma_f32_16x16x32_bf16 a[28:31], v[124:127], v[148:151], a[28:31]\n\t"
    "ds_read_b128 v[156:159], v181 offset:13056\n\t"
    "v_add_f32 v171, v171, v175\n\t"
    "v_mfma_f32_16x16x32_bf16 a[24:27], v[120:123], v[148:151], a[24:27]\n\t"
    "v_add_f32 v172, v176, v177\n\t"
    "v_cvt_pk_bf16_f32 v96, v176, v177\n\t"
    "v_mfma_f32_16x16x32_bf16 a[20:23], v[116:119], v[148:151], a[20:23]\n\t"
    "v_cvt_pk_bf16_f32 v244, v236, v237\n\t"
    "v_cvt_pk_bf16_f32 v245, v238, v239\n\t"
    "v_mfma_f32_16x16x32_bf16 a[16:19], v[112:115], v[148:151], a[16:19]\n\t"
    "s_waitcnt lgkmcnt(1)\n\t"
    "v_exp_f32 v178, v34\n\t"
    "v_mfma_f32_16x16x32_bf16 a[32:35], v[112:115], v[152:155], a[32:35]\n\t"
    "ds_read_b128 v[144:147], v181 offset:13312\n\t"
    "v_cvt_pk_bf16_f32 v246, v240, v241\n\t"
    "v_mfma_f32_16x16x32_bf16 a[36:39], v[116:119], v[152:155], a[36:39]\n\t"
    "v_exp_f32 v179, v35\n\t"
    "v_mfma_f32_16x16x32_bf16 a[40:43], v[120:123], v[152:155], a[40:43]\n\t"
    "v_add_f32 v172, v172, v178\n\t"
    "v_add_f32 v172, v172, v179\n\t"
    "v_mfma_f32_16x16x32_bf16 a[44:47], v[124:127], v[152:155], a[44:47]\n\t"
    "s_waitcnt lgkmcnt(1)\n\t"
    "v_cvt_pk_bf16_f32 v97, v178, v179\n\t"
    "v_cvt_pk_bf16_f32 v247, v242, v243\n\t"
    "v_mfma_f32_16x16x32_bf16 a[60:63], v[124:127], v[156:159], a[60:63]\n\t"
    "s_waitcnt vmcnt(36)\n\t"
    "ds_read_b128 v[148:151], v181 offset:13568\n\t"
    "v_fma_mix_f32 v160, v160, v204, v212 op_sel_hi:[1,0,0]\n\t"
    "v_mfma_f32_16x16x32_bf16 a[56:59], v[120:123], v[156:159], a[56:59]\n\t"
    "v_exp_f32 v176, v48\n\t"
    "v_mfma_f32_16x16x32_bf16 a[52:55], v[116:119], v[156:159], a[52:55]\n\t"
    "buffer_store_dwordx4 v[244:247], v199, s[48:51], s92 offen offset:1536 sc1\n\t"
    "v_mfma_f32_16x16x32_bf16 a[48:51], v[112:115], v[156:159], a[48:51]\n\t"
    "s_waitcnt lgkmcnt(1)\n\t"
    "v_exp_f32 v177, v49\n\t"
    "v_mfma_f32_16x16x32_bf16 a[64:67], v[112:115], v[144:147], a[64:67]\n\t"
    "ds_read_b128 v[152:155], v181 offset:13824\n\t"
    "v_add_f32 v172, v172, v176\n\t"
    "v_mfma_f32_16x16x32_bf16 a[68:71], v[116:119], v[144:147], a[68:71]\n\t"
    "v_add_f32 v172, v172, v177\n\t"
    "v_cvt_pk_bf16_f32 v98, v176, v177\n\t"
    "v_mfma_f32_16x16x32_bf16 a[72:75], v[120:123], v[144:147], a[72:75]\n\t"
    "global_load_dwordx4 v[236:239], v197, s[80:81] offset:1024\n\t"
    "v_mfma_f32_16x16x32_bf16 a[76:79], v[124:127], v[144:147], a[76:79]\n\t"
    "s_waitcnt lgkmcnt(1)\n\t"
    "v_exp_f32 v178, v50\n\t"
    "v_mfma_f32_16x16x32_bf16 a[92:95], v[124:127], v[148:151], a[92:95]\n\t"
    "ds_read_b128 v[156:159], v181 offset:14080\n\t"
    "v_fma_mix_f32 v161, v161, v205, v213 op_sel_hi:[1,0,0]\n\t"
    "v_mfma_f32_16x16x32_bf16 a[88:91], v[120:123], v[148:151], a[88:91]\n\t"
    "v_exp_f32 v179, v51\n\t"
    "v_mfma_f32_16x16x32_bf16 a[84:87], v[116:119], v[148:151], a[84:87]\n\t"
    "v_add_f32 v172, v172, v178\n\t"
    "v_add_f32 v172, v172, v179\n\t"
    "v_mfma_f32_16x16x32_bf16 a[80:83], v[112:115], v[148:151], a[80:83]\n\t"
    "s_waitcnt lgkmcnt(1)\n\t"
    "v_cvt_pk_bf16_f32 v99, v178, v179\n\t"
    "v_fma_mix_f32 v162, v162, v206, v214 op_sel_hi:[1,0,0]\n\t"
    "v_mfma_f32_16x16x32_bf16 a[96:99], v[112:115], v[152:155], a[96:99]\n\t"
    "ds_read_b128 v[144:147], v181 offset:14336\n\t"
    "v_fma_mix_f32 v163, v163, v207, v215 op_sel_hi:[1,0,0]\n\t"
    "v_mfma_f32_16x16x32_bf16 a[100:103], v[116:119], v[152:155], a[100:103]\n\t"
    "v_exp_f32 v176, v36\n\t"
    "v_mfma_f32_16x16x32_bf16 a[104:107], v[120:123], v[152:155], a[104:107]\n\t"
    "global_load_dwordx4 v[240:243], v197, s[80:81] offset:1792\n\t"
    "v_mfma_f32_16x16x32_bf16 a[108:111], v[124:127], v[152:155], a[108:111]\n\t"
    "s_waitcnt lgkmcnt(1)\n\t"
    "v_exp_f32 v177, v37\n\t"
    "v_mfma_f32_16x16x32_bf16 a[124:127], v[124:127], v[156:159], a[124:127]\n\t"
    "ds_read_b128 v[148:151], v181 offset:14592\n\t"
    "v_add_f32 v173, v176, v177\n\t"
    "v_mfma_f32_16x16x32_bf16 a[120:123], v[120:123], v[156:159], a[120:123]\n\t"
    "v_cvt_pk_bf16_f32 v100, v176, v177\n\t"
    "v_fma_mix_f32 v164, v164, v208, v216 op_sel_hi:[1,0,0]\n\t"
    "v_mfma_f32_16x16x32_bf16 a[116:119], v[116:119], v[156:159], a[116:119]\n\t"
    "v_fma_mix_f32 v165, v165, v209, v217 op_sel_hi:[1,0,0]\n\t"
    "v_fma_mix_f32 v166, v166, v210, v218 op_sel_hi:[1,0,0]\n\t"
    "v_mfma_f32_16x16x32_bf16 a[112:115], v[112:115], v[156:159], a[112:115]\n\t"
    "s_waitcnt lgkmcnt(1)\n\t"
    "v_exp_f32 v178, v38\n\t"
    "v_mfma_f32_16x16x32_bf16 a[128:131], v[112:115], v[144:147], a[128:131]\n\t"
    "ds_read_b128 v[152:155], v181 offset:14848\n\t"
    "v_fma_mix_f32 v167, v167, v211, v219 op_sel_hi:[1,0,0]\n\t"
    "v_mfma_f32_16x16x32_bf16 a[132:135], v[116:119], v[144:147], a[132:135]\n\t"
    "s_waitcnt vmcnt(16)\n\t"
    "v_exp_f32 v179, v39\n\t"
    "v_mfma_f32_16x16x32_bf16 a[136:139], v[120:123], v[144:147], a[136:139]\n\t"
    "s_barrier\n\t"
    "v_add_f32 v173, v173, v178\n\t"
    "v_add_f32 v173, v173, v179\n\t"
    "v_mfma_f32_16x16x32_bf16 a[140:143], v[124:127], v[144:147], a[140:143]\n\t"
    "s_waitcnt lgkmcnt(1)\n\t"
    "v_cvt_pk_bf16_f32 v101, v178, v179\n\t"
    "v_cvt_pk_bf16_f32 v248, v160, v161\n\t"
    "v_mfma_f32_16x16x32_bf16 a[156:159], v[124:127], v[148:151], a[156:159]\n\t"
    "ds_read_b128 v[156:159], v181 offset:15104\n\t"
    "v_cvt_pk_bf16_f32 v249, v162, v163\n\t"
    "v_mfma_f32_16x16x32_bf16 a[152:155], v[120:123], v[148:151], a[152:155]\n\t"
    "s_add_u32 m0, s60, 61440\n\t"
    "v_exp_f32 v176, v52\n\t"
    "v_mfma_f32_16x16x32_bf16 a[148:151], v[116:119], v[148:151], a[148:151]\n\t"
    "buffer_load_dwordx4 v195, s[40:43], s67 offen offset:0 sc1 lds\n\t"
    "v_mfma_f32_16x16x32_bf16 a[144:147], v[112:115], v[148:151], a[144:147]\n\t"
    "s_waitcnt lgkmcnt(1)\n\t"
    "v_exp_f32 v177, v53\n\t"
    "v_mfma_f32_16x16x32_bf16 a[160:163], v[112:115], v[152:155], a[160:163]\n\t"
    "ds_read_b128 v[144:147], v192 offset:12288\n\t"
    "v_add_f32 v173, v173, v176\n\t"
    "v_mfma_f32_16x16x32_bf16 a[164:167], v[116:119], v[152:155], a[164:167]\n\t"
    "ds_read_b128 v[148:151], v192 offset:13824\n\t"
    "v_add_f32 v173, v173, v177\n\t"
    "v_mfma_f32_16x16x32_bf16 a[168:171], v[120:123], v[152:155], a[168:171]\n\t"
    "buffer_load_dwordx4 v195, s[40:43], s67 offen offset:1024 sc1 lds\n\t"
    "v_mfma_f32_16x16x32_bf16 a[172:175], v[124:127], v[152:155], a[172:175]\n\t"
    "s_waitcnt lgkmcnt(2)\n\t"
    "v_exp_f32 v178, v54\n\t"
    "v_mfma_f32_16x16x32_bf16 a[188:191], v[124:127], v[156:159], a[188:191]\n\t"
    "v_cvt_pk_bf16_f32 v102, v176, v177\n\t"
    "v_cvt_pk_bf16_f32 v250, v164, v165\n\t"
    "v_mfma_f32_16x16x32_bf16 a[184:187], v[120:123], v[156:159], a[184:187]\n\t"
    "v_exp_f32 v179, v55\n\t"
    "v_mfma_f32_16x16x32_bf16 a[180:183], v[116:119], v[156:159], a[180:183]\n\t"
    "v_add_f32 v173, v173, v178\n\t"
    "v_add_f32 v173, v173, v179\n\t"
    "v_mfma_f32_16x16x32_bf16 a[176:179], v[112:115], v[156:159], a[176:179]\n\t"
    "s_waitcnt lgkmcnt(1)\n\t"
    "v_cvt_pk_bf16_f32 v103, v178, v179\n\t"
    "v_cvt_pk_bf16_f32 v251, v166, v167\n\t"
    "v_mfma_f32_16x16x32_bf16 v[64:67], v[144:147], a[192:195], v[128:131]\n\t"
    "s_waitcnt lgkmcnt(0)\n\t"
    "ds_read_b128 v[152:155], v193 offset:12288\n\t"
    "v_mfma_f32_16x16x32_bf16 v[80:83], v[148:151], a[192:195], v[128:131]\n\t"
    "s_waitcnt vmcnt(17)\n\t"
    "ds_read_b128 v[156:159], v193 offset:13824\n\t"
    "v_lshlrev_b32 v204, 16, v252\n\t"
    "v_mfma_f32_16x16x32_bf16 v[84:87], v[148:151], a[208:211], v[132:135]\n\t"
    "v_exp_f32 v176, v40\n\t"
    "v_mfma_f32_16x16x32_bf16 v[68:71], v[144:147], a[208:211], v[132:135]\n\t"
    "v_exp_f32 v177, v41\n\t"
    "v_mfma_f32_16x16x32_bf16 v[72:75], v[144:147], a[224:227], v[136:139]\n\t"
    "buffer_load_dwordx4 v195, s[40:43], s67 offen offset:2048 sc1 lds\n\t"
    "v_mfma_f32_16x16x32_bf16 v[88:91], v[148:151], a[224:227], v[136:139]\n\t"
    "v_exp_f32 v178, v42\n\t"
    "v_mfma_f32_16x16x32_bf16 v[92:95], v[148:151], a[240:243], v[140:143]\n\t"
    "v_add_f32 v174, v176, v177\n\t"
    "v_cvt_pk_bf16_f32 v104, v176, v177\n\t"
    "v_mfma_f32_16x16x32_bf16 v[76:79], v[144:147], a[240:243], v[140:143]\n\t"
    "s_waitcnt lgkmcnt(1)\n\t"
    "v_exp_f32 v179, v43\n\t"
    "v_mfma_f32_16x16x32_bf16 v[64:67], v[152:155], a[196:199], v[64:67]\n\t"
    "s_waitcnt lgkmcnt(0)\n\t"
    "ds_read_b128 v[144:147], v192 offset:12416\n\t"
    "v_add_f32 v174, v174, v178\n\t"
    "v_mfma_f32_16x16x32_bf16 v[80:83], v[156:159], a[196:199], v[80:83]\n\t"
    "ds_read_b128 v[148:151], v192 offset:13952\n\t"
    "v_add_f32 v174, v174, v179\n\t"
    "v_mfma_f32_16x16x32_bf16 v[84:87], v[156:159], a[212:215], v[84:87]\n\t"
    "v_cvt_pk_bf16_f32 v105, v178, v179\n\t"
    "v_and_b32 v205, 0xffff0000, v252\n\t"
    "v_mfma_f32_16x16x32_bf16 v[68:71], v[152:155], a[212:215], v[68:71]\n\t"
    "v_exp_f32 v176, v56\n\t"
    "v_mfma_f32_16x16x32_bf16 v[72:75], v[152:155], a[228:231], v[72:75]\n\t"
    "buffer_store_dwordx4 v[248:251], v200, s[48:51], s92 offen offset:2304 sc1\n\t"
    "v_mfma_f32_16x16x32_bf16 v[88:91], v[156:159], a[228:231], v[88:91]\n\t"
    "v_exp_f32 v177, v57\n\t"
    "v_mfma_f32_16x16x32_bf16 v[92:95], v[156:159], a[244:247], v[92:95]\n\t"
    "v_add_f32 v174, v174, v176\n\t"
    "v_add_f32 v174, v174, v177\n\t"
    "v_mfma_f32_16x16x32_bf16 v[76:79], v[152:155], a[244:247], v[76:79]\n\t"
    "s_waitcnt lgkmcnt(1)\n\t"
    "v_exp_f32 v178, v58\n\t"
    "v_mfma_f32_16x16x32_bf16 v[64:67], v[144:147], a[200:203], v[64:67]\n\t"
    "s_waitcnt lgkmcnt(0)\n\t"
    "ds_read_b128 v[152:155], v193 offset:12416\n\t"
    "v_cvt_pk_bf16_f32 v106, v176, v177\n\t"
    "v_mfma_f32_16x16x32_bf16 v[80:83], v[148:151], a[200:203], v[80:83]\n\t"
    "ds_read_b128 v[156:159], v193 offset:13952\n\t"
    "v_lshlrev_b32 v212, 16, v188\n\t"
    "v_mfma_f32_16x16x32_bf16 v[84:87], v[148:151], a[216:219], v[84:87]\n\t"
    "global_load_dwordx4 v[160:163], v197, s[80:81] offset:2560\n\t"
    "v_mfma_f32_16x16x32_bf16 v[68:71], v[144:147], a[216:219], v[68:71]\n\t"
    "s_add_u32 m0, s60, 73728\n\t"
    "v_exp_f32 v179, v59\n\t"
    "v_mfma_f32_16x16x32_bf16 v[72:75], v[144:147], a[232:235], v[72:75]\n\t"
    "buffer_load_dwordx4 v195, s[44:47], s66 offen offset:0 sc1 lds\n\t"
    "v_mfma_f32_16x16x32_bf16 v[88:91], v[148:151], a[232:235], v[88:91]\n\t"
    "v_exp_f32 v176, v44\n\t"
    "v_mfma_f32_16x16x32_bf16 v[92:95], v[148:151], a[248:251], v[92:95]\n\t"
    "v_add_f32 v174, v174, v178\n\t"
    "v_add_f32 v174, v174, v179\n\t"
    "v_mfma_f32_16x16x32_bf16 v[76:79], v[144:147], a[248:251], v[76:79]\n\t"
    "s_waitcnt lgkmcnt(1)\n\t"
    "v_exp_f32 v177, v45\n\t"
    "v_mfma_f32_16x16x32_bf16 v[64:67], v[152:155], a[204:207], v[64:67]\n\t"
    "s_waitcnt lgkmcnt(0)\n\t"
    "ds_read_b128 v[144:147], v192 offset:12544\n\t"
    "v_cvt_pk_bf16_f32 v107, v178, v179\n\t"
    "v_mfma_f32_16x16x32_bf16 v[80:83], v[156:159], a[204:207], v[80:83]\n\t"
    "ds_read_b128 v[148:151], v192 offset:14080\n\t"
    "v_add_f32 v175, v176, v177\n\t"
    "v_mfma_f32_16x16x32_bf16 v[84:87], v[156:159], a[220:223], v[84:87]\n\t"
    "v_cvt_pk_bf16_f32 v108, v176, v177\n\t"
    "v_and_b32 v213, 0xffff0000, v188\n\t"
    "v_mfma_f32_16x16x32_bf16 v[68:71], v[152:155], a[220:223], v[68:71]\n\t"
    "v_exp_f32 v178, v46\n\t"
    "v_mfma_f32_16x16x32_bf16 v[72:75], v[152:155], a[236:239], v[72:75]\n\t"
    "buffer_load_dwordx4 v195, s[44:47], s66 offen offset:1024 sc1 lds\n\t"
    "v_mfma_f32_16x16x32_bf16 v[88:91], v[156:159], a[236:239], v[88:91]\n\t"
    "v_exp_f32 v179, v47\n\t"
    "v_mfma_f32_16x16x32_bf16 v[92:95], v[156:159], a[252:255], v[92:95]\n\t"
    "v_add_f32 v175, v175, v178\n\t"
    "v_add_f32 v175, v175, v179\n\t"
    "v_mfma_f32_16x16x32_bf16 v[76:79], v[152:155], a[252:255], v[76:79]\n\t"
    "s_waitcnt lgkmcnt(1)\n\t"
    "v_exp_f32 v176, v60\n\t"
    "v_mfma_f32_16x16x32_bf16 v[64:67], v[144:147], v[0:3], v[64:67]\n\t"
    "s_waitcnt lgkmcnt(0)\n\t"
    "ds_read_b128 v[152:155], v193 offset:12544\n\t"
    "v_cvt_pk_bf16_f32 v109, v178, v179\n\t"
    "v_mfma_f32_16x16x32_bf16 v[80:83], v[148:151], v[0:3], v[80:83]\n\t"
    "ds_read_b128 v[156:159], v193 offset:14080\n\t"
    "v_lshlrev_b32 v206, 16, v253\n\t"
    "v_mfma_f32_16x16x32_bf16 v[84:87], v[148:151], v[8:11], v[84:87]\n\t"
    "global_load_dwordx4 v[164:167], v197, s[80:81] offset:3328\n\t"
    "v_mfma_f32_16x16x32_bf16 v[68:71], v[144:147], v[8:11], v[68:71]\n\t"
    "v_exp_f32 v177, v61\n\t"
    "v_mfma_f32_16x16x32_bf16 v[72:75], v[144:147], v[16:19], v[72:75]\n\t"
    "buffer_load_dwordx4 v195, s[44:47], s66 offen offset:2048 sc1 lds\n\t"
    "v_mfma_f32_16x16x32_bf16 v[88:91], v[148:151], v[16:19], v[88:91]\n\t"
    "s_add_u32 s80, s80, 196608\n\t"
    "v_exp_f32 v178, v62\n\t"
    "v_mfma_f32_16x16x32_bf16 v[92:95], v[148:151], v[24:27], v[92:95]\n\t"
    "s_addc_u32 s81, s81, 0\n\t"
    "v_add_f32 v175, v175, v176\n\t"
    "v_add_f32 v175, v175, v177\n\t"
    "v_mfma_f32_16x16x32_bf16 v[76:79], v[144:147], v[24:27], v[76:79]\n\t"
    "s_waitcnt lgkmcnt(1)\n\t"
    "v_exp_f32 v179, v63\n\t"
    "v_mfma_f32_16x16x32_bf16 v[64:67], v[152:155], v[4:7], v[64:67]\n\t"
    "s_waitcnt lgkmcnt(0)\n\t"
    "ds_read_b128 v[144:147], v194 offset:0\n\t"
    "v_cvt_pk_bf16_f32 v110, v176, v177\n\t"
    "v_mfma_f32_16x16x32_bf16 v[80:83], v[156:159], v[4:7], v[80:83]\n\t"
    "ds_read_b128 v[148:151], v194 offset:256\n\t"
    "v_add_f32 v175, v175, v178\n\t"
    "v_mfma_f32_16x16x32_bf16 v[84:87], v[156:159], v[12:15], v[84:87]\n\t"
    "v_add_f32 v175, v175, v179\n\t"
    "v_cvt_pk_bf16_f32 v111, v178, v179\n\t"
    "v_mfma_f32_16x16x32_bf16 v[68:71], v[152:155], v[12:15], v[68:71]\n\t"
    "v_max3_f32 v180, v172, v173, v174\n\t"
    "v_max_f32 v180, v180, v175\n\t"
    "v_mfma_f32_16x16x32_bf16 v[72:75], v[152:155], v[20:23], v[72:75]\n\t"
    "v_cmp_lt_f32 vcc, s62, v180\n\t"
    "v_and_b32 v207, 0xffff0000, v253\n\t"
    "v_mfma_f32_16x16x32_bf16 v[88:91], v[156:159], v[20:23], v[88:91]\n\t"
    "v_lshlrev_b32 v214, 16, v189\n\t"
    "v_and_b32 v215, 0xffff0000, v189\n\t"
    "v_mfma_f32_16x16x32_bf16 v[92:95], v[156:159], v[28:31], v[92:95]\n\t"
    "v_lshlrev_b32 v208, 16, v254\n\t"
    "v_and_b32 v209, 0xffff0000, v254\n\t"
    "v_mfma_f32_16x16x32_bf16 v[76:79], v[152:155], v[28:31], v[76:79]\n\t"
    "s_waitcnt lgkmcnt(1)\n\t"
    "s_cbranch_vccnz L_stub_l3_0\n\t"
    "L_ret_l3_0:\n\t"
    "v_mfma_f32_16x16x32_bf16 a[0:3], v[96:99], v[144:147], a[0:3]\n\t"
    "ds_read_b128 v[152:155], v194 offset:512\n\t"
    "v_add_f32 v168, v168, v172\n\t"
    "v_mfma_f32_16x16x32_bf16 a[4:7], v[100:103], v[144:147], a[4:7]\n\t"
    "v_exp_f32 v176, v64\n\t"
    "v_mfma_f32_16x16x32_bf16 a[8:11], v[104:107], v[144:147], a[8:11]\n\t"
    "v_add_f32 v169, v169, v173\n\t"
    "v_add_f32 v170, v170, v174\n\t"
    "v_mfma_f32_16x16x32_bf16 a[12:15], v[108:111], v[144:147], a[12:15]\n\t"
    "s_waitcnt lgkmcnt(1)\n\t"
    "v_exp_f32 v177, v65\n\t"
    "v_mfma_f32_16x16x32_bf16 a[28:31], v[108:111], v[148:151], a[28:31]\n\t"
    "ds_read_b128 v[156:159], v194 offset:768\n\t"
    "v_add_f32 v171, v171, v175\n\t"
    "v_mfma_f32_16x16x32_bf16 a[24:27], v[104:107], v[148:151], a[24:27]\n\t"
    "v_add_f32 v172, v176, v177\n\t"
    "v_cvt_pk_bf16_f32 v112, v176, v177\n\t"
    "v_mfma_f32_16x16x32_bf16 a[20:23], v[100:103], v[148:151], a[20:23]\n\t"
    "v_lshlrev_b32 v216, 16, v190\n\t"
    "v_and_b32 v217, 0xffff0000, v190\n\t"
    "v_mfma_f32_16x16x32_bf16 a[16:19], v[96:99], v[148:151], a[16:19]\n\t"
    "s_waitcnt lgkmcnt(1)\n\t"
    "v_exp_f32 v178, v66\n\t"
    "v_mfma_f32_16x16x32_bf16 a[32:35], v[96:99], v[152:155], a[32:35]\n\t"
    "ds_read_b128 v[144:147], v194 offset:1024\n\t"
    "v_lshlrev_b32 v210, 16, v255\n\t"
    "v_mfma_f32_16x16x32_bf16 a[36:39], v[100:103], v[152:155], a[36:39]\n\t"
    "v_exp_f32 v179, v67\n\t"
    "v_mfma_f32_16x16x32_bf16 a[40:43], v[104:107], v[152:155], a[40:43]\n\t"
    "v_add_f32 v172, v172, v178\n\t"
    "v_add_f32 v172, v172, v179\n\t"
    "v_mfma_f32_16x16x32_bf16 a[44:47], v[108:111], v[152:155], a[44:47]\n\t"
    "s_waitcnt lgkmcnt(1)\n\t"
    "v_cvt_pk_bf16_f32 v113, v178, v179\n\t"
    "v_and_b32 v211, 0xffff0000, v255\n\t"
    "v_mfma_f32_16x16x32_bf16 a[60:63], v[108:111], v[156:159], a[60:63]\n\t"
    "ds_read_b128 v[148:151], v194 offset:1280\n\t"
    "v_lshlrev_b32 v218, 16, v191\n\t"
    "v_mfma_f32_16x16x32_bf16 a[56:59], v[104:107], v[156:159], a[56:59]\n\t"
    "v_exp_f32 v176, v80\n\t"
    "v_mfma_f32_16x16x32_bf16 a[52:55], v[100:103], v[156:159], a[52:55]\n\t"
    "v_and_b32 v219, 0xffff0000, v191\n\t"
    "v_mul_f32_e64 v212, -v212, v204\n\t"
    "v_mfma_f32_16x16x32_bf16 a[48:51], v[96:99], v[156:159], a[48:51]\n\t"
    "s_waitcnt lgkmcnt(1)\n\t"
    "v_exp_f32 v177, v81\n\t"
    "v_mfma_f32_16x16x32_bf16 a[64:67], v[96:99], v[144:147], a[64:67]\n\t"
    "ds_read_b128 v[152:155], v194 offset:1536\n\t"
    "v_add_f32 v172, v172, v176\n\t"
    "v_mfma_f32_16x16x32_bf16 a[68:71], v[100:103], v[144:147], a[68:71]\n\t"
    "v_add_f32 v172, v172, v177\n\t"
    "v_cvt_pk_bf16_f32 v114, v176, v177\n\t"
    "v_mfma_f32_16x16x32_bf16 a[72:75], v[104:107], v[144:147], a[72:75]\n\t"
    "v_mul_f32_e64 v213, -v213, v205\n\t"
    "v_mul_f32_e64 v214, -v214, v206\n\t"
    "v_mfma_f32_16x16x32_bf16 a[76:79], v[108:111], v[144:147], a[76:79]\n\t"
    "s_waitcnt lgkmcnt(1)\n\t"
    "v_exp_f32 v178, v82\n\t"
    "v_mfma_f32_16x16x32_bf16 a[92:95], v[108:111], v[148:151], a[92:95]\n\t"
    "ds_read_b128 v[156:159], v194 offset:1792\n\t"
    "v_mul_f32_e64 v215, -v215, v207\n\t"
    "v_mfma_f32_16x16x32_bf16 a[88:91], v[104:107], v[148:151], a[88:91]\n\t"
    "v_exp_f32 v179, v83\n\t"
    "v_mfma_f32_16x16x32_bf16 a[84:87], v[100:103], v[148:151], a[84:87]\n\t"
    "v_add_f32 v172, v172, v178\n\t"
    "v_add_f32 v172, v172, v179\n\t"
    "v_mfma_f32_16x16x32_bf16 a[80:83], v[96:99], v[148:151], a[80:83]\n\t"
    "s_waitcnt lgkmcnt(1)\n\t"
    "v_cvt_pk_bf16_f32 v115, v178, v179\n\t"
    "v_mul_f32_e64 v216, -v216, v208\n\t"
    "v_mfma_f32_16x16x32_bf16 a[96:99], v[96:99], v[152:155], a[96:99]\n\t"
    "ds_read_b128 v[144:147], v194 offset:2048\n\t"
    "v_mul_f32_e64 v217, -v217, v209\n\t"
    "v_mfma_f32_16x16x32_bf16 a[100:103], v[100:103], v[152:155], a[100:103]\n\t"
    "v_exp_f32 v176, v68\n\t"
    "v_mfma_f32_16x16x32_bf16 a[104:107], v[104:107], v[152:155], a[104:107]\n\t"
    "v_mul_f32_e64 v218, -v218, v210\n\t"
    "v_mul_f32_e64 v219, -v219, v211\n\t"
    "v_mfma_f32_16x16x32_bf16 a[108:111], v[108:111], v[152:155], a[108:111]\n\t"
    "s_waitcnt lgkmcnt(1)\n\t"
    "v_exp_f32 v177, v69\n\t"
    "v_mfma_f32_16x16x32_bf16 a[124:127], v[108:111], v[156:159], a[124:127]\n\t"
    "ds_read_b128 v[148:151], v194 offset:2304\n\t"
    "v_add_f32 v173, v176, v177\n\t"
    "v_mfma_f32_16x16x32_bf16 a[120:123], v[104:107], v[156:159], a[120:123]\n\t"
    "v_cvt_pk_bf16_f32 v116, v176, v177\n\t"
    "v_mul_f32 v204, 0x4b800000, v204\n\t"
    "v_mfma_f32_16x16x32_bf16 a[116:119], v[100:103], v[156:159], a[116:119]\n\t"
    "v_mul_f32 v205, 0x4b800000, v205\n\t"
    "v_mul_f32 v206, 0x4b800000, v206\n\t"
    "v_mfma_f32_16x16x32_bf16 a[112:115], v[96:99], v[156:159], a[112:115]\n\t"
    "s_waitcnt lgkmcnt(1)\n\t"
    "v_exp_f32 v178, v70\n\t"
    "v_mfma_f32_16x16x32_bf16 a[128:131], v[96:99], v[144:147], a[128:131]\n\t"
    "ds_read_b128 v[152:155], v194 offset:2560\n\t"
    "v_mul_f32 v207, 0x4b800000, v207\n\t"
    "v_mfma_f32_16x16x32_bf16 a[132:135], v[100:103], v[144:147], a[132:135]\n\t"
    "s_waitcnt vmcnt(12)\n\t"
    "v_exp_f32 v179, v71\n\t"
    "v_mfma_f32_16x16x32_bf16 a[136:139], v[104:107], v[144:147], a[136:139]\n\t"
    "s_barrier\n\t"
    "v_add_f32 v173, v173, v178\n\t"
    "v_add_f32 v173, v173, v179\n\t"
    "v_mfma_f32_16x16x32_bf16 a[140:143], v[108:111], v[144:147], a[140:143]\n\t"
    "s_waitcnt lgkmcnt(1)\n\t"
    "v_cvt_pk_bf16_f32 v117, v178, v179\n\t"
    "v_mul_f32 v208, 0x4b800000, v208\n\t"
    "v_mfma_f32_16x16x32_bf16 a[156:159], v[108:111], v[148:151], a[156:159]\n\t"
    "ds_read_b128 v[156:159], v194 offset:2816\n\t"
    "v_mul_f32 v209, 0x4b800000, v209\n\t"
    "v_mfma_f32_16x16x32_bf16 a[152:155], v[104:107], v[148:151], a[152:155]\n\t"
    "s_add_u32 m0, s60, 24576\n\t"
    "v_exp_f32 v176, v84\n\t"
    "v_mfma_f32_16x16x32_bf16 a[148:151], v[100:103], v[148:151], a[148:151]\n\t"
    "buffer_load_dwordx4 v195, s[40:43], s68 offen offset:0 sc1 lds\n\t"
    "v_mfma_f32_16x16x32_bf16 a[144:147], v[96:99], v[148:151], a[144:147]\n\t"
    "s_waitcnt lgkmcnt(1)\n\t"
    "v_exp_f32 v177, v85\n\t"
    "v_mfma_f32_16x16x32_bf16 a[160:163], v[96:99], v[152:155], a[160:163]\n\t"
    "ds_read_b128 v[144:147], v192 offset:24576\n\t"
    "v_add_f32 v173, v173, v176\n\t"
    "v_mfma_f32_16x16x32_bf16 a[164:167], v[100:103], v[152:155], a[164:167]\n\t"
    "ds_read_b128 v[148:151], v192 offset:26112\n\t"
    "v_add_f32 v173, v173, v177\n\t"
    "v_mfma_f32_16x16x32_bf16 a[168:171], v[104:107], v[152:155], a[168:171]\n\t"
    "buffer_load_dwordx4 v195, s[40:43], s68 offen offset:1024 sc1 lds\n\t"
    "v_mfma_f32_16x16x32_bf16 a[172:175], v[108:111], v[152:155], a[172:175]\n\t"
    "s_waitcnt lgkmcnt(2)\n\t"
    "v_exp_f32 v178, v86\n\t"
    "v_mfma_f32_16x16x32_bf16 a[188:191], v[108:111], v[156:159], a[188:191]\n\t"
    "v_cvt_pk_bf16_f32 v118, v176, v177\n\t"
    "v_mul_f32 v210, 0x4b800000, v210\n\t"
    "v_mfma_f32_16x16x32_bf16 a[184:187], v[104:107], v[156:159], a[184:187]\n\t"
    "v_exp_f32 v179, v87\n\t"
    "v_mfma_f32_16x16x32_bf16 a[180:183], v[100:103], v[156:159], a[180:183]\n\t"
    "v_add_f32 v173, v173, v178\n\t"
    "v_add_f32 v173, v173, v179\n\t"
    "v_mfma_f32_16x16x32_bf16 a[176:179], v[96:99], v[156:159], a[176:179]\n\t"
    "s_waitcnt lgkmcnt(1)\n\t"
    "v_cvt_pk_bf16_f32 v119, v178, v179\n\t"
    "v_mul_f32 v211, 0x4b800000, v211\n\t"
    "v_mfma_f32_16x16x32_bf16 v[32:35], v[144:147], a[192:195], v[128:131]\n\t"
    "s_waitcnt lgkmcnt(0)\n\t"
    "ds_read_b128 v[152:155], v193 offset:24576\n\t"
    "v_mfma_f32_16x16x32_bf16 v[48:51], v[148:151], a[192:195], v[128:131]\n\t"
    "s_waitcnt vmcnt(23)\n\t"
    "ds_read_b128 v[156:159], v193 offset:26112\n\t"
    "v_fma_mix_f32 v220, v220, v204, v212 op_sel_hi:[1,0,0]\n\t"
    "v_mfma_f32_16x16x32_bf16 v[52:55], v[148:151], a[208:211], v[132:135]\n\t"
    "v_exp_f32 v176, v72\n\t"
    "v_mfma_f32_16x16x32_bf16 v[36:39], v[144:147], a[208:211], v[132:135]\n\t"
    "v_exp_f32 v177, v73\n\t"
    "v_mfma_f32_16x16x32_bf16 v[40:43], v[144:147], a[224:227], v[136:139]\n\t"
    "buffer_load_dwordx4 v195, s[40:43], s68 offen offset:2048 sc1 lds\n\t"
    "v_mfma_f32_16x16x32_bf16 v[56:59], v[148:151], a[224:227], v[136:139]\n\t"
    "v_exp_f32 v178, v74\n\t"
    "v_mfma_f32_16x16x32_bf16 v[60:63], v[148:151], a[240:243], v[140:143]\n\t"
    "v_add_f32 v174, v176, v177\n\t"
    "v_cvt_pk_bf16_f32 v120, v176, v177\n\t"
    "v_mfma_f32_16x16x32_bf16 v[44:47], v[144:147], a[240:243], v[140:143]\n\t"
    "s_waitcnt lgkmcnt(1)\n\t"
    "v_exp_f32 v179, v75\n\t"
    "v_mfma_f32_16x16x32_bf16 v[32:35], v[152:155], a[196:199], v[32:35]\n\t"
    "s_waitcnt lgkmcnt(0)\n\t"
    "ds_read_b128 v[144:147], v192 offset:24704\n\t"
    "v_add_f32 v174, v174, v178\n\t"
    "v_mfma_f32_16x16x32_bf16 v[48:51], v[156:159], a[196:199], v[48:51]\n\t"
    "ds_read_b128 v[148:151], v192 offset:26240\n\t"
    "v_add_f32 v174, v174, v179\n\t"
    "v_mfma_f32_16x16x32_bf16 v[52:55], v[156:159], a[212:215], v[52:55]\n\t"
    "v_cvt_pk_bf16_f32 v121, v178, v179\n\t"
    "v_fma_mix_f32 v221, v221, v204, v212 op_sel_hi:[1,0,0]\n\t"
    "v_mfma_f32_16x16x32_bf16 v[36:39], v[152:155], a[212:215], v[36:39]\n\t"
    "v_exp_f32 v176, v88\n\t"
    "v_mfma_f32_16x16x32_bf16 v[40:43], v[152:155], a[228:231], v[40:43]\n\t"
    "v_fma_mix_f32 v222, v222, v204, v212 op_sel_hi:[1,0,0]\n\t"
    "v_fma_mix_f32 v223, v223, v204, v212 op_sel_hi:[1,0,0]\n\t"
    "v_mfma_f32_16x16x32_bf16 v[56:59], v[156:159], a[228:231], v[56:59]\n\t"
    "v_exp_f32 v177, v89\n\t"
    "v_mfma_f32_16x16x32_bf16 v[60:63], v[156:159], a[244:247], v[60:63]\n\t"
    "v_add_f32 v174, v174, v176\n\t"
    "v_add_f32 v174, v174, v177\n\t"
    "v_mfma_f32_16x16x32_bf16 v[44:47], v[152:155], a[244:247], v[44:47]\n\t"
    "s_waitcnt lgkmcnt(1)\n\t"
    "v_exp_f32 v178, v90\n\t"
    "v_mfma_f32_16x16x32_bf16 v[32:35], v[144:147], a[200:203], v[32:35]\n\t"
    "s_waitcnt lgkmcnt(0)\n\t"
    "ds_read_b128 v[152:155], v193 offset:24704\n\t"
    "v_cvt_pk_bf16_f32 v122, v176, v177\n\t"
    "v_mfma_f32_16x16x32_bf16 v[48:51], v[148:151], a[200:203], v[48:51]\n\t"
    "ds_read_b128 v[156:159], v193 offset:26240\n\t"
    "v_fma_mix_f32 v224, v224, v205, v213 op_sel_hi:[1,0,0]\n\t"
    "v_mfma_f32_16x16x32_bf16 v[52:55], v[148:151], a[216:219], v[52:55]\n\t"
    "v_fma_mix_f32 v225, v225, v205, v213 op_sel_hi:[1,0,0]\n\t"
    "v_fma_mix_f32 v226, v226, v205, v213 op_sel_hi:[1,0,0]\n\t"
    "v_mfma_f32_16x16x32_bf16 v[36:39], v[144:147], a[216:219], v[36:39]\n\t"
    "s_add_u32 m0, s60, 86016\n\t"
    "v_exp_f32 v179, v91\n\t"
    "v_mfma_f32_16x16x32_bf16 v[40:43], v[144:147], a[232:235], v[40:43]\n\t"
    "buffer_load_dwordx4 v195, s[44:47], s67 offen offset:0 sc1 lds\n\t"
    "v_mfma_f32_16x16x32_bf16 v[56:59], v[148:151], a[232:235], v[56:59]\n\t"
    "v_exp_f32 v176, v76\n\t"
    "v_mfma_f32_16x16x32_bf16 v[60:63], v[148:151], a[248:251], v[60:63]\n\t"
    "v_add_f32 v174, v174, v178\n\t"
    "v_add_f32 v174, v174, v179\n\t"
    "v_mfma_f32_16x16x32_bf16 v[44:47], v[144:147], a[248:251], v[44:47]\n\t"
    "s_waitcnt lgkmcnt(1)\n\t"
    "v_exp_f32 v177, v77\n\t"
    "v_mfma_f32_16x16x32_bf16 v[32:35], v[152:155], a[204:207], v[32:35]\n\t"
    "s_waitcnt lgkmcnt(0)\n\t"
    "ds_read_b128 v[144:147], v192 offset:24832\n\t"
    "v_cvt_pk_bf16_f32 v123, v178, v179\n\t"
    "v_mfma_f32_16x16x32_bf16 v[48:51], v[156:159], a[204:207], v[48:51]\n\t"
    "ds_read_b128 v[148:151], v192 offset:26368\n\t"
    "v_add_f32 v175, v176, v177\n\t"
    "v_mfma_f32_16x16x32_bf16 v[52:55], v[156:159], a[220:223], v[52:55]\n\t"
    "v_cvt_pk_bf16_f32 v124, v176, v177\n\t"
    "v_fma_mix_f32 v227, v227, v205, v213 op_sel_hi:[1,0,0]\n\t"
    "v_mfma_f32_16x16x32_bf16 v[36:39], v[152:155], a[220:223], v[36:39]\n\t"
    "v_exp_f32 v178, v78\n\t"
    "v_mfma_f32_16x16x32_bf16 v[40:43], v[152:155], a[236:239], v[40:43]\n\t"
    "buffer_load_dwordx4 v195, s[44:47], s67 offen offset:1024 sc1 lds\n\t"
    "v_mfma_f32_16x16x32_bf16 v[56:59], v[156:159], a[236:239], v[56:59]\n\t"
    "v_exp_f32 v179, v79\n\t"
    "v_mfma_f32_16x16x32_bf16 v[60:63], v[156:159], a[252:255], v[60:63]\n\t"
    "v_add_f32 v175, v175, v178\n\t"
    "v_add_f32 v175, v175, v179\n\t"
    "v_mfma_f32_16x16x32_bf16 v[44:47], v[152:155], a[252:255], v[44:47]\n\t"
    "s_waitcnt lgkmcnt(1)\n\t"
    "v_exp_f32 v176, v92\n\t"
    "v_mfma_f32_16x16x32_bf16 v[32:35], v[144:147], v[0:3], v[32:35]\n\t"
    "s_waitcnt lgkmcnt(0)\n\t"
    "ds_read_b128 v[152:155], v193 offset:24832\n\t"
    "v_cvt_pk_bf16_f32 v125, v178, v179\n\t"
    "v_mfma_f32_16x16x32_bf16 v[48:51], v[148:151], v[0:3], v[48:51]\n\t"
    "ds_read_b128 v[156:159], v193 offset:26368\n\t"
    "v_cvt_pk_bf16_f32 v244, v220, v224\n\t"
    "v_mfma_f32_16x16x32_bf16 v[52:55], v[148:151], v[8:11], v[52:55]\n\t"
    "v_cvt_pk_bf16_f32 v248, v221, v225\n\t"
    "v_cvt_pk_bf16_f32 v252, v222, v226\n\t"
    "v_mfma_f32_16x16x32_bf16 v[36:39], v[144:147], v[8:11], v[36:39]\n\t"
    "v_exp_f32 v177, v93\n\t"
    "v_mfma_f32_16x16x32_bf16 v[40:43], v[144:147], v[16:19], v[40:43]\n\t"
    "buffer_load_dwordx4 v195, s[44:47], s67 offen offset:2048 sc1 lds\n\t"
    "v_mfma_f32_16x16x32_bf16 v[56:59], v[148:151], v[16:19], v[56:59]\n\t"
    "v_exp_f32 v178, v94\n\t"
    "v_mfma_f32_16x16x32_bf16 v[60:63], v[148:151], v[24:27], v[60:63]\n\t"
    "v_add_f32 v175, v175, v176\n\t"
    "v_add_f32 v175, v175, v177\n\t"
    "v_mfma_f32_16x16x32_bf16 v[44:47], v[144:147], v[24:27], v[44:47]\n\t"
    "s_waitcnt lgkmcnt(1)\n\t"
    "v_exp_f32 v179, v95\n\t"
    "v_mfma_f32_16x16x32_bf16 v[32:35], v[152:155], v[4:7], v[32:35]\n\t"
    "s_waitcnt lgkmcnt(0)\n\t"
    "ds_read_b128 v[144:147], v194 offset:12288\n\t"
    "v_cvt_pk_bf16_f32 v126, v176, v177\n\t"
    "v_mfma_f32_16x16x32_bf16 v[48:51], v[156:159], v[4:7], v[48:51]\n\t"
    "ds_read_b128 v[148:151], v194 offset:12544\n\t"
    "v_add_f32 v175, v175, v178\n\t"
    "v_mfma_f32_16x16x32_bf16 v[52:55], v[156:159], v[12:15], v[52:55]\n\t"
    "v_add_f32 v175, v175, v179\n\t"
    "v_cvt_pk_bf16_f32 v127, v178, v179\n\t"
    "v_mfma_f32_16x16x32_bf16 v[36:39], v[152:155], v[12:15], v[36:39]\n\t"
    "v_max3_f32 v180, v172, v173, v174\n\t"
    "v_max_f32 v180, v180, v175\n\t"
    "v_mfma_f32_16x16x32_bf16 v[40:43], v[152:155], v[20:23], v[40:43]\n\t"
    "v_cmp_lt_f32 vcc, s62, v180\n\t"
    "v_cvt_pk_bf16_f32 v188, v223, v227\n\t"
    "v_mfma_f32_16x16x32_bf16 v[56:59], v[156:159], v[20:23], v[56:59]\n\t"
    "s_waitcnt vmcnt(19)\n\t"
    "global_load_dwordx4 v[220:223], v196, s[78:79] offset:-2048\n\t"
    "v_mfma_f32_16x16x32_bf16 v[60:63], v[156:159], v[28:31], v[60:63]\n\t"
    "global_load_dwordx4 v[224:227], v196, s[78:79] offset:-2032\n\t"
    "v_mfma_f32_16x16x32_bf16 v[44:47], v[152:155], v[28:31], v[44:47]\n\t"
    "s_waitcnt lgkmcnt(1)\n\t"
    "s_cbranch_vccnz L_stub_l4_1\n\t"
    "L_ret_l4_1:\n\t"
    "v_mfma_f32_16x16x32_bf16 a[0:3], v[112:115], v[144:147], a[0:3]\n\t"
    "ds_read_b128 v[152:155], v194 offset:12800\n\t"
    "v_add_f32 v168, v168, v172\n\t"
    "v_mfma_f32_16x16x32_bf16 a[4:7], v[116:119], v[144:147], a[4:7]\n\t"
    "v_exp_f32 v176, v32\n\t"
    "v_mfma_f32_16x16x32_bf16 a[8:11], v[120:123], v[144:147], a[8:11]\n\t"
    "v_add_f32 v169, v169, v173\n\t"
    "v_add_f32 v170, v170, v174\n\t"
    "v_mfma_f32_16x16x32_bf16 a[12:15], v[124:127], v[144:147], a[12:15]\n\t"
    "s_waitcnt lgkmcnt(1)\n\t"
    "v_exp_f32 v177, v33\n\t"
    "v_mfma_f32_16x16x32_bf16 a[28:31], v[124:127], v[148:151], a[28:31]\n\t"
    "ds_read_b128 v[156:159], v194 offset:13056\n\t"
    "v_add_f32 v171, v171, v175\n\t"
    "v_mfma_f32_16x16x32_bf16 a[24:27], v[120:123], v[148:151], a[24:27]\n\t"
    "v_add_f32 v172, v176, v177\n\t"
    "v_cvt_pk_bf16_f32 v96, v176, v177\n\t"
    "v_mfma_f32_16x16x32_bf16 a[20:23], v[116:119], v[148:151], a[20:23]\n\t"
    "v_fma_mix_f32 v228, v228, v206, v214 op_sel_hi:[1,0,0]\n\t"
    "v_fma_mix_f32 v229, v229, v206, v214 op_sel_hi:[1,0,0]\n\t"
    "v_mfma_f32_16x16x32_bf16 a[16:19], v[112:115], v[148:151], a[16:19]\n\t"
    "s_waitcnt lgkmcnt(1)\n\t"
    "v_exp_f32 v178, v34\n\t"
    "v_mfma_f32_16x16x32_bf16 a[32:35], v[112:115], v[152:155], a[32:35]\n\t"
    "ds_read_b128 v[144:147], v194 offset:13312\n\t"
    "v_fma_mix_f32 v230, v230, v206, v214 op_sel_hi:[1,0,0]\n\t"
    "v_mfma_f32_16x16x32_bf16 a[36:39], v[116:119], v[152:155], a[36:39]\n\t"
    "v_exp_f32 v179, v35\n\t"
    "v_mfma_f32_16x16x32_bf16 a[40:43], v[120:123], v[152:155], a[40:43]\n\t"
    "v_add_f32 v172, v172, v178\n\t"
    "v_add_f32 v172, v172, v179\n\t"
    "v_mfma_f32_16x16x32_bf16 a[44:47], v[124:127], v[152:155], a[44:47]\n\t"
    "s_waitcnt lgkmcnt(1)\n\t"
    "v_cvt_pk_bf16_f32 v97, v178, v179\n\t"
    "v_fma_mix_f32 v231, v231, v206, v214 op_sel_hi:[1,0,0]\n\t"
    "v_mfma_f32_16x16x32_bf16 a[60:63], v[124:127], v[156:159], a[60:63]\n\t"
    "ds_read_b128 v[148:151], v194 offset:13568\n\t"
    "v_fma_mix_f32 v232, v232, v207, v215 op_sel_hi:[1,0,0]\n\t"
    "v_mfma_f32_16x16x32_bf16 a[56:59], v[120:123], v[156:159], a[56:59]\n\t"
    "v_exp_f32 v176, v48\n\t"
    "v_mfma_f32_16x16x32_bf16 a[52:55], v[116:119], v[156:159], a[52:55]\n\t"
    "v_fma_mix_f32 v233, v233, v207, v215 op_sel_hi:[1,0,0]\n\t"
    "v_fma_mix_f32 v234, v234, v207, v215 op_sel_hi:[1,0,0]\n\t"
    "v_mfma_f32_16x16x32_bf16 a[48:51], v[112:115], v[156:159], a[48:51]\n\t"
    "s_waitcnt lgkmcnt(1)\n\t"
    "v_exp_f32 v177, v49\n\t"
    "v_mfma_f32_16x16x32_bf16 a[64:67], v[112:115], v[144:147], a[64:67]\n\t"
    "ds_read_b128 v[152:155], v194 offset:13824\n\t"
    "v_add_f32 v172, v172, v176\n\t"
    "v_mfma_f32_16x16x32_bf16 a[68:71], v[116:119], v[144:147], a[68:71]\n\t"
    "v_add_f32 v172, v172, v177\n\t"
    "v_cvt_pk_bf16_f32 v98, v176, v177\n\t"
    "v_mfma_f32_16x16x32_bf16 a[72:75], v[120:123], v[144:147], a[72:75]\n\t"
    "v_fma_mix_f32 v235, v235, v207, v215 op_sel_hi:[1,0,0]\n\t"
    "v_cvt_pk_bf16_f32 v245, v228, v232\n\t"
    "v_mfma_f32_16x16x32_bf16 a[76:79], v[124:127], v[144:147], a[76:79]\n\t"
    "s_waitcnt lgkmcnt(1)\n\t"
    "v_exp_f32 v178, v50\n\t"
    "v_mfma_f32_16x16x32_bf16 a[92:95], v[124:127], v[148:151], a[92:95]\n\t"
    "ds_read_b128 v[156:159], v194 offset:14080\n\t"
    "v_cvt_pk_bf16_f32 v249, v229, v233\n\t"
    "v_mfma_f32_16x16x32_bf16 a[88:91], v[120:123], v[148:151], a[88:91]\n\t"
    "v_exp_f32 v179, v51\n\t"
    "v_mfma_f32_16x16x32_bf16 a[84:87], v[116:119], v[148:151], a[84:87]\n\t"
    "v_add_f32 v172, v172, v178\n\t"
    "v_add_f32 v172, v172, v179\n\t"
    "v_mfma_f32_16x16x32_bf16 a[80:83], v[112:115], v[148:151], a[80:83]\n\t"
    "s_waitcnt lgkmcnt(1)\n\t"
    "v_cvt_pk_bf16_f32 v99, v178, v179\n\t"
    "v_cvt_pk_bf16_f32 v253, v230, v234\n\t"
    "v_mfma_f32_16x16x32_bf16 a[96:99], v[112:115], v[152:155], a[96:99]\n\t"
    "ds_read_b128 v[144:147], v194 offset:14336\n\t"
    "v_cvt_pk_bf16_f32 v189, v231, v235\n\t"
    "v_mfma_f32_16x16x32_bf16 a[100:103], v[116:119], v[152:155], a[100:103]\n\t"
    "s_waitcnt vmcnt(17)\n\t"
    "v_exp_f32 v176, v36\n\t"
    "v_mfma_f32_16x16x32_bf16 a[104:107], v[120:123], v[152:155], a[104:107]\n\t"
    "global_load_dwordx4 v[228:231], v196, s[78:79] offset:-512\n\t"
    "v_mfma_f32_16x16x32_bf16 a[108:111], v[124:127], v[152:155], a[108:111]\n\t"
    "s_waitcnt lgkmcnt(1)\n\t"
    "v_exp_f32 v177, v37\n\t"
    "v_mfma_f32_16x16x32_bf16 a[124:127], v[124:127], v[156:159], a[124:127]\n\t"
    "ds_read_b128 v[148:151], v194 offset:14592\n\t"
    "v_add_f32 v173, v176, v177\n\t"
    "v_mfma_f32_16x16x32_bf16 a[120:123], v[120:123], v[156:159], a[120:123]\n\t"
    "v_cvt_pk_bf16_f32 v100, v176, v177\n\t"
    "v_fma_mix_f32 v236, v236, v208, v216 op_sel_hi:[1,0,0]\n\t"
    "v_mfma_f32_16x16x32_bf16 a[116:119], v[116:119], v[156:159], a[116:119]\n\t"
    "global_load_dwordx4 v[232:235], v196, s[78:79] offset:-496\n\t"
    "v_mfma_f32_16x16x32_bf16 a[112:115], v[112:115], v[156:159], a[112:115]\n\t"
    "s_waitcnt lgkmcnt(1)\n\t"
    "v_exp_f32 v178, v38\n\t"
    "v_mfma_f32_16x16x32_bf16 a[128:131], v[112:115], v[144:147], a[128:131]\n\t"
    "ds_read_b128 v[152:155], v194 offset:14848\n\t"
    "v_fma_mix_f32 v237, v237, v208, v216 op_sel_hi:[1,0,0]\n\t"
    "v_mfma_f32_16x16x32_bf16 a[132:135], v[116:119], v[144:147], a[132:135]\n\t"
    "s_waitcnt vmcnt(10)\n\t"
    "v_exp_f32 v179, v39\n\t"
    "v_mfma_f32_16x16x32_bf16 a[136:139], v[120:123], v[144:147], a[136:139]\n\t"
    "s_barrier\n\t"
    "v_add_f32 v173, v173, v178\n\t"
    "v_add_f32 v173, v173, v179\n\t"
    "v_mfma_f32_16x16x32_bf16 a[140:143], v[124:127], v[144:147], a[140:143]\n\t"
    "s_waitcnt lgkmcnt(1)\n\t"
    "v_cvt_pk_bf16_f32 v101, v178, v179\n\t"
    "v_fma_mix_f32 v238, v238, v208, v216 op_sel_hi:[1,0,0]\n\t"
    "v_mfma_f32_16x16x32_bf16 a[156:159], v[124:127], v[148:151], a[156:159]\n\t"
    "ds_read_b128 v[156:159], v194 offset:15104\n\t"
    "v_fma_mix_f32 v239, v239, v208, v216 op_sel_hi:[1,0,0]\n\t"
    "v_mfma_f32_16x16x32_bf16 a[152:155], v[120:123], v[148:151], a[152:155]\n\t"
    "s_add_u32 m0, s60, 36864\n\t"
    "v_exp_f32 v176, v52\n\t"
    "v_mfma_f32_16x16x32_bf16 a[148:151], v[116:119], v[148:151], a[148:151]\n\t"
    "buffer_load_dwordx4 v195, s[40:43], s69 offen offset:0 sc1 lds\n\t"
    "v_mfma_f32_16x16x32_bf16 a[144:147], v[112:115], v[148:151], a[144:147]\n\t"
    "s_waitcnt lgkmcnt(1)\n\t"
    "v_exp_f32 v177, v53\n\t"
    "v_mfma_f32_16x16x32_bf16 a[160:163], v[112:115], v[152:155], a[160:163]\n\t"
    "ds_read_b128 v[144:147], v192 offset:36864\n\t"
    "v_add_f32 v173, v173, v176\n\t"
    "v_mfma_f32_16x16x32_bf16 a[164:167], v[116:119], v[152:155], a[164:167]\n\t"
    "ds_read_b128 v[148:151], v192 offset:38400\n\t"
    "v_add_f32 v173, v173, v177\n\t"
    "v_mfma_f32_16x16x32_bf16 a[168:171], v[120:123], v[152:155], a[168:171]\n\t"
    "buffer_load_dwordx4 v195, s[40:43], s69 offen offset:1024 sc1 lds\n\t"
    "v_mfma_f32_16x16x32_bf16 a[172:175], v[124:127], v[152:155], a[172:175]\n\t"
    "s_waitcnt lgkmcnt(2)\n\t"
    "v_exp_f32 v178, v54\n\t"
    "v_mfma_f32_16x16x32_bf16 a[188:191], v[124:127], v[156:159], a[188:191]\n\t"
    "v_cvt_pk_bf16_f32 v102, v176, v177\n\t"
    "v_fma_mix_f32 v240, v240, v209, v217 op_sel_hi:[1,0,0]\n\t"
    "v_mfma_f32_16x16x32_bf16 a[184:187], v[120:123], v[156:159], a[184:187]\n\t"
    "v_exp_f32 v179, v55\n\t"
    "v_mfma_f32_16x16x32_bf16 a[180:183], v[116:119], v[156:159], a[180:183]\n\t"
    "v_add_f32 v173, v173, v178\n\t"
    "v_add_f32 v173, v173, v179\n\t"
    "v_mfma_f32_16x16x32_bf16 a[176:179], v[112:115], v[156:159], a[176:179]\n\t"
    "s_waitcnt lgkmcnt(1)\n\t"
    "v_cvt_pk_bf16_f32 v103, v178, v179\n\t"
    "v_fma_mix_f32 v241, v241, v209, v217 op_sel_hi:[1,0,0]\n\t"
    "v_mfma_f32_16x16x32_bf16 v[64:67], v[144:147], a[192:195], v[128:131]\n\t"
    "s_waitcnt lgkmcnt(0)\n\t"
    "ds_read_b128 v[152:155], v193 offset:36864\n\t"
    "v_fma_mix_f32 v242, v242, v209, v217 op_sel_hi:[1,0,0]\n\t"
    "v_mfma_f32_16x16x32_bf16 v[80:83], v[148:151], a[192:195], v[128:131]\n\t"
    "ds_read_b128 v[156:159], v193 offset:38400\n\t"
    "v_fma_mix_f32 v243, v243, v209, v217 op_sel_hi:[1,0,0]\n\t"
    "v_mfma_f32_16x16x32_bf16 v[84:87], v[148:151], a[208:211], v[132:135]\n\t"
    "v_exp_f32 v176, v40\n\t"
    "v_mfma_f32_16x16x32_bf16 v[68:71], v[144:147], a[208:211], v[132:135]\n\t"
    "v_exp_f32 v177, v41\n\t"
    "v_mfma_f32_16x16x32_bf16 v[72:75], v[144:147], a[224:227], v[136:139]\n\t"
    "buffer_load_dwordx4 v195, s[40:43], s69 offen offset:2048 sc1 lds\n\t"
    "v_mfma_f32_16x16x32_bf16 v[88:91], v[148:151], a[224:227], v[136:139]\n\t"
    "v_exp_f32 v178, v42\n\t"
    "v_mfma_f32_16x16x32_bf16 v[92:95], v[148:151], a[240:243], v[140:143]\n\t"
    "v_add_f32 v174, v176, v177\n\t"
    "v_cvt_pk_bf16_f32 v104, v176, v177\n\t"
    "v_mfma_f32_16x16x32_bf16 v[76:79], v[144:147], a[240:243], v[140:143]\n\t"
    "s_waitcnt lgkmcnt(1)\n\t"
    "v_exp_f32 v179, v43\n\t"
    "v_mfma_f32_16x16x32_bf16 v[64:67], v[152:155], a[196:199], v[64:67]\n\t"
    "s_waitcnt lgkmcnt(0)\n\t"
    "ds_read_b128 v[144:147], v192 offset:36992\n\t"
    "v_add_f32 v174, v174, v178\n\t"
    "v_mfma_f32_16x16x32_bf16 v[80:83], v[156:159], a[196:199], v[80:83]\n\t"
    "ds_read_b128 v[148:151], v192 offset:38528\n\t"
    "v_add_f32 v174, v174, v179\n\t"
    "v_mfma_f32_16x16x32_bf16 v[84:87], v[156:159], a[212:215], v[84:87]\n\t"
    "v_cvt_pk_bf16_f32 v105, v178, v179\n\t"
    "v_cvt_pk_bf16_f32 v246, v236, v240\n\t"
    "v_mfma_f32_16x16x32_bf16 v[68:71], v[152:155], a[212:215], v[68:71]\n\t"
    "v_exp_f32 v176, v56\n\t"
    "v_mfma_f32_16x16x32_bf16 v[72:75], v[152:155], a[228:231], v[72:75]\n\t"
    "v_cvt_pk_bf16_f32 v250, v237, v241\n\t"
    "v_cvt_pk_bf16_f32 v254, v238, v242\n\t"
    "v_mfma_f32_16x16x32_bf16 v[88:91], v[156:159], a[228:231], v[88:91]\n\t"
    "v_exp_f32 v177, v57\n\t"
    "v_mfma_f32_16x16x32_bf16 v[92:95], v[156:159], a[244:247], v[92:95]\n\t"
    "v_add_f32 v174, v174, v176\n\t"
    "v_add_f32 v174, v174, v177\n\t"
    "v_mfma_f32_16x16x32_bf16 v[76:79], v[152:155], a[244:247], v[76:79]\n\t"
    "s_waitcnt lgkmcnt(1)\n\t"
    "v_exp_f32 v178, v58\n\t"
    "v_mfma_f32_16x16x32_bf16 v[64:67], v[144:147], a[200:203], v[64:67]\n\t"
    "s_waitcnt lgkmcnt(0)\n\t"
    "ds_read_b128 v[152:155], v193 offset:36992\n\t"
    "v_cvt_pk_bf16_f32 v106, v176, v177\n\t"
    "v_mfma_f32_16x16x32_bf16 v[80:83], v[148:151], a[200:203], v[80:83]\n\t"
    "ds_read_b128 v[156:159], v193 offset:38528\n\t"
    "v_cvt_pk_bf16_f32 v190, v239, v243\n\t"
    "v_mfma_f32_16x16x32_bf16 v[84:87], v[148:151], a[216:219], v[84:87]\n\t"
    "s_waitcnt vmcnt(14)\n\t"
    "global_load_dwordx4 v[236:239], v196, s[78:79] offset:1024\n\t"
    "v_mfma_f32_16x16x32_bf16 v[68:71], v[144:147], a[216:219], v[68:71]\n\t"
    "s_add_u32 m0, s60, 0\n\t"
    "v_exp_f32 v179, v59\n\t"
    "v_mfma_f32_16x16x32_bf16 v[72:75], v[144:147], a[232:235], v[72:75]\n\t"
    "buffer_load_dwordx4 v195, s[44:47], s68 offen offset:0 sc1 lds\n\t"
    "v_mfma_f32_16x16x32_bf16 v[88:91], v[148:151], a[232:235], v[88:91]\n\t"
    "v_exp_f32 v176, v44\n\t"
    "v_mfma_f32_16x16x32_bf16 v[92:95], v[148:151], a[248:251], v[92:95]\n\t"
    "v_add_f32 v174, v174, v178\n\t"
    "v_add_f32 v174, v174, v179\n\t"
    "v_mfma_f32_16x16x32_bf16 v[76:79], v[144:147], a[248:251], v[76:79]\n\t"
    "s_waitcnt lgkmcnt(1)\n\t"
    "v_exp_f32 v177, v45\n\t"
    "v_mfma_f32_16x16x32_bf16 v[64:67], v[152:155], a[204:207], v[64:67]\n\t"
    "s_waitcnt lgkmcnt(0)\n\t"
    "ds_read_b128 v[144:147], v192 offset:37120\n\t"
    "v_cvt_pk_bf16_f32 v107, v178, v179\n\t"
    "v_mfma_f32_16x16x32_bf16 v[80:83], v[156:159], a[204:207], v[80:83]\n\t"
    "ds_read_b128 v[148:151], v192 offset:38656\n\t"
    "v_add_f32 v175, v176, v177\n\t"
    "v_mfma_f32_16x16x32_bf16 v[84:87], v[156:159], a[220:223], v[84:87]\n\t"
    "v_cvt_pk_bf16_f32 v108, v176, v177\n\t"
    "v_fma_mix_f32 v160, v160, v210, v218 op_sel_hi:[1,0,0]\n\t"
    "v_mfma_f32_16x16x32_bf16 v[68:71], v[152:155], a[220:223], v[68:71]\n\t"
    "v_exp_f32 v178, v46\n\t"
    "v_mfma_f32_16x16x32_bf16 v[72:75], v[152:155], a[236:239], v[72:75]\n\t"
    "buffer_load_dwordx4 v195, s[44:47], s68 offen offset:1024 sc1 lds\n\t"
    "v_mfma_f32_16x16x32_bf16 v[88:91], v[156:159], a[236:239], v[88:91]\n\t"
    "v_exp_f32 v179, v47\n\t"
    "v_mfma_f32_16x16x32_bf16 v[92:95], v[156:159], a[252:255], v[92:95]\n\t"
    "v_add_f32 v175, v175, v178\n\t"
    "v_add_f32 v175, v175, v179\n\t"
    "v_mfma_f32_16x16x32_bf16 v[76:79], v[152:155], a[252:255], v[76:79]\n\t"
    "s_waitcnt lgkmcnt(1)\n\t"
    "v_exp_f32 v176, v60\n\t"
    "v_mfma_f32_16x16x32_bf16 v[64:67], v[144:147], v[0:3], v[64:67]\n\t"
    "s_waitcnt lgkmcnt(0)\n\t"
    "ds_read_b128 v[152:155], v193 offset:37120\n\t"
    "v_cvt_pk_bf16_f32 v109, v178, v179\n\t"
    "v_mfma_f32_16x16x32_bf16 v[80:83], v[148:151], v[0:3], v[80:83]\n\t"
    "ds_read_b128 v[156:159], v193 offset:38656\n\t"
    "v_fma_mix_f32 v161, v161, v210, v218 op_sel_hi:[1,0,0]\n\t"
    "v_mfma_f32_16x16x32_bf16 v[84:87], v[148:151], v[8:11], v[84:87]\n\t"
    "global_load_dwordx4 v[240:243], v196, s[78:79] offset:1040\n\t"
    "v_mfma_f32_16x16x32_bf16 v[68:71], v[144:147], v[8:11], v[68:71]\n\t"
    "v_exp_f32 v177, v61\n\t"
    "v_mfma_f32_16x16x32_bf16 v[72:75], v[144:147], v[16:19], v[72:75]\n\t"
    "buffer_load_dwordx4 v195, s[44:47], s68 offen offset:2048 sc1 lds\n\t"
    "v_mfma_f32_16x16x32_bf16 v[88:91], v[148:151], v[16:19], v[88:91]\n\t"
    "v_exp_f32 v178, v62\n\t"
    "v_mfma_f32_16x16x32_bf16 v[92:95], v[148:151], v[24:27], v[92:95]\n\t"
    "v_add_f32 v175, v175, v176\n\t"
    "v_add_f32 v175, v175, v177\n\t"
    "v_mfma_f32_16x16x32_bf16 v[76:79], v[144:147], v[24:27], v[76:79]\n\t"
    "s_waitcnt lgkmcnt(1)\n\t"
    "v_exp_f32 v179, v63\n\t"
    "v_mfma_f32_16x16x32_bf16 v[64:67], v[152:155], v[4:7], v[64:67]\n\t"
    "s_waitcnt lgkmcnt(0)\n\t"
    "ds_read_b128 v[144:147], v181 offset:0\n\t"
    "v_cvt_pk_bf16_f32 v110, v176, v177\n\t"
    "v_mfma_f32_16x16x32_bf16 v[80:83], v[156:159], v[4:7], v[80:83]\n\t"
    "ds_read_b128 v[148:151], v181 offset:256\n\t"
    "v_add_f32 v175, v175, v178\n\t"
    "v_mfma_f32_16x16x32_bf16 v[84:87], v[156:159], v[12:15], v[84:87]\n\t"
    "v_add_f32 v175, v175, v179\n\t"
    "v_cvt_pk_bf16_f32 v111, v178, v179\n\t"
    "v_mfma_f32_16x16x32_bf16 v[68:71], v[152:155], v[12:15], v[68:71]\n\t"
    "v_max3_f32 v180, v172, v173, v174\n\t"
    "v_max_f32 v180, v180, v175\n\t"
    "v_mfma_f32_16x16x32_bf16 v[72:75], v[152:155], v[20:23], v[72:75]\n\t"
    "v_cmp_lt_f32 vcc, s62, v180\n\t"
    "v_fma_mix_f32 v162, v162, v210, v218 op_sel_hi:[1,0,0]\n\t"
    "v_mfma_f32_16x16x32_bf16 v[88:91], v[156:159], v[20:23], v[88:91]\n\t"
    "v_fma_mix_f32 v163, v163, v210, v218 op_sel_hi:[1,0,0]\n\t"
    "v_fma_mix_f32 v164, v164, v211, v219 op_sel_hi:[1,0,0]\n\t"
    "v_mfma_f32_16x16x32_bf16 v[92:95], v[156:159], v[28:31], v[92:95]\n\t"
    "v_fma_mix_f32 v165, v165, v211, v219 op_sel_hi:[1,0,0]\n\t"
    "v_fma_mix_f32 v166, v166, v211, v219 op_sel_hi:[1,0,0]\n\t"
    "v_mfma_f32_16x16x32_bf16 v[76:79], v[152:155], v[28:31], v[76:79]\n\t"
    "s_waitcnt lgkmcnt(1)\n\t"
    "s_cbranch_vccnz L_stub_l5_0\n\t"
    "L_ret_l5_0:\n\t"
    "v_mfma_f32_16x16x32_bf16 a[0:3], v[96:99], v[144:147], a[0:3]\n\t"
    "ds_read_b128 v[152:155], v181 offset:512\n\t"
    "v_add_f32 v168, v168, v172\n\t"
    "v_mfma_f32_16x16x32_bf16 a[4:7], v[100:103], v[144:147], a[4:7]\n\t"
    "v_exp_f32 v176, v64\n\t"
    "v_mfma_f32_16x16x32_bf16 a[8:11], v[104:107], v[144:147], a[8:11]\n\t"
    "v_add_f32 v169, v169, v173\n\t"
    "v_add_f32 v170, v170, v174\n\t"
    "v_mfma_f32_16x16x32_bf16 a[12:15], v[108:111], v[144:147], a[12:15]\n\t"
    "s_waitcnt lgkmcnt(1)\n\t"
    "v_exp_f32 v177, v65\n\t"
    "v_mfma_f32_16x16x32_bf16 a[28:31], v[108:111], v[148:151], a[28:31]\n\t"
    "ds_read_b128 v[156:159], v181 offset:768\n\t"
    "v_add_f32 v171, v171, v175\n\t"
    "v_mfma_f32_16x16x32_bf16 a[24:27], v[104:107], v[148:151], a[24:27]\n\t"
    "v_add_f32 v172, v176, v177\n\t"
    "v_cvt_pk_bf16_f32 v112, v176, v177\n\t"
    "v_mfma_f32_16x16x32_bf16 a[20:23], v[100:103], v[148:151], a[20:23]\n\t"
    "v_fma_mix_f32 v167, v167, v211, v219 op_sel_hi:[1,0,0]\n\t"
    "v_cvt_pk_bf16_f32 v247, v160, v164\n\t"
    "v_mfma_f32_16x16x32_bf16 a[16:19], v[96:99], v[148:151], a[16:19]\n\t"
    "s_waitcnt lgkmcnt(1)\n\t"
    "v_exp_f32 v178, v66\n\t"
    "v_mfma_f32_16x16x32_bf16 a[32:35], v[96:99], v[152:155], a[32:35]\n\t"
    "ds_read_b128 v[144:147], v181 offset:1024\n\t"
    "v_cvt_pk_bf16_f32 v251, v161, v165\n\t"
    "v_mfma_f32_16x16x32_bf16 a[36:39], v[100:103], v[152:155], a[36:39]\n\t"
    "v_exp_f32 v179, v67\n\t"
    "v_mfma_f32_16x16x32_bf16 a[40:43], v[104:107], v[152:155], a[40:43]\n\t"
    "v_add_f32 v172, v172, v178\n\t"
    "v_add_f32 v172, v172, v179\n\t"
    "v_mfma_f32_16x16x32_bf16 a[44:47], v[108:111], v[152:155], a[44:47]\n\t"
    "s_waitcnt lgkmcnt(1)\n\t"
    "v_cvt_pk_bf16_f32 v113, v178, v179\n\t"
    "v_cvt_pk_bf16_f32 v255, v162, v166\n\t"
    "v_mfma_f32_16x16x32_bf16 a[60:63], v[108:111], v[156:159], a[60:63]\n\t"
    "ds_read_b128 v[148:151], v181 offset:1280\n\t"
    "v_cvt_pk_bf16_f32 v191, v163, v167\n\t"
    "v_mfma_f32_16x16x32_bf16 a[56:59], v[104:107], v[156:159], a[56:59]\n\t"
    "v_exp_f32 v176, v80\n\t"
    "v_mfma_f32_16x16x32_bf16 a[52:55], v[100:103], v[156:159], a[52:55]\n\t"
    "buffer_store_dwordx4 v[244:247], v201, s[48:51], s92 offen offset:0 sc1\n\t"
    "v_mfma_f32_16x16x32_bf16 a[48:51], v[96:99], v[156:159], a[48:51]\n\t"
    "s_waitcnt lgkmcnt(1)\n\t"
    "v_exp_f32 v177, v81\n\t"
    "v_mfma_f32_16x16x32_bf16 a[64:67], v[96:99], v[144:147], a[64:67]\n\t"
    "ds_read_b128 v[152:155], v181 offset:1536\n\t"
    "v_add_f32 v172, v172, v176\n\t"
    "v_mfma_f32_16x16x32_bf16 a[68:71], v[100:103], v[144:147], a[68:71]\n\t"
    "v_add_f32 v172, v172, v177\n\t"
    "v_cvt_pk_bf16_f32 v114, v176, v177\n\t"
    "v_mfma_f32_16x16x32_bf16 a[72:75], v[104:107], v[144:147], a[72:75]\n\t"
    "buffer_store_dwordx4 v[248:251], v201, s[48:51], s92 offen offset:256 sc1\n\t"
    "v_mfma_f32_16x16x32_bf16 a[76:79], v[108:111], v[144:147], a[76:79]\n\t"
    "s_waitcnt lgkmcnt(1)\n\t"
    "v_exp_f32 v178, v82\n\t"
    "v_mfma_f32_16x16x32_bf16 a[92:95], v[108:111], v[148:151], a[92:95]\n\t"
    "ds_read_b128 v[156:159], v181 offset:1792\n\t"
    "v_lshl_add_u32 v185, v182, 4, s54\n\t"
    "v_mfma_f32_16x16x32_bf16 a[88:91], v[104:107], v[148:151], a[88:91]\n\t"
    "v_exp_f32 v179, v83\n\t"
    "v_mfma_f32_16x16x32_bf16 a[84:87], v[100:103], v[148:151], a[84:87]\n\t"
    "v_add_f32 v172, v172, v178\n\t"
    "v_add_f32 v172, v172, v179\n\t"
    "v_mfma_f32_16x16x32_bf16 a[80:83], v[96:99], v[148:151], a[80:83]\n\t"
    "s_waitcnt lgkmcnt(1)\n\t"
    "v_cvt_pk_bf16_f32 v115, v178, v179\n\t"
    "v_mfma_f32_16x16x32_bf16 a[96:99], v[96:99], v[152:155], a[96:99]\n\t"
    "ds_read_b128 v[144:147], v181 offset:2048\n\t"
    "v_mfma_f32_16x16x32_bf16 a[100:103], v[100:103], v[152:155], a[100:103]\n\t"
    "v_exp_f32 v176, v68\n\t"
    "v_mfma_f32_16x16x32_bf16 a[104:107], v[104:107], v[152:155], a[104:107]\n\t"
    "buffer_store_dwordx4 v[252:255], v201, s[48:51], s92 offen offset:512 sc1\n\t"
    "v_mfma_f32_16x16x32_bf16 a[108:111], v[108:111], v[152:155], a[108:111]\n\t"
    "s_waitcnt lgkmcnt(1)\n\t"
    "v_exp_f32 v177, v69\n\t"
    "v_mfma_f32_16x16x32_bf16 a[124:127], v[108:111], v[156:159], a[124:127]\n\t"
    "ds_read_b128 v[148:151], v181 offset:2304\n\t"
    "v_add_f32 v173, v176, v177\n\t"
    "v_mfma_f32_16x16x32_bf16 a[120:123], v[104:107], v[156:159], a[120:123]\n\t"
    "v_cvt_pk_bf16_f32 v116, v176, v177\n\t"
    "v_mfma_f32_16x16x32_bf16 a[116:119], v[100:103], v[156:159], a[116:119]\n\t"
    "buffer_store_dwordx4 v[188:191], v201, s[48:51], s92 offen offset:768 sc1\n\t"
    "v_mfma_f32_16x16x32_bf16 a[112:115], v[96:99], v[156:159], a[112:115]\n\t"
    "s_waitcnt lgkmcnt(1)\n\t"
    "v_exp_f32 v178, v70\n\t"
    "v_mfma_f32_16x16x32_bf16 a[128:131], v[96:99], v[144:147], a[128:131]\n\t"
    "s_add_u32 s92, s92, 196608\n\t"
    "ds_read_b128 v[152:155], v181 offset:2560\n\t"
    "v_mfma_f32_16x16x32_bf16 a[132:135], v[100:103], v[144:147], a[132:135]\n\t"
    "s_waitcnt vmcnt(16)\n\t"
    "v_exp_f32 v179, v71\n\t"
    "v_mfma_f32_16x16x32_bf16 a[136:139], v[104:107], v[144:147], a[136:139]\n\t"
    "s_barrier\n\t"
    "v_add_f32 v173, v173, v178\n\t"
    "v_add_f32 v173, v173, v179\n\t"
    "v_mfma_f32_16x16x32_bf16 a[140:143], v[108:111], v[144:147], a[140:143]\n\t"
    "s_waitcnt lgkmcnt(1)\n\t"
    "v_cvt_pk_bf16_f32 v117, v178, v179\n\t"
    "v_mfma_f32_16x16x32_bf16 a[156:159], v[108:111], v[148:151], a[156:159]\n\t"
    "ds_read_b128 v[156:159], v181 offset:2816\n\t"
    "v_mfma_f32_16x16x32_bf16 a[152:155], v[104:107], v[148:151], a[152:155]\n\t"
    "s_add_u32 m0, s60, 49152\n\t"
    "v_exp_f32 v176, v84\n\t"
    "v_mfma_f32_16x16x32_bf16 a[148:151], v[100:103], v[148:151], a[148:151]\n\t"
    "buffer_load_dwordx4 v195, s[40:43], s70 offen offset:0 sc1 lds\n\t"
    "v_mfma_f32_16x16x32_bf16 a[144:147], v[96:99], v[148:151], a[144:147]\n\t"
    "s_waitcnt lgkmcnt(1)\n\t"
    "v_exp_f32 v177, v85\n\t"
    "v_mfma_f32_16x16x32_bf16 a[160:163], v[96:99], v[152:155], a[160:163]\n\t"
    "ds_read_b128 v[144:147], v192 offset:0\n\t"
    "v_add_f32 v173, v173, v176\n\t"
    "v_mfma_f32_16x16x32_bf16 a[164:167], v[100:103], v[152:155], a[164:167]\n\t"
    "ds_read_b128 v[148:151], v192 offset:1536\n\t"
    "v_add_f32 v173, v173, v177\n\t"
    "v_mfma_f32_16x16x32_bf16 a[168:171], v[104:107], v[152:155], a[168:171]\n\t"
    "buffer_load_dwordx4 v195, s[40:43], s70 offen offset:1024 sc1 lds\n\t"
    "v_mfma_f32_16x16x32_bf16 a[172:175], v[108:111], v[152:155], a[172:175]\n\t"
    "s_waitcnt lgkmcnt(2)\n\t"
    "v_exp_f32 v178, v86\n\t"
    "v_mfma_f32_16x16x32_bf16 a[188:191], v[108:111], v[156:159], a[188:191]\n\t"
    "s_cmp_lt_u32 s61, 4\n\t"
    "v_cvt_pk_bf16_f32 v118, v176, v177\n\t"
    "v_mfma_f32_16x16x32_bf16 a[184:187], v[104:107], v[156:159], a[184:187]\n\t"
    "s_cselect_b32 s37, 196608, 0\n\t"
    "v_exp_f32 v179, v87\n\t"
    "v_mfma_f32_16x16x32_bf16 a[180:183], v[100:103], v[156:159], a[180:183]\n\t"
    "s_cselect_b32 s38, 3072, 0\n\t"
    "v_add_f32 v173, v173, v178\n\t"
    "v_add_f32 v173, v173, v179\n\t"
    "v_mfma_f32_16x16x32_bf16 a[176:179], v[96:99], v[156:159], a[176:179]\n\t"
    "s_waitcnt lgkmcnt(1)\n\t"
    "v_cvt_pk_bf16_f32 v119, v178, v179\n\t"
    "v_mfma_f32_16x16x32_bf16 v[32:35], v[144:147], a[192:195], v[128:131]\n\t"
    "s_waitcnt lgkmcnt(0)\n\t"
    "ds_read_b128 v[152:155], v193 offset:0\n\t"
    "v_mfma_f32_16x16x32_bf16 v[48:51], v[148:151], a[192:195], v[128:131]\n\t"
    "ds_read_b128 v[156:159], v193 offset:1536\n\t"
    "v_mfma_f32_16x16x32_bf16 v[52:55], v[148:151], a[208:211], v[132:135]\n\t"
    "v_exp_f32 v176, v72\n\t"
    "v_mfma_f32_16x16x32_bf16 v[36:39], v[144:147], a[208:211], v[132:135]\n\t"
    "v_exp_f32 v177, v73\n\t"
    "v_mfma_f32_16x16x32_bf16 v[40:43], v[144:147], a[224:227], v[136:139]\n\t"
    "buffer_load_dwordx4 v195, s[40:43], s70 offen offset:2048 sc1 lds\n\t"
    "v_mfma_f32_16x16x32_bf16 v[56:59], v[148:151], a[224:227], v[136:139]\n\t"
    "v_exp_f32 v178, v74\n\t"
    "v_mfma_f32_16x16x32_bf16 v[60:63], v[148:151], a[240:243], v[140:143]\n\t"
    "v_add_f32 v174, v176, v177\n\t"
    "v_cvt_pk_bf16_f32 v120, v176, v177\n\t"
    "v_mfma_f32_16x16x32_bf16 v[44:47], v[144:147], a[240:243], v[140:143]\n\t"
    "s_waitcnt lgkmcnt(1)\n\t"
    "v_exp_f32 v179, v75\n\t"
    "v_mfma_f32_16x16x32_bf16 v[32:35], v[152:155], a[196:199], v[32:35]\n\t"
    "s_waitcnt lgkmcnt(0)\n\t"
    "ds_read_b128 v[144:147], v192 offset:128\n\t"
    "v_add_f32 v174, v174, v178\n\t"
    "v_mfma_f32_16x16x32_bf16 v[48:51], v[156:159], a[196:199], v[48:51]\n\t"
    "ds_read_b128 v[148:151], v192 offset:1664\n\t"
    "v_add_f32 v174, v174, v179\n\t"
    "v_mfma_f32_16x16x32_bf16 v[52:55], v[156:159], a[212:215], v[52:55]\n\t"
    "v_cvt_pk_bf16_f32 v121, v178, v179\n\t"
    "v_mfma_f32_16x16x32_bf16 v[36:39], v[152:155], a[212:215], v[36:39]\n\t"
    "v_exp_f32 v176, v88\n\t"
    "v_mfma_f32_16x16x32_bf16 v[40:43], v[152:155], a[228:231], v[40:43]\n\t"
    "global_load_dwordx4 v[160:163], v196, s[78:79] offset:2560\n\t"
    "v_mfma_f32_16x16x32_bf16 v[56:59], v[156:159], a[228:231], v[56:59]\n\t"
    "v_exp_f32 v177, v89\n\t"
    "v_mfma_f32_16x16x32_bf16 v[60:63], v[156:159], a[244:247], v[60:63]\n\t"
    "v_add_f32 v174, v174, v176\n\t"
    "v_add_f32 v174, v174, v177\n\t"
    "v_mfma_f32_16x16x32_bf16 v[44:47], v[152:155], a[244:247], v[44:47]\n\t"
    "s_waitcnt lgkmcnt(1)\n\t"
    "v_exp_f32 v178, v90\n\t"
    "v_mfma_f32_16x16x32_bf16 v[32:35], v[144:147], a[200:203], v[32:35]\n\t"
    "s_waitcnt lgkmcnt(0)\n\t"
    "ds_read_b128 v[152:155], v193 offset:128\n\t"
    "v_cvt_pk_bf16_f32 v122, v176, v177\n\t"
    "v_mfma_f32_16x16x32_bf16 v[48:51], v[148:151], a[200:203], v[48:51]\n\t"
    "ds_read_b128 v[156:159], v193 offset:1664\n\t"
    "v_mfma_f32_16x16x32_bf16 v[52:55], v[148:151], a[216:219], v[52:55]\n\t"
    "global_load_dwordx4 v[164:167], v196, s[78:79] offset:2576\n\t"
    "v_mfma_f32_16x16x32_bf16 v[36:39], v[144:147], a[216:219], v[36:39]\n\t"
    "s_add_u32 m0, s60, 12288\n\t"
    "v_exp_f32 v179, v91\n\t"
    "v_mfma_f32_16x16x32_bf16 v[40:43], v[144:147], a[232:235], v[40:43]\n\t"
    "buffer_load_dwordx4 v195, s[44:47], s69 offen offset:0 sc1 lds\n\t"
    "v_mfma_f32_16x16x32_bf16 v[56:59], v[148:151], a[232:235], v[56:59]\n\t"
    "s_add_u32 s78, s78, s37\n\t"
    "v_exp_f32 v176, v76\n\t"
    "v_mfma_f32_16x16x32_bf16 v[60:63], v[148:151], a[248:251], v[60:63]\n\t"
    "s_addc_u32 s79, s79, 0\n\t"
    "v_add_f32 v174, v174, v178\n\t"
    "v_add_f32 v174, v174, v179\n\t"
    "v_mfma_f32_16x16x32_bf16 v[44:47], v[144:147], a[248:251], v[44:47]\n\t"
    "s_waitcnt lgkmcnt(1)\n\t"
    "v_exp_f32 v177, v77\n\t"
    "v_mfma_f32_16x16x32_bf16 v[32:35], v[152:155], a[204:207], v[32:35]\n\t"
    "s_waitcnt lgkmcnt(0)\n\t"
    "ds_read_b128 v[144:147], v192 offset:256\n\t"
    "v_cvt_pk_bf16_f32 v123, v178, v179\n\t"
    "v_mfma_f32_16x16x32_bf16 v[48:51], v[156:159], a[204:207], v[48:51]\n\t"
    "ds_read_b128 v[148:151], v192 offset:1792\n\t"
    "v_add_f32 v175, v176, v177\n\t"
    "v_mfma_f32_16x16x32_bf16 v[52:55], v[156:159], a[220:223], v[52:55]\n\t"
    "v_cvt_pk_bf16_f32 v124, v176, v177\n\t"
    "v_mfma_f32_16x16x32_bf16 v[36:39], v[152:155], a[220:223], v[36:39]\n\t"
    "v_exp_f32 v178, v78\n\t"
    "v_mfma_f32_16x16x32_bf16 v[40:43], v[152:155], a[236:239], v[40:43]\n\t"
    "buffer_load_dwordx4 v195, s[44:47], s69 offen offset:1024 sc1 lds\n\t"
    "v_mfma_f32_16x16x32_bf16 v[56:59], v[156:159], a[236:239], v[56:59]\n\t"
    "v_exp_f32 v179, v79\n\t"
    "v_mfma_f32_16x16x32_bf16 v[60:63], v[156:159], a[252:255], v[60:63]\n\t"
    "v_add_f32 v175, v175, v178\n\t"
    "v_add_f32 v175, v175, v179\n\t"
    "v_mfma_f32_16x16x32_bf16 v[44:47], v[152:155], a[252:255], v[44:47]\n\t"
    "s_waitcnt lgkmcnt(1)\n\t"
    "v_exp_f32 v176, v92\n\t"
    "v_mfma_f32_16x16x32_bf16 v[32:35], v[144:147], v[0:3], v[32:35]\n\t"
    "s_waitcnt lgkmcnt(0)\n\t"
    "ds_read_b128 v[152:155], v193 offset:256\n\t"
    "v_cvt_pk_bf16_f32 v125, v178, v179\n\t"
    "v_mfma_f32_16x16x32_bf16 v[48:51], v[148:151], v[0:3], v[48:51]\n\t"
    "ds_read_b128 v[156:159], v193 offset:1792\n\t"
    "v_mfma_f32_16x16x32_bf16 v[52:55], v[148:151], v[8:11], v[52:55]\n\t"
    "global_load_dwordx4 v[252:255], v198, s[82:83]\n\t"
    "v_mfma_f32_16x16x32_bf16 v[36:39], v[144:147], v[8:11], v[36:39]\n\t"
    "v_exp_f32 v177, v93\n\t"
    "v_mfma_f32_16x16x32_bf16 v[40:43], v[144:147], v[16:19], v[40:43]\n\t"
    "buffer_load_dwordx4 v195, s[44:47], s69 offen offset:2048 sc1 lds\n\t"
    "v_mfma_f32_16x16x32_bf16 v[56:59], v[148:151], v[16:19], v[56:59]\n\t"
    "s_add_u32 s82, s82, s38\n\t"
    "v_exp_f32 v178, v94\n\t"
    "v_mfma_f32_16x16x32_bf16 v[60:63], v[148:151], v[24:27], v[60:63]\n\t"
    "s_addc_u32 s83, s83, 0\n\t"
    "v_add_f32 v175, v175, v176\n\t"
    "v_add_f32 v175, v175, v177\n\t"
    "v_mfma_f32_16x16x32_bf16 v[44:47], v[144:147], v[24:27], v[44:47]\n\t"
    "s_waitcnt lgkmcnt(1)\n\t"
    "v_exp_f32 v179, v95\n\t"
    "v_mfma_f32_16x16x32_bf16 v[32:35], v[152:155], v[4:7], v[32:35]\n\t"
    "s_waitcnt lgkmcnt(0)\n\t"
    "ds_read_b128 v[144:147], v181 offset:12288\n\t"
    "v_cvt_pk_bf16_f32 v126, v176, v177\n\t"
    "v_mfma_f32_16x16x32_bf16 v[48:51], v[156:159], v[4:7], v[48:51]\n\t"
    "ds_read_b128 v[148:151], v181 offset:12544\n\t"
    "v_add_f32 v175, v175, v178\n\t"
    "v_mfma_f32_16x16x32_bf16 v[52:55], v[156:159], v[12:15], v[52:55]\n\t"
    "v_add_f32 v175, v175, v179\n\t"
    "v_cvt_pk_bf16_f32 v127, v178, v179\n\t"
    "v_mfma_f32_16x16x32_bf16 v[36:39], v[152:155], v[12:15], v[36:39]\n\t"
    "v_max3_f32 v180, v172, v173, v174\n\t"
    "v_max_f32 v180, v180, v175\n\t"
    "v_mfma_f32_16x16x32_bf16 v[40:43], v[152:155], v[20:23], v[40:43]\n\t"
    "v_cmp_lt_f32 vcc, s62, v180\n\t"
    "v_mfma_f32_16x16x32_bf16 v[56:59], v[156:159], v[20:23], v[56:59]\n\t"
    "global_load_dwordx4 v[188:191], v198, s[84:85]\n\t"
    "v_mfma_f32_16x16x32_bf16 v[60:63], v[156:159], v[28:31], v[60:63]\n\t"
    "global_load_dword v185, v185, s[52:53]\n\t"
    "v_mfma_f32_16x16x32_bf16 v[44:47], v[152:155], v[28:31], v[44:47]\n\t"
    "s_waitcnt lgkmcnt(1)\n\t"
    "s_cbranch_vccnz L_stub_l6_1\n\t"
    "L_ret_l6_1:\n\t"
    "v_mfma_f32_16x16x32_bf16 a[0:3], v[112:115], v[144:147], a[0:3]\n\t"
    "s_add_u32 s84, s84, s38\n\t"
    "ds_read_b128 v[152:155], v181 offset:12800\n\t"
    "v_add_f32 v168, v168, v172\n\t"
    "v_mfma_f32_16x16x32_bf16 a[4:7], v[116:119], v[144:147], a[4:7]\n\t"
    "s_addc_u32 s85, s85, 0\n\t"
    "v_exp_f32 v176, v32\n\t"
    "v_mfma_f32_16x16x32_bf16 a[8:11], v[120:123], v[144:147], a[8:11]\n\t"
    "v_add_f32 v169, v169, v173\n\t"
    "v_add_f32 v170, v170, v174\n\t"
    "v_mfma_f32_16x16x32_bf16 a[12:15], v[124:127], v[144:147], a[12:15]\n\t"
    "s_waitcnt lgkmcnt(1)\n\t"
    "v_exp_f32 v177, v33\n\t"
    "v_mfma_f32_16x16x32_bf16 a[28:31], v[124:127], v[148:151], a[28:31]\n\t"
    "s_waitcnt vmcnt(11)\n\t"
    "ds_read_b128 v[156:159], v181 offset:13056\n\t"
    "v_add_f32 v171, v171, v175\n\t"
    "v_mfma_f32_16x16x32_bf16 a[24:27], v[120:123], v[148:151], a[24:27]\n\t"
    "v_add_f32 v172, v176, v177\n\t"
    "v_cvt_pk_bf16_f32 v96, v176, v177\n\t"
    "v_mfma_f32_16x16x32_bf16 a[20:23], v[116:119], v[148:151], a[20:23]\n\t"
    "v_add_u32 v202, s94, v183\n\t"
    "v_mfma_f32_16x16x32_bf16 a[16:19], v[112:115], v[148:151], a[16:19]\n\t"
    "s_waitcnt lgkmcnt(1)\n\t"
    "v_exp_f32 v178, v34\n\t"
    "v_mfma_f32_16x16x32_bf16 a[32:35], v[112:115], v[152:155], a[32:35]\n\t"
    "s_add_u32 s94, s94, 1\n\t"
    "ds_read_b128 v[144:147], v181 offset:13312\n\t"
    "v_mfma_f32_16x16x32_bf16 a[36:39], v[116:119], v[152:155], a[36:39]\n\t"
    "v_exp_f32 v179, v35\n\t"
    "v_mfma_f32_16x16x32_bf16 a[40:43], v[120:123], v[152:155], a[40:43]\n\t"
    "v_add_f32 v172, v172, v178\n\t"
    "v_add_f32 v172, v172, v179\n\t"
    "v_mfma_f32_16x16x32_bf16 a[44:47], v[124:127], v[152:155], a[44:47]\n\t"
    "s_waitcnt lgkmcnt(1)\n\t"
    "v_cvt_pk_bf16_f32 v97, v178, v179\n\t"
    "v_mfma_f32_16x16x32_bf16 a[60:63], v[124:127], v[156:159], a[60:63]\n\t"
    "ds_read_b128 v[148:151], v181 offset:13568\n\t"
    "v_mfma_f32_16x16x32_bf16 a[56:59], v[120:123], v[156:159], a[56:59]\n\t"
    "v_exp_f32 v176, v48\n\t"
    "v_mfma_f32_16x16x32_bf16 a[52:55], v[116:119], v[156:159], a[52:55]\n\t"
    "global_store_dwordx2 v184, v[202:203], s[56:57] sc1\n\t"
    "v_mfma_f32_16x16x32_bf16 a[48:51], v[112:115], v[156:159], a[48:51]\n\t"
    "s_waitcnt lgkmcnt(1)\n\t"
    "v_exp_f32 v177, v49\n\t"
    "v_mfma_f32_16x16x32_bf16 a[64:67], v[112:115], v[144:147], a[64:67]\n\t"
    "ds_read_b128 v[152:155], v181 offset:13824\n\t"
    "v_add_f32 v172, v172, v176\n\t"
    "v_mfma_f32_16x16x32_bf16 a[68:71], v[116:119], v[144:147], a[68:71]\n\t"
    "v_add_f32 v172, v172, v177\n\t"
    "v_cvt_pk_bf16_f32 v98, v176, v177\n\t"
    "v_mfma_f32_16x16x32_bf16 a[72:75], v[120:123], v[144:147], a[72:75]\n\t"
    "v_mfma_f32_16x16x32_bf16 a[76:79], v[124:127], v[144:147], a[76:79]\n\t"
    "s_waitcnt lgkmcnt(1)\n\t"
    "v_exp_f32 v178, v50\n\t"
    "v_mfma_f32_16x16x32_bf16 a[92:95], v[124:127], v[148:151], a[92:95]\n\t"
    "ds_read_b128 v[156:159], v181 offset:14080\n\t"
    "v_mfma_f32_16x16x32_bf16 a[88:91], v[120:123], v[148:151], a[88:91]\n\t"
    "v_exp_f32 v179, v51\n\t"
    "v_mfma_f32_16x16x32_bf16 a[84:87], v[116:119], v[148:151], a[84:87]\n\t"
    "v_add_f32 v172, v172, v178\n\t"
    "v_add_f32 v172, v172, v179\n\t"
    "v_mfma_f32_16x16x32_bf16 a[80:83], v[112:115], v[148:151], a[80:83]\n\t"
    "s_waitcnt lgkmcnt(1)\n\t"
    "v_cvt_pk_bf16_f32 v99, v178, v179\n\t"
    "v_mfma_f32_16x16x32_bf16 a[96:99], v[112:115], v[152:155], a[96:99]\n\t"
    "ds_read_b128 v[144:147], v181 offset:14336\n\t"
    "v_mfma_f32_16x16x32_bf16 a[100:103], v[116:119], v[152:155], a[100:103]\n\t"
    "v_exp_f32 v176, v36\n\t"
    "v_mfma_f32_16x16x32_bf16 a[104:107], v[120:123], v[152:155], a[104:107]\n\t"
    "v_mfma_f32_16x16x32_bf16 a[108:111], v[124:127], v[152:155], a[108:111]\n\t"
    "s_waitcnt lgkmcnt(1)\n\t"
    "v_exp_f32 v177, v37\n\t"
    "v_mfma_f32_16x16x32_bf16 a[124:127], v[124:127], v[156:159], a[124:127]\n\t"
    "ds_read_b128 v[148:151], v181 offset:14592\n\t"
    "v_add_f32 v173, v176, v177\n\t"
    "v_mfma_f32_16x16x32_bf16 a[120:123], v[120:123], v[156:159], a[120:123]\n\t"
    "v_cvt_pk_bf16_f32 v100, v176, v177\n\t"
    "v_mfma_f32_16x16x32_bf16 a[116:119], v[116:119], v[156:159], a[116:119]\n\t"
    "v_mfma_f32_16x16x32_bf16 a[112:115], v[112:115], v[156:159], a[112:115]\n\t"
    "s_waitcnt lgkmcnt(1)\n\t"
    "v_exp_f32 v178, v38\n\t"
    "v_mfma_f32_16x16x32_bf16 a[128:131], v[112:115], v[144:147], a[128:131]\n\t"
    "ds_read_b128 v[152:155], v181 offset:14848\n\t"
    "v_mfma_f32_16x16x32_bf16 a[132:135], v[116:119], v[144:147], a[132:135]\n\t"
    "s_waitcnt vmcnt(16)\n\t"
    "v_exp_f32 v179, v39\n\t"
    "v_mfma_f32_16x16x32_bf16 a[136:139], v[120:123], v[144:147], a[136:139]\n\t"
    "s_barrier\n\t"
    "v_add_f32 v173, v173, v178\n\t"
    "v_add_f32 v173, v173, v179\n\t"
    "v_mfma_f32_16x16x32_bf16 a[140:143], v[124:127], v[144:147], a[140:143]\n\t"
    "s_waitcnt lgkmcnt(1)\n\t"
    "v_cvt_pk_bf16_f32 v101, v178, v179\n\t"
    "v_mfma_f32_16x16x32_bf16 a[156:159], v[124:127], v[148:151], a[156:159]\n\t"
    "ds_read_b128 v[156:159], v181 offset:15104\n\t"
    "v_mfma_f32_16x16x32_bf16 a[152:155], v[120:123], v[148:151], a[152:155]\n\t"
    "s_add_u32 m0, s60, 61440\n\t"
    "v_exp_f32 v176, v52\n\t"
    "v_mfma_f32_16x16x32_bf16 a[148:151], v[116:119], v[148:151], a[148:151]\n\t"
    "buffer_load_dwordx4 v195, s[40:43], s71 offen offset:0 sc1 lds\n\t"
    "v_mfma_f32_16x16x32_bf16 a[144:147], v[112:115], v[148:151], a[144:147]\n\t"
    "s_waitcnt lgkmcnt(1)\n\t"
    "v_exp_f32 v177, v53\n\t"
    "v_mfma_f32_16x16x32_bf16 a[160:163], v[112:115], v[152:155], a[160:163]\n\t"
    "ds_read_b128 v[144:147], v192 offset:12288\n\t"
    "v_add_f32 v173, v173, v176\n\t"
    "v_mfma_f32_16x16x32_bf16 a[164:167], v[116:119], v[152:155], a[164:167]\n\t"
    "ds_read_b128 v[148:151], v192 offset:13824\n\t"
    "v_add_f32 v173, v173, v177\n\t"
    "v_mfma_f32_16x16x32_bf16 a[168:171], v[120:123], v[152:155], a[168:171]\n\t"
    "buffer_load_dwordx4 v195, s[40:43], s71 offen offset:1024 sc1 lds\n\t"
    "v_mfma_f32_16x16x32_bf16 a[172:175], v[124:127], v[152:155], a[172:175]\n\t"
    "s_waitcnt lgkmcnt(2)\n\t"
    "v_exp_f32 v178, v54\n\t"
    "v_mfma_f32_16x16x32_bf16 a[188:191], v[124:127], v[156:159], a[188:191]\n\t"
    "v_cvt_pk_bf16_f32 v102, v176, v177\n\t"
    "v_mfma_f32_16x16x32_bf16 a[184:187], v[120:123], v[156:159], a[184:187]\n\t"
    "v_exp_f32 v179, v55\n\t"
    "v_mfma_f32_16x16x32_bf16 a[180:183], v[116:119], v[156:159], a[180:183]\n\t"
    "v_add_f32 v173, v173, v178\n\t"
    "v_add_f32 v173, v173, v179\n\t"
    "v_mfma_f32_16x16x32_bf16 a[176:179], v[112:115], v[156:159], a[176:179]\n\t"
    "s_waitcnt lgkmcnt(1)\n\t"
    "v_cvt_pk_bf16_f32 v103, v178, v179\n\t"
    "v_mfma_f32_16x16x32_bf16 v[64:67], v[144:147], a[192:195], v[128:131]\n\t"
    "s_waitcnt lgkmcnt(0)\n\t"
    "ds_read_b128 v[152:155], v193 offset:12288\n\t"
    "v_mfma_f32_16x16x32_bf16 v[80:83], v[148:151], a[192:195], v[128:131]\n\t"
    "ds_read_b128 v[156:159], v193 offset:13824\n\t"
    "v_mfma_f32_16x16x32_bf16 v[84:87], v[148:151], a[208:211], v[132:135]\n\t"
    "v_exp_f32 v176, v40\n\t"
    "v_mfma_f32_16x16x32_bf16 v[68:71], v[144:147], a[208:211], v[132:135]\n\t"
    "v_exp_f32 v177, v41\n\t"
    "v_mfma_f32_16x16x32_bf16 v[72:75], v[144:147], a[224:227], v[136:139]\n\t"
    "buffer_load_dwordx4 v195, s[40:43], s71 offen offset:2048 sc1 lds\n\t"
    "v_mfma_f32_16x16x32_bf16 v[88:91], v[148:151], a[224:227], v[136:139]\n\t"
    "v_exp_f32 v178, v42\n\t"
    "v_mfma_f32_16x16x32_bf16 v[92:95], v[148:151], a[240:243], v[140:143]\n\t"
    "v_add_f32 v174, v176, v177\n\t"
    "v_cvt_pk_bf16_f32 v104, v176, v177\n\t"
    "v_mfma_f32_16x16x32_bf16 v[76:79], v[144:147], a[240:243], v[140:143]\n\t"
    "s_waitcnt lgkmcnt(1)\n\t"
    "v_exp_f32 v179, v43\n\t"
    "v_mfma_f32_16x16x32_bf16 v[64:67], v[152:155], a[196:199], v[64:67]\n\t"
    "s_waitcnt lgkmcnt(0)\n\t"
    "ds_read_b128 v[144:147], v192 offset:12416\n\t"
    "v_add_f32 v174, v174, v178\n\t"
    "v_mfma_f32_16x16x32_bf16 v[80:83], v[156:159], a[196:199], v[80:83]\n\t"
    "s_add_u32 s40, s40, 196608\n\t"
    "ds_read_b128 v[148:151], v192 offset:13952\n\t"
    "v_add_f32 v174, v174, v179\n\t"
    "v_mfma_f32_16x16x32_bf16 v[84:87], v[156:159], a[212:215], v[84:87]\n\t"
    "s_addc_u32 s41, s41, 0\n\t"
    "v_cvt_pk_bf16_f32 v105, v178, v179\n\t"
    "v_mfma_f32_16x16x32_bf16 v[68:71], v[152:155], a[212:215], v[68:71]\n\t"
    "v_exp_f32 v176, v56\n\t"
    "v_mfma_f32_16x16x32_bf16 v[72:75], v[152:155], a[228:231], v[72:75]\n\t"
    "v_mfma_f32_16x16x32_bf16 v[88:91], v[156:159], a[228:231], v[88:91]\n\t"
    "v_exp_f32 v177, v57\n\t"
    "v_mfma_f32_16x16x32_bf16 v[92:95], v[156:159], a[244:247], v[92:95]\n\t"
    "v_add_f32 v174, v174, v176\n\t"
    "v_add_f32 v174, v174, v177\n\t"
    "v_mfma_f32_16x16x32_bf16 v[76:79], v[152:155], a[244:247], v[76:79]\n\t"
    "s_waitcnt lgkmcnt(1)\n\t"
    "v_exp_f32 v178, v58\n\t"
    "v_mfma_f32_16x16x32_bf16 v[64:67], v[144:147], a[200:203], v[64:67]\n\t"
    "s_waitcnt lgkmcnt(0)\n\t"
    "ds_read_b128 v[152:155], v193 offset:12416\n\t"
    "v_cvt_pk_bf16_f32 v106, v176, v177\n\t"
    "v_mfma_f32_16x16x32_bf16 v[80:83], v[148:151], a[200:203], v[80:83]\n\t"
    "ds_read_b128 v[156:159], v193 offset:13952\n\t"
    "v_mfma_f32_16x16x32_bf16 v[84:87], v[148:151], a[216:219], v[84:87]\n\t"
    "v_mfma_f32_16x16x32_bf16 v[68:71], v[144:147], a[216:219], v[68:71]\n\t"
    "s_add_u32 m0, s60, 73728\n\t"
    "v_exp_f32 v179, v59\n\t"
    "v_mfma_f32_16x16x32_bf16 v[72:75], v[144:147], a[232:235], v[72:75]\n\t"
    "buffer_load_dwordx4 v195, s[44:47], s70 offen offset:0 sc1 lds\n\t"
    "v_mfma_f32_16x16x32_bf16 v[88:91], v[148:151], a[232:235], v[88:91]\n\t"
    "v_exp_f32 v176, v44\n\t"
    "v_mfma_f32_16x16x32_bf16 v[92:95], v[148:151], a[248:251], v[92:95]\n\t"
    "v_add_f32 v174, v174, v178\n\t"
    "v_add_f32 v174, v174, v179\n\t"
    "v_mfma_f32_16x16x32_bf16 v[76:79], v[144:147], a[248:251], v[76:79]\n\t"
    "s_waitcnt lgkmcnt(1)\n\t"
    "v_exp_f32 v177, v45\n\t"
    "v_mfma_f32_16x16x32_bf16 v[64:67], v[152:155], a[204:207], v[64:67]\n\t"
    "s_waitcnt lgkmcnt(0)\n\t"
    "ds_read_b128 v[144:147], v192 offset:12544\n\t"
    "v_cvt_pk_bf16_f32 v107, v178, v179\n\t"
    "v_mfma_f32_16x16x32_bf16 v[80:83], v[156:159], a[204:207], v[80:83]\n\t"
    "ds_read_b128 v[148:151], v192 offset:14080\n\t"
    "v_add_f32 v175, v176, v177\n\t"
    "v_mfma_f32_16x16x32_bf16 v[84:87], v[156:159], a[220:223], v[84:87]\n\t"
    "v_cvt_pk_bf16_f32 v108, v176, v177\n\t"
    "v_mfma_f32_16x16x32_bf16 v[68:71], v[152:155], a[220:223], v[68:71]\n\t"
    "v_exp_f32 v178, v46\n\t"
    "v_mfma_f32_16x16x32_bf16 v[72:75], v[152:155], a[236:239], v[72:75]\n\t"
    "buffer_load_dwordx4 v195, s[44:47], s70 offen offset:1024 sc1 lds\n\t"
    "v_mfma_f32_16x16x32_bf16 v[88:91], v[156:159], a[236:239], v[88:91]\n\t"
    "v_exp_f32 v179, v47\n\t"
    "v_mfma_f32_16x16x32_bf16 v[92:95], v[156:159], a[252:255], v[92:95]\n\t"
    "v_add_f32 v175, v175, v178\n\t"
    "v_add_f32 v175, v175, v179\n\t"
    "v_mfma_f32_16x16x32_bf16 v[76:79], v[152:155], a[252:255], v[76:79]\n\t"
    "s_waitcnt lgkmcnt(1)\n\t"
    "v_exp_f32 v176, v60\n\t"
    "v_mfma_f32_16x16x32_bf16 v[64:67], v[144:147], v[0:3], v[64:67]\n\t"
    "s_waitcnt lgkmcnt(0)\n\t"
    "ds_read_b128 v[152:155], v193 offset:12544\n\t"
    "v_cvt_pk_bf16_f32 v109, v178, v179\n\t"
    "v_mfma_f32_16x16x32_bf16 v[80:83], v[148:151], v[0:3], v[80:83]\n\t"
    "ds_read_b128 v[156:159], v193 offset:14080\n\t"
    "v_mfma_f32_16x16x32_bf16 v[84:87], v[148:151], v[8:11], v[84:87]\n\t"
    "v_mfma_f32_16x16x32_bf16 v[68:71], v[144:147], v[8:11], v[68:71]\n\t"
    "v_exp_f32 v177, v61\n\t"
    "v_mfma_f32_16x16x32_bf16 v[72:75], v[144:147], v[16:19], v[72:75]\n\t"
    "buffer_load_dwordx4 v195, s[44:47], s70 offen offset:2048 sc1 lds\n\t"
    "v_mfma_f32_16x16x32_bf16 v[88:91], v[148:151], v[16:19], v[88:91]\n\t"
    "v_exp_f32 v178, v62\n\t"
    "v_mfma_f32_16x16x32_bf16 v[92:95], v[148:151], v[24:27], v[92:95]\n\t"
    "v_add_f32 v175, v175, v176\n\t"
    "v_add_f32 v175, v175, v177\n\t"
    "v_mfma_f32_16x16x32_bf16 v[76:79], v[144:147], v[24:27], v[76:79]\n\t"
    "s_waitcnt lgkmcnt(1)\n\t"
    "v_exp_f32 v179, v63\n\t"
    "v_mfma_f32_16x16x32_bf16 v[64:67], v[152:155], v[4:7], v[64:67]\n\t"
    "s_waitcnt lgkmcnt(0)\n\t"
    "ds_read_b128 v[144:147], v194 offset:0\n\t"
    "v_cvt_pk_bf16_f32 v110, v176, v177\n\t"
    "v_mfma_f32_16x16x32_bf16 v[80:83], v[156:159], v[4:7], v[80:83]\n\t"
    "ds_read_b128 v[148:151], v194 offset:256\n\t"
    "v_add_f32 v175, v175, v178\n\t"
    "v_mfma_f32_16x16x32_bf16 v[84:87], v[156:159], v[12:15], v[84:87]\n\t"
    "v_add_f32 v175, v175, v179\n\t"
    "v_cvt_pk_bf16_f32 v111, v178, v179\n\t"
    "v_mfma_f32_16x16x32_bf16 v[68:71], v[152:155], v[12:15], v[68:71]\n\t"
    "v_max3_f32 v180, v172, v173, v174\n\t"
    "v_max_f32 v180, v180, v175\n\t"
    "v_mfma_f32_16x16x32_bf16 v[72:75], v[152:155], v[20:23], v[72:75]\n\t"
    "v_cmp_lt_f32 vcc, s62, v180\n\t"
    "v_mfma_f32_16x16x32_bf16 v[88:91], v[156:159], v[20:23], v[88:91]\n\t"
    "v_mfma_f32_16x16x32_bf16 v[92:95], v[156:159], v[28:31], v[92:95]\n\t"
    "v_mfma_f32_16x16x32_bf16 v[76:79], v[152:155], v[28:31], v[76:79]\n\t"
    "s_waitcnt lgkmcnt(1)\n\t"
    "s_cbranch_vccnz L_stub_l7_0\n\t"
    "L_ret_l7_0:\n\t"
    "v_mfma_f32_16x16x32_bf16 a[0:3], v[96:99], v[144:147], a[0:3]\n\t"
    "ds_read_b128 v[152:155], v194 offset:512\n\t"
    "v_add_f32 v168, v168, v172\n\t"
    "v_mfma_f32_16x16x32_bf16 a[4:7], v[100:103], v[144:147], a[4:7]\n\t"
    "v_exp_f32 v176, v64\n\t"
    "v_mfma_f32_16x16x32_bf16 a[8:11], v[104:107], v[144:147], a[8:11]\n\t"
    "v_add_f32 v169, v169, v173\n\t"
    "v_add_f32 v170, v170, v174\n\t"
    "v_mfma_f32_16x16x32_bf16 a[12:15], v[108:111], v[144:147], a[12:15]\n\t"
    "s_waitcnt lgkmcnt(1)\n\t"
    "v_exp_f32 v177, v65\n\t"
    "v_mfma_f32_16x16x32_bf16 a[28:31], v[108:111], v[148:151], a[28:31]\n\t"
    "ds_read_b128 v[156:159], v194 offset:768\n\t"
    "v_add_f32 v171, v171, v175\n\t"
    "v_mfma_f32_16x16x32_bf16 a[24:27], v[104:107], v[148:151], a[24:27]\n\t"
    "v_add_f32 v172, v176, v177\n\t"
    "v_cvt_pk_bf16_f32 v112, v176, v177\n\t"
    "v_mfma_f32_16x16x32_bf16 a[20:23], v[100:103], v[148:151], a[20:23]\n\t"
    "v_mfma_f32_16x16x32_bf16 a[16:19], v[96:99], v[148:151], a[16:19]\n\t"
    "s_waitcnt lgkmcnt(1)\n\t"
    "v_exp_f32 v178, v66\n\t"
    "v_mfma_f32_16x16x32_bf16 a[32:35], v[96:99], v[152:155], a[32:35]\n\t"
    "ds_read_b128 v[144:147], v194 offset:1024\n\t"
    "v_mfma_f32_16x16x32_bf16 a[36:39], v[100:103], v[152:155], a[36:39]\n\t"
    "v_exp_f32 v179, v67\n\t"
    "v_mfma_f32_16x16x32_bf16 a[40:43], v[104:107], v[152:155], a[40:43]\n\t"
    "v_add_f32 v172, v172, v178\n\t"
    "v_add_f32 v172, v172, v179\n\t"
    "v_mfma_f32_16x16x32_bf16 a[44:47], v[108:111], v[152:155], a[44:47]\n\t"
    "s_waitcnt lgkmcnt(1)\n\t"
    "v_cvt_pk_bf16_f32 v113, v178, v179\n\t"
    "v_mfma_f32_16x16x32_bf16 a[60:63], v[108:111], v[156:159], a[60:63]\n\t"
    "ds_read_b128 v[148:151], v194 offset:1280\n\t"
    "v_mfma_f32_16x16x32_bf16 a[56:59], v[104:107], v[156:159], a[56:59]\n\t"
    "v_exp_f32 v176, v80\n\t"
    "v_mfma_f32_16x16x32_bf16 a[52:55], v[100:103], v[156:159], a[52:55]\n\t"
    "v_mfma_f32_16x16x32_bf16 a[48:51], v[96:99], v[156:159], a[48:51]\n\t"
    "s_waitcnt lgkmcnt(1)\n\t"
    "v_exp_f32 v177, v81\n\t"
    "v_mfma_f32_16x16x32_bf16 a[64:67], v[96:99], v[144:147], a[64:67]\n\t"
    "ds_read_b128 v[152:155], v194 offset:1536\n\t"
    "v_add_f32 v172, v172, v176\n\t"
    "v_mfma_f32_16x16x32_bf16 a[68:71], v[100:103], v[144:147], a[68:71]\n\t"
    "v_add_f32 v172, v172, v177\n\t"
    "v_cvt_pk_bf16_f32 v114, v176, v177\n\t"
    "v_mfma_f32_16x16x32_bf16 a[72:75], v[104:107], v[144:147], a[72:75]\n\t"
    "v_mfma_f32_16x16x32_bf16 a[76:79], v[108:111], v[144:147], a[76:79]\n\t"
    "s_waitcnt lgkmcnt(1)\n\t"
    "v_exp_f32 v178, v82\n\t"
    "v_mfma_f32_16x16x32_bf16 a[92:95], v[108:111], v[148:151], a[92:95]\n\t"
    "ds_read_b128 v[156:159], v194 offset:1792\n\t"
    "v_mfma_f32_16x16x32_bf16 a[88:91], v[104:107], v[148:151], a[88:91]\n\t"
    "v_exp_f32 v179, v83\n\t"
    "v_mfma_f32_16x16x32_bf16 a[84:87], v[100:103], v[148:151], a[84:87]\n\t"
    "v_add_f32 v172, v172, v178\n\t"
    "v_add_f32 v172, v172, v179\n\t"
    "v_mfma_f32_16x16x32_bf16 a[80:83], v[96:99], v[148:151], a[80:83]\n\t"
    "s_waitcnt lgkmcnt(1)\n\t"
    "v_cvt_pk_bf16_f32 v115, v178, v179\n\t"
    "v_mfma_f32_16x16x32_bf16 a[96:99], v[96:99], v[152:155], a[96:99]\n\t"
    "ds_read_b128 v[144:147], v194 offset:2048\n\t"
    "v_mfma_f32_16x16x32_bf16 a[100:103], v[100:103], v[152:155], a[100:103]\n\t"
    "v_exp_f32 v176, v68\n\t"
    "v_mfma_f32_16x16x32_bf16 a[104:107], v[104:107], v[152:155], a[104:107]\n\t"
    "v_mfma_f32_16x16x32_bf16 a[108:111], v[108:111], v[152:155], a[108:111]\n\t"
    "s_waitcnt lgkmcnt(1)\n\t"
    "v_exp_f32 v177, v69\n\t"
    "v_mfma_f32_16x16x32_bf16 a[124:127], v[108:111], v[156:159], a[124:127]\n\t"
    "ds_read_b128 v[148:151], v194 offset:2304\n\t"
    "v_add_f32 v173, v176, v177\n\t"
    "v_mfma_f32_16x16x32_bf16 a[120:123], v[104:107], v[156:159], a[120:123]\n\t"
    "v_cvt_pk_bf16_f32 v116, v176, v177\n\t"
    "v_mfma_f32_16x16x32_bf16 a[116:119], v[100:103], v[156:159], a[116:119]\n\t"
    "v_mfma_f32_16x16x32_bf16 a[112:115], v[96:99], v[156:159], a[112:115]\n\t"
    "s_waitcnt lgkmcnt(1)\n\t"
    "v_exp_f32 v178, v70\n\t"
    "v_mfma_f32_16x16x32_bf16 a[128:131], v[96:99], v[144:147], a[128:131]\n\t"
    "ds_read_b128 v[152:155], v194 offset:2560\n\t"
    "v_mfma_f32_16x16x32_bf16 a[132:135], v[100:103], v[144:147], a[132:135]\n\t"
    "s_waitcnt vmcnt(9)\n\t"
    "v_exp_f32 v179, v71\n\t"
    "v_mfma_f32_16x16x32_bf16 a[136:139], v[104:107], v[144:147], a[136:139]\n\t"
    "s_barrier\n\t"
    "v_add_f32 v173, v173, v178\n\t"
    "v_add_f32 v173, v173, v179\n\t"
    "v_mfma_f32_16x16x32_bf16 a[140:143], v[108:111], v[144:147], a[140:143]\n\t"
    "s_waitcnt lgkmcnt(1)\n\t"
    "v_cvt_pk_bf16_f32 v117, v178, v179\n\t"
    "v_mfma_f32_16x16x32_bf16 a[156:159], v[108:111], v[148:151], a[156:159]\n\t"
    "ds_read_b128 v[156:159], v194 offset:2816\n\t"
    "v_mfma_f32_16x16x32_bf16 a[152:155], v[104:107], v[148:151], a[152:155]\n\t"
    "s_add_u32 m0, s60, 24576\n\t"
    "v_exp_f32 v176, v84\n\t"
    "v_mfma_f32_16x16x32_bf16 a[148:151], v[100:103], v[148:151], a[148:151]\n\t"
    "buffer_load_dwordx4 v195, s[40:43], s64 offen offset:0 sc1 lds\n\t"
    "v_mfma_f32_16x16x32_bf16 a[144:147], v[96:99], v[148:151], a[144:147]\n\t"
    "s_waitcnt lgkmcnt(1)\n\t"
    "v_exp_f32 v177, v85\n\t"
    "v_mfma_f32_16x16x32_bf16 a[160:163], v[96:99], v[152:155], a[160:163]\n\t"
    "ds_read_b128 v[144:147], v192 offset:24576\n\t"
    "v_add_f32 v173, v173, v176\n\t"
    "v_mfma_f32_16x16x32_bf16 a[164:167], v[100:103], v[152:155], a[164:167]\n\t"
    "ds_read_b128 v[148:151], v192 offset:26112\n\t"
    "v_add_f32 v173, v173, v177\n\t"
    "v_mfma_f32_16x16x32_bf16 a[168:171], v[104:107], v[152:155], a[168:171]\n\t"
    "buffer_load_dwordx4 v195, s[40:43], s64 offen offset:1024 sc1 lds\n\t"
    "v_mfma_f32_16x16x32_bf16 a[172:175], v[108:111], v[152:155], a[172:175]\n\t"
    "s_waitcnt lgkmcnt(2)\n\t"
    "v_exp_f32 v178, v86\n\t"
    "v_mfma_f32_16x16x32_bf16 a[188:191], v[108:111], v[156:159], a[188:191]\n\t"
    "v_cvt_pk_bf16_f32 v118, v176, v177\n\t"
    "v_mfma_f32_16x16x32_bf16 a[184:187], v[104:107], v[156:159], a[184:187]\n\t"
    "v_exp_f32 v179, v87\n\t"
    "v_mfma_f32_16x16x32_bf16 a[180:183], v[100:103], v[156:159], a[180:183]\n\t"
    "v_add_f32 v173, v173, v178\n\t"
    "v_add_f32 v173, v173, v179\n\t"
    "v_mfma_f32_16x16x32_bf16 a[176:179], v[96:99], v[156:159], a[176:179]\n\t"
    "s_waitcnt lgkmcnt(1)\n\t"
    "v_cvt_pk_bf16_f32 v119, v178, v179\n\t"
    "s_add_u32 s61, s61, 1\n\t"
    "s_cmp_lt_u32 s61, 6\n\t"
    "s_cbranch_scc1 L_loop\n\t"
    "L_sfx:\n\t"
    "v_mfma_f32_16x16x32_bf16 v[32:35], v[144:147], a[192:195], v[128:131]\n\t"
    "s_waitcnt lgkmcnt(0)\n\t"
    "ds_read_b128 v[152:155], v193 offset:24576\n\t"
    "v_mfma_f32_16x16x32_bf16 v[48:51], v[148:151], a[192:195], v[128:131]\n\t"
    "ds_read_b128 v[156:159], v193 offset:26112\n\t"
    "v_mfma_f32_16x16x32_bf16 v[52:55], v[148:151], a[208:211], v[132:135]\n\t"
    "v_exp_f32 v176, v72\n\t"
    "v_mfma_f32_16x16x32_bf16 v[36:39], v[144:147], a[208:211], v[132:135]\n\t"
    "v_exp_f32 v177, v73\n\t"
    "v_mfma_f32_16x16x32_bf16 v[40:43], v[144:147], a[224:227], v[136:139]\n\t"
    "buffer_load_dwordx4 v195, s[40:43], s64 offen offset:2048 sc1 lds\n\t"
    "v_mfma_f32_16x16x32_bf16 v[56:59], v[148:151], a[224:227], v[136:139]\n\t"
    "v_exp_f32 v178, v74\n\t"
    "v_mfma_f32_16x16x32_bf16 v[60:63], v[148:151], a[240:243], v[140:143]\n\t"
    "v_add_f32 v174, v176, v177\n\t"
    "v_cvt_pk_bf16_f32 v120, v176, v177\n\t"
    "v_mfma_f32_16x16x32_bf16 v[44:47], v[144:147], a[240:243], v[140:143]\n\t"
    "s_waitcnt lgkmcnt(1)\n\t"
    "v_exp_f32 v179, v75\n\t"
    "v_mfma_f32_16x16x32_bf16 v[32:35], v[152:155], a[196:199], v[32:35]\n\t"
    "s_waitcnt lgkmcnt(0)\n\t"
    "ds_read_b128 v[144:147], v192 offset:24704\n\t"
    "v_add_f32 v174, v174, v178\n\t"
    "v_mfma_f32_16x16x32_bf16 v[48:51], v[156:159], a[196:199], v[48:51]\n\t"
    "ds_read_b128 v[148:151], v192 offset:26240\n\t"
    "v_add_f32 v174, v174, v179\n\t"
    "v_mfma_f32_16x16x32_bf16 v[52:55], v[156:159], a[212:215], v[52:55]\n\t"
    "v_cvt_pk_bf16_f32 v121, v178, v179\n\t"
    "v_mfma_f32_16x16x32_bf16 v[36:39], v[152:155], a[212:215], v[36:39]\n\t"
    "v_exp_f32 v176, v88\n\t"
    "v_mfma_f32_16x16x32_bf16 v[40:43], v[152:155], a[228:231], v[40:43]\n\t"
    "v_mfma_f32_16x16x32_bf16 v[56:59], v[156:159], a[228:231], v[56:59]\n\t"
    "v_exp_f32 v177, v89\n\t"
    "v_mfma_f32_16x16x32_bf16 v[60:63], v[156:159], a[244:247], v[60:63]\n\t"
    "v_add_f32 v174, v174, v176\n\t"
    "v_add_f32 v174, v174, v177\n\t"
    "v_mfma_f32_16x16x32_bf16 v[44:47], v[152:155], a[244:247], v[44:47]\n\t"
    "s_waitcnt lgkmcnt(1)\n\t"
    "v_exp_f32 v178, v90\n\t"
    "v_mfma_f32_16x16x32_bf16 v[32:35], v[144:147], a[200:203], v[32:35]\n\t"
    "s_waitcnt lgkmcnt(0)\n\t"
    "ds_read_b128 v[152:155], v193 offset:24704\n\t"
    "v_cvt_pk_bf16_f32 v122, v176, v177\n\t"
    "v_mfma_f32_16x16x32_bf16 v[48:51], v[148:151], a[200:203], v[48:51]\n\t"
    "ds_read_b128 v[156:159], v193 offset:26240\n\t"
    "v_mfma_f32_16x16x32_bf16 v[52:55], v[148:151], a[216:219], v[52:55]\n\t"
    "v_mfma_f32_16x16x32_bf16 v[36:39], v[144:147], a[216:219], v[36:39]\n\t"
    "v_exp_f32 v179, v91\n\t"
    "v_mfma_f32_16x16x32_bf16 v[40:43], v[144:147], a[232:235], v[40:43]\n\t"
    "v_add_f32 v174, v174, v178\n\t"
    "v_add_f32 v174, v174, v179\n\t"
    "v_mfma_f32_16x16x32_bf16 v[56:59], v[148:151], a[232:235], v[56:59]\n\t"
    "v_exp_f32 v176, v76\n\t"
    "v_mfma_f32_16x16x32_bf16 v[60:63], v[148:151], a[248:251], v[60:63]\n\t"
    "v_cvt_pk_bf16_f32 v123, v178, v179\n\t"
    "v_mfma_f32_16x16x32_bf16 v[44:47], v[144:147], a[248:251], v[44:47]\n\t"
    "s_waitcnt lgkmcnt(1)\n\t"
    "v_exp_f32 v177, v77\n\t"
    "v_mfma_f32_16x16x32_bf16 v[32:35], v[152:155], a[204:207], v[32:35]\n\t"
    "s_waitcnt lgkmcnt(0)\n\t"
    "ds_read_b128 v[144:147], v192 offset:24832\n\t"
    "v_add_f32 v175, v176, v177\n\t"
    "v_mfma_f32_16x16x32_bf16 v[48:51], v[156:159], a[204:207], v[48:51]\n\t"
    "ds_read_b128 v[148:151], v192 offset:26368\n\t"
    "v_cvt_pk_bf16_f32 v124, v176, v177\n\t"
    "v_mfma_f32_16x16x32_bf16 v[52:55], v[156:159], a[220:223], v[52:55]\n\t"
    "v_mfma_f32_16x16x32_bf16 v[36:39], v[152:155], a[220:223], v[36:39]\n\t"
    "v_exp_f32 v178, v78\n\t"
    "v_mfma_f32_16x16x32_bf16 v[40:43], v[152:155], a[236:239], v[40:43]\n\t"
    "v_mfma_f32_16x16x32_bf16 v[56:59], v[156:159], a[236:239], v[56:59]\n\t"
    "v_exp_f32 v179, v79\n\t"
    "v_mfma_f32_16x16x32_bf16 v[60:63], v[156:159], a[252:255], v[60:63]\n\t"
    "v_add_f32 v175, v175, v178\n\t"
    "v_add_f32 v175, v175, v179\n\t"
    "v_mfma_f32_16x16x32_bf16 v[44:47], v[152:155], a[252:255], v[44:47]\n\t"
    "s_waitcnt lgkmcnt(1)\n\t"
    "v_exp_f32 v176, v92\n\t"
    "v_mfma_f32_16x16x32_bf16 v[32:35], v[144:147], v[0:3], v[32:35]\n\t"
    "s_waitcnt lgkmcnt(0)\n\t"
    "ds_read_b128 v[152:155], v193 offset:24832\n\t"
    "v_cvt_pk_bf16_f32 v125, v178, v179\n\t"
    "v_mfma_f32_16x16x32_bf16 v[48:51], v[148:151], v[0:3], v[48:51]\n\t"
    "ds_read_b128 v[156:159], v193 offset:26368\n\t"
    "v_mfma_f32_16x16x32_bf16 v[52:55], v[148:151], v[8:11], v[52:55]\n\t"
    "v_mfma_f32_16x16x32_bf16 v[36:39], v[144:147], v[8:11], v[36:39]\n\t"
    "v_exp_f32 v177, v93\n\t"
    "v_mfma_f32_16x16x32_bf16 v[40:43], v[144:147], v[16:19], v[40:43]\n\t"
    "v_add_f32 v175, v175, v176\n\t"
    "v_add_f32 v175, v175, v177\n\t"
    "v_mfma_f32_16x16x32_bf16 v[56:59], v[148:151], v[16:19], v[56:59]\n\t"
    "v_exp_f32 v178, v94\n\t"
    "v_mfma_f32_16x16x32_bf16 v[60:63], v[148:151], v[24:27], v[60:63]\n\t"
    "v_cvt_pk_bf16_f32 v126, v176, v177\n\t"
    "v_mfma_f32_16x16x32_bf16 v[44:47], v[144:147], v[24:27], v[44:47]\n\t"
    "s_waitcnt lgkmcnt(1)\n\t"
    "v_exp_f32 v179, v95\n\t"
    "v_mfma_f32_16x16x32_bf16 v[32:35], v[152:155], v[4:7], v[32:35]\n\t"
    "s_waitcnt lgkmcnt(0)\n\t"
    "ds_read_b128 v[144:147], v194 offset:12288\n\t"
    "v_add_f32 v175, v175, v178\n\t"
    "v_mfma_f32_16x16x32_bf16 v[48:51], v[156:159], v[4:7], v[48:51]\n\t"
    "ds_read_b128 v[148:151], v194 offset:12544\n\t"
    "v_add_f32 v175, v175, v179\n\t"
    "v_mfma_f32_16x16x32_bf16 v[52:55], v[156:159], v[12:15], v[52:55]\n\t"
    "v_cvt_pk_bf16_f32 v127, v178, v179\n\t"
    "v_max3_f32 v180, v172, v173, v174\n\t"
    "v_mfma_f32_16x16x32_bf16 v[36:39], v[152:155], v[12:15], v[36:39]\n\t"
    "v_max_f32 v180, v180, v175\n\t"
    "v_cmp_lt_f32 vcc, s62, v180\n\t"
    "v_mfma_f32_16x16x32_bf16 v[40:43], v[152:155], v[20:23], v[40:43]\n\t"
    "global_load_dword v202, v182, s[90:91] sc1\n\t"
    "v_mfma_f32_16x16x32_bf16 v[56:59], v[156:159], v[20:23], v[56:59]\n\t"
    "s_add_u32 m0, s60, 86016\n\t"
    "v_mfma_f32_16x16x32_bf16 v[60:63], v[156:159], v[28:31], v[60:63]\n\t"
    "buffer_load_dwordx4 v195, s[44:47], s71 offen offset:0 sc1 lds\n\t"
    "v_mfma_f32_16x16x32_bf16 v[44:47], v[152:155], v[28:31], v[44:47]\n\t"
    "s_waitcnt lgkmcnt(1)\n\t"
    "s_cbranch_vccnz L_stub_s53_1\n\t"
    "L_ret_s53_1:\n\t"
    "v_mfma_f32_16x16x32_bf16 a[0:3], v[112:115], v[144:147], a[0:3]\n\t"
    "ds_read_b128 v[152:155], v194 offset:12800\n\t"
    "v_add_f32 v168, v168, v172\n\t"
    "v_mfma_f32_16x16x32_bf16 a[4:7], v[116:119], v[144:147], a[4:7]\n\t"
    "v_exp_f32 v176, v32\n\t"
    "v_mfma_f32_16x16x32_bf16 a[8:11], v[120:123], v[144:147], a[8:11]\n\t"
    "v_add_f32 v169, v169, v173\n\t"
    "v_add_f32 v170, v170, v174\n\t"
    "v_mfma_f32_16x16x32_bf16 a[12:15], v[124:127], v[144:147], a[12:15]\n\t"
    "s_waitcnt lgkmcnt(1)\n\t"
    "v_exp_f32 v177, v33\n\t"
    "v_mfma_f32_16x16x32_bf16 a[28:31], v[124:127], v[148:151], a[28:31]\n\t"
    "ds_read_b128 v[156:159], v194 offset:13056\n\t"
    "v_add_f32 v171, v171, v175\n\t"
    "v_mfma_f32_16x16x32_bf16 a[24:27], v[120:123], v[148:151], a[24:27]\n\t"
    "v_add_f32 v172, v176, v177\n\t"
    "v_cvt_pk_bf16_f32 v96, v176, v177\n\t"
    "v_mfma_f32_16x16x32_bf16 a[20:23], v[116:119], v[148:151], a[20:23]\n\t"
    "buffer_load_dwordx4 v195, s[44:47], s71 offen offset:1024 sc1 lds\n\t"
    "v_mfma_f32_16x16x32_bf16 a[16:19], v[112:115], v[148:151], a[16:19]\n\t"
    "s_waitcnt lgkmcnt(1)\n\t"
    "v_exp_f32 v178, v34\n\t"
    "v_mfma_f32_16x16x32_bf16 a[32:35], v[112:115], v[152:155], a[32:35]\n\t"
    "ds_read_b128 v[144:147], v194 offset:13312\n\t"
    "v_mfma_f32_16x16x32_bf16 a[36:39], v[116:119], v[152:155], a[36:39]\n\t"
    "v_exp_f32 v179, v35\n\t"
    "v_mfma_f32_16x16x32_bf16 a[40:43], v[120:123], v[152:155], a[40:43]\n\t"
    "v_add_f32 v172, v172, v178\n\t"
    "v_add_f32 v172, v172, v179\n\t"
    "v_mfma_f32_16x16x32_bf16 a[44:47], v[124:127], v[152:155], a[44:47]\n\t"
    "s_waitcnt lgkmcnt(1)\n\t"
    "v_cvt_pk_bf16_f32 v97, v178, v179\n\t"
    "v_mfma_f32_16x16x32_bf16 a[60:63], v[124:127], v[156:159], a[60:63]\n\t"
    "ds_read_b128 v[148:151], v194 offset:13568\n\t"
    "v_mfma_f32_16x16x32_bf16 a[56:59], v[120:123], v[156:159], a[56:59]\n\t"
    "v_exp_f32 v176, v48\n\t"
    "v_mfma_f32_16x16x32_bf16 a[52:55], v[116:119], v[156:159], a[52:55]\n\t"
    "buffer_load_dwordx4 v195, s[44:47], s71 offen offset:2048 sc1 lds\n\t"
    "v_mfma_f32_16x16x32_bf16 a[48:51], v[112:115], v[156:159], a[48:51]\n\t"
    "s_waitcnt lgkmcnt(1)\n\t"
    "v_exp_f32 v177, v49\n\t"
    "v_mfma_f32_16x16x32_bf16 a[64:67], v[112:115], v[144:147], a[64:67]\n\t"
    "s_add_u32 s44, s44, 196608\n\t"
    "ds_read_b128 v[152:155], v194 offset:13824\n\t"
    "v_add_f32 v172, v172, v176\n\t"
    "v_mfma_f32_16x16x32_bf16 a[68:71], v[116:119], v[144:147], a[68:71]\n\t"
    "s_addc_u32 s45, s45, 0\n\t"
    "v_add_f32 v172, v172, v177\n\t"
    "v_cvt_pk_bf16_f32 v98, v176, v177\n\t"
    "v_mfma_f32_16x16x32_bf16 a[72:75], v[120:123], v[144:147], a[72:75]\n\t"
    "v_mfma_f32_16x16x32_bf16 a[76:79], v[124:127], v[144:147], a[76:79]\n\t"
    "s_waitcnt lgkmcnt(1)\n\t"
    "v_exp_f32 v178, v50\n\t"
    "v_mfma_f32_16x16x32_bf16 a[92:95], v[124:127], v[148:151], a[92:95]\n\t"
    "ds_read_b128 v[156:159], v194 offset:14080\n\t"
    "v_mfma_f32_16x16x32_bf16 a[88:91], v[120:123], v[148:151], a[88:91]\n\t"
    "v_exp_f32 v179, v51\n\t"
    "v_mfma_f32_16x16x32_bf16 a[84:87], v[116:119], v[148:151], a[84:87]\n\t"
    "v_add_f32 v172, v172, v178\n\t"
    "v_add_f32 v172, v172, v179\n\t"
    "v_mfma_f32_16x16x32_bf16 a[80:83], v[112:115], v[148:151], a[80:83]\n\t"
    "s_waitcnt lgkmcnt(1)\n\t"
    "v_cvt_pk_bf16_f32 v99, v178, v179\n\t"
    "v_mfma_f32_16x16x32_bf16 a[96:99], v[112:115], v[152:155], a[96:99]\n\t"
    "ds_read_b128 v[144:147], v194 offset:14336\n\t"
    "v_mfma_f32_16x16x32_bf16 a[100:103], v[116:119], v[152:155], a[100:103]\n\t"
    "v_exp_f32 v176, v36\n\t"
    "v_mfma_f32_16x16x32_bf16 a[104:107], v[120:123], v[152:155], a[104:107]\n\t"
    "s_waitcnt vmcnt(3)\n\t"
    "v_subrev_u32 v202, s63, v202\n\t"
    "v_cmp_lt_u32 vcc, 7, v202\n\t"
    "v_mfma_f32_16x16x32_bf16 a[108:111], v[124:127], v[152:155], a[108:111]\n\t"
    "s_waitcnt lgkmcnt(1)\n\t"
    "v_exp_f32 v177, v37\n\t"
    "v_mfma_f32_16x16x32_bf16 a[124:127], v[124:127], v[156:159], a[124:127]\n\t"
    "s_add_u32 s63, s63, 1\n\t"
    "ds_read_b128 v[148:151], v194 offset:14592\n\t"
    "v_add_f32 v173, v176, v177\n\t"
    "v_mfma_f32_16x16x32_bf16 a[120:123], v[120:123], v[156:159], a[120:123]\n\t"
    "v_cvt_pk_bf16_f32 v100, v176, v177\n\t"
    "v_mfma_f32_16x16x32_bf16 a[116:119], v[116:119], v[156:159], a[116:119]\n\t"
    "s_cbranch_vccnz L_spin_53\n\t"
    "L_spinret_53:\n\t"
    "v_mfma_f32_16x16x32_bf16 a[112:115], v[112:115], v[156:159], a[112:115]\n\t"
    "s_waitcnt lgkmcnt(1)\n\t"
    "v_exp_f32 v178, v38\n\t"
    "v_mfma_f32_16x16x32_bf16 a[128:131], v[112:115], v[144:147], a[128:131]\n\t"
    "ds_read_b128 v[152:155], v194 offset:14848\n\t"
    "v_mfma_f32_16x16x32_bf16 a[132:135], v[116:119], v[144:147], a[132:135]\n\t"
    "s_waitcnt vmcnt(7)\n\t"
    "v_exp_f32 v179, v39\n\t"
    "v_mfma_f32_16x16x32_bf16 a[136:139], v[120:123], v[144:147], a[136:139]\n\t"
    "s_barrier\n\t"
    "v_add_f32 v173, v173, v178\n\t"
    "v_add_f32 v173, v173, v179\n\t"
    "v_mfma_f32_16x16x32_bf16 a[140:143], v[124:127], v[144:147], a[140:143]\n\t"
    "s_waitcnt lgkmcnt(1)\n\t"
    "v_cvt_pk_bf16_f32 v101, v178, v179\n\t"
    "v_mfma_f32_16x16x32_bf16 a[156:159], v[124:127], v[148:151], a[156:159]\n\t"
    "ds_read_b128 v[156:159], v194 offset:15104\n\t"
    "v_mfma_f32_16x16x32_bf16 a[152:155], v[120:123], v[148:151], a[152:155]\n\t"
    "s_add_u32 m0, s60, 36864\n\t"
    "v_exp_f32 v176, v52\n\t"
    "v_mfma_f32_16x16x32_bf16 a[148:151], v[116:119], v[148:151], a[148:151]\n\t"
    "buffer_load_dwordx4 v195, s[40:43], s65 offen offset:0 sc1 lds\n\t"
    "v_mfma_f32_16x16x32_bf16 a[144:147], v[112:115], v[148:151], a[144:147]\n\t"
    "s_waitcnt lgkmcnt(1)\n\t"
    "v_exp_f32 v177, v53\n\t"
    "v_mfma_f32_16x16x32_bf16 a[160:163], v[112:115], v[152:155], a[160:163]\n\t"
    "ds_read_b128 v[144:147], v192 offset:36864\n\t"
    "v_add_f32 v173, v173, v176\n\t"
    "v_mfma_f32_16x16x32_bf16 a[164:167], v[116:119], v[152:155], a[164:167]\n\t"
    "ds_read_b128 v[148:151], v192 offset:38400\n\t"
    "v_add_f32 v173, v173, v177\n\t"
    "v_mfma_f32_16x16x32_bf16 a[168:171], v[120:123], v[152:155], a[168:171]\n\t"
    "buffer_load_dwordx4 v195, s[40:43], s65 offen offset:1024 sc1 lds\n\t"
    "v_mfma_f32_16x16x32_bf16 a[172:175], v[124:127], v[152:155], a[172:175]\n\t"
    "s_waitcnt lgkmcnt(2)\n\t"
    "v_exp_f32 v178, v54\n\t"
    "v_mfma_f32_16x16x32_bf16 a[188:191], v[124:127], v[156:159], a[188:191]\n\t"
    "v_cvt_pk_bf16_f32 v102, v176, v177\n\t"
    "v_mfma_f32_16x16x32_bf16 a[184:187], v[120:123], v[156:159], a[184:187]\n\t"
    "v_exp_f32 v179, v55\n\t"
    "v_mfma_f32_16x16x32_bf16 a[180:183], v[116:119], v[156:159], a[180:183]\n\t"
    "v_add_f32 v173, v173, v178\n\t"
    "v_add_f32 v173, v173, v179\n\t"
    "v_mfma_f32_16x16x32_bf16 a[176:179], v[112:115], v[156:159], a[176:179]\n\t"
    "s_waitcnt lgkmcnt(1)\n\t"
    "v_cvt_pk_bf16_f32 v103, v178, v179\n\t"
    "v_mfma_f32_16x16x32_bf16 v[64:67], v[144:147], a[192:195], v[128:131]\n\t"
    "s_waitcnt lgkmcnt(0)\n\t"
    "ds_read_b128 v[152:155], v193 offset:36864\n\t"
    "v_mfma_f32_16x16x32_bf16 v[80:83], v[148:151], a[192:195], v[128:131]\n\t"
    "ds_read_b128 v[156:159], v193 offset:38400\n\t"
    "v_mfma_f32_16x16x32_bf16 v[84:87], v[148:151], a[208:211], v[132:135]\n\t"
    "v_exp_f32 v176, v40\n\t"
    "v_mfma_f32_16x16x32_bf16 v[68:71], v[144:147], a[208:211], v[132:135]\n\t"
    "v_exp_f32 v177, v41\n\t"
    "v_mfma_f32_16x16x32_bf16 v[72:75], v[144:147], a[224:227], v[136:139]\n\t"
    "buffer_load_dwordx4 v195, s[40:43], s65 offen offset:2048 sc1 lds\n\t"
    "v_mfma_f32_16x16x32_bf16 v[88:91], v[148:151], a[224:227], v[136:139]\n\t"
    "v_exp_f32 v178, v42\n\t"
    "v_mfma_f32_16x16x32_bf16 v[92:95], v[148:151], a[240:243], v[140:143]\n\t"
    "v_add_f32 v174, v176, v177\n\t"
    "v_cvt_pk_bf16_f32 v104, v176, v177\n\t"
    "v_mfma_f32_16x16x32_bf16 v[76:79], v[144:147], a[240:243], v[140:143]\n\t"
    "s_waitcnt lgkmcnt(1)\n\t"
    "v_exp_f32 v179, v43\n\t"
    "v_mfma_f32_16x16x32_bf16 v[64:67], v[152:155], a[196:199], v[64:67]\n\t"
    "s_waitcnt lgkmcnt(0)\n\t"
    "ds_read_b128 v[144:147], v192 offset:36992\n\t"
    "v_add_f32 v174, v174, v178\n\t"
    "v_mfma_f32_16x16x32_bf16 v[80:83], v[156:159], a[196:199], v[80:83]\n\t"
    "ds_read_b128 v[148:151], v192 offset:38528\n\t"
    "v_add_f32 v174, v174, v179\n\t"
    "v_mfma_f32_16x16x32_bf16 v[84:87], v[156:159], a[212:215], v[84:87]\n\t"
    "v_cvt_pk_bf16_f32 v105, v178, v179\n\t"
    "v_mfma_f32_16x16x32_bf16 v[68:71], v[152:155], a[212:215], v[68:71]\n\t"
    "v_exp_f32 v176, v56\n\t"
    "v_mfma_f32_16x16x32_bf16 v[72:75], v[152:155], a[228:231], v[72:75]\n\t"
    "v_mfma_f32_16x16x32_bf16 v[88:91], v[156:159], a[228:231], v[88:91]\n\t"
    "v_exp_f32 v177, v57\n\t"
    "v_mfma_f32_16x16x32_bf16 v[92:95], v[156:159], a[244:247], v[92:95]\n\t"
    "v_add_f32 v174, v174, v176\n\t"
    "v_add_f32 v174, v174, v177\n\t"
    "v_mfma_f32_16x16x32_bf16 v[76:79], v[152:155], a[244:247], v[76:79]\n\t"
    "s_waitcnt lgkmcnt(1)\n\t"
    "v_exp_f32 v178, v58\n\t"
    "v_mfma_f32_16x16x32_bf16 v[64:67], v[144:147], a[200:203], v[64:67]\n\t"
    "s_waitcnt lgkmcnt(0)\n\t"
    "ds_read_b128 v[152:155], v193 offset:36992\n\t"
    "v_cvt_pk_bf16_f32 v106, v176, v177\n\t"
    "v_mfma_f32_16x16x32_bf16 v[80:83], v[148:151], a[200:203], v[80:83]\n\t"
    "ds_read_b128 v[156:159], v193 offset:38528\n\t"
    "v_mfma_f32_16x16x32_bf16 v[84:87], v[148:151], a[216:219], v[84:87]\n\t"
    "v_mfma_f32_16x16x32_bf16 v[68:71], v[144:147], a[216:219], v[68:71]\n\t"
    "s_add_u32 m0, s60, 0\n\t"
    "v_exp_f32 v179, v59\n\t"
    "v_mfma_f32_16x16x32_bf16 v[72:75], v[144:147], a[232:235], v[72:75]\n\t"
    "buffer_load_dwordx4 v195, s[44:47], s64 offen offset:0 sc1 lds\n\t"
    "v_mfma_f32_16x16x32_bf16 v[88:91], v[148:151], a[232:235], v[88:91]\n\t"
    "v_exp_f32 v176, v44\n\t"
    "v_mfma_f32_16x16x32_bf16 v[92:95], v[148:151], a[248:251], v[92:95]\n\t"
    "v_add_f32 v174, v174, v178\n\t"
    "v_add_f32 v174, v174, v179\n\t"
    "v_mfma_f32_16x16x32_bf16 v[76:79], v[144:147], a[248:251], v[76:79]\n\t"
    "s_waitcnt lgkmcnt(1)\n\t"
    "v_exp_f32 v177, v45\n\t"
    "v_mfma_f32_16x16x32_bf16 v[64:67], v[152:155], a[204:207], v[64:67]\n\t"
    "s_waitcnt lgkmcnt(0)\n\t"
    "ds_read_b128 v[144:147], v192 offset:37120\n\t"
    "v_cvt_pk_bf16_f32 v107, v178, v179\n\t"
    "v_mfma_f32_16x16x32_bf16 v[80:83], v[156:159], a[204:207], v[80:83]\n\t"
    "ds_read_b128 v[148:151], v192 offset:38656\n\t"
    "v_add_f32 v175, v176, v177\n\t"
    "v_mfma_f32_16x16x32_bf16 v[84:87], v[156:159], a[220:223], v[84:87]\n\t"
    "v_cvt_pk_bf16_f32 v108, v176, v177\n\t"
    "v_mfma_f32_16x16x32_bf16 v[68:71], v[152:155], a[220:223], v[68:71]\n\t"
    "v_exp_f32 v178, v46\n\t"
    "v_mfma_f32_16x16x32_bf16 v[72:75], v[152:155], a[236:239], v[72:75]\n\t"
    "buffer_load_dwordx4 v195, s[44:47], s64 offen offset:1024 sc1 lds\n\t"
    "v_mfma_f32_16x16x32_bf16 v[88:91], v[156:159], a[236:239], v[88:91]\n\t"
    "v_exp_f32 v179, v47\n\t"
    "v_mfma_f32_16x16x32_bf16 v[92:95], v[156:159], a[252:255], v[92:95]\n\t"
    "v_add_f32 v175, v175, v178\n\t"
    "v_add_f32 v175, v175, v179\n\t"
    "v_mfma_f32_16x16x32_bf16 v[76:79], v[152:155], a[252:255], v[76:79]\n\t"
    "s_waitcnt lgkmcnt(1)\n\t"
    "v_exp_f32 v176, v60\n\t"
    "v_mfma_f32_16x16x32_bf16 v[64:67], v[144:147], v[0:3], v[64:67]\n\t"
    "s_waitcnt lgkmcnt(0)\n\t"
    "ds_read_b128 v[152:155], v193 offset:37120\n\t"
    "v_cvt_pk_bf16_f32 v109, v178, v179\n\t"
    "v_mfma_f32_16x16x32_bf16 v[80:83], v[148:151], v[0:3], v[80:83]\n\t"
    "ds_read_b128 v[156:159], v193 offset:38656\n\t"
    "v_mfma_f32_16x16x32_bf16 v[84:87], v[148:151], v[8:11], v[84:87]\n\t"
    "v_mfma_f32_16x16x32_bf16 v[68:71], v[144:147], v[8:11], v[68:71]\n\t"
    "v_exp_f32 v177, v61\n\t"
    "v_mfma_f32_16x16x32_bf16 v[72:75], v[144:147], v[16:19], v[72:75]\n\t"
    "buffer_load_dwordx4 v195, s[44:47], s64 offen offset:2048 sc1 lds\n\t"
    "v_mfma_f32_16x16x32_bf16 v[88:91], v[148:151], v[16:19], v[88:91]\n\t"
    "v_exp_f32 v178, v62\n\t"
    "v_mfma_f32_16x16x32_bf16 v[92:95], v[148:151], v[24:27], v[92:95]\n\t"
    "v_add_f32 v175, v175, v176\n\t"
    "v_add_f32 v175, v175, v177\n\t"
    "v_mfma_f32_16x16x32_bf16 v[76:79], v[144:147], v[24:27], v[76:79]\n\t"
    "s_waitcnt lgkmcnt(1)\n\t"
    "v_exp_f32 v179, v63\n\t"
    "v_mfma_f32_16x16x32_bf16 v[64:67], v[152:155], v[4:7], v[64:67]\n\t"
    "s_waitcnt lgkmcnt(0)\n\t"
    "ds_read_b128 v[144:147], v181 offset:0\n\t"
    "v_cvt_pk_bf16_f32 v110, v176, v177\n\t"
    "v_mfma_f32_16x16x32_bf16 v[80:83], v[156:159], v[4:7], v[80:83]\n\t"
    "ds_read_b128 v[148:151], v181 offset:256\n\t"
    "v_add_f32 v175, v175, v178\n\t"
    "v_mfma_f32_16x16x32_bf16 v[84:87], v[156:159], v[12:15], v[84:87]\n\t"
    "v_add_f32 v175, v175, v179\n\t"
    "v_cvt_pk_bf16_f32 v111, v178, v179\n\t"
    "v_mfma_f32_16x16x32_bf16 v[68:71], v[152:155], v[12:15], v[68:71]\n\t"
    "v_max3_f32 v180, v172, v173, v174\n\t"
    "v_max_f32 v180, v180, v175\n\t"
    "v_mfma_f32_16x16x32_bf16 v[72:75], v[152:155], v[20:23], v[72:75]\n\t"
    "v_cmp_lt_f32 vcc, s62, v180\n\t"
    "v_mfma_f32_16x16x32_bf16 v[88:91], v[156:159], v[20:23], v[88:91]\n\t"
    "v_mfma_f32_16x16x32_bf16 v[92:95], v[156:159], v[28:31], v[92:95]\n\t"
    "v_mfma_f32_16x16x32_bf16 v[76:79], v[152:155], v[28:31], v[76:79]\n\t"
    "s_waitcnt lgkmcnt(1)\n\t"
    "s_cbranch_vccnz L_stub_s54_0\n\t"
    "L_ret_s54_0:\n\t"
    "v_mfma_f32_16x16x32_bf16 a[0:3], v[96:99], v[144:147], a[0:3]\n\t"
    "ds_read_b128 v[152:155], v181 offset:512\n\t"
    "v_add_f32 v168, v168, v172\n\t"
    "v_mfma_f32_16x16x32_bf16 a[4:7], v[100:103], v[144:147], a[4:7]\n\t"
    "v_exp_f32 v176, v64\n\t"
    "v_mfma_f32_16x16x32_bf16 a[8:11], v[104:107], v[144:147], a[8:11]\n\t"
    "v_add_f32 v169, v169, v173\n\t"
    "v_add_f32 v170, v170, v174\n\t"
    "v_mfma_f32_16x16x32_bf16 a[12:15], v[108:111], v[144:147], a[12:15]\n\t"
    "s_waitcnt lgkmcnt(1)\n\t"
    "v_exp_f32 v177, v65\n\t"
    "v_mfma_f32_16x16x32_bf16 a[28:31], v[108:111], v[148:151], a[28:31]\n\t"
    "ds_read_b128 v[156:159], v181 offset:768\n\t"
    "v_add_f32 v171, v171, v175\n\t"
    "v_mfma_f32_16x16x32_bf16 a[24:27], v[104:107], v[148:151], a[24:27]\n\t"
    "v_add_f32 v172, v176, v177\n\t"
    "v_cvt_pk_bf16_f32 v112, v176, v177\n\t"
    "v_mfma_f32_16x16x32_bf16 a[20:23], v[100:103], v[148:151], a[20:23]\n\t"
    "v_mfma_f32_16x16x32_bf16 a[16:19], v[96:99], v[148:151], a[16:19]\n\t"
    "s_waitcnt lgkmcnt(1)\n\t"
    "v_exp_f32 v178, v66\n\t"
    "v_mfma_f32_16x16x32_bf16 a[32:35], v[96:99], v[152:155], a[32:35]\n\t"
    "ds_read_b128 v[144:147], v181 offset:1024\n\t"
    "v_mfma_f32_16x16x32_bf16 a[36:39], v[100:103], v[152:155], a[36:39]\n\t"
    "v_exp_f32 v179, v67\n\t"
    "v_mfma_f32_16x16x32_bf16 a[40:43], v[104:107], v[152:155], a[40:43]\n\t"
    "v_add_f32 v172, v172, v178\n\t"
    "v_add_f32 v172, v172, v179\n\t"
    "v_mfma_f32_16x16x32_bf16 a[44:47], v[108:111], v[152:155], a[44:47]\n\t"
    "s_waitcnt lgkmcnt(1)\n\t"
    "v_cvt_pk_bf16_f32 v113, v178, v179\n\t"
    "v_mfma_f32_16x16x32_bf16 a[60:63], v[108:111], v[156:159], a[60:63]\n\t"
    "ds_read_b128 v[148:151], v181 offset:1280\n\t"
    "v_mfma_f32_16x16x32_bf16 a[56:59], v[104:107], v[156:159], a[56:59]\n\t"
    "v_exp_f32 v176, v80\n\t"
    "v_mfma_f32_16x16x32_bf16 a[52:55], v[100:103], v[156:159], a[52:55]\n\t"
    "v_mfma_f32_16x16x32_bf16 a[48:51], v[96:99], v[156:159], a[48:51]\n\t"
    "s_waitcnt lgkmcnt(1)\n\t"
    "v_exp_f32 v177, v81\n\t"
    "v_mfma_f32_16x16x32_bf16 a[64:67], v[96:99], v[144:147], a[64:67]\n\t"
    "ds_read_b128 v[152:155], v181 offset:1536\n\t"
    "v_add_f32 v172, v172, v176\n\t"
    "v_mfma_f32_16x16x32_bf16 a[68:71], v[100:103], v[144:147], a[68:71]\n\t"
    "v_add_f32 v172, v172, v177\n\t"
    "v_cvt_pk_bf16_f32 v114, v176, v177\n\t"
    "v_mfma_f32_16x16x32_bf16 a[72:75], v[104:107], v[144:147], a[72:75]\n\t"
    "v_mfma_f32_16x16x32_bf16 a[76:79], v[108:111], v[144:147], a[76:79]\n\t"
    "s_waitcnt lgkmcnt(1)\n\t"
    "v_exp_f32 v178, v82\n\t"
    "v_mfma_f32_16x16x32_bf16 a[92:95], v[108:111], v[148:151], a[92:95]\n\t"
    "ds_read_b128 v[156:159], v181 offset:1792\n\t"
    "v_mfma_f32_16x16x32_bf16 a[88:91], v[104:107], v[148:151], a[88:91]\n\t"
    "v_exp_f32 v179, v83\n\t"
    "v_mfma_f32_16x16x32_bf16 a[84:87], v[100:103], v[148:151], a[84:87]\n\t"
    "v_add_f32 v172, v172, v178\n\t"
    "v_add_f32 v172, v172, v179\n\t"
    "v_mfma_f32_16x16x32_bf16 a[80:83], v[96:99], v[148:151], a[80:83]\n\t"
    "s_waitcnt lgkmcnt(1)\n\t"
    "v_cvt_pk_bf16_f32 v115, v178, v179\n\t"
    "v_mfma_f32_16x16x32_bf16 a[96:99], v[96:99], v[152:155], a[96:99]\n\t"
    "ds_read_b128 v[144:147], v181 offset:2048\n\t"
    "v_mfma_f32_16x16x32_bf16 a[100:103], v[100:103], v[152:155], a[100:103]\n\t"
    "v_exp_f32 v176, v68\n\t"
    "v_mfma_f32_16x16x32_bf16 a[104:107], v[104:107], v[152:155], a[104:107]\n\t"
    "v_mfma_f32_16x16x32_bf16 a[108:111], v[108:111], v[152:155], a[108:111]\n\t"
    "s_waitcnt lgkmcnt(1)\n\t"
    "v_exp_f32 v177, v69\n\t"
    "v_mfma_f32_16x16x32_bf16 a[124:127], v[108:111], v[156:159], a[124:127]\n\t"
    "ds_read_b128 v[148:151], v181 offset:2304\n\t"
    "v_add_f32 v173, v176, v177\n\t"
    "v_mfma_f32_16x16x32_bf16 a[120:123], v[104:107], v[156:159], a[120:123]\n\t"
    "v_cvt_pk_bf16_f32 v116, v176, v177\n\t"
    "v_mfma_f32_16x16x32_bf16 a[116:119], v[100:103], v[156:159], a[116:119]\n\t"
    "v_mfma_f32_16x16x32_bf16 a[112:115], v[96:99], v[156:159], a[112:115]\n\t"
    "s_waitcnt lgkmcnt(1)\n\t"
    "v_exp_f32 v178, v70\n\t"
    "v_mfma_f32_16x16x32_bf16 a[128:131], v[96:99], v[144:147], a[128:131]\n\t"
    "ds_read_b128 v[152:155], v181 offset:2560\n\t"
    "v_mfma_f32_16x16x32_bf16 a[132:135], v[100:103], v[144:147], a[132:135]\n\t"
    "s_waitcnt vmcnt(6)\n\t"
    "v_exp_f32 v179, v71\n\t"
    "v_mfma_f32_16x16x32_bf16 a[136:139], v[104:107], v[144:147], a[136:139]\n\t"
    "s_barrier\n\t"
    "v_add_f32 v173, v173, v178\n\t"
    "v_add_f32 v173, v173, v179\n\t"
    "v_mfma_f32_16x16x32_bf16 a[140:143], v[108:111], v[144:147], a[140:143]\n\t"
    "s_waitcnt lgkmcnt(1)\n\t"
    "v_cvt_pk_bf16_f32 v117, v178, v179\n\t"
    "v_mfma_f32_16x16x32_bf16 a[156:159], v[108:111], v[148:151], a[156:159]\n\t"
    "ds_read_b128 v[156:159], v181 offset:2816\n\t"
    "v_mfma_f32_16x16x32_bf16 a[152:155], v[104:107], v[148:151], a[152:155]\n\t"
    "s_add_u32 m0, s60, 49152\n\t"
    "v_exp_f32 v176, v84\n\t"
    "v_mfma_f32_16x16x32_bf16 a[148:151], v[100:103], v[148:151], a[148:151]\n\t"
    "buffer_load_dwordx4 v195, s[40:43], s66 offen offset:0 sc1 lds\n\t"
    "v_mfma_f32_16x16x32_bf16 a[144:147], v[96:99], v[148:151], a[144:147]\n\t"
    "s_waitcnt lgkmcnt(1)\n\t"
    "v_exp_f32 v177, v85\n\t"
    "v_mfma_f32_16x16x32_bf16 a[160:163], v[96:99], v[152:155], a[160:163]\n\t"
    "ds_read_b128 v[144:147], v192 offset:0\n\t"
    "v_add_f32 v173, v173, v176\n\t"
    "v_mfma_f32_16x16x32_bf16 a[164:167], v[100:103], v[152:155], a[164:167]\n\t"
    "ds_read_b128 v[148:151], v192 offset:1536\n\t"
    "v_add_f32 v173, v173, v177\n\t"
    "v_mfma_f32_16x16x32_bf16 a[168:171], v[104:107], v[152:155], a[168:171]\n\t"
    "buffer_load_dwordx4 v195, s[40:43], s66 offen offset:1024 sc1 lds\n\t"
    "v_mfma_f32_16x16x32_bf16 a[172:175], v[108:111], v[152:155], a[172:175]\n\t"
    "s_waitcnt lgkmcnt(2)\n\t"
    "v_exp_f32 v178, v86\n\t"
    "v_mfma_f32_16x16x32_bf16 a[188:191], v[108:111], v[156:159], a[188:191]\n\t"
    "v_cvt_pk_bf16_f32 v118, v176, v177\n\t"
    "v_mfma_f32_16x16x32_bf16 a[184:187], v[104:107], v[156:159], a[184:187]\n\t"
    "v_exp_f32 v179, v87\n\t"
    "v_mfma_f32_16x16x32_bf16 a[180:183], v[100:103], v[156:159], a[180:183]\n\t"
    "v_add_f32 v173, v173, v178\n\t"
    "v_add_f32 v173, v173, v179\n\t"
    "v_mfma_f32_16x16x32_bf16 a[176:179], v[96:99], v[156:159], a[176:179]\n\t"
    "s_waitcnt lgkmcnt(1)\n\t"
    "v_cvt_pk_bf16_f32 v119, v178, v179\n\t"
    "v_mfma_f32_16x16x32_bf16 v[32:35], v[144:147], a[192:195], v[128:131]\n\t"
    "s_waitcnt lgkmcnt(0)\n\t"
    "ds_read_b128 v[152:155], v193 offset:0\n\t"
    "v_mfma_f32_16x16x32_bf16 v[48:51], v[148:151], a[192:195], v[128:131]\n\t"
    "ds_read_b128 v[156:159], v193 offset:1536\n\t"
    "v_mfma_f32_16x16x32_bf16 v[52:55], v[148:151], a[208:211], v[132:135]\n\t"
    "v_exp_f32 v176, v72\n\t"
    "v_mfma_f32_16x16x32_bf16 v[36:39], v[144:147], a[208:211], v[132:135]\n\t"
    "v_exp_f32 v177, v73\n\t"
    "v_mfma_f32_16x16x32_bf16 v[40:43], v[144:147], a[224:227], v[136:139]\n\t"
    "buffer_load_dwordx4 v195, s[40:43], s66 offen offset:2048 sc1 lds\n\t"
    "v_mfma_f32_16x16x32_bf16 v[56:59], v[148:151], a[224:227], v[136:139]\n\t"
    "v_exp_f32 v178, v74\n\t"
    "v_mfma_f32_16x16x32_bf16 v[60:63], v[148:151], a[240:243], v[140:143]\n\t"
    "v_add_f32 v174, v176, v177\n\t"
    "v_cvt_pk_bf16_f32 v120, v176, v177\n\t"
    "v_mfma_f32_16x16x32_bf16 v[44:47], v[144:147], a[240:243], v[140:143]\n\t"
    "s_waitcnt lgkmcnt(1)\n\t"
    "v_exp_f32 v179, v75\n\t"
    "v_mfma_f32_16x16x32_bf16 v[32:35], v[152:155], a[196:199], v[32:35]\n\t"
    "s_waitcnt lgkmcnt(0)\n\t"
    "ds_read_b128 v[144:147], v192 offset:128\n\t"
    "v_add_f32 v174, v174, v178\n\t"
    "v_mfma_f32_16x16x32_bf16 v[48:51], v[156:159], a[196:199], v[48:51]\n\t"
    "ds_read_b128 v[148:151], v192 offset:1664\n\t"
    "v_add_f32 v174, v174, v179\n\t"
    "v_mfma_f32_16x16x32_bf16 v[52:55], v[156:159], a[212:215], v[52:55]\n\t"
    "v_cvt_pk_bf16_f32 v121, v178, v179\n\t"
    "v_mfma_f32_16x16x32_bf16 v[36:39], v[152:155], a[212:215], v[36:39]\n\t"
    "v_exp_f32 v176, v88\n\t"
    "v_mfma_f32_16x16x32_bf16 v[40:43], v[152:155], a[228:231], v[40:43]\n\t"
    "v_mfma_f32_16x16x32_bf16 v[56:59], v[156:159], a[228:231], v[56:59]\n\t"
    "v_exp_f32 v177, v89\n\t"
    "v_mfma_f32_16x16x32_bf16 v[60:63], v[156:159], a[244:247], v[60:63]\n\t"
    "v_add_f32 v174, v174, v176\n\t"
    "v_add_f32 v174, v174, v177\n\t"
    "v_mfma_f32_16x16x32_bf16 v[44:47], v[152:155], a[244:247], v[44:47]\n\t"
    "s_waitcnt lgkmcnt(1)\n\t"
    "v_exp_f32 v178, v90\n\t"
    "v_mfma_f32_16x16x32_bf16 v[32:35], v[144:147], a[200:203], v[32:35]\n\t"
    "s_waitcnt lgkmcnt(0)\n\t"
    "ds_read_b128 v[152:155], v193 offset:128\n\t"
    "v_cvt_pk_bf16_f32 v122, v176, v177\n\t"
    "v_mfma_f32_16x16x32_bf16 v[48:51], v[148:151], a[200:203], v[48:51]\n\t"
    "ds_read_b128 v[156:159], v193 offset:1664\n\t"
    "v_mfma_f32_16x16x32_bf16 v[52:55], v[148:151], a[216:219], v[52:55]\n\t"
    "v_mfma_f32_16x16x32_bf16 v[36:39], v[144:147], a[216:219], v[36:39]\n\t"
    "s_add_u32 m0, s60, 12288\n\t"
    "v_exp_f32 v179, v91\n\t"
    "v_mfma_f32_16x16x32_bf16 v[40:43], v[144:147], a[232:235], v[40:43]\n\t"
    "buffer_load_dwordx4 v195, s[44:47], s65 offen offset:0 sc1 lds\n\t"
    "v_mfma_f32_16x16x32_bf16 v[56:59], v[148:151], a[232:235], v[56:59]\n\t"
    "v_exp_f32 v176, v76\n\t"
    "v_mfma_f32_16x16x32_bf16 v[60:63], v[148:151], a[248:251], v[60:63]\n\t"
    "v_add_f32 v174, v174, v178\n\t"
    "v_add_f32 v174, v174, v179\n\t"
    "v_mfma_f32_16x16x32_bf16 v[44:47], v[144:147], a[248:251], v[44:47]\n\t"
    "s_waitcnt lgkmcnt(1)\n\t"
    "v_exp_f32 v177, v77\n\t"
    "v_mfma_f32_16x16x32_bf16 v[32:35], v[152:155], a[204:207], v[32:35]\n\t"
    "s_waitcnt lgkmcnt(0)\n\t"
    "ds_read_b128 v[144:147], v192 offset:256\n\t"
    "v_cvt_pk_bf16_f32 v123, v178, v179\n\t"
    "v_mfma_f32_16x16x32_bf16 v[48:51], v[156:159], a[204:207], v[48:51]\n\t"
    "ds_read_b128 v[148:151], v192 offset:1792\n\t"
    "v_add_f32 v175, v176, v177\n\t"
    "v_mfma_f32_16x16x32_bf16 v[52:55], v[156:159], a[220:223], v[52:55]\n\t"
    "v_cvt_pk_bf16_f32 v124, v176, v177\n\t"
    "v_mfma_f32_16x16x32_bf16 v[36:39], v[152:155], a[220:223], v[36:39]\n\t"
    "v_exp_f32 v178, v78\n\t"
    "v_mfma_f32_16x16x32_bf16 v[40:43], v[152:155], a[236:239], v[40:43]\n\t"
    "buffer_load_dwordx4 v195, s[44:47], s65 offen offset:1024 sc1 lds\n\t"
    "v_mfma_f32_16x16x32_bf16 v[56:59], v[156:159], a[236:239], v[56:59]\n\t"
    "v_exp_f32 v179, v79\n\t"
    "v_mfma_f32_16x16x32_bf16 v[60:63], v[156:159], a[252:255], v[60:63]\n\t"
    "v_add_f32 v175, v175, v178\n\t"
    "v_add_f32 v175, v175, v179\n\t"
    "v_mfma_f32_16x16x32_bf16 v[44:47], v[152:155], a[252:255], v[44:47]\n\t"
    "s_waitcnt lgkmcnt(1)\n\t"
    "v_exp_f32 v176, v92\n\t"
    "v_mfma_f32_16x16x32_bf16 v[32:35], v[144:147], v[0:3], v[32:35]\n\t"
    "s_waitcnt lgkmcnt(0)\n\t"
    "ds_read_b128 v[152:155], v193 offset:256\n\t"
    "v_cvt_pk_bf16_f32 v125, v178, v179\n\t"
    "v_mfma_f32_16x16x32_bf16 v[48:51], v[148:151], v[0:3], v[48:51]\n\t"
    "ds_read_b128 v[156:159], v193 offset:1792\n\t"
    "v_mfma_f32_16x16x32_bf16 v[52:55], v[148:151], v[8:11], v[52:55]\n\t"
    "v_mfma_f32_16x16x32_bf16 v[36:39], v[144:147], v[8:11], v[36:39]\n\t"
    "v_exp_f32 v177, v93\n\t"
    "v_mfma_f32_16x16x32_bf16 v[40:43], v[144:147], v[16:19], v[40:43]\n\t"
    "buffer_load_dwordx4 v195, s[44:47], s65 offen offset:2048 sc1 lds\n\t"
    "v_mfma_f32_16x16x32_bf16 v[56:59], v[148:151], v[16:19], v[56:59]\n\t"
    "v_exp_f32 v178, v94\n\t"
    "v_mfma_f32_16x16x32_bf16 v[60:63], v[148:151], v[24:27], v[60:63]\n\t"
    "v_add_f32 v175, v175, v176\n\t"
    "v_add_f32 v175, v175, v177\n\t"
    "v_mfma_f32_16x16x32_bf16 v[44:47], v[144:147], v[24:27], v[44:47]\n\t"
    "s_waitcnt lgkmcnt(1)\n\t"
    "v_exp_f32 v179, v95\n\t"
    "v_mfma_f32_16x16x32_bf16 v[32:35], v[152:155], v[4:7], v[32:35]\n\t"
    "s_waitcnt lgkmcnt(0)\n\t"
    "ds_read_b128 v[144:147], v181 offset:12288\n\t"
    "v_cvt_pk_bf16_f32 v126, v176, v177\n\t"
    "v_mfma_f32_16x16x32_bf16 v[48:51], v[156:159], v[4:7], v[48:51]\n\t"
    "ds_read_b128 v[148:151], v181 offset:12544\n\t"
    "v_add_f32 v175, v175, v178\n\t"
    "v_mfma_f32_16x16x32_bf16 v[52:55], v[156:159], v[12:15], v[52:55]\n\t"
    "v_add_f32 v175, v175, v179\n\t"
    "v_cvt_pk_bf16_f32 v127, v178, v179\n\t"
    "v_mfma_f32_16x16x32_bf16 v[36:39], v[152:155], v[12:15], v[36:39]\n\t"
    "v_max3_f32 v180, v172, v173, v174\n\t"
    "v_max_f32 v180, v180, v175\n\t"
    "v_mfma_f32_16x16x32_bf16 v[40:43], v[152:155], v[20:23], v[40:43]\n\t"
    "v_cmp_lt_f32 vcc, s62, v180\n\t"
    "v_mfma_f32_16x16x32_bf16 v[56:59], v[156:159], v[20:23], v[56:59]\n\t"
    "v_mfma_f32_16x16x32_bf16 v[60:63], v[156:159], v[28:31], v[60:63]\n\t"
    "v_mfma_f32_16x16x32_bf16 v[44:47], v[152:155], v[28:31], v[44:47]\n\t"
    "s_waitcnt lgkmcnt(1)\n\t"
    "s_cbranch_vccnz L_stub_s55_1\n\t"
    "L_ret_s55_1:\n\t"
    "v_mfma_f32_16x16x32_bf16 a[0:3], v[112:115], v[144:147], a[0:3]\n\t"
    "ds_read_b128 v[152:155], v181 offset:12800\n\t"
    "v_add_f32 v168, v168, v172\n\t"
    "v_mfma_f32_16x16x32_bf16 a[4:7], v[116:119], v[144:147], a[4:7]\n\t"
    "v_exp_f32 v176, v32\n\t"
    "v_mfma_f32_16x16x32_bf16 a[8:11], v[120:123], v[144:147], a[8:11]\n\t"
    "v_add_f32 v169, v169, v173\n\t"
    "v_add_f32 v170, v170, v174\n\t"
    "v_mfma_f32_16x16x32_bf16 a[12:15], v[124:127], v[144:147], a[12:15]\n\t"
    "s_waitcnt lgkmcnt(1)\n\t"
    "v_exp_f32 v177, v33\n\t"
    "v_mfma_f32_16x16x32_bf16 a[28:31], v[124:127], v[148:151], a[28:31]\n\t"
    "ds_read_b128 v[156:159], v181 offset:13056\n\t"
    "v_add_f32 v171, v171, v175\n\t"
    "v_mfma_f32_16x16x32_bf16 a[24:27], v[120:123], v[148:151], a[24:27]\n\t"
    "v_add_f32 v172, v176, v177\n\t"
    "v_cvt_pk_bf16_f32 v96, v176, v177\n\t"
    "v_mfma_f32_16x16x32_bf16 a[20:23], v[116:119], v[148:151], a[20:23]\n\t"
    "v_mfma_f32_16x16x32_bf16 a[16:19], v[112:115], v[148:151], a[16:19]\n\t"
    "s_waitcnt lgkmcnt(1)\n\t"
    "v_exp_f32 v178, v34\n\t"
    "v_mfma_f32_16x16x32_bf16 a[32:35], v[112:115], v[152:155], a[32:35]\n\t"
    "ds_read_b128 v[144:147], v181 offset:13312\n\t"
    "v_mfma_f32_16x16x32_bf16 a[36:39], v[116:119], v[152:155], a[36:39]\n\t"
    "v_exp_f32 v179, v35\n\t"
    "v_mfma_f32_16x16x32_bf16 a[40:43], v[120:123], v[152:155], a[40:43]\n\t"
    "v_add_f32 v172, v172, v178\n\t"
    "v_add_f32 v172, v172, v179\n\t"
    "v_mfma_f32_16x16x32_bf16 a[44:47], v[124:127], v[152:155], a[44:47]\n\t"
    "s_waitcnt lgkmcnt(1)\n\t"
    "v_cvt_pk_bf16_f32 v97, v178, v179\n\t"
    "v_mfma_f32_16x16x32_bf16 a[60:63], v[124:127], v[156:159], a[60:63]\n\t"
    "ds_read_b128 v[148:151], v181 offset:13568\n\t"
    "v_mfma_f32_16x16x32_bf16 a[56:59], v[120:123], v[156:159], a[56:59]\n\t"
    "v_exp_f32 v176, v48\n\t"
    "v_mfma_f32_16x16x32_bf16 a[52:55], v[116:119], v[156:159], a[52:55]\n\t"
    "v_mfma_f32_16x16x32_bf16 a[48:51], v[112:115], v[156:159], a[48:51]\n\t"
    "s_waitcnt lgkmcnt(1)\n\t"
    "v_exp_f32 v177, v49\n\t"
    "v_mfma_f32_16x16x32_bf16 a[64:67], v[112:115], v[144:147], a[64:67]\n\t"
    "ds_read_b128 v[152:155], v181 offset:13824\n\t"
    "v_add_f32 v172, v172, v176\n\t"
    "v_mfma_f32_16x16x32_bf16 a[68:71], v[116:119], v[144:147], a[68:71]\n\t"
    "v_add_f32 v172, v172, v177\n\t"
    "v_cvt_pk_bf16_f32 v98, v176, v177\n\t"
    "v_mfma_f32_16x16x32_bf16 a[72:75], v[120:123], v[144:147], a[72:75]\n\t"
    "v_mfma_f32_16x16x32_bf16 a[76:79], v[124:127], v[144:147], a[76:79]\n\t"
    "s_waitcnt lgkmcnt(1)\n\t"
    "v_exp_f32 v178, v50\n\t"
    "v_mfma_f32_16x16x32_bf16 a[92:95], v[124:127], v[148:151], a[92:95]\n\t"
    "ds_read_b128 v[156:159], v181 offset:14080\n\t"
    "v_mfma_f32_16x16x32_bf16 a[88:91], v[120:123], v[148:151], a[88:91]\n\t"
    "v_exp_f32 v179, v51\n\t"
    "v_mfma_f32_16x16x32_bf16 a[84:87], v[116:119], v[148:151], a[84:87]\n\t"
    "v_add_f32 v172, v172, v178\n\t"
    "v_add_f32 v172, v172, v179\n\t"
    "v_mfma_f32_16x16x32_bf16 a[80:83], v[112:115], v[148:151], a[80:83]\n\t"
    "s_waitcnt lgkmcnt(1)\n\t"
    "v_cvt_pk_bf16_f32 v99, v178, v179\n\t"
    "v_mfma_f32_16x16x32_bf16 a[96:99], v[112:115], v[152:155], a[96:99]\n\t"
    "ds_read_b128 v[144:147], v181 offset:14336\n\t"
    "v_mfma_f32_16x16x32_bf16 a[100:103], v[116:119], v[152:155], a[100:103]\n\t"
    "v_exp_f32 v176, v36\n\t"
    "v_mfma_f32_16x16x32_bf16 a[104:107], v[120:123], v[152:155], a[104:107]\n\t"
    "v_mfma_f32_16x16x32_bf16 a[108:111], v[124:127], v[152:155], a[108:111]\n\t"
    "s_waitcnt lgkmcnt(1)\n\t"
    "v_exp_f32 v177, v37\n\t"
    "v_mfma_f32_16x16x32_bf16 a[124:127], v[124:127], v[156:159], a[124:127]\n\t"
    "ds_read_b128 v[148:151], v181 offset:14592\n\t"
    "v_add_f32 v173, v176, v177\n\t"
    "v_mfma_f32_16x16x32_bf16 a[120:123], v[120:123], v[156:159], a[120:123]\n\t"
    "v_cvt_pk_bf16_f32 v100, v176, v177\n\t"
    "v_mfma_f32_16x16x32_bf16 a[116:119], v[116:119], v[156:159], a[116:119]\n\t"
    "v_mfma_f32_16x16x32_bf16 a[112:115], v[112:115], v[156:159], a[112:115]\n\t"
    "s_waitcnt lgkmcnt(1)\n\t"
    "v_exp_f32 v178, v38\n\t"
    "v_mfma_f32_16x16x32_bf16 a[128:131], v[112:115], v[144:147], a[128:131]\n\t"
    "ds_read_b128 v[152:155], v181 offset:14848\n\t"
    "v_mfma_f32_16x16x32_bf16 a[132:135], v[116:119], v[144:147], a[132:135]\n\t"
    "s_waitcnt vmcnt(6)\n\t"
    "v_exp_f32 v179, v39\n\t"
    "v_mfma_f32_16x16x32_bf16 a[136:139], v[120:123], v[144:147], a[136:139]\n\t"
    "s_barrier\n\t"
    "v_add_f32 v173, v173, v178\n\t"
    "v_add_f32 v173, v173, v179\n\t"
    "v_mfma_f32_16x16x32_bf16 a[140:143], v[124:127], v[144:147], a[140:143]\n\t"
    "s_waitcnt lgkmcnt(1)\n\t"
    "v_cvt_pk_bf16_f32 v101, v178, v179\n\t"
    "v_mfma_f32_16x16x32_bf16 a[156:159], v[124:127], v[148:151], a[156:159]\n\t"
    "ds_read_b128 v[156:159], v181 offset:15104\n\t"
    "v_mfma_f32_16x16x32_bf16 a[152:155], v[120:123], v[148:151], a[152:155]\n\t"
    "s_add_u32 m0, s60, 61440\n\t"
    "v_exp_f32 v176, v52\n\t"
    "v_mfma_f32_16x16x32_bf16 a[148:151], v[116:119], v[148:151], a[148:151]\n\t"
    "buffer_load_dwordx4 v195, s[40:43], s67 offen offset:0 sc1 lds\n\t"
    "v_mfma_f32_16x16x32_bf16 a[144:147], v[112:115], v[148:151], a[144:147]\n\t"
    "s_waitcnt lgkmcnt(1)\n\t"
    "v_exp_f32 v177, v53\n\t"
    "v_mfma_f32_16x16x32_bf16 a[160:163], v[112:115], v[152:155], a[160:163]\n\t"
    "ds_read_b128 v[144:147], v192 offset:12288\n\t"
    "v_add_f32 v173, v173, v176\n\t"
    "v_mfma_f32_16x16x32_bf16 a[164:167], v[116:119], v[152:155], a[164:167]\n\t"
    "ds_read_b128 v[148:151], v192 offset:13824\n\t"
    "v_add_f32 v173, v173, v177\n\t"
    "v_mfma_f32_16x16x32_bf16 a[168:171], v[120:123], v[152:155], a[168:171]\n\t"
    "buffer_load_dwordx4 v195, s[40:43], s67 offen offset:1024 sc1 lds\n\t"
    "v_mfma_f32_16x16x32_bf16 a[172:175], v[124:127], v[152:155], a[172:175]\n\t"
    "s_waitcnt lgkmcnt(2)\n\t"
    "v_exp_f32 v178, v54\n\t"
    "v_mfma_f32_16x16x32_bf16 a[188:191], v[124:127], v[156:159], a[188:191]\n\t"
    "v_cvt_pk_bf16_f32 v102, v176, v177\n\t"
    "v_mfma_f32_16x16x32_bf16 a[184:187], v[120:123], v[156:159], a[184:187]\n\t"
    "v_exp_f32 v179, v55\n\t"
    "v_mfma_f32_16x16x32_bf16 a[180:183], v[116:119], v[156:159], a[180:183]\n\t"
    "v_add_f32 v173, v173, v178\n\t"
    "v_add_f32 v173, v173, v179\n\t"
    "v_mfma_f32_16x16x32_bf16 a[176:179], v[112:115], v[156:159], a[176:179]\n\t"
    "s_waitcnt lgkmcnt(1)\n\t"
    "v_cvt_pk_bf16_f32 v103, v178, v179\n\t"
    "v_mfma_f32_16x16x32_bf16 v[64:67], v[144:147], a[192:195], v[128:131]\n\t"
    "s_waitcnt lgkmcnt(0)\n\t"
    "ds_read_b128 v[152:155], v193 offset:12288\n\t"
    "v_mfma_f32_16x16x32_bf16 v[80:83], v[148:151], a[192:195], v[128:131]\n\t"
    "ds_read_b128 v[156:159], v193 offset:13824\n\t"
    "v_mfma_f32_16x16x32_bf16 v[84:87], v[148:151], a[208:211], v[132:135]\n\t"
    "v_exp_f32 v176, v40\n\t"
    "v_mfma_f32_16x16x32_bf16 v[68:71], v[144:147], a[208:211], v[132:135]\n\t"
    "v_exp_f32 v177, v41\n\t"
    "v_mfma_f32_16x16x32_bf16 v[72:75], v[144:147], a[224:227], v[136:139]\n\t"
    "buffer_load_dwordx4 v195, s[40:43], s67 offen offset:2048 sc1 lds\n\t"
    "v_mfma_f32_16x16x32_bf16 v[88:91], v[148:151], a[224:227], v[136:139]\n\t"
    "v_exp_f32 v178, v42\n\t"
    "v_mfma_f32_16x16x32_bf16 v[92:95], v[148:151], a[240:243], v[140:143]\n\t"
    "v_add_f32 v174, v176, v177\n\t"
    "v_cvt_pk_bf16_f32 v104, v176, v177\n\t"
    "v_mfma_f32_16x16x32_bf16 v[76:79], v[144:147], a[240:243], v[140:143]\n\t"
    "s_waitcnt lgkmcnt(1)\n\t"
    "v_exp_f32 v179, v43\n\t"
    "v_mfma_f32_16x16x32_bf16 v[64:67], v[152:155], a[196:199], v[64:67]\n\t"
    "s_waitcnt lgkmcnt(0)\n\t"
    "ds_read_b128 v[144:147], v192 offset:12416\n\t"
    "v_add_f32 v174, v174, v178\n\t"
    "v_mfma_f32_16x16x32_bf16 v[80:83], v[156:159], a[196:199], v[80:83]\n\t"
    "ds_read_b128 v[148:151], v192 offset:13952\n\t"
    "v_add_f32 v174, v174, v179\n\t"
    "v_mfma_f32_16x16x32_bf16 v[84:87], v[156:159], a[212:215], v[84:87]\n\t"
    "v_cvt_pk_bf16_f32 v105, v178, v179\n\t"
    "v_mfma_f32_16x16x32_bf16 v[68:71], v[152:155], a[212:215], v[68:71]\n\t"
    "v_exp_f32 v176, v56\n\t"
    "v_mfma_f32_16x16x32_bf16 v[72:75], v[152:155], a[228:231], v[72:75]\n\t"
    "v_mfma_f32_16x16x32_bf16 v[88:91], v[156:159], a[228:231], v[88:91]\n\t"
    "v_exp_f32 v177, v57\n\t"
    "v_mfma_f32_16x16x32_bf16 v[92:95], v[156:159], a[244:247], v[92:95]\n\t"
    "v_add_f32 v174, v174, v176\n\t"
    "v_add_f32 v174, v174, v177\n\t"
    "v_mfma_f32_16x16x32_bf16 v[76:79], v[152:155], a[244:247], v[76:79]\n\t"
    "s_waitcnt lgkmcnt(1)\n\t"
    "v_exp_f32 v178, v58\n\t"
    "v_mfma_f32_16x16x32_bf16 v[64:67], v[144:147], a[200:203], v[64:67]\n\t"
    "s_waitcnt lgkmcnt(0)\n\t"
    "ds_read_b128 v[152:155], v193 offset:12416\n\t"
    "v_cvt_pk_bf16_f32 v106, v176, v177\n\t"
    "v_mfma_f32_16x16x32_bf16 v[80:83], v[148:151], a[200:203], v[80:83]\n\t"
    "ds_read_b128 v[156:159], v193 offset:13952\n\t"
    "v_mfma_f32_16x16x32_bf16 v[84:87], v[148:151], a[216:219], v[84:87]\n\t"
    "v_mfma_f32_16x16x32_bf16 v[68:71], v[144:147], a[216:219], v[68:71]\n\t"
    "s_add_u32 m0, s60, 73728\n\t"
    "v_exp_f32 v179, v59\n\t"
    "v_mfma_f32_16x16x32_bf16 v[72:75], v[144:147], a[232:235], v[72:75]\n\t"
    "buffer_load_dwordx4 v195, s[44:47], s66 offen offset:0 sc1 lds\n\t"
    "v_mfma_f32_16x16x32_bf16 v[88:91], v[148:151], a[232:235], v[88:91]\n\t"
    "v_exp_f32 v176, v44\n\t"
    "v_mfma_f32_16x16x32_bf16 v[92:95], v[148:151], a[248:251], v[92:95]\n\t"
    "v_add_f32 v174, v174, v178\n\t"
    "v_add_f32 v174, v174, v179\n\t"
    "v_mfma_f32_16x16x32_bf16 v[76:79], v[144:147], a[248:251], v[76:79]\n\t"
    "s_waitcnt lgkmcnt(1)\n\t"
    "v_exp_f32 v177, v45\n\t"
    "v_mfma_f32_16x16x32_bf16 v[64:67], v[152:155], a[204:207], v[64:67]\n\t"
    "s_waitcnt lgkmcnt(0)\n\t"
    "ds_read_b128 v[144:147], v192 offset:12544\n\t"
    "v_cvt_pk_bf16_f32 v107, v178, v179\n\t"
    "v_mfma_f32_16x16x32_bf16 v[80:83], v[156:159], a[204:207], v[80:83]\n\t"
    "ds_read_b128 v[148:151], v192 offset:14080\n\t"
    "v_add_f32 v175, v176, v177\n\t"
    "v_mfma_f32_16x16x32_bf16 v[84:87], v[156:159], a[220:223], v[84:87]\n\t"
    "v_cvt_pk_bf16_f32 v108, v176, v177\n\t"
    "v_mfma_f32_16x16x32_bf16 v[68:71], v[152:155], a[220:223], v[68:71]\n\t"
    "v_exp_f32 v178, v46\n\t"
    "v_mfma_f32_16x16x32_bf16 v[72:75], v[152:155], a[236:239], v[72:75]\n\t"
    "buffer_load_dwordx4 v195, s[44:47], s66 offen offset:1024 sc1 lds\n\t"
    "v_mfma_f32_16x16x32_bf16 v[88:91], v[156:159], a[236:239], v[88:91]\n\t"
    "v_exp_f32 v179, v47\n\t"
    "v_mfma_f32_16x16x32_bf16 v[92:95], v[156:159], a[252:255], v[92:95]\n\t"
    "v_add_f32 v175, v175, v178\n\t"
    "v_add_f32 v175, v175, v179\n\t"
    "v_mfma_f32_16x16x32_bf16 v[76:79], v[152:155], a[252:255], v[76:79]\n\t"
    "s_waitcnt lgkmcnt(1)\n\t"
    "v_exp_f32 v176, v60\n\t"
    "v_mfma_f32_16x16x32_bf16 v[64:67], v[144:147], v[0:3], v[64:67]\n\t"
    "s_waitcnt lgkmcnt(0)\n\t"
    "ds_read_b128 v[152:155], v193 offset:12544\n\t"
    "v_cvt_pk_bf16_f32 v109, v178, v179\n\t"
    "v_mfma_f32_16x16x32_bf16 v[80:83], v[148:151], v[0:3], v[80:83]\n\t"
    "ds_read_b128 v[156:159], v193 offset:14080\n\t"
    "v_mfma_f32_16x16x32_bf16 v[84:87], v[148:151], v[8:11], v[84:87]\n\t"
    "v_mfma_f32_16x16x32_bf16 v[68:71], v[144:147], v[8:11], v[68:71]\n\t"
    "v_exp_f32 v177, v61\n\t"
    "v_mfma_f32_16x16x32_bf16 v[72:75], v[144:147], v[16:19], v[72:75]\n\t"
    "buffer_load_dwordx4 v195, s[44:47], s66 offen offset:2048 sc1 lds\n\t"
    "v_mfma_f32_16x16x32_bf16 v[88:91], v[148:151], v[16:19], v[88:91]\n\t"
    "v_exp_f32 v178, v62\n\t"
    "v_mfma_f32_16x16x32_bf16 v[92:95], v[148:151], v[24:27], v[92:95]\n\t"
    "v_add_f32 v175, v175, v176\n\t"
    "v_add_f32 v175, v175, v177\n\t"
    "v_mfma_f32_16x16x32_bf16 v[76:79], v[144:147], v[24:27], v[76:79]\n\t"
    "s_waitcnt lgkmcnt(1)\n\t"
    "v_exp_f32 v179, v63\n\t"
    "v_mfma_f32_16x16x32_bf16 v[64:67], v[152:155], v[4:7], v[64:67]\n\t"
    "s_waitcnt lgkmcnt(0)\n\t"
    "ds_read_b128 v[144:147], v194 offset:0\n\t"
    "v_cvt_pk_bf16_f32 v110, v176, v177\n\t"
    "v_mfma_f32_16x16x32_bf16 v[80:83], v[156:159], v[4:7], v[80:83]\n\t"
    "ds_read_b128 v[148:151], v194 offset:256\n\t"
    "v_add_f32 v175, v175, v178\n\t"
    "v_mfma_f32_16x16x32_bf16 v[84:87], v[156:159], v[12:15], v[84:87]\n\t"
    "v_add_f32 v175, v175, v179\n\t"
    "v_cvt_pk_bf16_f32 v111, v178, v179\n\t"
    "v_mfma_f32_16x16x32_bf16 v[68:71], v[152:155], v[12:15], v[68:71]\n\t"
    "v_max3_f32 v180, v172, v173, v174\n\t"
    "v_max_f32 v180, v180, v175\n\t"
    "v_mfma_f32_16x16x32_bf16 v[72:75], v[152:155], v[20:23], v[72:75]\n\t"
    "v_cmp_lt_f32 vcc, s62, v180\n\t"
    "v_mfma_f32_16x16x32_bf16 v[88:91], v[156:159], v[20:23], v[88:91]\n\t"
    "v_mfma_f32_16x16x32_bf16 v[92:95], v[156:159], v[28:31], v[92:95]\n\t"
    "v_mfma_f32_16x16x32_bf16 v[76:79], v[152:155], v[28:31], v[76:79]\n\t"
    "s_waitcnt lgkmcnt(1)\n\t"
    "s_cbranch_vccnz L_stub_s56_0\n\t"
    "L_ret_s56_0:\n\t"
    "v_mfma_f32_16x16x32_bf16 a[0:3], v[96:99], v[144:147], a[0:3]\n\t"
    "ds_read_b128 v[152:155], v194 offset:512\n\t"
    "v_add_f32 v168, v168, v172\n\t"
    "v_mfma_f32_16x16x32_bf16 a[4:7], v[100:103], v[144:147], a[4:7]\n\t"
    "v_exp_f32 v176, v64\n\t"
    "v_mfma_f32_16x16x32_bf16 a[8:11], v[104:107], v[144:147], a[8:11]\n\t"
    "v_add_f32 v169, v169, v173\n\t"
    "v_add_f32 v170, v170, v174\n\t"
    "v_mfma_f32_16x16x32_bf16 a[12:15], v[108:111], v[144:147], a[12:15]\n\t"
    "s_waitcnt lgkmcnt(1)\n\t"
    "v_exp_f32 v177, v65\n\t"
    "v_mfma_f32_16x16x32_bf16 a[28:31], v[108:111], v[148:151], a[28:31]\n\t"
    "ds_read_b128 v[156:159], v194 offset:768\n\t"
    "v_add_f32 v171, v171, v175\n\t"
    "v_mfma_f32_16x16x32_bf16 a[24:27], v[104:107], v[148:151], a[24:27]\n\t"
    "v_add_f32 v172, v176, v177\n\t"
    "v_cvt_pk_bf16_f32 v112, v176, v177\n\t"
    "v_mfma_f32_16x16x32_bf16 a[20:23], v[100:103], v[148:151], a[20:23]\n\t"
    "v_mfma_f32_16x16x32_bf16 a[16:19], v[96:99], v[148:151], a[16:19]\n\t"
    "s_waitcnt lgkmcnt(1)\n\t"
    "v_exp_f32 v178, v66\n\t"
    "v_mfma_f32_16x16x32_bf16 a[32:35], v[96:99], v[152:155], a[32:35]\n\t"
    "ds_read_b128 v[144:147], v194 offset:1024\n\t"
    "v_mfma_f32_16x16x32_bf16 a[36:39], v[100:103], v[152:155], a[36:39]\n\t"
    "v_exp_f32 v179, v67\n\t"
    "v_mfma_f32_16x16x32_bf16 a[40:43], v[104:107], v[152:155], a[40:43]\n\t"
    "v_add_f32 v172, v172, v178\n\t"
    "v_add_f32 v172, v172, v179\n\t"
    "v_mfma_f32_16x16x32_bf16 a[44:47], v[108:111], v[152:155], a[44:47]\n\t"
    "s_waitcnt lgkmcnt(1)\n\t"
    "v_cvt_pk_bf16_f32 v113, v178, v179\n\t"
    "v_mfma_f32_16x16x32_bf16 a[60:63], v[108:111], v[156:159], a[60:63]\n\t"
    "ds_read_b128 v[148:151], v194 offset:1280\n\t"
    "v_mfma_f32_16x16x32_bf16 a[56:59], v[104:107], v[156:159], a[56:59]\n\t"
    "v_exp_f32 v176, v80\n\t"
    "v_mfma_f32_16x16x32_bf16 a[52:55], v[100:103], v[156:159], a[52:55]\n\t"
    "v_mfma_f32_16x16x32_bf16 a[48:51], v[96:99], v[156:159], a[48:51]\n\t"
    "s_waitcnt lgkmcnt(1)\n\t"
    "v_exp_f32 v177, v81\n\t"
    "v_mfma_f32_16x16x32_bf16 a[64:67], v[96:99], v[144:147], a[64:67]\n\t"
    "ds_read_b128 v[152:155], v194 offset:1536\n\t"
    "v_add_f32 v172, v172, v176\n\t"
    "v_mfma_f32_16x16x32_bf16 a[68:71], v[100:103], v[144:147], a[68:71]\n\t"
    "v_add_f32 v172, v172, v177\n\t"
    "v_cvt_pk_bf16_f32 v114, v176, v177\n\t"
    "v_mfma_f32_16x16x32_bf16 a[72:75], v[104:107], v[144:147], a[72:75]\n\t"
    "v_mfma_f32_16x16x32_bf16 a[76:79], v[108:111], v[144:147], a[76:79]\n\t"
    "s_waitcnt lgkmcnt(1)\n\t"
    "v_exp_f32 v178, v82\n\t"
    "v_mfma_f32_16x16x32_bf16 a[92:95], v[108:111], v[148:151], a[92:95]\n\t"
    "ds_read_b128 v[156:159], v194 offset:1792\n\t"
    "v_mfma_f32_16x16x32_bf16 a[88:91], v[104:107], v[148:151], a[88:91]\n\t"
    "v_exp_f32 v179, v83\n\t"
    "v_mfma_f32_16x16x32_bf16 a[84:87], v[100:103], v[148:151], a[84:87]\n\t"
    "v_add_f32 v172, v172, v178\n\t"
    "v_add_f32 v172, v172, v179\n\t"
    "v_mfma_f32_16x16x32_bf16 a[80:83], v[96:99], v[148:151], a[80:83]\n\t"
    "s_waitcnt lgkmcnt(1)\n\t"
    "v_cvt_pk_bf16_f32 v115, v178, v179\n\t"
    "v_mfma_f32_16x16x32_bf16 a[96:99], v[96:99], v[152:155], a[96:99]\n\t"
    "ds_read_b128 v[144:147], v194 offset:2048\n\t"
    "v_mfma_f32_16x16x32_bf16 a[100:103], v[100:103], v[152:155], a[100:103]\n\t"
    "v_exp_f32 v176, v68\n\t"
    "v_mfma_f32_16x16x32_bf16 a[104:107], v[104:107], v[152:155], a[104:107]\n\t"
    "v_mfma_f32_16x16x32_bf16 a[108:111], v[108:111], v[152:155], a[108:111]\n\t"
    "s_waitcnt lgkmcnt(1)\n\t"
    "v_exp_f32 v177, v69\n\t"
    "v_mfma_f32_16x16x32_bf16 a[124:127], v[108:111], v[156:159], a[124:127]\n\t"
    "ds_read_b128 v[148:151], v194 offset:2304\n\t"
    "v_add_f32 v173, v176, v177\n\t"
    "v_mfma_f32_16x16x32_bf16 a[120:123], v[104:107], v[156:159], a[120:123]\n\t"
    "v_cvt_pk_bf16_f32 v116, v176, v177\n\t"
    "v_mfma_f32_16x16x32_bf16 a[116:119], v[100:103], v[156:159], a[116:119]\n\t"
    "v_mfma_f32_16x16x32_bf16 a[112:115], v[96:99], v[156:159], a[112:115]\n\t"
    "s_waitcnt lgkmcnt(1)\n\t"
    "v_exp_f32 v178, v70\n\t"
    "v_mfma_f32_16x16x32_bf16 a[128:131], v[96:99], v[144:147], a[128:131]\n\t"
    "ds_read_b128 v[152:155], v194 offset:2560\n\t"
    "v_mfma_f32_16x16x32_bf16 a[132:135], v[100:103], v[144:147], a[132:135]\n\t"
    "s_waitcnt vmcnt(6)\n\t"
    "v_exp_f32 v179, v71\n\t"
    "v_mfma_f32_16x16x32_bf16 a[136:139], v[104:107], v[144:147], a[136:139]\n\t"
    "s_barrier\n\t"
    "v_add_f32 v173, v173, v178\n\t"
    "v_add_f32 v173, v173, v179\n\t"
    "v_mfma_f32_16x16x32_bf16 a[140:143], v[108:111], v[144:147], a[140:143]\n\t"
    "s_waitcnt lgkmcnt(1)\n\t"
    "v_cvt_pk_bf16_f32 v117, v178, v179\n\t"
    "v_mfma_f32_16x16x32_bf16 a[156:159], v[108:111], v[148:151], a[156:159]\n\t"
    "ds_read_b128 v[156:159], v194 offset:2816\n\t"
    "v_mfma_f32_16x16x32_bf16 a[152:155], v[104:107], v[148:151], a[152:155]\n\t"
    "s_add_u32 m0, s60, 24576\n\t"
    "v_exp_f32 v176, v84\n\t"
    "v_mfma_f32_16x16x32_bf16 a[148:151], v[100:103], v[148:151], a[148:151]\n\t"
    "buffer_load_dwordx4 v195, s[40:43], s68 offen offset:0 sc1 lds\n\t"
    "v_mfma_f32_16x16x32_bf16 a[144:147], v[96:99], v[148:151], a[144:147]\n\t"
    "s_waitcnt lgkmcnt(1)\n\t"
    "v_exp_f32 v177, v85\n\t"
    "v_mfma_f32_16x16x32_bf16 a[160:163], v[96:99], v[152:155], a[160:163]\n\t"
    "ds_read_b128 v[144:147], v192 offset:24576\n\t"
    "v_add_f32 v173, v173, v176\n\t"
    "v_mfma_f32_16x16x32_bf16 a[164:167], v[100:103], v[152:155], a[164:167]\n\t"
    "ds_read_b128 v[148:151], v192 offset:26112\n\t"
    "v_add_f32 v173, v173, v177\n\t"
    "v_mfma_f32_16x16x32_bf16 a[168:171], v[104:107], v[152:155], a[168:171]\n\t"
    "buffer_load_dwordx4 v195, s[40:43], s68 offen offset:1024 sc1 lds\n\t"
    "v_mfma_f32_16x16x32_bf16 a[172:175], v[108:111], v[152:155], a[172:175]\n\t"
    "s_waitcnt lgkmcnt(2)\n\t"
    "v_exp_f32 v178, v86\n\t"
    "v_mfma_f32_16x16x32_bf16 a[188:191], v[108:111], v[156:159], a[188:191]\n\t"
    "v_cvt_pk_bf16_f32 v118, v176, v177\n\t"
    "v_mfma_f32_16x16x32_bf16 a[184:187], v[104:107], v[156:159], a[184:187]\n\t"
    "v_exp_f32 v179, v87\n\t"
    "v_mfma_f32_16x16x32_bf16 a[180:183], v[100:103], v[156:159], a[180:183]\n\t"
    "v_add_f32 v173, v173, v178\n\t"
    "v_add_f32 v173, v173, v179\n\t"
    "v_mfma_f32_16x16x32_bf16 a[176:179], v[96:99], v[156:159], a[176:179]\n\t"
    "s_waitcnt lgkmcnt(1)\n\t"
    "v_cvt_pk_bf16_f32 v119, v178, v179\n\t"
    "v_mfma_f32_16x16x32_bf16 v[32:35], v[144:147], a[192:195], v[128:131]\n\t"
    "s_waitcnt lgkmcnt(0)\n\t"
    "ds_read_b128 v[152:155], v193 offset:24576\n\t"
    "v_mfma_f32_16x16x32_bf16 v[48:51], v[148:151], a[192:195], v[128:131]\n\t"
    "ds_read_b128 v[156:159], v193 offset:26112\n\t"
    "v_mfma_f32_16x16x32_bf16 v[52:55], v[148:151], a[208:211], v[132:135]\n\t"
    "v_exp_f32 v176, v72\n\t"
    "v_mfma_f32_16x16x32_bf16 v[36:39], v[144:147], a[208:211], v[132:135]\n\t"
    "v_exp_f32 v177, v73\n\t"
    "v_mfma_f32_16x16x32_bf16 v[40:43], v[144:147], a[224:227], v[136:139]\n\t"
    "buffer_load_dwordx4 v195, s[40:43], s68 offen offset:2048 sc1 lds\n\t"
    "v_mfma_f32_16x16x32_bf16 v[56:59], v[148:151], a[224:227], v[136:139]\n\t"
    "v_exp_f32 v178, v74\n\t"
    "v_mfma_f32_16x16x32_bf16 v[60:63], v[148:151], a[240:243], v[140:143]\n\t"
    "v_add_f32 v174, v176, v177\n\t"
    "v_cvt_pk_bf16_f32 v120, v176, v177\n\t"
    "v_mfma_f32_16x16x32_bf16 v[44:47], v[144:147], a[240:243], v[140:143]\n\t"
    "s_waitcnt lgkmcnt(1)\n\t"
    "v_exp_f32 v179, v75\n\t"
    "v_mfma_f32_16x16x32_bf16 v[32:35], v[152:155], a[196:199], v[32:35]\n\t"
    "s_waitcnt lgkmcnt(0)\n\t"
    "ds_read_b128 v[144:147], v192 offset:24704\n\t"
    "v_add_f32 v174, v174, v178\n\t"
    "v_mfma_f32_16x16x32_bf16 v[48:51], v[156:159], a[196:199], v[48:51]\n\t"
    "ds_read_b128 v[148:151], v192 offset:26240\n\t"
    "v_add_f32 v174, v174, v179\n\t"
    "v_mfma_f32_16x16x32_bf16 v[52:55], v[156:159], a[212:215], v[52:55]\n\t"
    "v_cvt_pk_bf16_f32 v121, v178, v179\n\t"
    "v_mfma_f32_16x16x32_bf16 v[36:39], v[152:155], a[212:215], v[36:39]\n\t"
    "v_exp_f32 v176, v88\n\t"
    "v_mfma_f32_16x16x32_bf16 v[40:43], v[152:155], a[228:231], v[40:43]\n\t"
    "v_mfma_f32_16x16x32_bf16 v[56:59], v[156:159], a[228:231], v[56:59]\n\t"
    "v_exp_f32 v177, v89\n\t"
    "v_mfma_f32_16x16x32_bf16 v[60:63], v[156:159], a[244:247], v[60:63]\n\t"
    "v_add_f32 v174, v174, v176\n\t"
    "v_add_f32 v174, v174, v177\n\t"
    "v_mfma_f32_16x16x32_bf16 v[44:47], v[152:155], a[244:247], v[44:47]\n\t"
    "s_waitcnt lgkmcnt(1)\n\t"
    "v_exp_f32 v178, v90\n\t"
    "v_mfma_f32_16x16x32_bf16 v[32:35], v[144:147], a[200:203], v[32:35]\n\t"
    "s_waitcnt lgkmcnt(0)\n\t"
    "ds_read_b128 v[152:155], v193 offset:24704\n\t"
    "v_cvt_pk_bf16_f32 v122, v176, v177\n\t"
    "v_mfma_f32_16x16x32_bf16 v[48:51], v[148:151], a[200:203], v[48:51]\n\t"
    "ds_read_b128 v[156:159], v193 offset:26240\n\t"
    "v_mfma_f32_16x16x32_bf16 v[52:55], v[148:151], a[216:219], v[52:55]\n\t"
    "v_mfma_f32_16x16x32_bf16 v[36:39], v[144:147], a[216:219], v[36:39]\n\t"
    "s_add_u32 m0, s60, 86016\n\t"
    "v_exp_f32 v179, v91\n\t"
    "v_mfma_f32_16x16x32_bf16 v[40:43], v[144:147], a[232:235], v[40:43]\n\t"
    "buffer_load_dwordx4 v195, s[44:47], s67 offen offset:0 sc1 lds\n\t"
    "v_mfma_f32_16x16x32_bf16 v[56:59], v[148:151], a[232:235], v[56:59]\n\t"
    "v_exp_f32 v176, v76\n\t"
    "v_mfma_f32_16x16x32_bf16 v[60:63], v[148:151], a[248:251], v[60:63]\n\t"
    "v_add_f32 v174, v174, v178\n\t"
    "v_add_f32 v174, v174, v179\n\t"
    "v_mfma_f32_16x16x32_bf16 v[44:47], v[144:147], a[248:251], v[44:47]\n\t"
    "s_waitcnt lgkmcnt(1)\n\t"
    "v_exp_f32 v177, v77\n\t"
    "v_mfma_f32_16x16x32_bf16 v[32:35], v[152:155], a[204:207], v[32:35]\n\t"
    "s_waitcnt lgkmcnt(0)\n\t"
    "ds_read_b128 v[144:147], v192 offset:24832\n\t"
    "v_cvt_pk_bf16_f32 v123, v178, v179\n\t"
    "v_mfma_f32_16x16x32_bf16 v[48:51], v[156:159], a[204:207], v[48:51]\n\t"
    "ds_read_b128 v[148:151], v192 offset:26368\n\t"
    "v_add_f32 v175, v176, v177\n\t"
    "v_mfma_f32_16x16x32_bf16 v[52:55], v[156:159], a[220:223], v[52:55]\n\t"
    "v_cvt_pk_bf16_f32 v124, v176, v177\n\t"
    "v_mfma_f32_16x16x32_bf16 v[36:39], v[152:155], a[220:223], v[36:39]\n\t"
    "v_exp_f32 v178, v78\n\t"
    "v_mfma_f32_16x16x32_bf16 v[40:43], v[152:155], a[236:239], v[40:43]\n\t"
    "buffer_load_dwordx4 v195, s[44:47], s67 offen offset:1024 sc1 lds\n\t"
    "v_mfma_f32_16x16x32_bf16 v[56:59], v[156:159], a[236:239], v[56:59]\n\t"
    "v_exp_f32 v179, v79\n\t"
    "v_mfma_f32_16x16x32_bf16 v[60:63], v[156:159], a[252:255], v[60:63]\n\t"
    "v_add_f32 v175, v175, v178\n\t"
    "v_add_f32 v175, v175, v179\n\t"
    "v_mfma_f32_16x16x32_bf16 v[44:47], v[152:155], a[252:255], v[44:47]\n\t"
    "s_waitcnt lgkmcnt(1)\n\t"
    "v_exp_f32 v176, v92\n\t"
    "v_mfma_f32_16x16x32_bf16 v[32:35], v[144:147], v[0:3], v[32:35]\n\t"
    "s_waitcnt lgkmcnt(0)\n\t"
    "ds_read_b128 v[152:155], v193 offset:24832\n\t"
    "v_cvt_pk_bf16_f32 v125, v178, v179\n\t"
    "v_mfma_f32_16x16x32_bf16 v[48:51], v[148:151], v[0:3], v[48:51]\n\t"
    "ds_read_b128 v[156:159], v193 offset:26368\n\t"
    "v_mfma_f32_16x16x32_bf16 v[52:55], v[148:151], v[8:11], v[52:55]\n\t"
    "v_mfma_f32_16x16x32_bf16 v[36:39], v[144:147], v[8:11], v[36:39]\n\t"
    "v_exp_f32 v177, v93\n\t"
    "v_mfma_f32_16x16x32_bf16 v[40:43], v[144:147], v[16:19], v[40:43]\n\t"
    "buffer_load_dwordx4 v195, s[44:47], s67 offen offset:2048 sc1 lds\n\t"
    "v_mfma_f32_16x16x32_bf16 v[56:59], v[148:151], v[16:19], v[56:59]\n\t"
    "v_exp_f32 v178, v94\n\t"
    "v_mfma_f32_16x16x32_bf16 v[60:63], v[148:151], v[24:27], v[60:63]\n\t"
    "v_add_f32 v175, v175, v176\n\t"
    "v_add_f32 v175, v175, v177\n\t"
    "v_mfma_f32_16x16x32_bf16 v[44:47], v[144:147], v[24:27], v[44:47]\n\t"
    "s_waitcnt lgkmcnt(1)\n\t"
    "v_exp_f32 v179, v95\n\t"
    "v_mfma_f32_16x16x32_bf16 v[32:35], v[152:155], v[4:7], v[32:35]\n\t"
    "s_waitcnt lgkmcnt(0)\n\t"
    "ds_read_b128 v[144:147], v194 offset:12288\n\t"
    "v_cvt_pk_bf16_f32 v126, v176, v177\n\t"
    "v_mfma_f32_16x16x32_bf16 v[48:51], v[156:159], v[4:7], v[48:51]\n\t"
    "ds_read_b128 v[148:151], v194 offset:12544\n\t"
    "v_add_f32 v175, v175, v178\n\t"
    "v_mfma_f32_16x16x32_bf16 v[52:55], v[156:159], v[12:15], v[52:55]\n\t"
    "v_add_f32 v175, v175, v179\n\t"
    "v_cvt_pk_bf16_f32 v127, v178, v179\n\t"
    "v_mfma_f32_16x16x32_bf16 v[36:39], v[152:155], v[12:15], v[36:39]\n\t"
    "v_max3_f32 v180, v172, v173, v174\n\t"
    "v_max_f32 v180, v180, v175\n\t"
    "v_mfma_f32_16x16x32_bf16 v[40:43], v[152:155], v[20:23], v[40:43]\n\t"
    "v_cmp_lt_f32 vcc, s62, v180\n\t"
    "v_mfma_f32_16x16x32_bf16 v[56:59], v[156:159], v[20:23], v[56:59]\n\t"
    "v_mfma_f32_16x16x32_bf16 v[60:63], v[156:159], v[28:31], v[60:63]\n\t"
    "v_mfma_f32_16x16x32_bf16 v[44:47], v[152:155], v[28:31], v[44:47]\n\t"
    "s_waitcnt lgkmcnt(1)\n\t"
    "s_cbranch_vccnz L_stub_s57_1\n\t"
    "L_ret_s57_1:\n\t"
    "v_mfma_f32_16x16x32_bf16 a[0:3], v[112:115], v[144:147], a[0:3]\n\t"
    "ds_read_b128 v[152:155], v194 offset:12800\n\t"
    "v_add_f32 v168, v168, v172\n\t"
    "v_mfma_f32_16x16x32_bf16 a[4:7], v[116:119], v[144:147], a[4:7]\n\t"
    "v_exp_f32 v176, v32\n\t"
    "v_mfma_f32_16x16x32_bf16 a[8:11], v[120:123], v[144:147], a[8:11]\n\t"
    "v_add_f32 v169, v169, v173\n\t"
    "v_add_f32 v170, v170, v174\n\t"
    "v_mfma_f32_16x16x32_bf16 a[12:15], v[124:127], v[144:147], a[12:15]\n\t"
    "s_waitcnt lgkmcnt(1)\n\t"
    "v_exp_f32 v177, v33\n\t"
    "v_mfma_f32_16x16x32_bf16 a[28:31], v[124:127], v[148:151], a[28:31]\n\t"
    "ds_read_b128 v[156:159], v194 offset:13056\n\t"
    "v_add_f32 v171, v171, v175\n\t"
    "v_mfma_f32_16x16x32_bf16 a[24:27], v[120:123], v[148:151], a[24:27]\n\t"
    "v_add_f32 v172, v176, v177\n\t"
    "v_cvt_pk_bf16_f32 v96, v176, v177\n\t"
    "v_mfma_f32_16x16x32_bf16 a[20:23], v[116:119], v[148:151], a[20:23]\n\t"
    "v_mfma_f32_16x16x32_bf16 a[16:19], v[112:115], v[148:151], a[16:19]\n\t"
    "s_waitcnt lgkmcnt(1)\n\t"
    "v_exp_f32 v178, v34\n\t"
    "v_mfma_f32_16x16x32_bf16 a[32:35], v[112:115], v[152:155], a[32:35]\n\t"
    "ds_read_b128 v[144:147], v194 offset:13312\n\t"
    "v_mfma_f32_16x16x32_bf16 a[36:39], v[116:119], v[152:155], a[36:39]\n\t"
    "v_exp_f32 v179, v35\n\t"
    "v_mfma_f32_16x16x32_bf16 a[40:43], v[120:123], v[152:155], a[40:43]\n\t"
    "v_add_f32 v172, v172, v178\n\t"
    "v_add_f32 v172, v172, v179\n\t"
    "v_mfma_f32_16x16x32_bf16 a[44:47], v[124:127], v[152:155], a[44:47]\n\t"
    "s_waitcnt lgkmcnt(1)\n\t"
    "v_cvt_pk_bf16_f32 v97, v178, v179\n\t"
    "v_mfma_f32_16x16x32_bf16 a[60:63], v[124:127], v[156:159], a[60:63]\n\t"
    "ds_read_b128 v[148:151], v194 offset:13568\n\t"
    "v_mfma_f32_16x16x32_bf16 a[56:59], v[120:123], v[156:159], a[56:59]\n\t"
    "v_exp_f32 v176, v48\n\t"
    "v_mfma_f32_16x16x32_bf16 a[52:55], v[116:119], v[156:159], a[52:55]\n\t"
    "v_mfma_f32_16x16x32_bf16 a[48:51], v[112:115], v[156:159], a[48:51]\n\t"
    "s_waitcnt lgkmcnt(1)\n\t"
    "v_exp_f32 v177, v49\n\t"
    "v_mfma_f32_16x16x32_bf16 a[64:67], v[112:115], v[144:147], a[64:67]\n\t"
    "ds_read_b128 v[152:155], v194 offset:13824\n\t"
    "v_add_f32 v172, v172, v176\n\t"
    "v_mfma_f32_16x16x32_bf16 a[68:71], v[116:119], v[144:147], a[68:71]\n\t"
    "v_add_f32 v172, v172, v177\n\t"
    "v_cvt_pk_bf16_f32 v98, v176, v177\n\t"
    "v_mfma_f32_16x16x32_bf16 a[72:75], v[120:123], v[144:147], a[72:75]\n\t"
    "v_mfma_f32_16x16x32_bf16 a[76:79], v[124:127], v[144:147], a[76:79]\n\t"
    "s_waitcnt lgkmcnt(1)\n\t"
    "v_exp_f32 v178, v50\n\t"
    "v_mfma_f32_16x16x32_bf16 a[92:95], v[124:127], v[148:151], a[92:95]\n\t"
    "ds_read_b128 v[156:159], v194 offset:14080\n\t"
    "v_mfma_f32_16x16x32_bf16 a[88:91], v[120:123], v[148:151], a[88:91]\n\t"
    "v_exp_f32 v179, v51\n\t"
    "v_mfma_f32_16x16x32_bf16 a[84:87], v[116:119], v[148:151], a[84:87]\n\t"
    "v_add_f32 v172, v172, v178\n\t"
    "v_add_f32 v172, v172, v179\n\t"
    "v_mfma_f32_16x16x32_bf16 a[80:83], v[112:115], v[148:151], a[80:83]\n\t"
    "s_waitcnt lgkmcnt(1)\n\t"
    "v_cvt_pk_bf16_f32 v99, v178, v179\n\t"
    "v_mfma_f32_16x16x32_bf16 a[96:99], v[112:115], v[152:155], a[96:99]\n\t"
    "ds_read_b128 v[144:147], v194 offset:14336\n\t"
    "v_mfma_f32_16x16x32_bf16 a[100:103], v[116:119], v[152:155], a[100:103]\n\t"
    "v_exp_f32 v176, v36\n\t"
    "v_mfma_f32_16x16x32_bf16 a[104:107], v[120:123], v[152:155], a[104:107]\n\t"
    "v_mfma_f32_16x16x32_bf16 a[108:111], v[124:127], v[152:155], a[108:111]\n\t"
    "s_waitcnt lgkmcnt(1)\n\t"
    "v_exp_f32 v177, v37\n\t"
    "v_mfma_f32_16x16x32_bf16 a[124:127], v[124:127], v[156:159], a[124:127]\n\t"
    "ds_read_b128 v[148:151], v194 offset:14592\n\t"
    "v_add_f32 v173, v176, v177\n\t"
    "v_mfma_f32_16x16x32_bf16 a[120:123], v[120:123], v[156:159], a[120:123]\n\t"
    "v_cvt_pk_bf16_f32 v100, v176, v177\n\t"
    "v_mfma_f32_16x16x32_bf16 a[116:119], v[116:119], v[156:159], a[116:119]\n\t"
    "v_mfma_f32_16x16x32_bf16 a[112:115], v[112:115], v[156:159], a[112:115]\n\t"
    "s_waitcnt lgkmcnt(1)\n\t"
    "v_exp_f32 v178, v38\n\t"
    "v_mfma_f32_16x16x32_bf16 a[128:131], v[112:115], v[144:147], a[128:131]\n\t"
    "ds_read_b128 v[152:155], v194 offset:14848\n\t"
    "v_mfma_f32_16x16x32_bf16 a[132:135], v[116:119], v[144:147], a[132:135]\n\t"
    "s_waitcnt vmcnt(6)\n\t"
    "v_exp_f32 v179, v39\n\t"
    "v_mfma_f32_16x16x32_bf16 a[136:139], v[120:123], v[144:147], a[136:139]\n\t"
    "s_barrier\n\t"
    "v_add_f32 v173, v173, v178\n\t"
    "v_add_f32 v173, v173, v179\n\t"
    "v_mfma_f32_16x16x32_bf16 a[140:143], v[124:127], v[144:147], a[140:143]\n\t"
    "s_waitcnt lgkmcnt(1)\n\t"
    "v_cvt_pk_bf16_f32 v101, v178, v179\n\t"
    "v_mfma_f32_16x16x32_bf16 a[156:159], v[124:127], v[148:151], a[156:159]\n\t"
    "ds_read_b128 v[156:159], v194 offset:15104\n\t"
    "v_mfma_f32_16x16x32_bf16 a[152:155], v[120:123], v[148:151], a[152:155]\n\t"
    "s_add_u32 m0, s60, 36864\n\t"
    "v_exp_f32 v176, v52\n\t"
    "v_mfma_f32_16x16x32_bf16 a[148:151], v[116:119], v[148:151], a[148:151]\n\t"
    "buffer_load_dwordx4 v195, s[40:43], s69 offen offset:0 sc1 lds\n\t"
    "v_mfma_f32_16x16x32_bf16 a[144:147], v[112:115], v[148:151], a[144:147]\n\t"
    "s_waitcnt lgkmcnt(1)\n\t"
    "v_exp_f32 v177, v53\n\t"
    "v_mfma_f32_16x16x32_bf16 a[160:163], v[112:115], v[152:155], a[160:163]\n\t"
    "ds_read_b128 v[144:147], v192 offset:36864\n\t"
    "v_add_f32 v173, v173, v176\n\t"
    "v_mfma_f32_16x16x32_bf16 a[164:167], v[116:119], v[152:155], a[164:167]\n\t"
    "ds_read_b128 v[148:151], v192 offset:38400\n\t"
    "v_add_f32 v173, v173, v177\n\t"
    "v_mfma_f32_16x16x32_bf16 a[168:171], v[120:123], v[152:155], a[168:171]\n\t"
    "buffer_load_dwordx4 v195, s[40:43], s69 offen offset:1024 sc1 lds\n\t"
    "v_mfma_f32_16x16x32_bf16 a[172:175], v[124:127], v[152:155], a[172:175]\n\t"
    "s_waitcnt lgkmcnt(2)\n\t"
    "v_exp_f32 v178, v54\n\t"
    "v_mfma_f32_16x16x32_bf16 a[188:191], v[124:127], v[156:159], a[188:191]\n\t"
    "v_cvt_pk_bf16_f32 v102, v176, v177\n\t"
    "v_mfma_f32_16x16x32_bf16 a[184:187], v[120:123], v[156:159], a[184:187]\n\t"
    "v_exp_f32 v179, v55\n\t"
    "v_mfma_f32_16x16x32_bf16 a[180:183], v[116:119], v[156:159], a[180:183]\n\t"
    "v_add_f32 v173, v173, v178\n\t"
    "v_add_f32 v173, v173, v179\n\t"
    "v_mfma_f32_16x16x32_bf16 a[176:179], v[112:115], v[156:159], a[176:179]\n\t"
    "s_waitcnt lgkmcnt(1)\n\t"
    "v_cvt_pk_bf16_f32 v103, v178, v179\n\t"
    "v_mfma_f32_16x16x32_bf16 v[64:67], v[144:147], a[192:195], v[128:131]\n\t"
    "s_waitcnt lgkmcnt(0)\n\t"
    "ds_read_b128 v[152:155], v193 offset:36864\n\t"
    "v_mfma_f32_16x16x32_bf16 v[80:83], v[148:151], a[192:195], v[128:131]\n\t"
    "ds_read_b128 v[156:159], v193 offset:38400\n\t"
    "v_mfma_f32_16x16x32_bf16 v[84:87], v[148:151], a[208:211], v[132:135]\n\t"
    "v_exp_f32 v176, v40\n\t"
    "v_mfma_f32_16x16x32_bf16 v[68:71], v[144:147], a[208:211], v[132:135]\n\t"
    "v_exp_f32 v177, v41\n\t"
    "v_mfma_f32_16x16x32_bf16 v[72:75], v[144:147], a[224:227], v[136:139]\n\t"
    "buffer_load_dwordx4 v195, s[40:43], s69 offen offset:2048 sc1 lds\n\t"
    "v_mfma_f32_16x16x32_bf16 v[88:91], v[148:151], a[224:227], v[136:139]\n\t"
    "v_exp_f32 v178, v42\n\t"
    "v_mfma_f32_16x16x32_bf16 v[92:95], v[148:151], a[240:243], v[140:143]\n\t"
    "v_add_f32 v174, v176, v177\n\t"
    "v_cvt_pk_bf16_f32 v104, v176, v177\n\t"
    "v_mfma_f32_16x16x32_bf16 v[76:79], v[144:147], a[240:243], v[140:143]\n\t"
    "s_waitcnt lgkmcnt(1)\n\t"
    "v_exp_f32 v179, v43\n\t"
    "v_mfma_f32_16x16x32_bf16 v[64:67], v[152:155], a[196:199], v[64:67]\n\t"
    "s_waitcnt lgkmcnt(0)\n\t"
    "ds_read_b128 v[144:147], v192 offset:36992\n\t"
    "v_add_f32 v174, v174, v178\n\t"
    "v_mfma_f32_16x16x32_bf16 v[80:83], v[156:159], a[196:199], v[80:83]\n\t"
    "ds_read_b128 v[148:151], v192 offset:38528\n\t"
    "v_add_f32 v174, v174, v179\n\t"
    "v_mfma_f32_16x16x32_bf16 v[84:87], v[156:159], a[212:215], v[84:87]\n\t"
    "v_cvt_pk_bf16_f32 v105, v178, v179\n\t"
    "v_mfma_f32_16x16x32_bf16 v[68:71], v[152:155], a[212:215], v[68:71]\n\t"
    "v_exp_f32 v176, v56\n\t"
    "v_mfma_f32_16x16x32_bf16 v[72:75], v[152:155], a[228:231], v[72:75]\n\t"
    "v_mfma_f32_16x16x32_bf16 v[88:91], v[156:159], a[228:231], v[88:91]\n\t"
    "v_exp_f32 v177, v57\n\t"
    "v_mfma_f32_16x16x32_bf16 v[92:95], v[156:159], a[244:247], v[92:95]\n\t"
    "v_add_f32 v174, v174, v176\n\t"
    "v_add_f32 v174, v174, v177\n\t"
    "v_mfma_f32_16x16x32_bf16 v[76:79], v[152:155], a[244:247], v[76:79]\n\t"
    "s_waitcnt lgkmcnt(1)\n\t"
    "v_exp_f32 v178, v58\n\t"
    "v_mfma_f32_16x16x32_bf16 v[64:67], v[144:147], a[200:203], v[64:67]\n\t"
    "s_waitcnt lgkmcnt(0)\n\t"
    "ds_read_b128 v[152:155], v193 offset:36992\n\t"
    "v_cvt_pk_bf16_f32 v106, v176, v177\n\t"
    "v_mfma_f32_16x16x32_bf16 v[80:83], v[148:151], a[200:203], v[80:83]\n\t"
    "ds_read_b128 v[156:159], v193 offset:38528\n\t"
    "v_mfma_f32_16x16x32_bf16 v[84:87], v[148:151], a[216:219], v[84:87]\n\t"
    "v_mfma_f32_16x16x32_bf16 v[68:71], v[144:147], a[216:219], v[68:71]\n\t"
    "s_add_u32 m0, s60, 0\n\t"
    "v_exp_f32 v179, v59\n\t"
    "v_mfma_f32_16x16x32_bf16 v[72:75], v[144:147], a[232:235], v[72:75]\n\t"
    "buffer_load_dwordx4 v195, s[44:47], s68 offen offset:0 sc1 lds\n\t"
    "v_mfma_f32_16x16x32_bf16 v[88:91], v[148:151], a[232:235], v[88:91]\n\t"
    "v_exp_f32 v176, v44\n\t"
    "v_mfma_f32_16x16x32_bf16 v[92:95], v[148:151], a[248:251], v[92:95]\n\t"
    "v_add_f32 v174, v174, v178\n\t"
    "v_add_f32 v174, v174, v179\n\t"
    "v_mfma_f32_16x16x32_bf16 v[76:79], v[144:147], a[248:251], v[76:79]\n\t"
    "s_waitcnt lgkmcnt(1)\n\t"
    "v_exp_f32 v177, v45\n\t"
    "v_mfma_f32_16x16x32_bf16 v[64:67], v[152:155], a[204:207], v[64:67]\n\t"
    "s_waitcnt lgkmcnt(0)\n\t"
    "ds_read_b128 v[144:147], v192 offset:37120\n\t"
    "v_cvt_pk_bf16_f32 v107, v178, v179\n\t"
    "v_mfma_f32_16x16x32_bf16 v[80:83], v[156:159], a[204:207], v[80:83]\n\t"
    "ds_read_b128 v[148:151], v192 offset:38656\n\t"
    "v_add_f32 v175, v176, v177\n\t"
    "v_mfma_f32_16x16x32_bf16 v[84:87], v[156:159], a[220:223], v[84:87]\n\t"
    "v_cvt_pk_bf16_f32 v108, v176, v177\n\t"
    "v_mfma_f32_16x16x32_bf16 v[68:71], v[152:155], a[220:223], v[68:71]\n\t"
    "v_exp_f32 v178, v46\n\t"
    "v_mfma_f32_16x16x32_bf16 v[72:75], v[152:155], a[236:239], v[72:75]\n\t"
    "buffer_load_dwordx4 v195, s[44:47], s68 offen offset:1024 sc1 lds\n\t"
    "v_mfma_f32_16x16x32_bf16 v[88:91], v[156:159], a[236:239], v[88:91]\n\t"
    "v_exp_f32 v179, v47\n\t"
    "v_mfma_f32_16x16x32_bf16 v[92:95], v[156:159], a[252:255], v[92:95]\n\t"
    "v_add_f32 v175, v175, v178\n\t"
    "v_add_f32 v175, v175, v179\n\t"
    "v_mfma_f32_16x16x32_bf16 v[76:79], v[152:155], a[252:255], v[76:79]\n\t"
    "s_waitcnt lgkmcnt(1)\n\t"
    "v_exp_f32 v176, v60\n\t"
    "v_mfma_f32_16x16x32_bf16 v[64:67], v[144:147], v[0:3], v[64:67]\n\t"
    "s_waitcnt lgkmcnt(0)\n\t"
    "ds_read_b128 v[152:155], v193 offset:37120\n\t"
    "v_cvt_pk_bf16_f32 v109, v178, v179\n\t"
    "v_mfma_f32_16x16x32_bf16 v[80:83], v[148:151], v[0:3], v[80:83]\n\t"
    "ds_read_b128 v[156:159], v193 offset:38656\n\t"
    "v_mfma_f32_16x16x32_bf16 v[84:87], v[148:151], v[8:11], v[84:87]\n\t"
    "v_mfma_f32_16x16x32_bf16 v[68:71], v[144:147], v[8:11], v[68:71]\n\t"
    "v_exp_f32 v177, v61\n\t"
    "v_mfma_f32_16x16x32_bf16 v[72:75], v[144:147], v[16:19], v[72:75]\n\t"
    "buffer_load_dwordx4 v195, s[44:47], s68 offen offset:2048 sc1 lds\n\t"
    "v_mfma_f32_16x16x32_bf16 v[88:91], v[148:151], v[16:19], v[88:91]\n\t"
    "v_exp_f32 v178, v62\n\t"
    "v_mfma_f32_16x16x32_bf16 v[92:95], v[148:151], v[24:27], v[92:95]\n\t"
    "v_add_f32 v175, v175, v176\n\t"
    "v_add_f32 v175, v175, v177\n\t"
    "v_mfma_f32_16x16x32_bf16 v[76:79], v[144:147], v[24:27], v[76:79]\n\t"
    "s_waitcnt lgkmcnt(1)\n\t"
    "v_exp_f32 v179, v63\n\t"
    "v_mfma_f32_16x16x32_bf16 v[64:67], v[152:155], v[4:7], v[64:67]\n\t"
    "s_waitcnt lgkmcnt(0)\n\t"
    "ds_read_b128 v[144:147], v181 offset:0\n\t"
    "v_cvt_pk_bf16_f32 v110, v176, v177\n\t"
    "v_mfma_f32_16x16x32_bf16 v[80:83], v[156:159], v[4:7], v[80:83]\n\t"
    "ds_read_b128 v[148:151], v181 offset:256\n\t"
    "v_add_f32 v175, v175, v178\n\t"
    "v_mfma_f32_16x16x32_bf16 v[84:87], v[156:159], v[12:15], v[84:87]\n\t"
    "v_add_f32 v175, v175, v179\n\t"
    "v_cvt_pk_bf16_f32 v111, v178, v179\n\t"
    "v_mfma_f32_16x16x32_bf16 v[68:71], v[152:155], v[12:15], v[68:71]\n\t"
    "v_max3_f32 v180, v172, v173, v174\n\t"
    "v_max_f32 v180, v180, v175\n\t"
    "v_mfma_f32_16x16x32_bf16 v[72:75], v[152:155], v[20:23], v[72:75]\n\t"
    "v_cmp_lt_f32 vcc, s62, v180\n\t"
    "v_mfma_f32_16x16x32_bf16 v[88:91], v[156:159], v[20:23], v[88:91]\n\t"
    "v_mfma_f32_16x16x32_bf16 v[92:95], v[156:159], v[28:31], v[92:95]\n\t"
    "v_mfma_f32_16x16x32_bf16 v[76:79], v[152:155], v[28:31], v[76:79]\n\t"
    "s_waitcnt lgkmcnt(1)\n\t"
    "s_cbranch_vccnz L_stub_s58_0\n\t"
    "L_ret_s58_0:\n\t"
    "v_mfma_f32_16x16x32_bf16 a[0:3], v[96:99], v[144:147], a[0:3]\n\t"
    "ds_read_b128 v[152:155], v181 offset:512\n\t"
    "v_add_f32 v168, v168, v172\n\t"
    "v_mfma_f32_16x16x32_bf16 a[4:7], v[100:103], v[144:147], a[4:7]\n\t"
    "v_exp_f32 v176, v64\n\t"
    "v_mfma_f32_16x16x32_bf16 a[8:11], v[104:107], v[144:147], a[8:11]\n\t"
    "v_add_f32 v169, v169, v173\n\t"
    "v_add_f32 v170, v170, v174\n\t"
    "v_mfma_f32_16x16x32_bf16 a[12:15], v[108:111], v[144:147], a[12:15]\n\t"
    "s_waitcnt lgkmcnt(1)\n\t"
    "v_exp_f32 v177, v65\n\t"
    "v_mfma_f32_16x16x32_bf16 a[28:31], v[108:111], v[148:151], a[28:31]\n\t"
    "ds_read_b128 v[156:159], v181 offset:768\n\t"
    "v_add_f32 v171, v171, v175\n\t"
    "v_mfma_f32_16x16x32_bf16 a[24:27], v[104:107], v[148:151], a[24:27]\n\t"
    "v_add_f32 v172, v176, v177\n\t"
    "v_cvt_pk_bf16_f32 v112, v176, v177\n\t"
    "v_mfma_f32_16x16x32_bf16 a[20:23], v[100:103], v[148:151], a[20:23]\n\t"
    "v_mfma_f32_16x16x32_bf16 a[16:19], v[96:99], v[148:151], a[16:19]\n\t"
    "s_waitcnt lgkmcnt(1)\n\t"
    "v_exp_f32 v178, v66\n\t"
    "v_mfma_f32_16x16x32_bf16 a[32:35], v[96:99], v[152:155], a[32:35]\n\t"
    "ds_read_b128 v[144:147], v181 offset:1024\n\t"
    "v_mfma_f32_16x16x32_bf16 a[36:39], v[100:103], v[152:155], a[36:39]\n\t"
    "v_exp_f32 v179, v67\n\t"
    "v_mfma_f32_16x16x32_bf16 a[40:43], v[104:107], v[152:155], a[40:43]\n\t"
    "v_add_f32 v172, v172, v178\n\t"
    "v_add_f32 v172, v172, v179\n\t"
    "v_mfma_f32_16x16x32_bf16 a[44:47], v[108:111], v[152:155], a[44:47]\n\t"
    "s_waitcnt lgkmcnt(1)\n\t"
    "v_cvt_pk_bf16_f32 v113, v178, v179\n\t"
    "v_mfma_f32_16x16x32_bf16 a[60:63], v[108:111], v[156:159], a[60:63]\n\t"
    "ds_read_b128 v[148:151], v181 offset:1280\n\t"
    "v_mfma_f32_16x16x32_bf16 a[56:59], v[104:107], v[156:159], a[56:59]\n\t"
    "v_exp_f32 v176, v80\n\t"
    "v_mfma_f32_16x16x32_bf16 a[52:55], v[100:103], v[156:159], a[52:55]\n\t"
    "v_mfma_f32_16x16x32_bf16 a[48:51], v[96:99], v[156:159], a[48:51]\n\t"
    "s_waitcnt lgkmcnt(1)\n\t"
    "v_exp_f32 v177, v81\n\t"
    "v_mfma_f32_16x16x32_bf16 a[64:67], v[96:99], v[144:147], a[64:67]\n\t"
    "ds_read_b128 v[152:155], v181 offset:1536\n\t"
    "v_add_f32 v172, v172, v176\n\t"
    "v_mfma_f32_16x16x32_bf16 a[68:71], v[100:103], v[144:147], a[68:71]\n\t"
    "v_add_f32 v172, v172, v177\n\t"
    "v_cvt_pk_bf16_f32 v114, v176, v177\n\t"
    "v_mfma_f32_16x16x32_bf16 a[72:75], v[104:107], v[144:147], a[72:75]\n\t"
    "v_mfma_f32_16x16x32_bf16 a[76:79], v[108:111], v[144:147], a[76:79]\n\t"
    "s_waitcnt lgkmcnt(1)\n\t"
    "v_exp_f32 v178, v82\n\t"
    "v_mfma_f32_16x16x32_bf16 a[92:95], v[108:111], v[148:151], a[92:95]\n\t"
    "ds_read_b128 v[156:159], v181 offset:1792\n\t"
    "v_mfma_f32_16x16x32_bf16 a[88:91], v[104:107], v[148:151], a[88:91]\n\t"
    "v_exp_f32 v179, v83\n\t"
    "v_mfma_f32_16x16x32_bf16 a[84:87], v[100:103], v[148:151], a[84:87]\n\t"
    "v_add_f32 v172, v172, v178\n\t"
    "v_add_f32 v172, v172, v179\n\t"
    "v_mfma_f32_16x16x32_bf16 a[80:83], v[96:99], v[148:151], a[80:83]\n\t"
    "s_waitcnt lgkmcnt(1)\n\t"
    "v_cvt_pk_bf16_f32 v115, v178, v179\n\t"
    "v_mfma_f32_16x16x32_bf16 a[96:99], v[96:99], v[152:155], a[96:99]\n\t"
    "ds_read_b128 v[144:147], v181 offset:2048\n\t"
    "v_mfma_f32_16x16x32_bf16 a[100:103], v[100:103], v[152:155], a[100:103]\n\t"
    "v_exp_f32 v176, v68\n\t"
    "v_mfma_f32_16x16x32_bf16 a[104:107], v[104:107], v[152:155], a[104:107]\n\t"
    "v_mfma_f32_16x16x32_bf16 a[108:111], v[108:111], v[152:155], a[108:111]\n\t"
    "s_waitcnt lgkmcnt(1)\n\t"
    "v_exp_f32 v177, v69\n\t"
    "v_mfma_f32_16x16x32_bf16 a[124:127], v[108:111], v[156:159], a[124:127]\n\t"
    "ds_read_b128 v[148:151], v181 offset:2304\n\t"
    "v_add_f32 v173, v176, v177\n\t"
    "v_mfma_f32_16x16x32_bf16 a[120:123], v[104:107], v[156:159], a[120:123]\n\t"
    "v_cvt_pk_bf16_f32 v116, v176, v177\n\t"
    "v_mfma_f32_16x16x32_bf16 a[116:119], v[100:103], v[156:159], a[116:119]\n\t"
    "v_mfma_f32_16x16x32_bf16 a[112:115], v[96:99], v[156:159], a[112:115]\n\t"
    "s_waitcnt lgkmcnt(1)\n\t"
    "v_exp_f32 v178, v70\n\t"
    "v_mfma_f32_16x16x32_bf16 a[128:131], v[96:99], v[144:147], a[128:131]\n\t"
    "ds_read_b128 v[152:155], v181 offset:2560\n\t"
    "v_mfma_f32_16x16x32_bf16 a[132:135], v[100:103], v[144:147], a[132:135]\n\t"
    "s_waitcnt vmcnt(6)\n\t"
    "v_exp_f32 v179, v71\n\t"
    "v_mfma_f32_16x16x32_bf16 a[136:139], v[104:107], v[144:147], a[136:139]\n\t"
    "s_barrier\n\t"
    "v_add_f32 v173, v173, v178\n\t"
    "v_add_f32 v173, v173, v179\n\t"
    "v_mfma_f32_16x16x32_bf16 a[140:143], v[108:111], v[144:147], a[140:143]\n\t"
    "s_waitcnt lgkmcnt(1)\n\t"
    "v_cvt_pk_bf16_f32 v117, v178, v179\n\t"
    "v_mfma_f32_16x16x32_bf16 a[156:159], v[108:111], v[148:151], a[156:159]\n\t"
    "ds_read_b128 v[156:159], v181 offset:2816\n\t"
    "v_mfma_f32_16x16x32_bf16 a[152:155], v[104:107], v[148:151], a[152:155]\n\t"
    "s_add_u32 m0, s60, 49152\n\t"
    "v_exp_f32 v176, v84\n\t"
    "v_mfma_f32_16x16x32_bf16 a[148:151], v[100:103], v[148:151], a[148:151]\n\t"
    "buffer_load_dwordx4 v195, s[40:43], s70 offen offset:0 sc1 lds\n\t"
    "v_mfma_f32_16x16x32_bf16 a[144:147], v[96:99], v[148:151], a[144:147]\n\t"
    "s_waitcnt lgkmcnt(1)\n\t"
    "v_exp_f32 v177, v85\n\t"
    "v_mfma_f32_16x16x32_bf16 a[160:163], v[96:99], v[152:155], a[160:163]\n\t"
    "ds_read_b128 v[144:147], v192 offset:0\n\t"
    "v_add_f32 v173, v173, v176\n\t"
    "v_mfma_f32_16x16x32_bf16 a[164:167], v[100:103], v[152:155], a[164:167]\n\t"
    "ds_read_b128 v[148:151], v192 offset:1536\n\t"
    "v_add_f32 v173, v173, v177\n\t"
    "v_mfma_f32_16x16x32_bf16 a[168:171], v[104:107], v[152:155], a[168:171]\n\t"
    "buffer_load_dwordx4 v195, s[40:43], s70 offen offset:1024 sc1 lds\n\t"
    "v_mfma_f32_16x16x32_bf16 a[172:175], v[108:111], v[152:155], a[172:175]\n\t"
    "s_waitcnt lgkmcnt(2)\n\t"
    "v_exp_f32 v178, v86\n\t"
    "v_mfma_f32_16x16x32_bf16 a[188:191], v[108:111], v[156:159], a[188:191]\n\t"
    "v_cvt_pk_bf16_f32 v118, v176, v177\n\t"
    "v_mfma_f32_16x16x32_bf16 a[184:187], v[104:107], v[156:159], a[184:187]\n\t"
    "v_exp_f32 v179, v87\n\t"
    "v_mfma_f32_16x16x32_bf16 a[180:183], v[100:103], v[156:159], a[180:183]\n\t"
    "v_add_f32 v173, v173, v178\n\t"
    "v_add_f32 v173, v173, v179\n\t"
    "v_mfma_f32_16x16x32_bf16 a[176:179], v[96:99], v[156:159], a[176:179]\n\t"
    "s_waitcnt lgkmcnt(1)\n\t"
    "v_cvt_pk_bf16_f32 v119, v178, v179\n\t"
    "v_mfma_f32_16x16x32_bf16 v[32:35], v[144:147], a[192:195], v[128:131]\n\t"
    "s_waitcnt lgkmcnt(0)\n\t"
    "ds_read_b128 v[152:155], v193 offset:0\n\t"
    "v_mfma_f32_16x16x32_bf16 v[48:51], v[148:151], a[192:195], v[128:131]\n\t"
    "ds_read_b128 v[156:159], v193 offset:1536\n\t"
    "v_mfma_f32_16x16x32_bf16 v[52:55], v[148:151], a[208:211], v[132:135]\n\t"
    "v_exp_f32 v176, v72\n\t"
    "v_mfma_f32_16x16x32_bf16 v[36:39], v[144:147], a[208:211], v[132:135]\n\t"
    "v_exp_f32 v177, v73\n\t"
    "v_mfma_f32_16x16x32_bf16 v[40:43], v[144:147], a[224:227], v[136:139]\n\t"
    "buffer_load_dwordx4 v195, s[40:43], s70 offen offset:2048 sc1 lds\n\t"
    "v_mfma_f32_16x16x32_bf16 v[56:59], v[148:151], a[224:227], v[136:139]\n\t"
    "v_exp_f32 v178, v74\n\t"
    "v_mfma_f32_16x16x32_bf16 v[60:63], v[148:151], a[240:243], v[140:143]\n\t"
    "v_add_f32 v174, v176, v177\n\t"
    "v_cvt_pk_bf16_f32 v120, v176, v177\n\t"
    "v_mfma_f32_16x16x32_bf16 v[44:47], v[144:147], a[240:243], v[140:143]\n\t"
    "s_waitcnt lgkmcnt(1)\n\t"
    "v_exp_f32 v179, v75\n\t"
    "v_mfma_f32_16x16x32_bf16 v[32:35], v[152:155], a[196:199], v[32:35]\n\t"
    "s_waitcnt lgkmcnt(0)\n\t"
    "ds_read_b128 v[144:147], v192 offset:128\n\t"
    "v_add_f32 v174, v174, v178\n\t"
    "v_mfma_f32_16x16x32_bf16 v[48:51], v[156:159], a[196:199], v[48:51]\n\t"
    "ds_read_b128 v[148:151], v192 offset:1664\n\t"
    "v_add_f32 v174, v174, v179\n\t"
    "v_mfma_f32_16x16x32_bf16 v[52:55], v[156:159], a[212:215], v[52:55]\n\t"
    "v_cvt_pk_bf16_f32 v121, v178, v179\n\t"
    "v_mfma_f32_16x16x32_bf16 v[36:39], v[152:155], a[212:215], v[36:39]\n\t"
    "v_exp_f32 v176, v88\n\t"
    "v_mfma_f32_16x16x32_bf16 v[40:43], v[152:155], a[228:231], v[40:43]\n\t"
    "v_mfma_f32_16x16x32_bf16 v[56:59], v[156:159], a[228:231], v[56:59]\n\t"
    "v_exp_f32 v177, v89\n\t"
    "v_mfma_f32_16x16x32_bf16 v[60:63], v[156:159], a[244:247], v[60:63]\n\t"
    "v_add_f32 v174, v174, v176\n\t"
    "v_add_f32 v174, v174, v177\n\t"
    "v_mfma_f32_16x16x32_bf16 v[44:47], v[152:155], a[244:247], v[44:47]\n\t"
    "s_waitcnt lgkmcnt(1)\n\t"
    "v_exp_f32 v178, v90\n\t"
    "v_mfma_f32_16x16x32_bf16 v[32:35], v[144:147], a[200:203], v[32:35]\n\t"
    "s_waitcnt lgkmcnt(0)\n\t"
    "ds_read_b128 v[152:155], v193 offset:128\n\t"
    "v_cvt_pk_bf16_f32 v122, v176, v177\n\t"
    "v_mfma_f32_16x16x32_bf16 v[48:51], v[148:151], a[200:203], v[48:51]\n\t"
    "ds_read_b128 v[156:159], v193 offset:1664\n\t"
    "v_mfma_f32_16x16x32_bf16 v[52:55], v[148:151], a[216:219], v[52:55]\n\t"
    "v_mfma_f32_16x16x32_bf16 v[36:39], v[144:147], a[216:219], v[36:39]\n\t"
    "s_add_u32 m0, s60, 12288\n\t"
    "v_exp_f32 v179, v91\n\t"
    "v_mfma_f32_16x16x32_bf16 v[40:43], v[144:147], a[232:235], v[40:43]\n\t"
    "buffer_load_dwordx4 v195, s[44:47], s69 offen offset:0 sc1 lds\n\t"
    "v_mfma_f32_16x16x32_bf16 v[56:59], v[148:151], a[232:235], v[56:59]\n\t"
    "v_exp_f32 v176, v76\n\t"
    "v_mfma_f32_16x16x32_bf16 v[60:63], v[148:151], a[248:251], v[60:63]\n\t"
    "v_add_f32 v174, v174, v178\n\t"
    "v_add_f32 v174, v174, v179\n\t"
    "v_mfma_f32_16x16x32_bf16 v[44:47], v[144:147], a[248:251], v[44:47]\n\t"
    "s_waitcnt lgkmcnt(1)\n\t"
    "v_exp_f32 v177, v77\n\t"
    "v_mfma_f32_16x16x32_bf16 v[32:35], v[152:155], a[204:207], v[32:35]\n\t"
    "s_waitcnt lgkmcnt(0)\n\t"
    "ds_read_b128 v[144:147], v192 offset:256\n\t"
    "v_cvt_pk_bf16_f32 v123, v178, v179\n\t"
    "v_mfma_f32_16x16x32_bf16 v[48:51], v[156:159], a[204:207], v[48:51]\n\t"
    "ds_read_b128 v[148:151], v192 offset:1792\n\t"
    "v_add_f32 v175, v176, v177\n\t"
    "v_mfma_f32_16x16x32_bf16 v[52:55], v[156:159], a[220:223], v[52:55]\n\t"
    "v_cvt_pk_bf16_f32 v124, v176, v177\n\t"
    "v_mfma_f32_16x16x32_bf16 v[36:39], v[152:155], a[220:223], v[36:39]\n\t"
    "v_exp_f32 v178, v78\n\t"
    "v_mfma_f32_16x16x32_bf16 v[40:43], v[152:155], a[236:239], v[40:43]\n\t"
    "buffer_load_dwordx4 v195, s[44:47], s69 offen offset:1024 sc1 lds\n\t"
    "v_mfma_f32_16x16x32_bf16 v[56:59], v[156:159], a[236:239], v[56:59]\n\t"
    "v_exp_f32 v179, v79\n\t"
    "v_mfma_f32_16x16x32_bf16 v[60:63], v[156:159], a[252:255], v[60:63]\n\t"
    "v_add_f32 v175, v175, v178\n\t"
    "v_add_f32 v175, v175, v179\n\t"
    "v_mfma_f32_16x16x32_bf16 v[44:47], v[152:155], a[252:255], v[44:47]\n\t"
    "s_waitcnt lgkmcnt(1)\n\t"
    "v_exp_f32 v176, v92\n\t"
    "v_mfma_f32_16x16x32_bf16 v[32:35], v[144:147], v[0:3], v[32:35]\n\t"
    "s_waitcnt lgkmcnt(0)\n\t"
    "ds_read_b128 v[152:155], v193 offset:256\n\t"
    "v_cvt_pk_bf16_f32 v125, v178, v179\n\t"
    "v_mfma_f32_16x16x32_bf16 v[48:51], v[148:151], v[0:3], v[48:51]\n\t"
    "ds_read_b128 v[156:159], v193 offset:1792\n\t"
    "v_mfma_f32_16x16x32_bf16 v[52:55], v[148:151], v[8:11], v[52:55]\n\t"
    "v_mfma_f32_16x16x32_bf16 v[36:39], v[144:147], v[8:11], v[36:39]\n\t"
    "v_exp_f32 v177, v93\n\t"
    "v_mfma_f32_16x16x32_bf16 v[40:43], v[144:147], v[16:19], v[40:43]\n\t"
    "buffer_load_dwordx4 v195, s[44:47], s69 offen offset:2048 sc1 lds\n\t"
    "v_mfma_f32_16x16x32_bf16 v[56:59], v[148:151], v[16:19], v[56:59]\n\t"
    "v_exp_f32 v178, v94\n\t"
    "v_mfma_f32_16x16x32_bf16 v[60:63], v[148:151], v[24:27], v[60:63]\n\t"
    "v_add_f32 v175, v175, v176\n\t"
    "v_add_f32 v175, v175, v177\n\t"
    "v_mfma_f32_16x16x32_bf16 v[44:47], v[144:147], v[24:27], v[44:47]\n\t"
    "s_waitcnt lgkmcnt(1)\n\t"
    "v_exp_f32 v179, v95\n\t"
    "v_mfma_f32_16x16x32_bf16 v[32:35], v[152:155], v[4:7], v[32:35]\n\t"
    "s_waitcnt lgkmcnt(0)\n\t"
    "ds_read_b128 v[144:147], v181 offset:12288\n\t"
    "v_cvt_pk_bf16_f32 v126, v176, v177\n\t"
    "v_mfma_f32_16x16x32_bf16 v[48:51], v[156:159], v[4:7], v[48:51]\n\t"
    "ds_read_b128 v[148:151], v181 offset:12544\n\t"
    "v_add_f32 v175, v175, v178\n\t"
    "v_mfma_f32_16x16x32_bf16 v[52:55], v[156:159], v[12:15], v[52:55]\n\t"
    "v_add_f32 v175, v175, v179\n\t"
    "v_cvt_pk_bf16_f32 v127, v178, v179\n\t"
    "v_mfma_f32_16x16x32_bf16 v[36:39], v[152:155], v[12:15], v[36:39]\n\t"
    "v_max3_f32 v180, v172, v173, v174\n\t"
    "v_max_f32 v180, v180, v175\n\t"
    "v_mfma_f32_16x16x32_bf16 v[40:43], v[152:155], v[20:23], v[40:43]\n\t"
    "v_cmp_lt_f32 vcc, s62, v180\n\t"
    "v_mfma_f32_16x16x32_bf16 v[56:59], v[156:159], v[20:23], v[56:59]\n\t"
    "v_mfma_f32_16x16x32_bf16 v[60:63], v[156:159], v[28:31], v[60:63]\n\t"
    "v_mfma_f32_16x16x32_bf16 v[44:47], v[152:155], v[28:31], v[44:47]\n\t"
    "s_waitcnt lgkmcnt(1)\n\t"
    "s_cbranch_vccnz L_stub_s59_1\n\t"
    "L_ret_s59_1:\n\t"
    "v_mfma_f32_16x16x32_bf16 a[0:3], v[112:115], v[144:147], a[0:3]\n\t"
    "ds_read_b128 v[152:155], v181 offset:12800\n\t"
    "v_add_f32 v168, v168, v172\n\t"
    "v_mfma_f32_16x16x32_bf16 a[4:7], v[116:119], v[144:147], a[4:7]\n\t"
    "v_exp_f32 v176, v32\n\t"
    "v_mfma_f32_16x16x32_bf16 a[8:11], v[120:123], v[144:147], a[8:11]\n\t"
    "v_add_f32 v169, v169, v173\n\t"
    "v_add_f32 v170, v170, v174\n\t"
    "v_mfma_f32_16x16x32_bf16 a[12:15], v[124:127], v[144:147], a[12:15]\n\t"
    "s_waitcnt lgkmcnt(1)\n\t"
    "v_exp_f32 v177, v33\n\t"
    "v_mfma_f32_16x16x32_bf16 a[28:31], v[124:127], v[148:151], a[28:31]\n\t"
    "ds_read_b128 v[156:159], v181 offset:13056\n\t"
    "v_add_f32 v171, v171, v175\n\t"
    "v_mfma_f32_16x16x32_bf16 a[24:27], v[120:123], v[148:151], a[24:27]\n\t"
    "v_add_f32 v172, v176, v177\n\t"
    "v_cvt_pk_bf16_f32 v96, v176, v177\n\t"
    "v_mfma_f32_16x16x32_bf16 a[20:23], v[116:119], v[148:151], a[20:23]\n\t"
    "v_mfma_f32_16x16x32_bf16 a[16:19], v[112:115], v[148:151], a[16:19]\n\t"
    "s_waitcnt lgkmcnt(1)\n\t"
    "v_exp_f32 v178, v34\n\t"
    "v_mfma_f32_16x16x32_bf16 a[32:35], v[112:115], v[152:155], a[32:35]\n\t"
    "ds_read_b128 v[144:147], v181 offset:13312\n\t"
    "v_mfma_f32_16x16x32_bf16 a[36:39], v[116:119], v[152:155], a[36:39]\n\t"
    "v_exp_f32 v179, v35\n\t"
    "v_mfma_f32_16x16x32_bf16 a[40:43], v[120:123], v[152:155], a[40:43]\n\t"
    "v_add_f32 v172, v172, v178\n\t"
    "v_add_f32 v172, v172, v179\n\t"
    "v_mfma_f32_16x16x32_bf16 a[44:47], v[124:127], v[152:155], a[44:47]\n\t"
    "s_waitcnt lgkmcnt(1)\n\t"
    "v_cvt_pk_bf16_f32 v97, v178, v179\n\t"
    "v_mfma_f32_16x16x32_bf16 a[60:63], v[124:127], v[156:159], a[60:63]\n\t"
    "ds_read_b128 v[148:151], v181 offset:13568\n\t"
    "v_mfma_f32_16x16x32_bf16 a[56:59], v[120:123], v[156:159], a[56:59]\n\t"
    "v_exp_f32 v176, v48\n\t"
    "v_mfma_f32_16x16x32_bf16 a[52:55], v[116:119], v[156:159], a[52:55]\n\t"
    "v_mfma_f32_16x16x32_bf16 a[48:51], v[112:115], v[156:159], a[48:51]\n\t"
    "s_waitcnt lgkmcnt(1)\n\t"
    "v_exp_f32 v177, v49\n\t"
    "v_mfma_f32_16x16x32_bf16 a[64:67], v[112:115], v[144:147], a[64:67]\n\t"
    "ds_read_b128 v[152:155], v181 offset:13824\n\t"
    "v_add_f32 v172, v172, v176\n\t"
    "v_mfma_f32_16x16x32_bf16 a[68:71], v[116:119], v[144:147], a[68:71]\n\t"
    "v_add_f32 v172, v172, v177\n\t"
    "v_cvt_pk_bf16_f32 v98, v176, v177\n\t"
    "v_mfma_f32_16x16x32_bf16 a[72:75], v[120:123], v[144:147], a[72:75]\n\t"
    "v_mfma_f32_16x16x32_bf16 a[76:79], v[124:127], v[144:147], a[76:79]\n\t"
    "s_waitcnt lgkmcnt(1)\n\t"
    "v_exp_f32 v178, v50\n\t"
    "v_mfma_f32_16x16x32_bf16 a[92:95], v[124:127], v[148:151], a[92:95]\n\t"
    "ds_read_b128 v[156:159], v181 offset:14080\n\t"
    "v_mfma_f32_16x16x32_bf16 a[88:91], v[120:123], v[148:151], a[88:91]\n\t"
    "v_exp_f32 v179, v51\n\t"
    "v_mfma_f32_16x16x32_bf16 a[84:87], v[116:119], v[148:151], a[84:87]\n\t"
    "v_add_f32 v172, v172, v178\n\t"
    "v_add_f32 v172, v172, v179\n\t"
    "v_mfma_f32_16x16x32_bf16 a[80:83], v[112:115], v[148:151], a[80:83]\n\t"
    "s_waitcnt lgkmcnt(1)\n\t"
    "v_cvt_pk_bf16_f32 v99, v178, v179\n\t"
    "v_mfma_f32_16x16x32_bf16 a[96:99], v[112:115], v[152:155], a[96:99]\n\t"
    "ds_read_b128 v[144:147], v181 offset:14336\n\t"
    "v_mfma_f32_16x16x32_bf16 a[100:103], v[116:119], v[152:155], a[100:103]\n\t"
    "v_exp_f32 v176, v36\n\t"
    "v_mfma_f32_16x16x32_bf16 a[104:107], v[120:123], v[152:155], a[104:107]\n\t"
    "v_mfma_f32_16x16x32_bf16 a[108:111], v[124:127], v[152:155], a[108:111]\n\t"
    "s_waitcnt lgkmcnt(1)\n\t"
    "v_exp_f32 v177, v37\n\t"
    "v_mfma_f32_16x16x32_bf16 a[124:127], v[124:127], v[156:159], a[124:127]\n\t"
    "ds_read_b128 v[148:151], v181 offset:14592\n\t"
    "v_add_f32 v173, v176, v177\n\t"
    "v_mfma_f32_16x16x32_bf16 a[120:123], v[120:123], v[156:159], a[120:123]\n\t"
    "v_cvt_pk_bf16_f32 v100, v176, v177\n\t"
    "v_mfma_f32_16x16x32_bf16 a[116:119], v[116:119], v[156:159], a[116:119]\n\t"
    "v_mfma_f32_16x16x32_bf16 a[112:115], v[112:115], v[156:159], a[112:115]\n\t"
    "s_waitcnt lgkmcnt(1)\n\t"
    "v_exp_f32 v178, v38\n\t"
    "v_mfma_f32_16x16x32_bf16 a[128:131], v[112:115], v[144:147], a[128:131]\n\t"
    "ds_read_b128 v[152:155], v181 offset:14848\n\t"
    "v_mfma_f32_16x16x32_bf16 a[132:135], v[116:119], v[144:147], a[132:135]\n\t"
    "s_waitcnt vmcnt(6)\n\t"
    "v_exp_f32 v179, v39\n\t"
    "v_mfma_f32_16x16x32_bf16 a[136:139], v[120:123], v[144:147], a[136:139]\n\t"
    "s_barrier\n\t"
    "v_add_f32 v173, v173, v178\n\t"
    "v_add_f32 v173, v173, v179\n\t"
    "v_mfma_f32_16x16x32_bf16 a[140:143], v[124:127], v[144:147], a[140:143]\n\t"
    "s_waitcnt lgkmcnt(1)\n\t"
    "v_cvt_pk_bf16_f32 v101, v178, v179\n\t"
    "v_mfma_f32_16x16x32_bf16 a[156:159], v[124:127], v[148:151], a[156:159]\n\t"
    "ds_read_b128 v[156:159], v181 offset:15104\n\t"
    "v_mfma_f32_16x16x32_bf16 a[152:155], v[120:123], v[148:151], a[152:155]\n\t"
    "s_add_u32 m0, s60, 61440\n\t"
    "v_exp_f32 v176, v52\n\t"
    "v_mfma_f32_16x16x32_bf16 a[148:151], v[116:119], v[148:151], a[148:151]\n\t"
    "buffer_load_dwordx4 v195, s[40:43], s71 offen offset:0 sc1 lds\n\t"
    "v_mfma_f32_16x16x32_bf16 a[144:147], v[112:115], v[148:151], a[144:147]\n\t"
    "s_waitcnt lgkmcnt(1)\n\t"
    "v_exp_f32 v177, v53\n\t"
    "v_mfma_f32_16x16x32_bf16 a[160:163], v[112:115], v[152:155], a[160:163]\n\t"
    "ds_read_b128 v[144:147], v192 offset:12288\n\t"
    "v_add_f32 v173, v173, v176\n\t"
    "v_mfma_f32_16x16x32_bf16 a[164:167], v[116:119], v[152:155], a[164:167]\n\t"
    "ds_read_b128 v[148:151], v192 offset:13824\n\t"
    "v_add_f32 v173, v173, v177\n\t"
    "v_mfma_f32_16x16x32_bf16 a[168:171], v[120:123], v[152:155], a[168:171]\n\t"
    "buffer_load_dwordx4 v195, s[40:43], s71 offen offset:1024 sc1 lds\n\t"
    "v_mfma_f32_16x16x32_bf16 a[172:175], v[124:127], v[152:155], a[172:175]\n\t"
    "s_waitcnt lgkmcnt(2)\n\t"
    "v_exp_f32 v178, v54\n\t"
    "v_mfma_f32_16x16x32_bf16 a[188:191], v[124:127], v[156:159], a[188:191]\n\t"
    "v_cvt_pk_bf16_f32 v102, v176, v177\n\t"
    "v_mfma_f32_16x16x32_bf16 a[184:187], v[120:123], v[156:159], a[184:187]\n\t"
    "v_exp_f32 v179, v55\n\t"
    "v_mfma_f32_16x16x32_bf16 a[180:183], v[116:119], v[156:159], a[180:183]\n\t"
    "v_add_f32 v173, v173, v178\n\t"
    "v_add_f32 v173, v173, v179\n\t"
    "v_mfma_f32_16x16x32_bf16 a[176:179], v[112:115], v[156:159], a[176:179]\n\t"
    "s_waitcnt lgkmcnt(1)\n\t"
    "v_cvt_pk_bf16_f32 v103, v178, v179\n\t"
    "v_mfma_f32_16x16x32_bf16 v[64:67], v[144:147], a[192:195], v[128:131]\n\t"
    "s_waitcnt lgkmcnt(0)\n\t"
    "ds_read_b128 v[152:155], v193 offset:12288\n\t"
    "v_mfma_f32_16x16x32_bf16 v[80:83], v[148:151], a[192:195], v[128:131]\n\t"
    "ds_read_b128 v[156:159], v193 offset:13824\n\t"
    "v_mfma_f32_16x16x32_bf16 v[84:87], v[148:151], a[208:211], v[132:135]\n\t"
    "v_exp_f32 v176, v40\n\t"
    "v_mfma_f32_16x16x32_bf16 v[68:71], v[144:147], a[208:211], v[132:135]\n\t"
    "v_exp_f32 v177, v41\n\t"
    "v_mfma_f32_16x16x32_bf16 v[72:75], v[144:147], a[224:227], v[136:139]\n\t"
    "buffer_load_dwordx4 v195, s[40:43], s71 offen offset:2048 sc1 lds\n\t"
    "v_mfma_f32_16x16x32_bf16 v[88:91], v[148:151], a[224:227], v[136:139]\n\t"
    "v_exp_f32 v178, v42\n\t"
    "v_mfma_f32_16x16x32_bf16 v[92:95], v[148:151], a[240:243], v[140:143]\n\t"
    "v_add_f32 v174, v176, v177\n\t"
    "v_cvt_pk_bf16_f32 v104, v176, v177\n\t"
    "v_mfma_f32_16x16x32_bf16 v[76:79], v[144:147], a[240:243], v[140:143]\n\t"
    "s_waitcnt lgkmcnt(1)\n\t"
    "v_exp_f32 v179, v43\n\t"
    "v_mfma_f32_16x16x32_bf16 v[64:67], v[152:155], a[196:199], v[64:67]\n\t"
    "s_waitcnt lgkmcnt(0)\n\t"
    "ds_read_b128 v[144:147], v192 offset:12416\n\t"
    "v_add_f32 v174, v174, v178\n\t"
    "v_mfma_f32_16x16x32_bf16 v[80:83], v[156:159], a[196:199], v[80:83]\n\t"
    "ds_read_b128 v[148:151], v192 offset:13952\n\t"
    "v_add_f32 v174, v174, v179\n\t"
    "v_mfma_f32_16x16x32_bf16 v[84:87], v[156:159], a[212:215], v[84:87]\n\t"
    "v_cvt_pk_bf16_f32 v105, v178, v179\n\t"
    "v_mfma_f32_16x16x32_bf16 v[68:71], v[152:155], a[212:215], v[68:71]\n\t"
    "v_exp_f32 v176, v56\n\t"
    "v_mfma_f32_16x16x32_bf16 v[72:75], v[152:155], a[228:231], v[72:75]\n\t"
    "v_mfma_f32_16x16x32_bf16 v[88:91], v[156:159], a[228:231], v[88:91]\n\t"
    "v_exp_f32 v177, v57\n\t"
    "v_mfma_f32_16x16x32_bf16 v[92:95], v[156:159], a[244:247], v[92:95]\n\t"
    "v_add_f32 v174, v174, v176\n\t"
    "v_add_f32 v174, v174, v177\n\t"
    "v_mfma_f32_16x16x32_bf16 v[76:79], v[152:155], a[244:247], v[76:79]\n\t"
    "s_waitcnt lgkmcnt(1)\n\t"
    "v_exp_f32 v178, v58\n\t"
    "v_mfma_f32_16x16x32_bf16 v[64:67], v[144:147], a[200:203], v[64:67]\n\t"
    "s_waitcnt lgkmcnt(0)\n\t"
    "ds_read_b128 v[152:155], v193 offset:12416\n\t"
    "v_cvt_pk_bf16_f32 v106, v176, v177\n\t"
    "v_mfma_f32_16x16x32_bf16 v[80:83], v[148:151], a[200:203], v[80:83]\n\t"
    "ds_read_b128 v[156:159], v193 offset:13952\n\t"
    "v_mfma_f32_16x16x32_bf16 v[84:87], v[148:151], a[216:219], v[84:87]\n\t"
    "v_mfma_f32_16x16x32_bf16 v[68:71], v[144:147], a[216:219], v[68:71]\n\t"
    "s_add_u32 m0, s60, 73728\n\t"
    "v_exp_f32 v179, v59\n\t"
    "v_mfma_f32_16x16x32_bf16 v[72:75], v[144:147], a[232:235], v[72:75]\n\t"
    "buffer_load_dwordx4 v195, s[44:47], s70 offen offset:0 sc1 lds\n\t"
    "v_mfma_f32_16x16x32_bf16 v[88:91], v[148:151], a[232:235], v[88:91]\n\t"
    "v_exp_f32 v176, v44\n\t"
    "v_mfma_f32_16x16x32_bf16 v[92:95], v[148:151], a[248:251], v[92:95]\n\t"
    "v_add_f32 v174, v174, v178\n\t"
    "v_add_f32 v174, v174, v179\n\t"
    "v_mfma_f32_16x16x32_bf16 v[76:79], v[144:147], a[248:251], v[76:79]\n\t"
    "s_waitcnt lgkmcnt(1)\n\t"
    "v_exp_f32 v177, v45\n\t"
    "v_mfma_f32_16x16x32_bf16 v[64:67], v[152:155], a[204:207], v[64:67]\n\t"
    "s_waitcnt lgkmcnt(0)\n\t"
    "ds_read_b128 v[144:147], v192 offset:12544\n\t"
    "v_cvt_pk_bf16_f32 v107, v178, v179\n\t"
    "v_mfma_f32_16x16x32_bf16 v[80:83], v[156:159], a[204:207], v[80:83]\n\t"
    "ds_read_b128 v[148:151], v192 offset:14080\n\t"
    "v_add_f32 v175, v176, v177\n\t"
    "v_mfma_f32_16x16x32_bf16 v[84:87], v[156:159], a[220:223], v[84:87]\n\t"
    "v_cvt_pk_bf16_f32 v108, v176, v177\n\t"
    "v_mfma_f32_16x16x32_bf16 v[68:71], v[152:155], a[220:223], v[68:71]\n\t"
    "v_exp_f32 v178, v46\n\t"
    "v_mfma_f32_16x16x32_bf16 v[72:75], v[152:155], a[236:239], v[72:75]\n\t"
    "buffer_load_dwordx4 v195, s[44:47], s70 offen offset:1024 sc1 lds\n\t"
    "v_mfma_f32_16x16x32_bf16 v[88:91], v[156:159], a[236:239], v[88:91]\n\t"
    "v_exp_f32 v179, v47\n\t"
    "v_mfma_f32_16x16x32_bf16 v[92:95], v[156:159], a[252:255], v[92:95]\n\t"
    "v_add_f32 v175, v175, v178\n\t"
    "v_add_f32 v175, v175, v179\n\t"
    "v_mfma_f32_16x16x32_bf16 v[76:79], v[152:155], a[252:255], v[76:79]\n\t"
    "s_waitcnt lgkmcnt(1)\n\t"
    "v_exp_f32 v176, v60\n\t"
    "v_mfma_f32_16x16x32_bf16 v[64:67], v[144:147], v[0:3], v[64:67]\n\t"
    "s_waitcnt lgkmcnt(0)\n\t"
    "ds_read_b128 v[152:155], v193 offset:12544\n\t"
    "v_cvt_pk_bf16_f32 v109, v178, v179\n\t"
    "v_mfma_f32_16x16x32_bf16 v[80:83], v[148:151], v[0:3], v[80:83]\n\t"
    "ds_read_b128 v[156:159], v193 offset:14080\n\t"
    "v_mfma_f32_16x16x32_bf16 v[84:87], v[148:151], v[8:11], v[84:87]\n\t"
    "v_mfma_f32_16x16x32_bf16 v[68:71], v[144:147], v[8:11], v[68:71]\n\t"
    "v_exp_f32 v177, v61\n\t"
    "v_mfma_f32_16x16x32_bf16 v[72:75], v[144:147], v[16:19], v[72:75]\n\t"
    "buffer_load_dwordx4 v195, s[44:47], s70 offen offset:2048 sc1 lds\n\t"
    "v_mfma_f32_16x16x32_bf16 v[88:91], v[148:151], v[16:19], v[88:91]\n\t"
    "v_exp_f32 v178, v62\n\t"
    "v_mfma_f32_16x16x32_bf16 v[92:95], v[148:151], v[24:27], v[92:95]\n\t"
    "v_add_f32 v175, v175, v176\n\t"
    "v_add_f32 v175, v175, v177\n\t"
    "v_mfma_f32_16x16x32_bf16 v[76:79], v[144:147], v[24:27], v[76:79]\n\t"
    "s_waitcnt lgkmcnt(1)\n\t"
    "v_exp_f32 v179, v63\n\t"
    "v_mfma_f32_16x16x32_bf16 v[64:67], v[152:155], v[4:7], v[64:67]\n\t"
    "s_waitcnt lgkmcnt(0)\n\t"
    "ds_read_b128 v[144:147], v194 offset:0\n\t"
    "v_cvt_pk_bf16_f32 v110, v176, v177\n\t"
    "v_mfma_f32_16x16x32_bf16 v[80:83], v[156:159], v[4:7], v[80:83]\n\t"
    "ds_read_b128 v[148:151], v194 offset:256\n\t"
    "v_add_f32 v175, v175, v178\n\t"
    "v_mfma_f32_16x16x32_bf16 v[84:87], v[156:159], v[12:15], v[84:87]\n\t"
    "v_add_f32 v175, v175, v179\n\t"
    "v_cvt_pk_bf16_f32 v111, v178, v179\n\t"
    "v_mfma_f32_16x16x32_bf16 v[68:71], v[152:155], v[12:15], v[68:71]\n\t"
    "v_max3_f32 v180, v172, v173, v174\n\t"
    "v_max_f32 v180, v180, v175\n\t"
    "v_mfma_f32_16x16x32_bf16 v[72:75], v[152:155], v[20:23], v[72:75]\n\t"
    "v_cmp_lt_f32 vcc, s62, v180\n\t"
    "v_mfma_f32_16x16x32_bf16 v[88:91], v[156:159], v[20:23], v[88:91]\n\t"
    "v_mfma_f32_16x16x32_bf16 v[92:95], v[156:159], v[28:31], v[92:95]\n\t"
    "v_mfma_f32_16x16x32_bf16 v[76:79], v[152:155], v[28:31], v[76:79]\n\t"
    "s_waitcnt lgkmcnt(1)\n\t"
    "s_cbranch_vccnz L_stub_s60_0\n\t"
    "L_ret_s60_0:\n\t"
    "v_mfma_f32_16x16x32_bf16 a[0:3], v[96:99], v[144:147], a[0:3]\n\t"
    "ds_read_b128 v[152:155], v194 offset:512\n\t"
    "v_add_f32 v168, v168, v172\n\t"
    "v_mfma_f32_16x16x32_bf16 a[4:7], v[100:103], v[144:147], a[4:7]\n\t"
    "v_exp_f32 v176, v64\n\t"
    "v_mfma_f32_16x16x32_bf16 a[8:11], v[104:107], v[144:147], a[8:11]\n\t"
    "v_add_f32 v169, v169, v173\n\t"
    "v_add_f32 v170, v170, v174\n\t"
    "v_mfma_f32_16x16x32_bf16 a[12:15], v[108:111], v[144:147], a[12:15]\n\t"
    "s_waitcnt lgkmcnt(1)\n\t"
    "v_exp_f32 v177, v65\n\t"
    "v_mfma_f32_16x16x32_bf16 a[28:31], v[108:111], v[148:151], a[28:31]\n\t"
    "ds_read_b128 v[156:159], v194 offset:768\n\t"
    "v_add_f32 v171, v171, v175\n\t"
    "v_mfma_f32_16x16x32_bf16 a[24:27], v[104:107], v[148:151], a[24:27]\n\t"
    "v_add_f32 v172, v176, v177\n\t"
    "v_cvt_pk_bf16_f32 v112, v176, v177\n\t"
    "v_mfma_f32_16x16x32_bf16 a[20:23], v[100:103], v[148:151], a[20:23]\n\t"
    "v_mfma_f32_16x16x32_bf16 a[16:19], v[96:99], v[148:151], a[16:19]\n\t"
    "s_waitcnt lgkmcnt(1)\n\t"
    "v_exp_f32 v178, v66\n\t"
    "v_mfma_f32_16x16x32_bf16 a[32:35], v[96:99], v[152:155], a[32:35]\n\t"
    "ds_read_b128 v[144:147], v194 offset:1024\n\t"
    "v_mfma_f32_16x16x32_bf16 a[36:39], v[100:103], v[152:155], a[36:39]\n\t"
    "v_exp_f32 v179, v67\n\t"
    "v_mfma_f32_16x16x32_bf16 a[40:43], v[104:107], v[152:155], a[40:43]\n\t"
    "v_add_f32 v172, v172, v178\n\t"
    "v_add_f32 v172, v172, v179\n\t"
    "v_mfma_f32_16x16x32_bf16 a[44:47], v[108:111], v[152:155], a[44:47]\n\t"
    "s_waitcnt lgkmcnt(1)\n\t"
    "v_cvt_pk_bf16_f32 v113, v178, v179\n\t"
    "v_mfma_f32_16x16x32_bf16 a[60:63], v[108:111], v[156:159], a[60:63]\n\t"
    "ds_read_b128 v[148:151], v194 offset:1280\n\t"
    "v_mfma_f32_16x16x32_bf16 a[56:59], v[104:107], v[156:159], a[56:59]\n\t"
    "v_exp_f32 v176, v80\n\t"
    "v_mfma_f32_16x16x32_bf16 a[52:55], v[100:103], v[156:159], a[52:55]\n\t"
    "v_mfma_f32_16x16x32_bf16 a[48:51], v[96:99], v[156:159], a[48:51]\n\t"
    "s_waitcnt lgkmcnt(1)\n\t"
    "v_exp_f32 v177, v81\n\t"
    "v_mfma_f32_16x16x32_bf16 a[64:67], v[96:99], v[144:147], a[64:67]\n\t"
    "ds_read_b128 v[152:155], v194 offset:1536\n\t"
    "v_add_f32 v172, v172, v176\n\t"
    "v_mfma_f32_16x16x32_bf16 a[68:71], v[100:103], v[144:147], a[68:71]\n\t"
    "v_add_f32 v172, v172, v177\n\t"
    "v_cvt_pk_bf16_f32 v114, v176, v177\n\t"
    "v_mfma_f32_16x16x32_bf16 a[72:75], v[104:107], v[144:147], a[72:75]\n\t"
    "v_mfma_f32_16x16x32_bf16 a[76:79], v[108:111], v[144:147], a[76:79]\n\t"
    "s_waitcnt lgkmcnt(1)\n\t"
    "v_exp_f32 v178, v82\n\t"
    "v_mfma_f32_16x16x32_bf16 a[92:95], v[108:111], v[148:151], a[92:95]\n\t"
    "ds_read_b128 v[156:159], v194 offset:1792\n\t"
    "v_mfma_f32_16x16x32_bf16 a[88:91], v[104:107], v[148:151], a[88:91]\n\t"
    "v_exp_f32 v179, v83\n\t"
    "v_mfma_f32_16x16x32_bf16 a[84:87], v[100:103], v[148:151], a[84:87]\n\t"
    "v_add_f32 v172, v172, v178\n\t"
    "v_add_f32 v172, v172, v179\n\t"
    "v_mfma_f32_16x16x32_bf16 a[80:83], v[96:99], v[148:151], a[80:83]\n\t"
    "s_waitcnt lgkmcnt(1)\n\t"
    "v_cvt_pk_bf16_f32 v115, v178, v179\n\t"
    "v_mfma_f32_16x16x32_bf16 a[96:99], v[96:99], v[152:155], a[96:99]\n\t"
    "ds_read_b128 v[144:147], v194 offset:2048\n\t"
    "v_mfma_f32_16x16x32_bf16 a[100:103], v[100:103], v[152:155], a[100:103]\n\t"
    "v_exp_f32 v176, v68\n\t"
    "v_mfma_f32_16x16x32_bf16 a[104:107], v[104:107], v[152:155], a[104:107]\n\t"
    "v_mfma_f32_16x16x32_bf16 a[108:111], v[108:111], v[152:155], a[108:111]\n\t"
    "s_waitcnt lgkmcnt(1)\n\t"
    "v_exp_f32 v177, v69\n\t"
    "v_mfma_f32_16x16x32_bf16 a[124:127], v[108:111], v[156:159], a[124:127]\n\t"
    "ds_read_b128 v[148:151], v194 offset:2304\n\t"
    "v_add_f32 v173, v176, v177\n\t"
    "v_mfma_f32_16x16x32_bf16 a[120:123], v[104:107], v[156:159], a[120:123]\n\t"
    "v_cvt_pk_bf16_f32 v116, v176, v177\n\t"
    "v_mfma_f32_16x16x32_bf16 a[116:119], v[100:103], v[156:159], a[116:119]\n\t"
    "v_mfma_f32_16x16x32_bf16 a[112:115], v[96:99], v[156:159], a[112:115]\n\t"
    "s_waitcnt lgkmcnt(1)\n\t"
    "v_exp_f32 v178, v70\n\t"
    "v_mfma_f32_16x16x32_bf16 a[128:131], v[96:99], v[144:147], a[128:131]\n\t"
    "ds_read_b128 v[152:155], v194 offset:2560\n\t"
    "v_mfma_f32_16x16x32_bf16 a[132:135], v[100:103], v[144:147], a[132:135]\n\t"
    "s_waitcnt vmcnt(6)\n\t"
    "v_exp_f32 v179, v71\n\t"
    "v_mfma_f32_16x16x32_bf16 a[136:139], v[104:107], v[144:147], a[136:139]\n\t"
    "s_barrier\n\t"
    "v_add_f32 v173, v173, v178\n\t"
    "v_add_f32 v173, v173, v179\n\t"
    "v_mfma_f32_16x16x32_bf16 a[140:143], v[108:111], v[144:147], a[140:143]\n\t"
    "s_waitcnt lgkmcnt(1)\n\t"
    "v_cvt_pk_bf16_f32 v117, v178, v179\n\t"
    "v_mfma_f32_16x16x32_bf16 a[156:159], v[108:111], v[148:151], a[156:159]\n\t"
    "ds_read_b128 v[156:159], v194 offset:2816\n\t"
    "v_mfma_f32_16x16x32_bf16 a[152:155], v[104:107], v[148:151], a[152:155]\n\t"
    "v_exp_f32 v176, v84\n\t"
    "v_mfma_f32_16x16x32_bf16 a[148:151], v[100:103], v[148:151], a[148:151]\n\t"
    "v_mfma_f32_16x16x32_bf16 a[144:147], v[96:99], v[148:151], a[144:147]\n\t"
    "s_waitcnt lgkmcnt(1)\n\t"
    "v_exp_f32 v177, v85\n\t"
    "v_mfma_f32_16x16x32_bf16 a[160:163], v[96:99], v[152:155], a[160:163]\n\t"
    "ds_read_b128 v[144:147], v192 offset:24576\n\t"
    "v_add_f32 v173, v173, v176\n\t"
    "v_mfma_f32_16x16x32_bf16 a[164:167], v[100:103], v[152:155], a[164:167]\n\t"
    "ds_read_b128 v[148:151], v192 offset:26112\n\t"
    "v_add_f32 v173, v173, v177\n\t"
    "v_mfma_f32_16x16x32_bf16 a[168:171], v[104:107], v[152:155], a[168:171]\n\t"
    "v_cvt_pk_bf16_f32 v118, v176, v177\n\t"
    "v_mfma_f32_16x16x32_bf16 a[172:175], v[108:111], v[152:155], a[172:175]\n\t"
    "s_waitcnt lgkmcnt(2)\n\t"
    "v_exp_f32 v178, v86\n\t"
    "v_mfma_f32_16x16x32_bf16 a[188:191], v[108:111], v[156:159], a[188:191]\n\t"
    "v_mfma_f32_16x16x32_bf16 a[184:187], v[104:107], v[156:159], a[184:187]\n\t"
    "v_exp_f32 v179, v87\n\t"
    "v_mfma_f32_16x16x32_bf16 a[180:183], v[100:103], v[156:159], a[180:183]\n\t"
    "v_add_f32 v173, v173, v178\n\t"
    "v_add_f32 v173, v173, v179\n\t"
    "v_mfma_f32_16x16x32_bf16 a[176:179], v[96:99], v[156:159], a[176:179]\n\t"
    "s_waitcnt lgkmcnt(1)\n\t"
    "v_cvt_pk_bf16_f32 v119, v178, v179\n\t"
    "ds_read_b128 v[204:207], v193 offset:24576\n\t"
    "ds_read_b128 v[208:211], v193 offset:26112\n\t"
    "ds_read_b128 v[212:215], v192 offset:24704\n\t"
    "ds_read_b128 v[216:219], v192 offset:26240\n\t"
    "s_waitcnt lgkmcnt(5)\n\t"
    "s_add_u32 m0, s60, 86016\n\t"
    "s_nop 0\n\t"
    "buffer_load_dwordx4 v195, s[44:47], s71 offen offset:0 sc1 lds\n\t"
    "v_max_f32 v180, v172, v173\n\t"
    "v_cmp_lt_f32 vcc, s62, v180\n\t"
    "s_nop 1\n\t"
    "s_cbranch_vccnz L_stub_h0\n\t"
    "L_ret_h0:\n\t"
    "v_mfma_f32_16x16x32_bf16 v[32:35], v[144:147], a[192:195], v[128:131]\n\t"
    "s_waitcnt lgkmcnt(4)\n\t"
    "ds_read_b128 v[220:223], v193 offset:24704\n\t"
    "v_add_f32 v168, v168, v172\n\t"
    "v_mfma_f32_16x16x32_bf16 v[48:51], v[148:151], a[192:195], v[128:131]\n\t"
    "s_mov_b64 s[56:57], %2\n\t"
    "v_add_f32 v169, v169, v173\n\t"
    "v_mfma_f32_16x16x32_bf16 v[52:55], v[148:151], a[208:211], v[132:135]\n\t"
    "s_and_b32 s57, s57, 0xffff\n\t"
    "ds_read_b128 v[224:227], v193 offset:26240\n\t"
    "v_mfma_f32_16x16x32_bf16 v[36:39], v[144:147], a[208:211], v[132:135]\n\t"
    "s_waitcnt lgkmcnt(4)\n\t"
    "buffer_load_dwordx4 v195, s[44:47], s71 offen offset:1024 sc1 lds\n\t"
    "v_mfma_f32_16x16x32_bf16 v[32:35], v[204:207], a[196:199], v[32:35]\n\t"
    "s_mov_b32 s58, 24576\n\t"
    "ds_read_b128 v[228:231], v192 offset:24832\n\t"
    "v_mfma_f32_16x16x32_bf16 v[48:51], v[208:211], a[196:199], v[48:51]\n\t"
    "s_mov_b32 s59, 0x20000\n\t"
    "v_exp_f32 v176, v72\n\t"
    "v_mfma_f32_16x16x32_bf16 v[52:55], v[208:211], a[212:215], v[52:55]\n\t"
    "s_mov_b32 s95, 0\n\t"
    "ds_read_b128 v[232:235], v192 offset:26368\n\t"
    "v_mfma_f32_16x16x32_bf16 v[36:39], v[204:207], a[212:215], v[36:39]\n\t"
    "s_waitcnt lgkmcnt(4)\n\t"
    "buffer_load_dwordx4 v195, s[44:47], s71 offen offset:2048 sc1 lds\n\t"
    "v_mfma_f32_16x16x32_bf16 v[32:35], v[212:215], a[200:203], v[32:35]\n\t"
    "s_mov_b32 s96, 6144\n\t"
    "ds_read_b128 v[204:207], v193 offset:24832\n\t"
    "v_mfma_f32_16x16x32_bf16 v[48:51], v[216:219], a[200:203], v[48:51]\n\t"
    "v_exp_f32 v177, v73\n\t"
    "v_mfma_f32_16x16x32_bf16 v[52:55], v[216:219], a[216:219], v[52:55]\n\t"
    "ds_read_b128 v[208:211], v193 offset:26368\n\t"
    "v_mfma_f32_16x16x32_bf16 v[36:39], v[212:215], a[216:219], v[36:39]\n\t"
    "s_waitcnt lgkmcnt(4)\n\t"
    "v_exp_f32 v178, v74\n\t"
    "v_mfma_f32_16x16x32_bf16 v[32:35], v[220:223], a[204:207], v[32:35]\n\t"
    "ds_read_b128 v[212:215], v194 offset:12288\n\t"
    "v_add_f32 v174, v176, v177\n\t"
    "v_mfma_f32_16x16x32_bf16 v[48:51], v[224:227], a[204:207], v[48:51]\n\t"
    "v_exp_f32 v179, v75\n\t"
    "v_mfma_f32_16x16x32_bf16 v[52:55], v[224:227], a[220:223], v[52:55]\n\t"
    "ds_read_b128 v[216:219], v194 offset:12544\n\t"
    "v_cvt_pk_bf16_f32 v120, v176, v177\n\t"
    "v_mfma_f32_16x16x32_bf16 v[36:39], v[220:223], a[220:223], v[36:39]\n\t"
    "s_waitcnt lgkmcnt(4)\n\t"
    "v_exp_f32 v176, v88\n\t"
    "v_mfma_f32_16x16x32_bf16 v[32:35], v[228:231], v[0:3], v[32:35]\n\t"
    "ds_read_b128 v[220:223], v194 offset:12800\n\t"
    "v_add_f32 v174, v174, v178\n\t"
    "v_mfma_f32_16x16x32_bf16 v[48:51], v[232:235], v[0:3], v[48:51]\n\t"
    "v_exp_f32 v177, v89\n\t"
    "v_add_f32 v174, v174, v179\n\t"
    "v_mfma_f32_16x16x32_bf16 v[52:55], v[232:235], v[8:11], v[52:55]\n\t"
    "ds_read_b128 v[224:227], v194 offset:13056\n\t"
    "v_cvt_pk_bf16_f32 v121, v178, v179\n\t"
    "v_mfma_f32_16x16x32_bf16 v[36:39], v[228:231], v[8:11], v[36:39]\n\t"
    "s_waitcnt lgkmcnt(4)\n\t"
    "v_exp_f32 v178, v90\n\t"
    "v_mfma_f32_16x16x32_bf16 v[32:35], v[204:207], v[4:7], v[32:35]\n\t"
    "ds_read_b128 v[228:231], v194 offset:13312\n\t"
    "v_add_f32 v174, v174, v176\n\t"
    "v_mfma_f32_16x16x32_bf16 v[48:51], v[208:211], v[4:7], v[48:51]\n\t"
    "v_exp_f32 v179, v91\n\t"
    "v_add_f32 v174, v174, v177\n\t"
    "v_mfma_f32_16x16x32_bf16 v[52:55], v[208:211], v[12:15], v[52:55]\n\t"
    "ds_read_b128 v[232:235], v194 offset:13568\n\t"
    "v_cvt_pk_bf16_f32 v122, v176, v177\n\t"
    "v_mfma_f32_16x16x32_bf16 v[36:39], v[204:207], v[12:15], v[36:39]\n\t"
    "s_waitcnt lgkmcnt(5)\n\t"
    "v_exp_f32 v176, v76\n\t"
    "v_mfma_f32_16x16x32_bf16 a[0:3], v[112:115], v[212:215], a[0:3]\n\t"
    "ds_read_b128 v[204:207], v194 offset:13824\n\t"
    "v_add_f32 v174, v174, v178\n\t"
    "v_mfma_f32_16x16x32_bf16 a[4:7], v[116:119], v[212:215], a[4:7]\n\t"
    "s_waitcnt lgkmcnt(5)\n\t"
    "v_exp_f32 v177, v77\n\t"
    "v_add_f32 v174, v174, v179\n\t"
    "v_mfma_f32_16x16x32_bf16 a[20:23], v[116:119], v[216:219], a[20:23]\n\t"
    "ds_read_b128 v[208:211], v194 offset:14080\n\t"
    "v_cvt_pk_bf16_f32 v123, v178, v179\n\t"
    "v_mfma_f32_16x16x32_bf16 a[16:19], v[112:115], v[216:219], a[16:19]\n\t"
    "s_waitcnt lgkmcnt(5)\n\t"
    "v_add_f32 v175, v176, v177\n\t"
    "v_cvt_pk_bf16_f32 v124, v176, v177\n\t"
    "v_mfma_f32_16x16x32_bf16 a[32:35], v[112:115], v[220:223], a[32:35]\n\t"
    "ds_read_b128 v[212:215], v194 offset:14336\n\t"
    "v_mfma_f32_16x16x32_bf16 a[36:39], v[116:119], v[220:223], a[36:39]\n\t"
    "s_waitcnt lgkmcnt(5)\n\t"
    "v_exp_f32 v176, v32\n\t"
    "v_mfma_f32_16x16x32_bf16 a[52:55], v[116:119], v[224:227], a[52:55]\n\t"
    "ds_read_b128 v[216:219], v194 offset:14592\n\t"
    "v_mfma_f32_16x16x32_bf16 a[48:51], v[112:115], v[224:227], a[48:51]\n\t"
    "s_waitcnt lgkmcnt(5)\n\t"
    "v_exp_f32 v177, v33\n\t"
    "v_mfma_f32_16x16x32_bf16 a[64:67], v[112:115], v[228:231], a[64:67]\n\t"
    "ds_read_b128 v[220:223], v194 offset:14848\n\t"
    "v_mfma_f32_16x16x32_bf16 a[68:71], v[116:119], v[228:231], a[68:71]\n\t"
    "s_waitcnt lgkmcnt(5)\n\t"
    "v_exp_f32 v178, v34\n\t"
    "v_mfma_f32_16x16x32_bf16 a[84:87], v[116:119], v[232:235], a[84:87]\n\t"
    "ds_read_b128 v[224:227], v194 offset:15104\n\t"
    "v_add_f32 v172, v176, v177\n\t"
    "v_mfma_f32_16x16x32_bf16 a[80:83], v[112:115], v[232:235], a[80:83]\n\t"
    "s_waitcnt lgkmcnt(5)\n\t"
    "s_waitcnt vmcnt(3)\n\t"
    "s_barrier\n\t"
    "v_mfma_f32_16x16x32_bf16 a[96:99], v[112:115], v[204:207], a[96:99]\n\t"
    "ds_read_b128 v[228:231], v192 offset:36864\n\t"
    "v_mfma_f32_16x16x32_bf16 a[100:103], v[116:119], v[204:207], a[100:103]\n\t"
    "s_waitcnt lgkmcnt(5)\n\t"
    "v_exp_f32 v179, v35\n\t"
    "v_mfma_f32_16x16x32_bf16 a[116:119], v[116:119], v[208:211], a[116:119]\n\t"
    "ds_read_b128 v[232:235], v192 offset:38400\n\t"
    "v_cvt_pk_bf16_f32 v96, v176, v177\n\t"
    "v_mfma_f32_16x16x32_bf16 a[112:115], v[112:115], v[208:211], a[112:115]\n\t"
    "s_waitcnt lgkmcnt(5)\n\t"
    "v_exp_f32 v176, v48\n\t"
    "v_mfma_f32_16x16x32_bf16 a[128:131], v[112:115], v[212:215], a[128:131]\n\t"
    "ds_read_b128 v[204:207], v193 offset:36864\n\t"
    "v_add_f32 v172, v172, v178\n\t"
    "v_mfma_f32_16x16x32_bf16 a[132:135], v[116:119], v[212:215], a[132:135]\n\t"
    "s_waitcnt lgkmcnt(5)\n\t"
    "v_exp_f32 v177, v49\n\t"
    "v_add_f32 v172, v172, v179\n\t"
    "v_mfma_f32_16x16x32_bf16 a[148:151], v[116:119], v[216:219], a[148:151]\n\t"
    "ds_read_b128 v[208:211], v193 offset:38400\n\t"
    "v_cvt_pk_bf16_f32 v97, v178, v179\n\t"
    "v_mfma_f32_16x16x32_bf16 a[144:147], v[112:115], v[216:219], a[144:147]\n\t"
    "s_waitcnt lgkmcnt(5)\n\t"
    "v_exp_f32 v178, v50\n\t"
    "v_mfma_f32_16x16x32_bf16 a[160:163], v[112:115], v[220:223], a[160:163]\n\t"
    "ds_read_b128 v[212:215], v192 offset:36992\n\t"
    "v_add_f32 v172, v172, v176\n\t"
    "v_mfma_f32_16x16x32_bf16 a[164:167], v[116:119], v[220:223], a[164:167]\n\t"
    "s_waitcnt lgkmcnt(5)\n\t"
    "v_exp_f32 v179, v51\n\t"
    "v_add_f32 v172, v172, v177\n\t"
    "v_mfma_f32_16x16x32_bf16 a[180:183], v[116:119], v[224:227], a[180:183]\n\t"
    "ds_read_b128 v[216:219], v192 offset:38528\n\t"
    "v_cvt_pk_bf16_f32 v98, v176, v177\n\t"
    "v_mfma_f32_16x16x32_bf16 a[176:179], v[112:115], v[224:227], a[176:179]\n\t"
    "s_waitcnt lgkmcnt(4)\n\t"
    "v_exp_f32 v176, v36\n\t"
    "v_mfma_f32_16x16x32_bf16 v[64:67], v[228:231], a[192:195], v[128:131]\n\t"
    "ds_read_b128 v[220:223], v193 offset:36992\n\t"
    "v_add_f32 v172, v172, v178\n\t"
    "v_mfma_f32_16x16x32_bf16 v[80:83], v[232:235], a[192:195], v[128:131]\n\t"
    "v_exp_f32 v177, v37\n\t"
    "v_add_f32 v172, v172, v179\n\t"
    "v_mfma_f32_16x16x32_bf16 v[84:87], v[232:235], a[208:211], v[132:135]\n\t"
    "ds_read_b128 v[224:227], v193 offset:38528\n\t"
    "v_cvt_pk_bf16_f32 v99, v178, v179\n\t"
    "v_mfma_f32_16x16x32_bf16 v[68:71], v[228:231], a[208:211], v[132:135]\n\t"
    "s_waitcnt lgkmcnt(4)\n\t"
    "v_exp_f32 v178, v38\n\t"
    "v_mfma_f32_16x16x32_bf16 v[64:67], v[204:207], a[196:199], v[64:67]\n\t"
    "ds_read_b128 v[228:231], v192 offset:37120\n\t"
    "v_add_f32 v173, v176, v177\n\t"
    "v_mfma_f32_16x16x32_bf16 v[80:83], v[208:211], a[196:199], v[80:83]\n\t"
    "v_exp_f32 v179, v39\n\t"
    "v_mfma_f32_16x16x32_bf16 v[84:87], v[208:211], a[212:215], v[84:87]\n\t"
    "ds_read_b128 v[232:235], v192 offset:38656\n\t"
    "v_cvt_pk_bf16_f32 v100, v176, v177\n\t"
    "v_mfma_f32_16x16x32_bf16 v[68:71], v[204:207], a[212:215], v[68:71]\n\t"
    "s_waitcnt lgkmcnt(4)\n\t"
    "v_exp_f32 v176, v52\n\t"
    "v_mfma_f32_16x16x32_bf16 v[64:67], v[212:215], a[200:203], v[64:67]\n\t"
    "ds_read_b128 v[204:207], v193 offset:37120\n\t"
    "v_add_f32 v173, v173, v178\n\t"
    "v_mfma_f32_16x16x32_bf16 v[80:83], v[216:219], a[200:203], v[80:83]\n\t"
    "v_exp_f32 v177, v53\n\t"
    "v_add_f32 v173, v173, v179\n\t"
    "v_mfma_f32_16x16x32_bf16 v[84:87], v[216:219], a[216:219], v[84:87]\n\t"
    "ds_read_b128 v[208:211], v193 offset:38656\n\t"
    "v_cvt_pk_bf16_f32 v101, v178, v179\n\t"
    "v_mfma_f32_16x16x32_bf16 v[68:71], v[212:215], a[216:219], v[68:71]\n\t"
    "s_waitcnt lgkmcnt(4)\n\t"
    "v_exp_f32 v178, v54\n\t"
    "v_mfma_f32_16x16x32_bf16 v[64:67], v[220:223], a[204:207], v[64:67]\n\t"
    "ds_read_b128 v[212:215], v181 offset:0\n\t"
    "v_add_f32 v173, v173, v176\n\t"
    "v_mfma_f32_16x16x32_bf16 v[80:83], v[224:227], a[204:207], v[80:83]\n\t"
    "v_exp_f32 v179, v55\n\t"
    "v_add_f32 v173, v173, v177\n\t"
    "v_mfma_f32_16x16x32_bf16 v[84:87], v[224:227], a[220:223], v[84:87]\n\t"
    "ds_read_b128 v[216:219], v181 offset:256\n\t"
    "v_cvt_pk_bf16_f32 v102, v176, v177\n\t"
    "v_mfma_f32_16x16x32_bf16 v[68:71], v[220:223], a[220:223], v[68:71]\n\t"
    "s_waitcnt lgkmcnt(4)\n\t"
    "v_add_f32 v173, v173, v178\n\t"
    "v_mfma_f32_16x16x32_bf16 v[64:67], v[228:231], v[0:3], v[64:67]\n\t"
    "ds_read_b128 v[220:223], v181 offset:512\n\t"
    "v_add_f32 v173, v173, v179\n\t"
    "v_mfma_f32_16x16x32_bf16 v[80:83], v[232:235], v[0:3], v[80:83]\n\t"
    "v_cvt_pk_bf16_f32 v103, v178, v179\n\t"
    "v_max_f32 v180, v172, v173\n\t"
    "v_mfma_f32_16x16x32_bf16 v[84:87], v[232:235], v[8:11], v[84:87]\n\t"
    "ds_read_b128 v[224:227], v181 offset:768\n\t"
    "v_cmp_lt_f32 vcc, s62, v180\n\t"
    "v_mfma_f32_16x16x32_bf16 v[68:71], v[228:231], v[8:11], v[68:71]\n\t"
    "s_waitcnt lgkmcnt(4)\n\t"
    "v_mfma_f32_16x16x32_bf16 v[64:67], v[204:207], v[4:7], v[64:67]\n\t"
    "ds_read_b128 v[228:231], v181 offset:1024\n\t"
    "v_mfma_f32_16x16x32_bf16 v[80:83], v[208:211], v[4:7], v[80:83]\n\t"
    "v_mfma_f32_16x16x32_bf16 v[84:87], v[208:211], v[12:15], v[84:87]\n\t"
    "ds_read_b128 v[232:235], v181 offset:1280\n\t"
    "v_mfma_f32_16x16x32_bf16 v[68:71], v[204:207], v[12:15], v[68:71]\n\t"
    "s_waitcnt lgkmcnt(5)\n\t"
    "s_cbranch_vccnz L_stub_h2\n\t"
    "L_ret_h2:\n\t"
    "v_mfma_f32_16x16x32_bf16 a[0:3], v[96:99], v[212:215], a[0:3]\n\t"
    "ds_read_b128 v[204:207], v181 offset:1536\n\t"
    "v_add_f32 v168, v168, v172\n\t"
    "v_mfma_f32_16x16x32_bf16 a[4:7], v[100:103], v[212:215], a[4:7]\n\t"
    "s_waitcnt lgkmcnt(5)\n\t"
    "v_add_f32 v169, v169, v173\n\t"
    "v_mfma_f32_16x16x32_bf16 a[20:23], v[100:103], v[216:219], a[20:23]\n\t"
    "ds_read_b128 v[208:211], v181 offset:1792\n\t"
    "v_mfma_f32_16x16x32_bf16 a[16:19], v[96:99], v[216:219], a[16:19]\n\t"
    "s_waitcnt lgkmcnt(5)\n\t"
    "v_mfma_f32_16x16x32_bf16 a[32:35], v[96:99], v[220:223], a[32:35]\n\t"
    "ds_read_b128 v[212:215], v181 offset:2048\n\t"
    "v_mfma_f32_16x16x32_bf16 a[36:39], v[100:103], v[220:223], a[36:39]\n\t"
    "s_waitcnt lgkmcnt(5)\n\t"
    "v_exp_f32 v176, v64\n\t"
    "v_mfma_f32_16x16x32_bf16 a[52:55], v[100:103], v[224:227], a[52:55]\n\t"
    "ds_read_b128 v[216:219], v181 offset:2304\n\t"
    "v_mfma_f32_16x16x32_bf16 a[48:51], v[96:99], v[224:227], a[48:51]\n\t"
    "s_waitcnt lgkmcnt(5)\n\t"
    "v_exp_f32 v177, v65\n\t"
    "v_mfma_f32_16x16x32_bf16 a[64:67], v[96:99], v[228:231], a[64:67]\n\t"
    "ds_read_b128 v[220:223], v181 offset:2560\n\t"
    "v_mfma_f32_16x16x32_bf16 a[68:71], v[100:103], v[228:231], a[68:71]\n\t"
    "s_waitcnt lgkmcnt(5)\n\t"
    "v_exp_f32 v178, v66\n\t"
    "v_mfma_f32_16x16x32_bf16 a[84:87], v[100:103], v[232:235], a[84:87]\n\t"
    "ds_read_b128 v[224:227], v181 offset:2816\n\t"
    "v_add_f32 v172, v176, v177\n\t"
    "v_mfma_f32_16x16x32_bf16 a[80:83], v[96:99], v[232:235], a[80:83]\n\t"
    "s_waitcnt lgkmcnt(5)\n\t"
    "v_exp_f32 v179, v67\n\t"
    "v_mfma_f32_16x16x32_bf16 a[96:99], v[96:99], v[204:207], a[96:99]\n\t"
    "ds_read_b128 v[228:231], v192 offset:24576\n\t"
    "v_cvt_pk_bf16_f32 v112, v176, v177\n\t"
    "v_mfma_f32_16x16x32_bf16 a[100:103], v[100:103], v[204:207], a[100:103]\n\t"
    "s_waitcnt lgkmcnt(5)\n\t"
    "v_exp_f32 v176, v80\n\t"
    "v_mfma_f32_16x16x32_bf16 a[116:119], v[100:103], v[208:211], a[116:119]\n\t"
    "ds_read_b128 v[232:235], v192 offset:26112\n\t"
    "v_add_f32 v172, v172, v178\n\t"
    "v_mfma_f32_16x16x32_bf16 a[112:115], v[96:99], v[208:211], a[112:115]\n\t"
    "s_waitcnt lgkmcnt(5)\n\t"
    "v_exp_f32 v177, v81\n\t"
    "v_add_f32 v172, v172, v179\n\t"
    "v_mfma_f32_16x16x32_bf16 a[128:131], v[96:99], v[212:215], a[128:131]\n\t"
    "ds_read_b128 v[204:207], v193 offset:24576\n\t"
    "v_cvt_pk_bf16_f32 v113, v178, v179\n\t"
    "v_mfma_f32_16x16x32_bf16 a[132:135], v[100:103], v[212:215], a[132:135]\n\t"
    "s_waitcnt lgkmcnt(5)\n\t"
    "v_exp_f32 v178, v82\n\t"
    "v_mfma_f32_16x16x32_bf16 a[148:151], v[100:103], v[216:219], a[148:151]\n\t"
    "ds_read_b128 v[208:211], v193 offset:26112\n\t"
    "v_add_f32 v172, v172, v176\n\t"
    "v_mfma_f32_16x16x32_bf16 a[144:147], v[96:99], v[216:219], a[144:147]\n\t"
    "s_waitcnt lgkmcnt(5)\n\t"
    "v_exp_f32 v179, v83\n\t"
    "v_add_f32 v172, v172, v177\n\t"
    "v_mfma_f32_16x16x32_bf16 a[160:163], v[96:99], v[220:223], a[160:163]\n\t"
    "ds_read_b128 v[212:215], v192 offset:24704\n\t"
    "v_cvt_pk_bf16_f32 v114, v176, v177\n\t"
    "v_mfma_f32_16x16x32_bf16 a[164:167], v[100:103], v[220:223], a[164:167]\n\t"
    "s_waitcnt lgkmcnt(5)\n\t"
    "v_exp_f32 v176, v68\n\t"
    "v_mfma_f32_16x16x32_bf16 a[180:183], v[100:103], v[224:227], a[180:183]\n\t"
    "ds_read_b128 v[216:219], v192 offset:26240\n\t"
    "v_add_f32 v172, v172, v178\n\t"
    "v_mfma_f32_16x16x32_bf16 a[176:179], v[96:99], v[224:227], a[176:179]\n\t"
    "s_waitcnt lgkmcnt(4)\n\t"
    "v_exp_f32 v177, v69\n\t"
    "v_add_f32 v172, v172, v179\n\t"
    "v_mfma_f32_16x16x32_bf16 v[40:43], v[228:231], a[224:227], v[136:139]\n\t"
    "ds_read_b128 v[220:223], v193 offset:24704\n\t"
    "v_cvt_pk_bf16_f32 v115, v178, v179\n\t"
    "v_mfma_f32_16x16x32_bf16 v[56:59], v[232:235], a[224:227], v[136:139]\n\t"
    "v_exp_f32 v178, v70\n\t"
    "v_mfma_f32_16x16x32_bf16 v[60:63], v[232:235], a[240:243], v[140:143]\n\t"
    "ds_read_b128 v[224:227], v193 offset:26240\n\t"
    "v_add_f32 v173, v176, v177\n\t"
    "v_mfma_f32_16x16x32_bf16 v[44:47], v[228:231], a[240:243], v[140:143]\n\t"
    "s_waitcnt lgkmcnt(4)\n\t"
    "v_exp_f32 v179, v71\n\t"
    "v_mfma_f32_16x16x32_bf16 v[40:43], v[204:207], a[228:231], v[40:43]\n\t"
    "ds_read_b128 v[228:231], v192 offset:24832\n\t"
    "v_cvt_pk_bf16_f32 v116, v176, v177\n\t"
    "v_mfma_f32_16x16x32_bf16 v[56:59], v[208:211], a[228:231], v[56:59]\n\t"
    "v_exp_f32 v176, v84\n\t"
    "v_mfma_f32_16x16x32_bf16 v[60:63], v[208:211], a[244:247], v[60:63]\n\t"
    "ds_read_b128 v[232:235], v192 offset:26368\n\t"
    "v_add_f32 v173, v173, v178\n\t"
    "v_mfma_f32_16x16x32_bf16 v[44:47], v[204:207], a[244:247], v[44:47]\n\t"
    "s_waitcnt lgkmcnt(4)\n\t"
    "v_exp_f32 v177, v85\n\t"
    "v_add_f32 v173, v173, v179\n\t"
    "v_mfma_f32_16x16x32_bf16 v[40:43], v[212:215], a[232:235], v[40:43]\n\t"
    "ds_read_b128 v[204:207], v193 offset:24832\n\t"
    "v_cvt_pk_bf16_f32 v117, v178, v179\n\t"
    "v_mfma_f32_16x16x32_bf16 v[56:59], v[216:219], a[232:235], v[56:59]\n\t"
    "v_exp_f32 v178, v86\n\t"
    "v_mfma_f32_16x16x32_bf16 v[60:63], v[216:219], a[248:251], v[60:63]\n\t"
    "ds_read_b128 v[208:211], v193 offset:26368\n\t"
    "v_add_f32 v173, v173, v176\n\t"
    "v_mfma_f32_16x16x32_bf16 v[44:47], v[212:215], a[248:251], v[44:47]\n\t"
    "s_waitcnt lgkmcnt(4)\n\t"
    "s_waitcnt vmcnt(0)\n\t"
    "s_barrier\n\t"
    "v_mfma_f32_16x16x32_bf16 v[40:43], v[220:223], a[236:239], v[40:43]\n\t"
    "ds_read_b128 v[212:215], v181 offset:12288\n\t"
    "v_mfma_f32_16x16x32_bf16 v[56:59], v[224:227], a[236:239], v[56:59]\n\t"
    "v_exp_f32 v179, v87\n\t"
    "v_add_f32 v173, v173, v177\n\t"
    "v_mfma_f32_16x16x32_bf16 v[60:63], v[224:227], a[252:255], v[60:63]\n\t"
    "ds_read_b128 v[216:219], v181 offset:12544\n\t"
    "v_cvt_pk_bf16_f32 v118, v176, v177\n\t"
    "v_mfma_f32_16x16x32_bf16 v[44:47], v[220:223], a[252:255], v[44:47]\n\t"
    "s_waitcnt lgkmcnt(4)\n\t"
    "v_add_f32 v173, v173, v178\n\t"
    "v_mfma_f32_16x16x32_bf16 v[40:43], v[228:231], v[16:19], v[40:43]\n\t"
    "ds_read_b128 v[220:223], v181 offset:12800\n\t"
    "v_add_f32 v173, v173, v179\n\t"
    "v_mfma_f32_16x16x32_bf16 v[56:59], v[232:235], v[16:19], v[56:59]\n\t"
    "v_cvt_pk_bf16_f32 v119, v178, v179\n\t"
    "v_max_f32 v180, v172, v173\n\t"
    "v_mfma_f32_16x16x32_bf16 v[60:63], v[232:235], v[24:27], v[60:63]\n\t"
    "ds_read_b128 v[224:227], v181 offset:13056\n\t"
    "v_cmp_lt_f32 vcc, s62, v180\n\t"
    "v_mfma_f32_16x16x32_bf16 v[44:47], v[228:231], v[24:27], v[44:47]\n\t"
    "s_waitcnt lgkmcnt(4)\n\t"
    "v_mfma_f32_16x16x32_bf16 v[40:43], v[204:207], v[20:23], v[40:43]\n\t"
    "ds_read_b128 v[228:231], v181 offset:13312\n\t"
    "v_mfma_f32_16x16x32_bf16 v[56:59], v[208:211], v[20:23], v[56:59]\n\t"
    "v_mfma_f32_16x16x32_bf16 v[60:63], v[208:211], v[28:31], v[60:63]\n\t"
    "ds_read_b128 v[232:235], v181 offset:13568\n\t"
    "v_mfma_f32_16x16x32_bf16 v[44:47], v[204:207], v[28:31], v[44:47]\n\t"
    "s_waitcnt lgkmcnt(5)\n\t"
    "s_cbranch_vccnz L_stub_h3\n\t"
    "L_ret_h3:\n\t"
    "v_mfma_f32_16x16x32_bf16 a[0:3], v[112:115], v[212:215], a[0:3]\n\t"
    "ds_read_b128 v[204:207], v181 offset:13824\n\t"
    "v_add_f32 v168, v168, v172\n\t"
    "v_mfma_f32_16x16x32_bf16 a[4:7], v[116:119], v[212:215], a[4:7]\n\t"
    "s_waitcnt lgkmcnt(5)\n\t"
    "v_add_f32 v169, v169, v173\n\t"
    "v_lshrrev_b32 v48, 4, v195\n\t"
    "v_and_b32 v50, 48, v48\n\t"
    "v_mul_u32_u24 v52, 96, v50\n\t"
    "v_and_b32 v48, 15, v48\n\t"
    "v_lshl_add_u32 v51, v48, 4, v52\n\t"
    "v_lshl_add_u32 v52, v48, 3, v52\n\t"
    "v_mov_b32 v48, v168\n\t"
    "v_mov_b32 v49, v169\n\t"
    "v_nop\n\t"
    "v_nop\n\t"
    "v_permlane32_swap_b32 v168, v48\n\t"
    "v_permlane32_swap_b32 v169, v49\n\t"
    "v_nop\n\t"
    "v_add_f32 v168, v168, v48\n\t"
    "v_add_f32 v169, v169, v49\n\t"
    "v_mov_b32 v48, v168\n\t"
    "v_mov_b32 v49, v169\n\t"
    "v_nop\n\t"
    "v_nop\n\t"
    "v_permlane16_swap_b32 v168, v48\n\t"
    "v_permlane16_swap_b32 v169, v49\n\t"
    "v_nop\n\t"
    "v_add_f32 v168, v168, v48\n\t"
    "v_add_f32 v169, v169, v49\n\t"
    "v_rcp_f32 v168, v168\n\t"
    "v_rcp_f32 v169, v169\n\t"
    "v_nop\n\t"
    "ds_bpermute_b32 v32, v50, v168 offset:0\n\t"
    "ds_bpermute_b32 v33, v50, v168 offset:4\n\t"
    "ds_bpermute_b32 v34, v50, v168 offset:8\n\t"
    "ds_bpermute_b32 v35, v50, v168 offset:12\n\t"
    "ds_bpermute_b32 v36, v50, v169 offset:0\n\t"
    "ds_bpermute_b32 v37, v50, v169 offset:4\n\t"
    "ds_bpermute_b32 v38, v50, v169 offset:8\n\t"
    "ds_bpermute_b32 v39, v50, v169 offset:12\n\t"
    "v_mfma_f32_16x16x32_bf16 a[20:23], v[116:119], v[216:219], a[20:23]\n\t"
    "ds_read_b128 v[208:211], v181 offset:14080\n\t"
    "v_mfma_f32_16x16x32_bf16 a[16:19], v[112:115], v[216:219], a[16:19]\n\t"
    "s_waitcnt lgkmcnt(13)\n\t"
    "v_exp_f32 v178, v78\n\t"
    "v_mfma_f32_16x16x32_bf16 a[32:35], v[112:115], v[220:223], a[32:35]\n\t"
    "ds_read_b128 v[212:215], v181 offset:14336\n\t"
    "v_mfma_f32_16x16x32_bf16 a[36:39], v[116:119], v[220:223], a[36:39]\n\t"
    "s_waitcnt lgkmcnt(13)\n\t"
    "v_exp_f32 v179, v79\n\t"
    "v_mfma_f32_16x16x32_bf16 a[52:55], v[116:119], v[224:227], a[52:55]\n\t"
    "ds_read_b128 v[216:219], v181 offset:14592\n\t"
    "v_mfma_f32_16x16x32_bf16 a[48:51], v[112:115], v[224:227], a[48:51]\n\t"
    "s_waitcnt lgkmcnt(13)\n\t"
    "v_exp_f32 v176, v92\n\t"
    "v_mfma_f32_16x16x32_bf16 a[64:67], v[112:115], v[228:231], a[64:67]\n\t"
    "ds_read_b128 v[220:223], v181 offset:14848\n\t"
    "v_add_f32 v175, v175, v178\n\t"
    "v_mfma_f32_16x16x32_bf16 a[68:71], v[116:119], v[228:231], a[68:71]\n\t"
    "s_waitcnt lgkmcnt(13)\n\t"
    "v_exp_f32 v177, v93\n\t"
    "v_add_f32 v175, v175, v179\n\t"
    "v_mfma_f32_16x16x32_bf16 a[84:87], v[116:119], v[232:235], a[84:87]\n\t"
    "ds_read_b128 v[224:227], v181 offset:15104\n\t"
    "v_cvt_pk_bf16_f32 v125, v178, v179\n\t"
    "v_mfma_f32_16x16x32_bf16 a[80:83], v[112:115], v[232:235], a[80:83]\n\t"
    "s_waitcnt lgkmcnt(13)\n\t"
    "v_exp_f32 v178, v94\n\t"
    "v_mfma_f32_16x16x32_bf16 a[96:99], v[112:115], v[204:207], a[96:99]\n\t"
    "ds_read_b128 v[228:231], v194 offset:12288\n\t"
    "v_add_f32 v175, v175, v176\n\t"
    "v_mfma_f32_16x16x32_bf16 a[100:103], v[116:119], v[204:207], a[100:103]\n\t"
    "s_waitcnt lgkmcnt(5)\n\t"
    "v_exp_f32 v179, v95\n\t"
    "v_add_f32 v175, v175, v177\n\t"
    "v_mfma_f32_16x16x32_bf16 a[116:119], v[116:119], v[208:211], a[116:119]\n\t"
    "ds_read_b128 v[232:235], v194 offset:12544\n\t"
    "v_cvt_pk_bf16_f32 v126, v176, v177\n\t"
    "v_mfma_f32_16x16x32_bf16 a[112:115], v[112:115], v[208:211], a[112:115]\n\t"
    "s_waitcnt lgkmcnt(5)\n\t"
    "v_add_f32 v175, v175, v178\n\t"
    "v_mfma_f32_16x16x32_bf16 a[128:131], v[112:115], v[212:215], a[128:131]\n\t"
    "ds_read_b128 v[204:207], v194 offset:12800\n\t"
    "v_add_f32 v175, v175, v179\n\t"
    "v_mfma_f32_16x16x32_bf16 a[132:135], v[116:119], v[212:215], a[132:135]\n\t"
    "s_waitcnt lgkmcnt(5)\n\t"
    "v_cvt_pk_bf16_f32 v127, v178, v179\n\t"
    "v_max_f32 v180, v174, v175\n\t"
    "v_mfma_f32_16x16x32_bf16 a[148:151], v[116:119], v[216:219], a[148:151]\n\t"
    "ds_read_b128 v[208:211], v194 offset:13056\n\t"
    "v_cmp_lt_f32 vcc, s62, v180\n\t"
    "v_mfma_f32_16x16x32_bf16 a[144:147], v[112:115], v[216:219], a[144:147]\n\t"
    "s_waitcnt lgkmcnt(5)\n\t"
    "v_mfma_f32_16x16x32_bf16 a[160:163], v[112:115], v[220:223], a[160:163]\n\t"
    "ds_read_b128 v[212:215], v194 offset:13312\n\t"
    "v_mfma_f32_16x16x32_bf16 a[164:167], v[116:119], v[220:223], a[164:167]\n\t"
    "s_waitcnt lgkmcnt(5)\n\t"
    "v_mfma_f32_16x16x32_bf16 a[180:183], v[116:119], v[224:227], a[180:183]\n\t"
    "ds_read_b128 v[216:219], v194 offset:13568\n\t"
    "v_mfma_f32_16x16x32_bf16 a[176:179], v[112:115], v[224:227], a[176:179]\n\t"
    "s_waitcnt lgkmcnt(5)\n\t"
    "s_cbranch_vccnz L_stub_h1\n\t"
    "L_ret_h1:\n\t"
    "v_mfma_f32_16x16x32_bf16 a[8:11], v[120:123], v[228:231], a[8:11]\n\t"
    "ds_read_b128 v[220:223], v194 offset:13824\n\t"
    "v_add_f32 v170, v170, v174\n\t"
    "v_mfma_f32_16x16x32_bf16 a[12:15], v[124:127], v[228:231], a[12:15]\n\t"
    "s_waitcnt lgkmcnt(5)\n\t"
    "v_add_f32 v171, v171, v175\n\t"
    "v_mfma_f32_16x16x32_bf16 a[28:31], v[124:127], v[232:235], a[28:31]\n\t"
    "s_waitcnt lgkmcnt(12)\n\t"
    "ds_read_b128 v[224:227], v194 offset:14080\n\t"
    "v_accvgpr_read_b32 v236, a0\n\t"
    "v_accvgpr_read_b32 v237, a16\n\t"
    "v_accvgpr_read_b32 v238, a32\n\t"
    "v_accvgpr_read_b32 v239, a48\n\t"
    "v_accvgpr_read_b32 v240, a64\n\t"
    "v_accvgpr_read_b32 v241, a80\n\t"
    "v_accvgpr_read_b32 v242, a96\n\t"
    "v_accvgpr_read_b32 v243, a112\n\t"
    "v_accvgpr_read_b32 v244, a128\n\t"
    "v_mfma_f32_16x16x32_bf16 a[24:27], v[120:123], v[232:235], a[24:27]\n\t"
    "s_waitcnt lgkmcnt(5)\n\t"
    "v_exp_f32 v176, v40\n\t"
    "v_accvgpr_read_b32 v245, a144\n\t"
    "v_accvgpr_read_b32 v246, a160\n\t"
    "v_accvgpr_read_b32 v247, a176\n\t"
    "v_mul_f32 v236, v236, v32\n\t"
    "v_mul_f32 v237, v237, v32\n\t"
    "v_mul_f32 v238, v238, v32\n\t"
    "v_mul_f32 v239, v239, v32\n\t"
    "v_mul_f32 v240, v240, v32\n\t"
    "v_mfma_f32_16x16x32_bf16 a[40:43], v[120:123], v[204:207], a[40:43]\n\t"
    "ds_read_b128 v[228:231], v194 offset:14336\n\t"
    "v_mul_f32 v241, v241, v32\n\t"
    "v_mul_f32 v242, v242, v32\n\t"
    "v_mul_f32 v243, v243, v32\n\t"
    "v_mul_f32 v244, v244, v32\n\t"
    "v_mul_f32 v245, v245, v32\n\t"
    "v_mul_f32 v246, v246, v32\n\t"
    "v_mul_f32 v247, v247, v32\n\t"
    "v_cvt_pk_bf16_f32 v248, v236, v237\n\t"
    "v_cvt_pk_bf16_f32 v249, v238, v239\n\t"
    "v_mfma_f32_16x16x32_bf16 a[44:47], v[124:127], v[204:207], a[44:47]\n\t"
    "s_waitcnt lgkmcnt(5)\n\t"
    "v_exp_f32 v177, v41\n\t"
    "v_cvt_pk_bf16_f32 v250, v240, v241\n\t"
    "v_cvt_pk_bf16_f32 v251, v242, v243\n\t"
    "v_cvt_pk_bf16_f32 v252, v244, v245\n\t"
    "v_cvt_pk_bf16_f32 v253, v246, v247\n\t"
    "v_accvgpr_read_b32 v236, a1\n\t"
    "v_accvgpr_read_b32 v237, a17\n\t"
    "v_accvgpr_read_b32 v238, a33\n\t"
    "v_accvgpr_read_b32 v239, a49\n\t"
    "v_mfma_f32_16x16x32_bf16 a[60:63], v[124:127], v[208:211], a[60:63]\n\t"
    "ds_read_b128 v[232:235], v194 offset:14592\n\t"
    "v_accvgpr_read_b32 v240, a65\n\t"
    "v_accvgpr_read_b32 v241, a81\n\t"
    "v_accvgpr_read_b32 v242, a97\n\t"
    "v_accvgpr_read_b32 v243, a113\n\t"
    "v_accvgpr_read_b32 v244, a129\n\t"
    "v_accvgpr_read_b32 v245, a145\n\t"
    "v_accvgpr_read_b32 v246, a161\n\t"
    "v_accvgpr_read_b32 v247, a177\n\t"
    "v_mul_f32 v236, v236, v33\n\t"
    "v_mfma_f32_16x16x32_bf16 a[56:59], v[120:123], v[208:211], a[56:59]\n\t"
    "s_waitcnt lgkmcnt(5)\n\t"
    "v_exp_f32 v178, v42\n\t"
    "v_mul_f32 v237, v237, v33\n\t"
    "v_mul_f32 v238, v238, v33\n\t"
    "v_mul_f32 v239, v239, v33\n\t"
    "v_mul_f32 v240, v240, v33\n\t"
    "v_mul_f32 v241, v241, v33\n\t"
    "v_mul_f32 v242, v242, v33\n\t"
    "v_mul_f32 v243, v243, v33\n\t"
    "v_mul_f32 v244, v244, v33\n\t"
    "v_mfma_f32_16x16x32_bf16 a[72:75], v[120:123], v[212:215], a[72:75]\n\t"
    "ds_read_b128 v[204:207], v194 offset:14848\n\t"
    "v_add_f32 v174, v176, v177\n\t"
    "v_mul_f32 v245, v245, v33\n\t"
    "v_mul_f32 v246, v246, v33\n\t"
    "v_mul_f32 v247, v247, v33\n\t"
    "v_cvt_pk_bf16_f32 v160, v236, v237\n\t"
    "v_cvt_pk_bf16_f32 v161, v238, v239\n\t"
    "v_cvt_pk_bf16_f32 v162, v240, v241\n\t"
    "v_cvt_pk_bf16_f32 v163, v242, v243\n\t"
    "v_mfma_f32_16x16x32_bf16 a[76:79], v[124:127], v[212:215], a[76:79]\n\t"
    "s_waitcnt lgkmcnt(5)\n\t"
    "buffer_store_dwordx4 v[248:251], v51, s[56:59], s95 offen offset:0 sc1\n\t"
    "v_exp_f32 v179, v43\n\t"
    "v_cvt_pk_bf16_f32 v164, v244, v245\n\t"
    "v_cvt_pk_bf16_f32 v165, v246, v247\n\t"
    "v_accvgpr_read_b32 v236, a2\n\t"
    "v_accvgpr_read_b32 v237, a18\n\t"
    "v_accvgpr_read_b32 v238, a34\n\t"
    "v_accvgpr_read_b32 v239, a50\n\t"
    "v_accvgpr_read_b32 v240, a66\n\t"
    "v_accvgpr_read_b32 v241, a82\n\t"
    "v_mfma_f32_16x16x32_bf16 a[92:95], v[124:127], v[216:219], a[92:95]\n\t"
    "ds_read_b128 v[208:211], v194 offset:15104\n\t"
    "v_cvt_pk_bf16_f32 v104, v176, v177\n\t"
    "v_accvgpr_read_b32 v242, a98\n\t"
    "v_accvgpr_read_b32 v243, a114\n\t"
    "v_accvgpr_read_b32 v244, a130\n\t"
    "v_accvgpr_read_b32 v245, a146\n\t"
    "v_accvgpr_read_b32 v246, a162\n\t"
    "v_accvgpr_read_b32 v247, a178\n\t"
    "v_mul_f32 v236, v236, v34\n\t"
    "v_mul_f32 v237, v237, v34\n\t"
    "v_mfma_f32_16x16x32_bf16 a[88:91], v[120:123], v[216:219], a[88:91]\n\t"
    "s_waitcnt lgkmcnt(5)\n\t"
    "buffer_store_dwordx2 v[252:253], v52, s[56:59], s95 offen offset:256 sc1\n\t"
    "v_exp_f32 v176, v56\n\t"
    "v_mul_f32 v238, v238, v34\n\t"
    "v_mul_f32 v239, v239, v34\n\t"
    "v_mul_f32 v240, v240, v34\n\t"
    "v_mul_f32 v241, v241, v34\n\t"
    "v_mul_f32 v242, v242, v34\n\t"
    "v_mul_f32 v243, v243, v34\n\t"
    "v_mul_f32 v244, v244, v34\n\t"
    "v_mul_f32 v245, v245, v34\n\t"
    "v_mfma_f32_16x16x32_bf16 a[104:107], v[120:123], v[220:223], a[104:107]\n\t"
    "ds_read_b128 v[212:215], v192 offset:36864\n\t"
    "v_add_f32 v174, v174, v178\n\t"
    "v_mul_f32 v246, v246, v34\n\t"
    "v_mul_f32 v247, v247, v34\n\t"
    "v_cvt_pk_bf16_f32 v248, v236, v237\n\t"
    "v_cvt_pk_bf16_f32 v249, v238, v239\n\t"
    "v_cvt_pk_bf16_f32 v250, v240, v241\n\t"
    "v_cvt_pk_bf16_f32 v251, v242, v243\n\t"
    "v_cvt_pk_bf16_f32 v252, v244, v245\n\t"
    "v_mfma_f32_16x16x32_bf16 a[108:111], v[124:127], v[220:223], a[108:111]\n\t"
    "s_waitcnt lgkmcnt(5)\n\t"
    "buffer_store_dwordx4 v[160:163], v51, s[56:59], s95 offen offset:384 sc1\n\t"
    "v_exp_f32 v177, v57\n\t"
    "v_add_f32 v174, v174, v179\n\t"
    "v_cvt_pk_bf16_f32 v253, v246, v247\n\t"
    "v_accvgpr_read_b32 v236, a3\n\t"
    "v_accvgpr_read_b32 v237, a19\n\t"
    "v_accvgpr_read_b32 v238, a35\n\t"
    "v_accvgpr_read_b32 v239, a51\n\t"
    "v_accvgpr_read_b32 v240, a67\n\t"
    "v_accvgpr_read_b32 v241, a83\n\t"
    "v_mfma_f32_16x16x32_bf16 a[124:127], v[124:127], v[224:227], a[124:127]\n\t"
    "ds_read_b128 v[216:219], v192 offset:38400\n\t"
    "v_cvt_pk_bf16_f32 v105, v178, v179\n\t"
    "v_accvgpr_read_b32 v242, a99\n\t"
    "v_accvgpr_read_b32 v243, a115\n\t"
    "v_accvgpr_read_b32 v244, a131\n\t"
    "v_accvgpr_read_b32 v245, a147\n\t"
    "v_accvgpr_read_b32 v246, a163\n\t"
    "v_accvgpr_read_b32 v247, a179\n\t"
    "v_mul_f32 v236, v236, v35\n\t"
    "v_mul_f32 v237, v237, v35\n\t"
    "v_mfma_f32_16x16x32_bf16 a[120:123], v[120:123], v[224:227], a[120:123]\n\t"
    "s_waitcnt lgkmcnt(5)\n\t"
    "buffer_store_dwordx2 v[164:165], v52, s[56:59], s95 offen offset:640 sc1\n\t"
    "v_exp_f32 v178, v58\n\t"
    "v_mul_f32 v238, v238, v35\n\t"
    "v_mul_f32 v239, v239, v35\n\t"
    "v_mul_f32 v240, v240, v35\n\t"
    "v_mul_f32 v241, v241, v35\n\t"
    "v_mul_f32 v242, v242, v35\n\t"
    "v_mul_f32 v243, v243, v35\n\t"
    "v_mul_f32 v244, v244, v35\n\t"
    "v_mul_f32 v245, v245, v35\n\t"
    "v_mfma_f32_16x16x32_bf16 a[136:139], v[120:123], v[228:231], a[136:139]\n\t"
    "ds_read_b128 v[220:223], v193 offset:36864\n\t"
    "v_add_f32 v174, v174, v176\n\t"
    "v_mul_f32 v246, v246, v35\n\t"
    "v_mul_f32 v247, v247, v35\n\t"
    "v_cvt_pk_bf16_f32 v160, v236, v237\n\t"
    "v_cvt_pk_bf16_f32 v161, v238, v239\n\t"
    "v_cvt_pk_bf16_f32 v162, v240, v241\n\t"
    "v_cvt_pk_bf16_f32 v163, v242, v243\n\t"
    "v_cvt_pk_bf16_f32 v164, v244, v245\n\t"
    "v_mfma_f32_16x16x32_bf16 a[140:143], v[124:127], v[228:231], a[140:143]\n\t"
    "s_waitcnt lgkmcnt(5)\n\t"
    "buffer_store_dwordx4 v[248:251], v51, s[56:59], s95 offen offset:768 sc1\n\t"
    "v_exp_f32 v179, v59\n\t"
    "v_add_f32 v174, v174, v177\n\t"
    "v_cvt_pk_bf16_f32 v165, v246, v247\n\t"
    "v_accvgpr_read_b32 v236, a4\n\t"
    "v_accvgpr_read_b32 v237, a20\n\t"
    "v_accvgpr_read_b32 v238, a36\n\t"
    "v_accvgpr_read_b32 v239, a52\n\t"
    "v_accvgpr_read_b32 v240, a68\n\t"
    "v_accvgpr_read_b32 v241, a84\n\t"
    "v_mfma_f32_16x16x32_bf16 a[156:159], v[124:127], v[232:235], a[156:159]\n\t"
    "ds_read_b128 v[224:227], v193 offset:38400\n\t"
    "v_cvt_pk_bf16_f32 v106, v176, v177\n\t"
    "v_accvgpr_read_b32 v242, a100\n\t"
    "v_accvgpr_read_b32 v243, a116\n\t"
    "v_accvgpr_read_b32 v244, a132\n\t"
    "v_accvgpr_read_b32 v245, a148\n\t"
    "v_accvgpr_read_b32 v246, a164\n\t"
    "v_accvgpr_read_b32 v247, a180\n\t"
    "v_mul_f32 v236, v236, v36\n\t"
    "v_mul_f32 v237, v237, v36\n\t"
    "v_mfma_f32_16x16x32_bf16 a[152:155], v[120:123], v[232:235], a[152:155]\n\t"
    "s_waitcnt lgkmcnt(5)\n\t"
    "buffer_store_dwordx2 v[252:253], v52, s[56:59], s95 offen offset:1024 sc1\n\t"
    "v_exp_f32 v176, v44\n\t"
    "v_mul_f32 v238, v238, v36\n\t"
    "v_mul_f32 v239, v239, v36\n\t"
    "v_mul_f32 v240, v240, v36\n\t"
    "v_mul_f32 v241, v241, v36\n\t"
    "v_mul_f32 v242, v242, v36\n\t"
    "v_mul_f32 v243, v243, v36\n\t"
    "v_mul_f32 v244, v244, v36\n\t"
    "v_mul_f32 v245, v245, v36\n\t"
    "v_mfma_f32_16x16x32_bf16 a[168:171], v[120:123], v[204:207], a[168:171]\n\t"
    "ds_read_b128 v[228:231], v192 offset:36992\n\t"
    "v_add_f32 v174, v174, v178\n\t"
    "v_mul_f32 v246, v246, v36\n\t"
    "v_mul_f32 v247, v247, v36\n\t"
    "v_cvt_pk_bf16_f32 v248, v236, v237\n\t"
    "v_cvt_pk_bf16_f32 v249, v238, v239\n\t"
    "v_cvt_pk_bf16_f32 v250, v240, v241\n\t"
    "v_cvt_pk_bf16_f32 v251, v242, v243\n\t"
    "v_cvt_pk_bf16_f32 v252, v244, v245\n\t"
    "v_mfma_f32_16x16x32_bf16 a[172:175], v[124:127], v[204:207], a[172:175]\n\t"
    "s_waitcnt lgkmcnt(5)\n\t"
    "buffer_store_dwordx4 v[160:163], v51, s[56:59], s95 offen offset:1152 sc1\n\t"
    "v_exp_f32 v177, v45\n\t"
    "v_add_f32 v174, v174, v179\n\t"
    "v_cvt_pk_bf16_f32 v253, v246, v247\n\t"
    "v_accvgpr_read_b32 v236, a5\n\t"
    "v_accvgpr_read_b32 v237, a21\n\t"
    "v_accvgpr_read_b32 v238, a37\n\t"
    "v_accvgpr_read_b32 v239, a53\n\t"
    "v_accvgpr_read_b32 v240, a69\n\t"
    "v_accvgpr_read_b32 v241, a85\n\t"
    "v_mfma_f32_16x16x32_bf16 a[188:191], v[124:127], v[208:211], a[188:191]\n\t"
    "ds_read_b128 v[232:235], v192 offset:38528\n\t"
    "v_cvt_pk_bf16_f32 v107, v178, v179\n\t"
    "v_accvgpr_read_b32 v242, a101\n\t"
    "v_accvgpr_read_b32 v243, a117\n\t"
    "v_accvgpr_read_b32 v244, a133\n\t"
    "v_accvgpr_read_b32 v245, a149\n\t"
    "v_accvgpr_read_b32 v246, a165\n\t"
    "v_accvgpr_read_b32 v247, a181\n\t"
    "v_mul_f32 v236, v236, v37\n\t"
    "v_mul_f32 v237, v237, v37\n\t"
    "v_mfma_f32_16x16x32_bf16 a[184:187], v[120:123], v[208:211], a[184:187]\n\t"
    "s_waitcnt lgkmcnt(4)\n\t"
    "buffer_store_dwordx2 v[164:165], v52, s[56:59], s95 offen offset:1408 sc1\n\t"
    "v_exp_f32 v178, v46\n\t"
    "v_mul_f32 v238, v238, v37\n\t"
    "v_mul_f32 v239, v239, v37\n\t"
    "v_mul_f32 v240, v240, v37\n\t"
    "v_mul_f32 v241, v241, v37\n\t"
    "v_mul_f32 v242, v242, v37\n\t"
    "v_mul_f32 v243, v243, v37\n\t"
    "v_mul_f32 v244, v244, v37\n\t"
    "v_mul_f32 v245, v245, v37\n\t"
    "v_mfma_f32_16x16x32_bf16 v[72:75], v[212:215], a[224:227], v[136:139]\n\t"
    "ds_read_b128 v[204:207], v193 offset:36992\n\t"
    "v_add_f32 v175, v176, v177\n\t"
    "v_mul_f32 v246, v246, v37\n\t"
    "v_mul_f32 v247, v247, v37\n\t"
    "v_cvt_pk_bf16_f32 v160, v236, v237\n\t"
    "v_cvt_pk_bf16_f32 v161, v238, v239\n\t"
    "v_cvt_pk_bf16_f32 v162, v240, v241\n\t"
    "v_cvt_pk_bf16_f32 v163, v242, v243\n\t"
    "v_cvt_pk_bf16_f32 v164, v244, v245\n\t"
    "v_mfma_f32_16x16x32_bf16 v[88:91], v[216:219], a[224:227], v[136:139]\n\t"
    "buffer_store_dwordx4 v[248:251], v51, s[56:59], s96 offen offset:0 sc1\n\t"
    "v_exp_f32 v179, v47\n\t"
    "v_cvt_pk_bf16_f32 v165, v246, v247\n\t"
    "v_accvgpr_read_b32 v236, a6\n\t"
    "v_accvgpr_read_b32 v237, a22\n\t"
    "v_accvgpr_read_b32 v238, a38\n\t"
    "v_accvgpr_read_b32 v239, a54\n\t"
    "v_accvgpr_read_b32 v240, a70\n\t"
    "v_accvgpr_read_b32 v241, a86\n\t"
    "v_accvgpr_read_b32 v242, a102\n\t"
    "v_mfma_f32_16x16x32_bf16 v[92:95], v[216:219], a[240:243], v[140:143]\n\t"
    "ds_read_b128 v[208:211], v193 offset:38528\n\t"
    "v_cvt_pk_bf16_f32 v108, v176, v177\n\t"
    "v_accvgpr_read_b32 v243, a118\n\t"
    "v_accvgpr_read_b32 v244, a134\n\t"
    "v_accvgpr_read_b32 v245, a150\n\t"
    "v_accvgpr_read_b32 v246, a166\n\t"
    "v_accvgpr_read_b32 v247, a182\n\t"
    "v_mul_f32 v236, v236, v38\n\t"
    "v_mul_f32 v237, v237, v38\n\t"
    "v_mul_f32 v238, v238, v38\n\t"
    "v_mfma_f32_16x16x32_bf16 v[76:79], v[212:215], a[240:243], v[140:143]\n\t"
    "s_waitcnt lgkmcnt(4)\n\t"
    "buffer_store_dwordx2 v[252:253], v52, s[56:59], s96 offen offset:256 sc1\n\t"
    "v_exp_f32 v176, v60\n\t"
    "v_mul_f32 v239, v239, v38\n\t"
    "v_mul_f32 v240, v240, v38\n\t"
    "v_mul_f32 v241, v241, v38\n\t"
    "v_mul_f32 v242, v242, v38\n\t"
    "v_mul_f32 v243, v243, v38\n\t"
    "v_mul_f32 v244, v244, v38\n\t"
    "v_mul_f32 v245, v245, v38\n\t"
    "v_mul_f32 v246, v246, v38\n\t"
    "v_mfma_f32_16x16x32_bf16 v[72:75], v[220:223], a[228:231], v[72:75]\n\t"
    "ds_read_b128 v[212:215], v192 offset:37120\n\t"
    "v_add_f32 v175, v175, v178\n\t"
    "v_mul_f32 v247, v247, v38\n\t"
    "v_cvt_pk_bf16_f32 v248, v236, v237\n\t"
    "v_cvt_pk_bf16_f32 v249, v238, v239\n\t"
    "v_cvt_pk_bf16_f32 v250, v240, v241\n\t"
    "v_cvt_pk_bf16_f32 v251, v242, v243\n\t"
    "v_cvt_pk_bf16_f32 v252, v244, v245\n\t"
    "v_mfma_f32_16x16x32_bf16 v[88:91], v[224:227], a[228:231], v[88:91]\n\t"
    "buffer_store_dwordx4 v[160:163], v51, s[56:59], s96 offen offset:384 sc1\n\t"
    "v_exp_f32 v177, v61\n\t"
    "v_add_f32 v175, v175, v179\n\t"
    "v_cvt_pk_bf16_f32 v253, v246, v247\n\t"
    "v_accvgpr_read_b32 v236, a7\n\t"
    "v_accvgpr_read_b32 v237, a23\n\t"
    "v_accvgpr_read_b32 v238, a39\n\t"
    "v_accvgpr_read_b32 v239, a55\n\t"
    "v_accvgpr_read_b32 v240, a71\n\t"
    "v_accvgpr_read_b32 v241, a87\n\t"
    "v_mfma_f32_16x16x32_bf16 v[92:95], v[224:227], a[244:247], v[92:95]\n\t"
    "ds_read_b128 v[216:219], v192 offset:38656\n\t"
    "v_cvt_pk_bf16_f32 v109, v178, v179\n\t"
    "v_accvgpr_read_b32 v242, a103\n\t"
    "v_accvgpr_read_b32 v243, a119\n\t"
    "v_accvgpr_read_b32 v244, a135\n\t"
    "v_accvgpr_read_b32 v245, a151\n\t"
    "v_accvgpr_read_b32 v246, a167\n\t"
    "v_accvgpr_read_b32 v247, a183\n\t"
    "v_mul_f32 v236, v236, v39\n\t"
    "v_mul_f32 v237, v237, v39\n\t"
    "v_mfma_f32_16x16x32_bf16 v[76:79], v[220:223], a[244:247], v[76:79]\n\t"
    "s_waitcnt lgkmcnt(4)\n\t"
    "buffer_store_dwordx2 v[164:165], v52, s[56:59], s96 offen offset:640 sc1\n\t"
    "v_exp_f32 v178, v62\n\t"
    "v_mul_f32 v238, v238, v39\n\t"
    "v_mul_f32 v239, v239, v39\n\t"
    "v_mul_f32 v240, v240, v39\n\t"
    "v_mul_f32 v241, v241, v39\n\t"
    "v_mul_f32 v242, v242, v39\n\t"
    "v_mul_f32 v243, v243, v39\n\t"
    "v_mul_f32 v244, v244, v39\n\t"
    "v_mul_f32 v245, v245, v39\n\t"
    "v_mfma_f32_16x16x32_bf16 v[72:75], v[228:231], a[232:235], v[72:75]\n\t"
    "ds_read_b128 v[220:223], v193 offset:37120\n\t"
    "v_add_f32 v175, v175, v176\n\t"
    "v_mul_f32 v246, v246, v39\n\t"
    "v_mul_f32 v247, v247, v39\n\t"
    "v_cvt_pk_bf16_f32 v160, v236, v237\n\t"
    "v_cvt_pk_bf16_f32 v161, v238, v239\n\t"
    "v_cvt_pk_bf16_f32 v162, v240, v241\n\t"
    "v_cvt_pk_bf16_f32 v163, v242, v243\n\t"
    "v_cvt_pk_bf16_f32 v164, v244, v245\n\t"
    "v_mfma_f32_16x16x32_bf16 v[88:91], v[232:235], a[232:235], v[88:91]\n\t"
    "buffer_store_dwordx4 v[248:251], v51, s[56:59], s96 offen offset:768 sc1\n\t"
    "v_exp_f32 v179, v63\n\t"
    "v_add_f32 v175, v175, v177\n\t"
    "v_cvt_pk_bf16_f32 v165, v246, v247\n\t"
    "v_mfma_f32_16x16x32_bf16 v[92:95], v[232:235], a[248:251], v[92:95]\n\t"
    "ds_read_b128 v[224:227], v193 offset:38656\n\t"
    "v_cvt_pk_bf16_f32 v110, v176, v177\n\t"
    "v_mfma_f32_16x16x32_bf16 v[76:79], v[228:231], a[248:251], v[76:79]\n\t"
    "s_waitcnt lgkmcnt(4)\n\t"
    "buffer_store_dwordx2 v[252:253], v52, s[56:59], s96 offen offset:1024 sc1\n\t"
    "v_add_f32 v175, v175, v178\n\t"
    "v_mfma_f32_16x16x32_bf16 v[72:75], v[204:207], a[236:239], v[72:75]\n\t"
    "ds_read_b128 v[228:231], v181 offset:0\n\t"
    "v_add_f32 v175, v175, v179\n\t"
    "v_mfma_f32_16x16x32_bf16 v[88:91], v[208:211], a[236:239], v[88:91]\n\t"
    "buffer_store_dwordx4 v[160:163], v51, s[56:59], s96 offen offset:1152 sc1\n\t"
    "v_cvt_pk_bf16_f32 v111, v178, v179\n\t"
    "v_max_f32 v180, v174, v175\n\t"
    "v_mfma_f32_16x16x32_bf16 v[92:95], v[208:211], a[252:255], v[92:95]\n\t"
    "ds_read_b128 v[232:235], v181 offset:256\n\t"
    "v_cmp_lt_f32 vcc, s62, v180\n\t"
    "v_mfma_f32_16x16x32_bf16 v[76:79], v[204:207], a[252:255], v[76:79]\n\t"
    "s_waitcnt lgkmcnt(4)\n\t"
    "buffer_store_dwordx2 v[164:165], v52, s[56:59], s96 offen offset:1408 sc1\n\t"
    "v_mfma_f32_16x16x32_bf16 v[72:75], v[212:215], v[16:19], v[72:75]\n\t"
    "ds_read_b128 v[204:207], v181 offset:512\n\t"
    "v_mfma_f32_16x16x32_bf16 v[88:91], v[216:219], v[16:19], v[88:91]\n\t"
    "v_mfma_f32_16x16x32_bf16 v[92:95], v[216:219], v[24:27], v[92:95]\n\t"
    "ds_read_b128 v[208:211], v181 offset:768\n\t"
    "v_mfma_f32_16x16x32_bf16 v[76:79], v[212:215], v[24:27], v[76:79]\n\t"
    "s_waitcnt lgkmcnt(4)\n\t"
    "v_mfma_f32_16x16x32_bf16 v[72:75], v[220:223], v[20:23], v[72:75]\n\t"
    "ds_read_b128 v[212:215], v181 offset:1024\n\t"
    "v_mfma_f32_16x16x32_bf16 v[88:91], v[224:227], v[20:23], v[88:91]\n\t"
    "v_mfma_f32_16x16x32_bf16 v[92:95], v[224:227], v[28:31], v[92:95]\n\t"
    "ds_read_b128 v[216:219], v181 offset:1280\n\t"
    "v_mfma_f32_16x16x32_bf16 v[76:79], v[220:223], v[28:31], v[76:79]\n\t"
    "s_waitcnt lgkmcnt(5)\n\t"
    "s_cbranch_vccnz L_stub_h4\n\t"
    "L_ret_h4:\n\t"
    "v_mfma_f32_16x16x32_bf16 a[8:11], v[104:107], v[228:231], a[8:11]\n\t"
    "ds_read_b128 v[220:223], v181 offset:1536\n\t"
    "v_add_f32 v170, v170, v174\n\t"
    "v_mfma_f32_16x16x32_bf16 a[12:15], v[108:111], v[228:231], a[12:15]\n\t"
    "s_waitcnt lgkmcnt(5)\n\t"
    "v_add_f32 v171, v171, v175\n\t"
    "v_mfma_f32_16x16x32_bf16 a[28:31], v[108:111], v[232:235], a[28:31]\n\t"
    "ds_read_b128 v[224:227], v181 offset:1792\n\t"
    "v_mfma_f32_16x16x32_bf16 a[24:27], v[104:107], v[232:235], a[24:27]\n\t"
    "s_waitcnt lgkmcnt(5)\n\t"
    "v_mfma_f32_16x16x32_bf16 a[40:43], v[104:107], v[204:207], a[40:43]\n\t"
    "ds_read_b128 v[228:231], v181 offset:2048\n\t"
    "v_mfma_f32_16x16x32_bf16 a[44:47], v[108:111], v[204:207], a[44:47]\n\t"
    "s_waitcnt lgkmcnt(5)\n\t"
    "v_exp_f32 v176, v72\n\t"
    "v_mfma_f32_16x16x32_bf16 a[60:63], v[108:111], v[208:211], a[60:63]\n\t"
    "ds_read_b128 v[232:235], v181 offset:2304\n\t"
    "v_mfma_f32_16x16x32_bf16 a[56:59], v[104:107], v[208:211], a[56:59]\n\t"
    "s_waitcnt lgkmcnt(5)\n\t"
    "v_exp_f32 v177, v73\n\t"
    "v_mfma_f32_16x16x32_bf16 a[72:75], v[104:107], v[212:215], a[72:75]\n\t"
    "ds_read_b128 v[204:207], v181 offset:2560\n\t"
    "v_mfma_f32_16x16x32_bf16 a[76:79], v[108:111], v[212:215], a[76:79]\n\t"
    "s_waitcnt lgkmcnt(5)\n\t"
    "v_exp_f32 v178, v74\n\t"
    "v_mfma_f32_16x16x32_bf16 a[92:95], v[108:111], v[216:219], a[92:95]\n\t"
    "ds_read_b128 v[208:211], v181 offset:2816\n\t"
    "v_add_f32 v174, v176, v177\n\t"
    "v_mfma_f32_16x16x32_bf16 a[88:91], v[104:107], v[216:219], a[88:91]\n\t"
    "s_waitcnt lgkmcnt(5)\n\t"
    "v_exp_f32 v179, v75\n\t"
    "v_mfma_f32_16x16x32_bf16 a[104:107], v[104:107], v[220:223], a[104:107]\n\t"
    "ds_read_b128 v[212:215], v181 offset:12288\n\t"
    "v_cvt_pk_bf16_f32 v120, v176, v177\n\t"
    "v_mfma_f32_16x16x32_bf16 a[108:111], v[108:111], v[220:223], a[108:111]\n\t"
    "s_waitcnt lgkmcnt(5)\n\t"
    "v_exp_f32 v176, v88\n\t"
    "v_mfma_f32_16x16x32_bf16 a[124:127], v[108:111], v[224:227], a[124:127]\n\t"
    "ds_read_b128 v[216:219], v181 offset:12544\n\t"
    "v_add_f32 v174, v174, v178\n\t"
    "v_mfma_f32_16x16x32_bf16 a[120:123], v[104:107], v[224:227], a[120:123]\n\t"
    "s_waitcnt lgkmcnt(5)\n\t"
    "v_exp_f32 v177, v89\n\t"
    "v_add_f32 v174, v174, v179\n\t"
    "v_mfma_f32_16x16x32_bf16 a[136:139], v[104:107], v[228:231], a[136:139]\n\t"
    "ds_read_b128 v[220:223], v181 offset:12800\n\t"
    "v_cvt_pk_bf16_f32 v121, v178, v179\n\t"
    "v_mfma_f32_16x16x32_bf16 a[140:143], v[108:111], v[228:231], a[140:143]\n\t"
    "s_waitcnt lgkmcnt(5)\n\t"
    "v_exp_f32 v178, v90\n\t"
    "v_mfma_f32_16x16x32_bf16 a[156:159], v[108:111], v[232:235], a[156:159]\n\t"
    "ds_read_b128 v[224:227], v181 offset:13056\n\t"
    "v_add_f32 v174, v174, v176\n\t"
    "v_mfma_f32_16x16x32_bf16 a[152:155], v[104:107], v[232:235], a[152:155]\n\t"
    "s_waitcnt lgkmcnt(5)\n\t"
    "v_exp_f32 v179, v91\n\t"
    "v_add_f32 v174, v174, v177\n\t"
    "v_mfma_f32_16x16x32_bf16 a[168:171], v[104:107], v[204:207], a[168:171]\n\t"
    "ds_read_b128 v[228:231], v181 offset:13312\n\t"
    "v_cvt_pk_bf16_f32 v122, v176, v177\n\t"
    "v_mfma_f32_16x16x32_bf16 a[172:175], v[108:111], v[204:207], a[172:175]\n\t"
    "s_waitcnt lgkmcnt(5)\n\t"
    "v_exp_f32 v176, v76\n\t"
    "v_mfma_f32_16x16x32_bf16 a[188:191], v[108:111], v[208:211], a[188:191]\n\t"
    "ds_read_b128 v[232:235], v181 offset:13568\n\t"
    "v_add_f32 v174, v174, v178\n\t"
    "v_mfma_f32_16x16x32_bf16 a[184:187], v[104:107], v[208:211], a[184:187]\n\t"
    "s_waitcnt lgkmcnt(5)\n\t"
    "v_exp_f32 v177, v77\n\t"
    "v_add_f32 v174, v174, v179\n\t"
    "s_nop 0\n\t"
    "v_cvt_pk_bf16_f32 v123, v178, v179\n\t"
    "v_exp_f32 v178, v78\n\t"
    "s_nop 0\n\t"
    "v_add_f32 v175, v176, v177\n\t"
    "v_exp_f32 v179, v79\n\t"
    "s_nop 0\n\t"
    "v_cvt_pk_bf16_f32 v124, v176, v177\n\t"
    "v_exp_f32 v176, v92\n\t"
    "s_nop 0\n\t"
    "v_add_f32 v175, v175, v178\n\t"
    "v_exp_f32 v177, v93\n\t"
    "s_nop 0\n\t"
    "v_add_f32 v175, v175, v179\n\t"
    "v_cvt_pk_bf16_f32 v125, v178, v179\n\t"
    "v_exp_f32 v178, v94\n\t"
    "s_nop 0\n\t"
    "v_add_f32 v175, v175, v176\n\t"
    "v_exp_f32 v179, v95\n\t"
    "s_nop 0\n\t"
    "v_add_f32 v175, v175, v177\n\t"
    "v_cvt_pk_bf16_f32 v126, v176, v177\n\t"
    "v_add_f32 v175, v175, v178\n\t"
    "v_add_f32 v175, v175, v179\n\t"
    "v_cvt_pk_bf16_f32 v127, v178, v179\n\t"
    "v_max_f32 v180, v174, v175\n\t"
    "v_cmp_lt_f32 vcc, s62, v180\n\t"
    "s_nop 1\n\t"
    "s_cbranch_vccnz L_stub_h5\n\t"
    "L_ret_h5:\n\t"
    "v_mfma_f32_16x16x32_bf16 a[8:11], v[120:123], v[212:215], a[8:11]\n\t"
    "ds_read_b128 v[204:207], v181 offset:13824\n\t"
    "v_mfma_f32_16x16x32_bf16 a[12:15], v[124:127], v[212:215], a[12:15]\n\t"
    "s_waitcnt lgkmcnt(5)\n\t"
    "v_mfma_f32_16x16x32_bf16 a[28:31], v[124:127], v[216:219], a[28:31]\n\t"
    "ds_read_b128 v[208:211], v181 offset:14080\n\t"
    "v_mfma_f32_16x16x32_bf16 a[24:27], v[120:123], v[216:219], a[24:27]\n\t"
    "s_waitcnt lgkmcnt(5)\n\t"
    "v_mfma_f32_16x16x32_bf16 a[40:43], v[120:123], v[220:223], a[40:43]\n\t"
    "ds_read_b128 v[212:215], v181 offset:14336\n\t"
    "v_mfma_f32_16x16x32_bf16 a[44:47], v[124:127], v[220:223], a[44:47]\n\t"
    "s_waitcnt lgkmcnt(5)\n\t"
    "v_mfma_f32_16x16x32_bf16 a[60:63], v[124:127], v[224:227], a[60:63]\n\t"
    "ds_read_b128 v[216:219], v181 offset:14592\n\t"
    "v_mfma_f32_16x16x32_bf16 a[56:59], v[120:123], v[224:227], a[56:59]\n\t"
    "s_waitcnt lgkmcnt(5)\n\t"
    "v_mfma_f32_16x16x32_bf16 a[72:75], v[120:123], v[228:231], a[72:75]\n\t"
    "ds_read_b128 v[220:223], v181 offset:14848\n\t"
    "v_mfma_f32_16x16x32_bf16 a[76:79], v[124:127], v[228:231], a[76:79]\n\t"
    "s_waitcnt lgkmcnt(5)\n\t"
    "v_mfma_f32_16x16x32_bf16 a[92:95], v[124:127], v[232:235], a[92:95]\n\t"
    "ds_read_b128 v[224:227], v181 offset:15104\n\t"
    "v_mfma_f32_16x16x32_bf16 a[88:91], v[120:123], v[232:235], a[88:91]\n\t"
    "s_waitcnt lgkmcnt(5)\n\t"
    "v_mfma_f32_16x16x32_bf16 a[104:107], v[120:123], v[204:207], a[104:107]\n\t"
    "v_mfma_f32_16x16x32_bf16 a[108:111], v[124:127], v[204:207], a[108:111]\n\t"
    "s_waitcnt lgkmcnt(4)\n\t"
    "v_mfma_f32_16x16x32_bf16 a[124:127], v[124:127], v[208:211], a[124:127]\n\t"
    "v_mfma_f32_16x16x32_bf16 a[120:123], v[120:123], v[208:211], a[120:123]\n\t"
    "s_waitcnt lgkmcnt(3)\n\t"
    "v_mfma_f32_16x16x32_bf16 a[136:139], v[120:123], v[212:215], a[136:139]\n\t"
    "v_mfma_f32_16x16x32_bf16 a[140:143], v[124:127], v[212:215], a[140:143]\n\t"
    "s_waitcnt lgkmcnt(2)\n\t"
    "v_mfma_f32_16x16x32_bf16 a[156:159], v[124:127], v[216:219], a[156:159]\n\t"
    "v_mfma_f32_16x16x32_bf16 a[152:155], v[120:123], v[216:219], a[152:155]\n\t"
    "s_waitcnt lgkmcnt(1)\n\t"
    "v_mfma_f32_16x16x32_bf16 a[168:171], v[120:123], v[220:223], a[168:171]\n\t"
    "v_mfma_f32_16x16x32_bf16 a[172:175], v[124:127], v[220:223], a[172:175]\n\t"
    "s_waitcnt lgkmcnt(0)\n\t"
    "v_mfma_f32_16x16x32_bf16 a[188:191], v[124:127], v[224:227], a[188:191]\n\t"
    "v_mfma_f32_16x16x32_bf16 a[184:187], v[120:123], v[224:227], a[184:187]\n\t"
    "v_add_f32 v170, v170, v174\n\t"
    "v_add_f32 v171, v171, v175\n\t"
    "v_mov_b32 v48, v170\n\t"
    "v_mov_b32 v49, v171\n\t"
    "v_nop\n\t"
    "v_nop\n\t"
    "v_permlane32_swap_b32 v170, v48\n\t"
    "v_permlane32_swap_b32 v171, v49\n\t"
    "v_nop\n\t"
    "v_add_f32 v170, v170, v48\n\t"
    "v_add_f32 v171, v171, v49\n\t"
    "v_mov_b32 v48, v170\n\t"
    "v_mov_b32 v49, v171\n\t"
    "v_nop\n\t"
    "v_nop\n\t"
    "v_permlane16_swap_b32 v170, v48\n\t"
    "v_permlane16_swap_b32 v171, v49\n\t"
    "v_nop\n\t"
    "v_add_f32 v170, v170, v48\n\t"
    "v_add_f32 v171, v171, v49\n\t"
    "v_rcp_f32 v170, v170\n\t"
    "v_rcp_f32 v171, v171\n\t"
    "v_nop\n\t"
    "ds_bpermute_b32 v64, v50, v170 offset:0\n\t"
    "ds_bpermute_b32 v65, v50, v170 offset:4\n\t"
    "ds_bpermute_b32 v66, v50, v170 offset:8\n\t"
    "ds_bpermute_b32 v67, v50, v170 offset:12\n\t"
    "ds_bpermute_b32 v68, v50, v171 offset:0\n\t"
    "ds_bpermute_b32 v69, v50, v171 offset:4\n\t"
    "ds_bpermute_b32 v70, v50, v171 offset:8\n\t"
    "ds_bpermute_b32 v71, v50, v171 offset:12\n\t"
    "s_waitcnt lgkmcnt(0)\n\t"
    "s_mov_b32 s95, 12288\n\t"
    "v_accvgpr_read_b32 v0, a8\n\t"
    "v_accvgpr_read_b32 v1, a24\n\t"
    "v_accvgpr_read_b32 v2, a40\n\t"
    "v_accvgpr_read_b32 v3, a56\n\t"
    "v_accvgpr_read_b32 v4, a72\n\t"
    "v_accvgpr_read_b32 v5, a88\n\t"
    "v_accvgpr_read_b32 v6, a104\n\t"
    "v_accvgpr_read_b32 v7, a120\n\t"
    "v_accvgpr_read_b32 v8, a136\n\t"
    "v_accvgpr_read_b32 v9, a152\n\t"
    "v_accvgpr_read_b32 v10, a168\n\t"
    "v_accvgpr_read_b32 v11, a184\n\t"
    "v_pk_mul_f32 v[0:1], v[0:1], v[64:65] op_sel_hi:[1,0]\n\t"
    "v_pk_mul_f32 v[2:3], v[2:3], v[64:65] op_sel_hi:[1,0]\n\t"
    "v_pk_mul_f32 v[4:5], v[4:5], v[64:65] op_sel_hi:[1,0]\n\t"
    "v_pk_mul_f32 v[6:7], v[6:7], v[64:65] op_sel_hi:[1,0]\n\t"
    "v_pk_mul_f32 v[8:9], v[8:9], v[64:65] op_sel_hi:[1,0]\n\t"
    "v_pk_mul_f32 v[10:11], v[10:11], v[64:65] op_sel_hi:[1,0]\n\t"
    "v_cvt_pk_bf16_f32 v12, v0, v1\n\t"
    "v_cvt_pk_bf16_f32 v13, v2, v3\n\t"
    "v_cvt_pk_bf16_f32 v14, v4, v5\n\t"
    "v_cvt_pk_bf16_f32 v15, v6, v7\n\t"
    "v_cvt_pk_bf16_f32 v16, v8, v9\n\t"
    "v_cvt_pk_bf16_f32 v17, v10, v11\n\t"
    "buffer_store_dwordx4 v[12:15], v51, s[56:59], s95 offen offset:0 sc1\n\t"
    "buffer_store_dwordx2 v[16:17], v52, s[56:59], s95 offen offset:256 sc1\n\t"
    "v_accvgpr_read_b32 v96, a9\n\t"
    "v_accvgpr_read_b32 v97, a25\n\t"
    "v_accvgpr_read_b32 v98, a41\n\t"
    "v_accvgpr_read_b32 v99, a57\n\t"
    "v_accvgpr_read_b32 v100, a73\n\t"
    "v_accvgpr_read_b32 v101, a89\n\t"
    "v_accvgpr_read_b32 v102, a105\n\t"
    "v_accvgpr_read_b32 v103, a121\n\t"
    "v_accvgpr_read_b32 v104, a137\n\t"
    "v_accvgpr_read_b32 v105, a153\n\t"
    "v_accvgpr_read_b32 v106, a169\n\t"
    "v_accvgpr_read_b32 v107, a185\n\t"
    "v_pk_mul_f32 v[96:97], v[96:97], v[64:65] op_sel:[0,1] op_sel_hi:[1,1]\n\t"
    "v_pk_mul_f32 v[98:99], v[98:99], v[64:65] op_sel:[0,1] op_sel_hi:[1,1]\n\t"
    "v_pk_mul_f32 v[100:101], v[100:101], v[64:65] op_sel:[0,1] op_sel_hi:[1,1]\n\t"
    "v_pk_mul_f32 v[102:103], v[102:103], v[64:65] op_sel:[0,1] op_sel_hi:[1,1]\n\t"
    "v_pk_mul_f32 v[104:105], v[104:105], v[64:65] op_sel:[0,1] op_sel_hi:[1,1]\n\t"
    "v_pk_mul_f32 v[106:107], v[106:107], v[64:65] op_sel:[0,1] op_sel_hi:[1,1]\n\t"
    "v_cvt_pk_bf16_f32 v108, v96, v97\n\t"
    "v_cvt_pk_bf16_f32 v109, v98, v99\n\t"
    "v_cvt_pk_bf16_f32 v110, v100, v101\n\t"
    "v_cvt_pk_bf16_f32 v111, v102, v103\n\t"
    "v_cvt_pk_bf16_f32 v112, v104, v105\n\t"
    "v_cvt_pk_bf16_f32 v113, v106, v107\n\t"
    "buffer_store_dwordx4 v[108:111], v51, s[56:59], s95 offen offset:384 sc1\n\t"
    "buffer_store_dwordx2 v[112:113], v52, s[56:59], s95 offen offset:640 sc1\n\t"
    "v_accvgpr_read_b32 v204, a10\n\t"
    "v_accvgpr_read_b32 v205, a26\n\t"
    "v_accvgpr_read_b32 v206, a42\n\t"
    "v_accvgpr_read_b32 v207, a58\n\t"
    "v_accvgpr_read_b32 v208, a74\n\t"
    "v_accvgpr_read_b32 v209, a90\n\t"
    "v_accvgpr_read_b32 v210, a106\n\t"
    "v_accvgpr_read_b32 v211, a122\n\t"
    "v_accvgpr_read_b32 v212, a138\n\t"
    "v_accvgpr_read_b32 v213, a154\n\t"
    "v_accvgpr_read_b32 v214, a170\n\t"
    "v_accvgpr_read_b32 v215, a186\n\t"
    "v_pk_mul_f32 v[204:205], v[204:205], v[66:67] op_sel_hi:[1,0]\n\t"
    "v_pk_mul_f32 v[206:207], v[206:207], v[66:67] op_sel_hi:[1,0]\n\t"
    "v_pk_mul_f32 v[208:209], v[208:209], v[66:67] op_sel_hi:[1,0]\n\t"
    "v_pk_mul_f32 v[210:211], v[210:211], v[66:67] op_sel_hi:[1,0]\n\t"
    "v_pk_mul_f32 v[212:213], v[212:213], v[66:67] op_sel_hi:[1,0]\n\t"
    "v_pk_mul_f32 v[214:215], v[214:215], v[66:67] op_sel_hi:[1,0]\n\t"
    "v_cvt_pk_bf16_f32 v216, v204, v205\n\t"
    "v_cvt_pk_bf16_f32 v217, v206, v207\n\t"
    "v_cvt_pk_bf16_f32 v218, v208, v209\n\t"
    "v_cvt_pk_bf16_f32 v219, v210, v211\n\t"
    "v_cvt_pk_bf16_f32 v220, v212, v213\n\t"
    "v_cvt_pk_bf16_f32 v221, v214, v215\n\t"
    "buffer_store_dwordx4 v[216:219], v51, s[56:59], s95 offen offset:768 sc1\n\t"
    "buffer_store_dwordx2 v[220:221], v52, s[56:59], s95 offen offset:1024 sc1\n\t"
    "v_accvgpr_read_b32 v0, a11\n\t"
    "v_accvgpr_read_b32 v1, a27\n\t"
    "v_accvgpr_read_b32 v2, a43\n\t"
    "v_accvgpr_read_b32 v3, a59\n\t"
    "v_accvgpr_read_b32 v4, a75\n\t"
    "v_accvgpr_read_b32 v5, a91\n\t"
    "v_accvgpr_read_b32 v6, a107\n\t"
    "v_accvgpr_read_b32 v7, a123\n\t"
    "v_accvgpr_read_b32 v8, a139\n\t"
    "v_accvgpr_read_b32 v9, a155\n\t"
    "v_accvgpr_read_b32 v10, a171\n\t"
    "v_accvgpr_read_b32 v11, a187\n\t"
    "v_pk_mul_f32 v[0:1], v[0:1], v[66:67] op_sel:[0,1] op_sel_hi:[1,1]\n\t"
    "v_pk_mul_f32 v[2:3], v[2:3], v[66:67] op_sel:[0,1] op_sel_hi:[1,1]\n\t"
    "v_pk_mul_f32 v[4:5], v[4:5], v[66:67] op_sel:[0,1] op_sel_hi:[1,1]\n\t"
    "v_pk_mul_f32 v[6:7], v[6:7], v[66:67] op_sel:[0,1] op_sel_hi:[1,1]\n\t"
    "v_pk_mul_f32 v[8:9], v[8:9], v[66:67] op_sel:[0,1] op_sel_hi:[1,1]\n\t"
    "v_pk_mul_f32 v[10:11], v[10:11], v[66:67] op_sel:[0,1] op_sel_hi:[1,1]\n\t"
    "v_cvt_pk_bf16_f32 v12, v0, v1\n\t"
    "v_cvt_pk_bf16_f32 v13, v2, v3\n\t"
    "v_cvt_pk_bf16_f32 v14, v4, v5\n\t"
    "v_cvt_pk_bf16_f32 v15, v6, v7\n\t"
    "v_cvt_pk_bf16_f32 v16, v8, v9\n\t"
    "v_cvt_pk_bf16_f32 v17, v10, v11\n\t"
    "buffer_store_dwordx4 v[12:15], v51, s[56:59], s95 offen offset:1152 sc1\n\t"
    "buffer_store_dwordx2 v[16:17], v52, s[56:59], s95 offen offset:1408 sc1\n\t"
    "s_mov_b32 s95, 18432\n\t"
    "v_accvgpr_read_b32 v96, a12\n\t"
    "v_accvgpr_read_b32 v97, a28\n\t"
    "v_accvgpr_read_b32 v98, a44\n\t"
    "v_accvgpr_read_b32 v99, a60\n\t"
    "v_accvgpr_read_b32 v100, a76\n\t"
    "v_accvgpr_read_b32 v101, a92\n\t"
    "v_accvgpr_read_b32 v102, a108\n\t"
    "v_accvgpr_read_b32 v103, a124\n\t"
    "v_accvgpr_read_b32 v104, a140\n\t"
    "v_accvgpr_read_b32 v105, a156\n\t"
    "v_accvgpr_read_b32 v106, a172\n\t"
    "v_accvgpr_read_b32 v107, a188\n\t"
    "v_pk_mul_f32 v[96:97], v[96:97], v[68:69] op_sel_hi:[1,0]\n\t"
    "v_pk_mul_f32 v[98:99], v[98:99], v[68:69] op_sel_hi:[1,0]\n\t"
    "v_pk_mul_f32 v[100:101], v[100:101], v[68:69] op_sel_hi:[1,0]\n\t"
    "v_pk_mul_f32 v[102:103], v[102:103], v[68:69] op_sel_hi:[1,0]\n\t"
    "v_pk_mul_f32 v[104:105], v[104:105], v[68:69] op_sel_hi:[1,0]\n\t"
    "v_pk_mul_f32 v[106:107], v[106:107], v[68:69] op_sel_hi:[1,0]\n\t"
    "v_cvt_pk_bf16_f32 v108, v96, v97\n\t"
    "v_cvt_pk_bf16_f32 v109, v98, v99\n\t"
    "v_cvt_pk_bf16_f32 v110, v100, v101\n\t"
    "v_cvt_pk_bf16_f32 v111, v102, v103\n\t"
    "v_cvt_pk_bf16_f32 v112, v104, v105\n\t"
    "v_cvt_pk_bf16_f32 v113, v106, v107\n\t"
    "buffer_store_dwordx4 v[108:111], v51, s[56:59], s95 offen offset:0 sc1\n\t"
    "buffer_store_dwordx2 v[112:113], v52, s[56:59], s95 offen offset:256 sc1\n\t"
    "v_accvgpr_read_b32 v204, a13\n\t"
    "v_accvgpr_read_b32 v205, a29\n\t"
    "v_accvgpr_read_b32 v206, a45\n\t"
    "v_accvgpr_read_b32 v207, a61\n\t"
    "v_accvgpr_read_b32 v208, a77\n\t"
    "v_accvgpr_read_b32 v209, a93\n\t"
    "v_accvgpr_read_b32 v210, a109\n\t"
    "v_accvgpr_read_b32 v211, a125\n\t"
    "v_accvgpr_read_b32 v212, a141\n\t"
    "v_accvgpr_read_b32 v213, a157\n\t"
    "v_accvgpr_read_b32 v214, a173\n\t"
    "v_accvgpr_read_b32 v215, a189\n\t"
    "v_pk_mul_f32 v[204:205], v[204:205], v[68:69] op_sel:[0,1] op_sel_hi:[1,1]\n\t"
    "v_pk_mul_f32 v[206:207], v[206:207], v[68:69] op_sel:[0,1] op_sel_hi:[1,1]\n\t"
    "v_pk_mul_f32 v[208:209], v[208:209], v[68:69] op_sel:[0,1] op_sel_hi:[1,1]\n\t"
    "v_pk_mul_f32 v[210:211], v[210:211], v[68:69] op_sel:[0,1] op_sel_hi:[1,1]\n\t"
    "v_pk_mul_f32 v[212:213], v[212:213], v[68:69] op_sel:[0,1] op_sel_hi:[1,1]\n\t"
    "v_pk_mul_f32 v[214:215], v[214:215], v[68:69] op_sel:[0,1] op_sel_hi:[1,1]\n\t"
    "v_cvt_pk_bf16_f32 v216, v204, v205\n\t"
    "v_cvt_pk_bf16_f32 v217, v206, v207\n\t"
    "v_cvt_pk_bf16_f32 v218, v208, v209\n\t"
    "v_cvt_pk_bf16_f32 v219, v210, v211\n\t"
    "v_cvt_pk_bf16_f32 v220, v212, v213\n\t"
    "v_cvt_pk_bf16_f32 v221, v214, v215\n\t"
    "buffer_store_dwordx4 v[216:219], v51, s[56:59], s95 offen offset:384 sc1\n\t"
    "buffer_store_dwordx2 v[220:221], v52, s[56:59], s95 offen offset:640 sc1\n\t"
    "v_accvgpr_read_b32 v0, a14\n\t"
    "v_accvgpr_read_b32 v1, a30\n\t"
    "v_accvgpr_read_b32 v2, a46\n\t"
    "v_accvgpr_read_b32 v3, a62\n\t"
    "v_accvgpr_read_b32 v4, a78\n\t"
    "v_accvgpr_read_b32 v5, a94\n\t"
    "v_accvgpr_read_b32 v6, a110\n\t"
    "v_accvgpr_read_b32 v7, a126\n\t"
    "v_accvgpr_read_b32 v8, a142\n\t"
    "v_accvgpr_read_b32 v9, a158\n\t"
    "v_accvgpr_read_b32 v10, a174\n\t"
    "v_accvgpr_read_b32 v11, a190\n\t"
    "v_pk_mul_f32 v[0:1], v[0:1], v[70:71] op_sel_hi:[1,0]\n\t"
    "v_pk_mul_f32 v[2:3], v[2:3], v[70:71] op_sel_hi:[1,0]\n\t"
    "v_pk_mul_f32 v[4:5], v[4:5], v[70:71] op_sel_hi:[1,0]\n\t"
    "v_pk_mul_f32 v[6:7], v[6:7], v[70:71] op_sel_hi:[1,0]\n\t"
    "v_pk_mul_f32 v[8:9], v[8:9], v[70:71] op_sel_hi:[1,0]\n\t"
    "v_pk_mul_f32 v[10:11], v[10:11], v[70:71] op_sel_hi:[1,0]\n\t"
    "v_cvt_pk_bf16_f32 v12, v0, v1\n\t"
    "v_cvt_pk_bf16_f32 v13, v2, v3\n\t"
    "v_cvt_pk_bf16_f32 v14, v4, v5\n\t"
    "v_cvt_pk_bf16_f32 v15, v6, v7\n\t"
    "v_cvt_pk_bf16_f32 v16, v8, v9\n\t"
    "v_cvt_pk_bf16_f32 v17, v10, v11\n\t"
    "buffer_store_dwordx4 v[12:15], v51, s[56:59], s95 offen offset:768 sc1\n\t"
    "buffer_store_dwordx2 v[16:17], v52, s[56:59], s95 offen offset:1024 sc1\n\t"
    "v_accvgpr_read_b32 v96, a15\n\t"
    "v_accvgpr_read_b32 v97, a31\n\t"
    "v_accvgpr_read_b32 v98, a47\n\t"
    "v_accvgpr_read_b32 v99, a63\n\t"
    "v_accvgpr_read_b32 v100, a79\n\t"
    "v_accvgpr_read_b32 v101, a95\n\t"
    "v_accvgpr_read_b32 v102, a111\n\t"
    "v_accvgpr_read_b32 v103, a127\n\t"
    "v_accvgpr_read_b32 v104, a143\n\t"
    "v_accvgpr_read_b32 v105, a159\n\t"
    "v_accvgpr_read_b32 v106, a175\n\t"
    "v_accvgpr_read_b32 v107, a191\n\t"
    "v_pk_mul_f32 v[96:97], v[96:97], v[70:71] op_sel:[0,1] op_sel_hi:[1,1]\n\t"
    "v_pk_mul_f32 v[98:99], v[98:99], v[70:71] op_sel:[0,1] op_sel_hi:[1,1]\n\t"
    "v_pk_mul_f32 v[100:101], v[100:101], v[70:71] op_sel:[0,1] op_sel_hi:[1,1]\n\t"
    "v_pk_mul_f32 v[102:103], v[102:103], v[70:71] op_sel:[0,1] op_sel_hi:[1,1]\n\t"
    "v_pk_mul_f32 v[104:105], v[104:105], v[70:71] op_sel:[0,1] op_sel_hi:[1,1]\n\t"
    "v_pk_mul_f32 v[106:107], v[106:107], v[70:71] op_sel:[0,1] op_sel_hi:[1,1]\n\t"
    "v_cvt_pk_bf16_f32 v108, v96, v97\n\t"
    "v_cvt_pk_bf16_f32 v109, v98, v99\n\t"
    "v_cvt_pk_bf16_f32 v110, v100, v101\n\t"
    "v_cvt_pk_bf16_f32 v111, v102, v103\n\t"
    "v_cvt_pk_bf16_f32 v112, v104, v105\n\t"
    "v_cvt_pk_bf16_f32 v113, v106, v107\n\t"
    "buffer_store_dwordx4 v[108:111], v51, s[56:59], s95 offen offset:1152 sc1\n\t"
    "buffer_store_dwordx2 v[112:113], v52, s[56:59], s95 offen offset:1408 sc1\n\t"
    "s_waitcnt vmcnt(0)\n\t"
    "s_branch L_end\n\t"
    "L_cend:\n\t"
    "L_stub_s0_0:\n\t"
    "s_swappc_b64 s[76:77], s[72:73]\n\t"
    "s_branch L_ret_s0_0\n\t"
    "L_stub_s1_1:\n\t"
    "s_swappc_b64 s[76:77], s[74:75]\n\t"
    "s_branch L_ret_s1_1\n\t"
    "L_stub_s2_0:\n\t"
    "s_swappc_b64 s[76:77], s[72:73]\n\t"
    "s_branch L_ret_s2_0\n\t"
    "L_stub_s3_1:\n\t"
    "s_swappc_b64 s[76:77], s[74:75]\n\t"
    "s_branch L_ret_s3_1\n\t"
    "L_stub_s4_0:\n\t"
    "s_swappc_b64 s[76:77], s[72:73]\n\t"
    "s_branch L_ret_s4_0\n\t"
    "L_stub_l0_1:\n\t"
    "s_swappc_b64 s[76:77], s[74:75]\n\t"
    "s_branch L_ret_l0_1\n\t"
    "L_stub_l1_0:\n\t"
    "s_swappc_b64 s[76:77], s[72:73]\n\t"
    "s_branch L_ret_l1_0\n\t"
    "L_stub_l2_1:\n\t"
    "s_swappc_b64 s[76:77], s[74:75]\n\t"
    "s_branch L_ret_l2_1\n\t"
    "L_stub_l3_0:\n\t"
    "s_swappc_b64 s[76:77], s[72:73]\n\t"
    "s_branch L_ret_l3_0\n\t"
    "L_stub_l4_1:\n\t"
    "s_swappc_b64 s[76:77], s[74:75]\n\t"
    "s_branch L_ret_l4_1\n\t"
    "L_stub_l5_0:\n\t"
    "s_swappc_b64 s[76:77], s[72:73]\n\t"
    "s_branch L_ret_l5_0\n\t"
    "L_stub_l6_1:\n\t"
    "s_swappc_b64 s[76:77], s[74:75]\n\t"
    "s_branch L_ret_l6_1\n\t"
    "L_stub_l7_0:\n\t"
    "s_swappc_b64 s[76:77], s[72:73]\n\t"
    "s_branch L_ret_l7_0\n\t"
    "L_stub_s53_1:\n\t"
    "s_swappc_b64 s[76:77], s[74:75]\n\t"
    "s_branch L_ret_s53_1\n\t"
    "L_stub_s54_0:\n\t"
    "s_swappc_b64 s[76:77], s[72:73]\n\t"
    "s_branch L_ret_s54_0\n\t"
    "L_stub_s55_1:\n\t"
    "s_swappc_b64 s[76:77], s[74:75]\n\t"
    "s_branch L_ret_s55_1\n\t"
    "L_stub_s56_0:\n\t"
    "s_swappc_b64 s[76:77], s[72:73]\n\t"
    "s_branch L_ret_s56_0\n\t"
    "L_stub_s57_1:\n\t"
    "s_swappc_b64 s[76:77], s[74:75]\n\t"
    "s_branch L_ret_s57_1\n\t"
    "L_stub_s58_0:\n\t"
    "s_swappc_b64 s[76:77], s[72:73]\n\t"
    "s_branch L_ret_s58_0\n\t"
    "L_stub_s59_1:\n\t"
    "s_swappc_b64 s[76:77], s[74:75]\n\t"
    "s_branch L_ret_s59_1\n\t"
    "L_stub_s60_0:\n\t"
    "s_swappc_b64 s[76:77], s[72:73]\n\t"
    "s_branch L_ret_s60_0\n\t"
    "L_stub_h0:\n\t"
    "s_nop 7\n\t"
    "s_nop 7\n\t"
    "v_lshrrev_b32 v99, 4, v195\n\t"
    "v_and_b32 v99, 48, v99\n\t"
    "v_max3_f32 v96, v64, v65, v66\n\t"
    "v_max3_f32 v96, v96, v67, v80\n\t"
    "v_max3_f32 v96, v96, v81, v82\n\t"
    "v_max_f32 v96, v96, v83\n\t"
    "v_mov_b32 v97, v96\n\t"
    "s_nop 1\n\t"
    "v_permlane32_swap_b32 v96, v97\n\t"
    "s_nop 1\n\t"
    "v_max_f32 v96, v96, v97\n\t"
    "v_mov_b32 v97, v96\n\t"
    "s_nop 1\n\t"
    "v_permlane16_swap_b32 v96, v97\n\t"
    "s_nop 1\n\t"
    "v_max_f32 v96, v96, v97\n\t"
    "v_max_f32 v96, 0, v96\n\t"
    "v_sub_f32 v128, v128, v96\n\t"
    "v_sub_f32 v129, v129, v96\n\t"
    "v_sub_f32 v130, v130, v96\n\t"
    "v_sub_f32 v131, v131, v96\n\t"
    "v_sub_f32 v64, v64, v96\n\t"
    "v_sub_f32 v65, v65, v96\n\t"
    "v_sub_f32 v66, v66, v96\n\t"
    "v_sub_f32 v67, v67, v96\n\t"
    "v_sub_f32 v80, v80, v96\n\t"
    "v_sub_f32 v81, v81, v96\n\t"
    "v_sub_f32 v82, v82, v96\n\t"
    "v_sub_f32 v83, v83, v96\n\t"
    "v_sub_f32 v32, v32, v96\n\t"
    "v_sub_f32 v33, v33, v96\n\t"
    "v_sub_f32 v34, v34, v96\n\t"
    "v_sub_f32 v35, v35, v96\n\t"
    "v_sub_f32 v48, v48, v96\n\t"
    "v_sub_f32 v49, v49, v96\n\t"
    "v_sub_f32 v50, v50, v96\n\t"
    "v_sub_f32 v51, v51, v96\n\t"
    "v_exp_f32 v98, -v96\n\t"
    "s_nop 0\n\t"
    "v_mul_f32 v168, v168, v98\n\t"
    "ds_bpermute_b32 v100, v99, v98 offset:0\n\t"
    "ds_bpermute_b32 v101, v99, v98 offset:4\n\t"
    "ds_bpermute_b32 v102, v99, v98 offset:8\n\t"
    "ds_bpermute_b32 v103, v99, v98 offset:12\n\t"
    "v_exp_f32 v176, v64\n\t"
    "v_exp_f32 v177, v65\n\t"
    "s_nop 0\n\t"
    "v_add_f32 v172, v176, v177\n\t"
    "v_cvt_pk_bf16_f32 v112, v176, v177\n\t"
    "v_exp_f32 v178, v66\n\t"
    "v_exp_f32 v179, v67\n\t"
    "s_nop 0\n\t"
    "v_add_f32 v172, v172, v178\n\t"
    "v_add_f32 v172, v172, v179\n\t"
    "v_cvt_pk_bf16_f32 v113, v178, v179\n\t"
    "v_exp_f32 v176, v80\n\t"
    "v_exp_f32 v177, v81\n\t"
    "s_nop 0\n\t"
    "v_add_f32 v172, v172, v176\n\t"
    "v_add_f32 v172, v172, v177\n\t"
    "v_cvt_pk_bf16_f32 v114, v176, v177\n\t"
    "v_exp_f32 v178, v82\n\t"
    "v_exp_f32 v179, v83\n\t"
    "s_nop 0\n\t"
    "v_add_f32 v172, v172, v178\n\t"
    "v_add_f32 v172, v172, v179\n\t"
    "v_cvt_pk_bf16_f32 v115, v178, v179\n\t"
    "s_waitcnt lgkmcnt(0)\n\t"
    "v_accvgpr_read_b32 v96, a0\n\t"
    "v_mul_f32 v96, v96, v100\n\t"
    "v_accvgpr_write_b32 a0, v96\n\t"
    "v_accvgpr_read_b32 v97, a1\n\t"
    "v_mul_f32 v97, v97, v101\n\t"
    "v_accvgpr_write_b32 a1, v97\n\t"
    "v_accvgpr_read_b32 v96, a2\n\t"
    "v_mul_f32 v96, v96, v102\n\t"
    "v_accvgpr_write_b32 a2, v96\n\t"
    "v_accvgpr_read_b32 v97, a3\n\t"
    "v_mul_f32 v97, v97, v103\n\t"
    "v_accvgpr_write_b32 a3, v97\n\t"
    "v_accvgpr_read_b32 v96, a16\n\t"
    "v_mul_f32 v96, v96, v100\n\t"
    "v_accvgpr_write_b32 a16, v96\n\t"
    "v_accvgpr_read_b32 v97, a17\n\t"
    "v_mul_f32 v97, v97, v101\n\t"
    "v_accvgpr_write_b32 a17, v97\n\t"
    "v_accvgpr_read_b32 v96, a18\n\t"
    "v_mul_f32 v96, v96, v102\n\t"
    "v_accvgpr_write_b32 a18, v96\n\t"
    "v_accvgpr_read_b32 v97, a19\n\t"
    "v_mul_f32 v97, v97, v103\n\t"
    "v_accvgpr_write_b32 a19, v97\n\t"
    "v_accvgpr_read_b32 v96, a32\n\t"
    "v_mul_f32 v96, v96, v100\n\t"
    "v_accvgpr_write_b32 a32, v96\n\t"
    "v_accvgpr_read_b32 v97, a33\n\t"
    "v_mul_f32 v97, v97, v101\n\t"
    "v_accvgpr_write_b32 a33, v97\n\t"
    "v_accvgpr_read_b32 v96, a34\n\t"
    "v_mul_f32 v96, v96, v102\n\t"
    "v_accvgpr_write_b32 a34, v96\n\t"
    "v_accvgpr_read_b32 v97, a35\n\t"
    "v_mul_f32 v97, v97, v103\n\t"
    "v_accvgpr_write_b32 a35, v97\n\t"
    "v_accvgpr_read_b32 v96, a48\n\t"
    "v_mul_f32 v96, v96, v100\n\t"
    "v_accvgpr_write_b32 a48, v96\n\t"
    "v_accvgpr_read_b32 v97, a49\n\t"
    "v_mul_f32 v97, v97, v101\n\t"
    "v_accvgpr_write_b32 a49, v97\n\t"
    "v_accvgpr_read_b32 v96, a50\n\t"
    "v_mul_f32 v96, v96, v102\n\t"
    "v_accvgpr_write_b32 a50, v96\n\t"
    "v_accvgpr_read_b32 v97, a51\n\t"
    "v_mul_f32 v97, v97, v103\n\t"
    "v_accvgpr_write_b32 a51, v97\n\t"
    "v_accvgpr_read_b32 v96, a64\n\t"
    "v_mul_f32 v96, v96, v100\n\t"
    "v_accvgpr_write_b32 a64, v96\n\t"
    "v_accvgpr_read_b32 v97, a65\n\t"
    "v_mul_f32 v97, v97, v101\n\t"
    "v_accvgpr_write_b32 a65, v97\n\t"
    "v_accvgpr_read_b32 v96, a66\n\t"
    "v_mul_f32 v96, v96, v102\n\t"
    "v_accvgpr_write_b32 a66, v96\n\t"
    "v_accvgpr_read_b32 v97, a67\n\t"
    "v_mul_f32 v97, v97, v103\n\t"
    "v_accvgpr_write_b32 a67, v97\n\t"
    "v_accvgpr_read_b32 v96, a80\n\t"
    "v_mul_f32 v96, v96, v100\n\t"
    "v_accvgpr_write_b32 a80, v96\n\t"
    "v_accvgpr_read_b32 v97, a81\n\t"
    "v_mul_f32 v97, v97, v101\n\t"
    "v_accvgpr_write_b32 a81, v97\n\t"
    "v_accvgpr_read_b32 v96, a82\n\t"
    "v_mul_f32 v96, v96, v102\n\t"
    "v_accvgpr_write_b32 a82, v96\n\t"
    "v_accvgpr_read_b32 v97, a83\n\t"
    "v_mul_f32 v97, v97, v103\n\t"
    "v_accvgpr_write_b32 a83, v97\n\t"
    "v_accvgpr_read_b32 v96, a96\n\t"
    "v_mul_f32 v96, v96, v100\n\t"
    "v_accvgpr_write_b32 a96, v96\n\t"
    "v_accvgpr_read_b32 v97, a97\n\t"
    "v_mul_f32 v97, v97, v101\n\t"
    "v_accvgpr_write_b32 a97, v97\n\t"
    "v_accvgpr_read_b32 v96, a98\n\t"
    "v_mul_f32 v96, v96, v102\n\t"
    "v_accvgpr_write_b32 a98, v96\n\t"
    "v_accvgpr_read_b32 v97, a99\n\t"
    "v_mul_f32 v97, v97, v103\n\t"
    "v_accvgpr_write_b32 a99, v97\n\t"
    "v_accvgpr_read_b32 v96, a112\n\t"
    "v_mul_f32 v96, v96, v100\n\t"
    "v_accvgpr_write_b32 a112, v96\n\t"
    "v_accvgpr_read_b32 v97, a113\n\t"
    "v_mul_f32 v97, v97, v101\n\t"
    "v_accvgpr_write_b32 a113, v97\n\t"
    "v_accvgpr_read_b32 v96, a114\n\t"
    "v_mul_f32 v96, v96, v102\n\t"
    "v_accvgpr_write_b32 a114, v96\n\t"
    "v_accvgpr_read_b32 v97, a115\n\t"
    "v_mul_f32 v97, v97, v103\n\t"
    "v_accvgpr_write_b32 a115, v97\n\t"
    "v_accvgpr_read_b32 v96, a128\n\t"
    "v_mul_f32 v96, v96, v100\n\t"
    "v_accvgpr_write_b32 a128, v96\n\t"
    "v_accvgpr_read_b32 v97, a129\n\t"
    "v_mul_f32 v97, v97, v101\n\t"
    "v_accvgpr_write_b32 a129, v97\n\t"
    "v_accvgpr_read_b32 v96, a130\n\t"
    "v_mul_f32 v96, v96, v102\n\t"
    "v_accvgpr_write_b32 a130, v96\n\t"
    "v_accvgpr_read_b32 v97, a131\n\t"
    "v_mul_f32 v97, v97, v103\n\t"
    "v_accvgpr_write_b32 a131, v97\n\t"
    "v_accvgpr_read_b32 v96, a144\n\t"
    "v_mul_f32 v96, v96, v100\n\t"
    "v_accvgpr_write_b32 a144, v96\n\t"
    "v_accvgpr_read_b32 v97, a145\n\t"
    "v_mul_f32 v97, v97, v101\n\t"
    "v_accvgpr_write_b32 a145, v97\n\t"
    "v_accvgpr_read_b32 v96, a146\n\t"
    "v_mul_f32 v96, v96, v102\n\t"
    "v_accvgpr_write_b32 a146, v96\n\t"
    "v_accvgpr_read_b32 v97, a147\n\t"
    "v_mul_f32 v97, v97, v103\n\t"
    "v_accvgpr_write_b32 a147, v97\n\t"
    "v_accvgpr_read_b32 v96, a160\n\t"
    "v_mul_f32 v96, v96, v100\n\t"
    "v_accvgpr_write_b32 a160, v96\n\t"
    "v_accvgpr_read_b32 v97, a161\n\t"
    "v_mul_f32 v97, v97, v101\n\t"
    "v_accvgpr_write_b32 a161, v97\n\t"
    "v_accvgpr_read_b32 v96, a162\n\t"
    "v_mul_f32 v96, v96, v102\n\t"
    "v_accvgpr_write_b32 a162, v96\n\t"
    "v_accvgpr_read_b32 v97, a163\n\t"
    "v_mul_f32 v97, v97, v103\n\t"
    "v_accvgpr_write_b32 a163, v97\n\t"
    "v_accvgpr_read_b32 v96, a176\n\t"
    "v_mul_f32 v96, v96, v100\n\t"
    "v_accvgpr_write_b32 a176, v96\n\t"
    "v_accvgpr_read_b32 v97, a177\n\t"
    "v_mul_f32 v97, v97, v101\n\t"
    "v_accvgpr_write_b32 a177, v97\n\t"
    "v_accvgpr_read_b32 v96, a178\n\t"
    "v_mul_f32 v96, v96, v102\n\t"
    "v_accvgpr_write_b32 a178, v96\n\t"
    "v_accvgpr_read_b32 v97, a179\n\t"
    "v_mul_f32 v97, v97, v103\n\t"
    "v_accvgpr_write_b32 a179, v97\n\t"
    "v_max3_f32 v96, v68, v69, v70\n\t"
    "v_max3_f32 v96, v96, v71, v84\n\t"
    "v_max3_f32 v96, v96, v85, v86\n\t"
    "v_max_f32 v96, v96, v87\n\t"
    "v_mov_b32 v97, v96\n\t"
    "s_nop 1\n\t"
    "v_permlane32_swap_b32 v96, v97\n\t"
    "s_nop 1\n\t"
    "v_max_f32 v96, v96, v97\n\t"
    "v_mov_b32 v97, v96\n\t"
    "s_nop 1\n\t"
    "v_permlane16_swap_b32 v96, v97\n\t"
    "s_nop 1\n\t"
    "v_max_f32 v96, v96, v97\n\t"
    "v_max_f32 v96, 0, v96\n\t"
    "v_sub_f32 v132, v132, v96\n\t"
    "v_sub_f32 v133, v133, v96\n\t"
    "v_sub_f32 v134, v134, v96\n\t"
    "v_sub_f32 v135, v135, v96\n\t"
    "v_sub_f32 v68, v68, v96\n\t"
    "v_sub_f32 v69, v69, v96\n\t"
    "v_sub_f32 v70, v70, v96\n\t"
    "v_sub_f32 v71, v71, v96\n\t"
    "v_sub_f32 v84, v84, v96\n\t"
    "v_sub_f32 v85, v85, v96\n\t"
    "v_sub_f32 v86, v86, v96\n\t"
    "v_sub_f32 v87, v87, v96\n\t"
    "v_sub_f32 v36, v36, v96\n\t"
    "v_sub_f32 v37, v37, v96\n\t"
    "v_sub_f32 v38, v38, v96\n\t"
    "v_sub_f32 v39, v39, v96\n\t"
    "v_sub_f32 v52, v52, v96\n\t"
    "v_sub_f32 v53, v53, v96\n\t"
    "v_sub_f32 v54, v54, v96\n\t"
    "v_sub_f32 v55, v55, v96\n\t"
    "v_exp_f32 v98, -v96\n\t"
    "s_nop 0\n\t"
    "v_mul_f32 v169, v169, v98\n\t"
    "ds_bpermute_b32 v100, v99, v98 offset:0\n\t"
    "ds_bpermute_b32 v101, v99, v98 offset:4\n\t"
    "ds_bpermute_b32 v102, v99, v98 offset:8\n\t"
    "ds_bpermute_b32 v103, v99, v98 offset:12\n\t"
    "v_exp_f32 v176, v68\n\t"
    "v_exp_f32 v177, v69\n\t"
    "s_nop 0\n\t"
    "v_add_f32 v173, v176, v177\n\t"
    "v_cvt_pk_bf16_f32 v116, v176, v177\n\t"
    "v_exp_f32 v178, v70\n\t"
    "v_exp_f32 v179, v71\n\t"
    "s_nop 0\n\t"
    "v_add_f32 v173, v173, v178\n\t"
    "v_add_f32 v173, v173, v179\n\t"
    "v_cvt_pk_bf16_f32 v117, v178, v179\n\t"
    "v_exp_f32 v176, v84\n\t"
    "v_exp_f32 v177, v85\n\t"
    "s_nop 0\n\t"
    "v_add_f32 v173, v173, v176\n\t"
    "v_add_f32 v173, v173, v177\n\t"
    "v_cvt_pk_bf16_f32 v118, v176, v177\n\t"
    "v_exp_f32 v178, v86\n\t"
    "v_exp_f32 v179, v87\n\t"
    "s_nop 0\n\t"
    "v_add_f32 v173, v173, v178\n\t"
    "v_add_f32 v173, v173, v179\n\t"
    "v_cvt_pk_bf16_f32 v119, v178, v179\n\t"
    "s_waitcnt lgkmcnt(0)\n\t"
    "v_accvgpr_read_b32 v96, a4\n\t"
    "v_mul_f32 v96, v96, v100\n\t"
    "v_accvgpr_write_b32 a4, v96\n\t"
    "v_accvgpr_read_b32 v97, a5\n\t"
    "v_mul_f32 v97, v97, v101\n\t"
    "v_accvgpr_write_b32 a5, v97\n\t"
    "v_accvgpr_read_b32 v96, a6\n\t"
    "v_mul_f32 v96, v96, v102\n\t"
    "v_accvgpr_write_b32 a6, v96\n\t"
    "v_accvgpr_read_b32 v97, a7\n\t"
    "v_mul_f32 v97, v97, v103\n\t"
    "v_accvgpr_write_b32 a7, v97\n\t"
    "v_accvgpr_read_b32 v96, a20\n\t"
    "v_mul_f32 v96, v96, v100\n\t"
    "v_accvgpr_write_b32 a20, v96\n\t"
    "v_accvgpr_read_b32 v97, a21\n\t"
    "v_mul_f32 v97, v97, v101\n\t"
    "v_accvgpr_write_b32 a21, v97\n\t"
    "v_accvgpr_read_b32 v96, a22\n\t"
    "v_mul_f32 v96, v96, v102\n\t"
    "v_accvgpr_write_b32 a22, v96\n\t"
    "v_accvgpr_read_b32 v97, a23\n\t"
    "v_mul_f32 v97, v97, v103\n\t"
    "v_accvgpr_write_b32 a23, v97\n\t"
    "v_accvgpr_read_b32 v96, a36\n\t"
    "v_mul_f32 v96, v96, v100\n\t"
    "v_accvgpr_write_b32 a36, v96\n\t"
    "v_accvgpr_read_b32 v97, a37\n\t"
    "v_mul_f32 v97, v97, v101\n\t"
    "v_accvgpr_write_b32 a37, v97\n\t"
    "v_accvgpr_read_b32 v96, a38\n\t"
    "v_mul_f32 v96, v96, v102\n\t"
    "v_accvgpr_write_b32 a38, v96\n\t"
    "v_accvgpr_read_b32 v97, a39\n\t"
    "v_mul_f32 v97, v97, v103\n\t"
    "v_accvgpr_write_b32 a39, v97\n\t"
    "v_accvgpr_read_b32 v96, a52\n\t"
    "v_mul_f32 v96, v96, v100\n\t"
    "v_accvgpr_write_b32 a52, v96\n\t"
    "v_accvgpr_read_b32 v97, a53\n\t"
    "v_mul_f32 v97, v97, v101\n\t"
    "v_accvgpr_write_b32 a53, v97\n\t"
    "v_accvgpr_read_b32 v96, a54\n\t"
    "v_mul_f32 v96, v96, v102\n\t"
    "v_accvgpr_write_b32 a54, v96\n\t"
    "v_accvgpr_read_b32 v97, a55\n\t"
    "v_mul_f32 v97, v97, v103\n\t"
    "v_accvgpr_write_b32 a55, v97\n\t"
    "v_accvgpr_read_b32 v96, a68\n\t"
    "v_mul_f32 v96, v96, v100\n\t"
    "v_accvgpr_write_b32 a68, v96\n\t"
    "v_accvgpr_read_b32 v97, a69\n\t"
    "v_mul_f32 v97, v97, v101\n\t"
    "v_accvgpr_write_b32 a69, v97\n\t"
    "v_accvgpr_read_b32 v96, a70\n\t"
    "v_mul_f32 v96, v96, v102\n\t"
    "v_accvgpr_write_b32 a70, v96\n\t"
    "v_accvgpr_read_b32 v97, a71\n\t"
    "v_mul_f32 v97, v97, v103\n\t"
    "v_accvgpr_write_b32 a71, v97\n\t"
    "v_accvgpr_read_b32 v96, a84\n\t"
    "v_mul_f32 v96, v96, v100\n\t"
    "v_accvgpr_write_b32 a84, v96\n\t"
    "v_accvgpr_read_b32 v97, a85\n\t"
    "v_mul_f32 v97, v97, v101\n\t"
    "v_accvgpr_write_b32 a85, v97\n\t"
    "v_accvgpr_read_b32 v96, a86\n\t"
    "v_mul_f32 v96, v96, v102\n\t"
    "v_accvgpr_write_b32 a86, v96\n\t"
    "v_accvgpr_read_b32 v97, a87\n\t"
    "v_mul_f32 v97, v97, v103\n\t"
    "v_accvgpr_write_b32 a87, v97\n\t"
    "v_accvgpr_read_b32 v96, a100\n\t"
    "v_mul_f32 v96, v96, v100\n\t"
    "v_accvgpr_write_b32 a100, v96\n\t"
    "v_accvgpr_read_b32 v97, a101\n\t"
    "v_mul_f32 v97, v97, v101\n\t"
    "v_accvgpr_write_b32 a101, v97\n\t"
    "v_accvgpr_read_b32 v96, a102\n\t"
    "v_mul_f32 v96, v96, v102\n\t"
    "v_accvgpr_write_b32 a102, v96\n\t"
    "v_accvgpr_read_b32 v97, a103\n\t"
    "v_mul_f32 v97, v97, v103\n\t"
    "v_accvgpr_write_b32 a103, v97\n\t"
    "v_accvgpr_read_b32 v96, a116\n\t"
    "v_mul_f32 v96, v96, v100\n\t"
    "v_accvgpr_write_b32 a116, v96\n\t"
    "v_accvgpr_read_b32 v97, a117\n\t"
    "v_mul_f32 v97, v97, v101\n\t"
    "v_accvgpr_write_b32 a117, v97\n\t"
    "v_accvgpr_read_b32 v96, a118\n\t"
    "v_mul_f32 v96, v96, v102\n\t"
    "v_accvgpr_write_b32 a118, v96\n\t"
    "v_accvgpr_read_b32 v97, a119\n\t"
    "v_mul_f32 v97, v97, v103\n\t"
    "v_accvgpr_write_b32 a119, v97\n\t"
    "v_accvgpr_read_b32 v96, a132\n\t"
    "v_mul_f32 v96, v96, v100\n\t"
    "v_accvgpr_write_b32 a132, v96\n\t"
    "v_accvgpr_read_b32 v97, a133\n\t"
    "v_mul_f32 v97, v97, v101\n\t"
    "v_accvgpr_write_b32 a133, v97\n\t"
    "v_accvgpr_read_b32 v96, a134\n\t"
    "v_mul_f32 v96, v96, v102\n\t"
    "v_accvgpr_write_b32 a134, v96\n\t"
    "v_accvgpr_read_b32 v97, a135\n\t"
    "v_mul_f32 v97, v97, v103\n\t"
    "v_accvgpr_write_b32 a135, v97\n\t"
    "v_accvgpr_read_b32 v96, a148\n\t"
    "v_mul_f32 v96, v96, v100\n\t"
    "v_accvgpr_write_b32 a148, v96\n\t"
    "v_accvgpr_read_b32 v97, a149\n\t"
    "v_mul_f32 v97, v97, v101\n\t"
    "v_accvgpr_write_b32 a149, v97\n\t"
    "v_accvgpr_read_b32 v96, a150\n\t"
    "v_mul_f32 v96, v96, v102\n\t"
    "v_accvgpr_write_b32 a150, v96\n\t"
    "v_accvgpr_read_b32 v97, a151\n\t"
    "v_mul_f32 v97, v97, v103\n\t"
    "v_accvgpr_write_b32 a151, v97\n\t"
    "v_accvgpr_read_b32 v96, a164\n\t"
    "v_mul_f32 v96, v96, v100\n\t"
    "v_accvgpr_write_b32 a164, v96\n\t"
    "v_accvgpr_read_b32 v97, a165\n\t"
    "v_mul_f32 v97, v97, v101\n\t"
    "v_accvgpr_write_b32 a165, v97\n\t"
    "v_accvgpr_read_b32 v96, a166\n\t"
    "v_mul_f32 v96, v96, v102\n\t"
    "v_accvgpr_write_b32 a166, v96\n\t"
    "v_accvgpr_read_b32 v97, a167\n\t"
    "v_mul_f32 v97, v97, v103\n\t"
    "v_accvgpr_write_b32 a167, v97\n\t"
    "v_accvgpr_read_b32 v96, a180\n\t"
    "v_mul_f32 v96, v96, v100\n\t"
    "v_accvgpr_write_b32 a180, v96\n\t"
    "v_accvgpr_read_b32 v97, a181\n\t"
    "v_mul_f32 v97, v97, v101\n\t"
    "v_accvgpr_write_b32 a181, v97\n\t"
    "v_accvgpr_read_b32 v96, a182\n\t"
    "v_mul_f32 v96, v96, v102\n\t"
    "v_accvgpr_write_b32 a182, v96\n\t"
    "v_accvgpr_read_b32 v97, a183\n\t"
    "v_mul_f32 v97, v97, v103\n\t"
    "v_accvgpr_write_b32 a183, v97\n\t"
    "s_nop 1\n\t"
    "s_branch L_ret_h0\n\t"
    "L_stub_h2:\n\t"
    "s_nop 7\n\t"
    "s_nop 7\n\t"
    "v_lshrrev_b32 v115, 4, v195\n\t"
    "v_and_b32 v115, 48, v115\n\t"
    "v_max3_f32 v112, v32, v33, v34\n\t"
    "v_max3_f32 v112, v112, v35, v48\n\t"
    "v_max3_f32 v112, v112, v49, v50\n\t"
    "v_max_f32 v112, v112, v51\n\t"
    "v_mov_b32 v113, v112\n\t"
    "s_nop 1\n\t"
    "v_permlane32_swap_b32 v112, v113\n\t"
    "s_nop 1\n\t"
    "v_max_f32 v112, v112, v113\n\t"
    "v_mov_b32 v113, v112\n\t"
    "s_nop 1\n\t"
    "v_permlane16_swap_b32 v112, v113\n\t"
    "s_nop 1\n\t"
    "v_max_f32 v112, v112, v113\n\t"
    "v_max_f32 v112, 0, v112\n\t"
    "v_sub_f32 v128, v128, v112\n\t"
    "v_sub_f32 v129, v129, v112\n\t"
    "v_sub_f32 v130, v130, v112\n\t"
    "v_sub_f32 v131, v131, v112\n\t"
    "v_sub_f32 v32, v32, v112\n\t"
    "v_sub_f32 v33, v33, v112\n\t"
    "v_sub_f32 v34, v34, v112\n\t"
    "v_sub_f32 v35, v35, v112\n\t"
    "v_sub_f32 v48, v48, v112\n\t"
    "v_sub_f32 v49, v49, v112\n\t"
    "v_sub_f32 v50, v50, v112\n\t"
    "v_sub_f32 v51, v51, v112\n\t"
    "v_sub_f32 v64, v64, v112\n\t"
    "v_sub_f32 v65, v65, v112\n\t"
    "v_sub_f32 v66, v66, v112\n\t"
    "v_sub_f32 v67, v67, v112\n\t"
    "v_sub_f32 v80, v80, v112\n\t"
    "v_sub_f32 v81, v81, v112\n\t"
    "v_sub_f32 v82, v82, v112\n\t"
    "v_sub_f32 v83, v83, v112\n\t"
    "v_exp_f32 v114, -v112\n\t"
    "s_nop 0\n\t"
    "v_mul_f32 v168, v168, v114\n\t"
    "ds_bpermute_b32 v116, v115, v114 offset:0\n\t"
    "ds_bpermute_b32 v117, v115, v114 offset:4\n\t"
    "ds_bpermute_b32 v118, v115, v114 offset:8\n\t"
    "ds_bpermute_b32 v119, v115, v114 offset:12\n\t"
    "v_exp_f32 v176, v32\n\t"
    "v_exp_f32 v177, v33\n\t"
    "s_nop 0\n\t"
    "v_add_f32 v172, v176, v177\n\t"
    "v_cvt_pk_bf16_f32 v96, v176, v177\n\t"
    "v_exp_f32 v178, v34\n\t"
    "v_exp_f32 v179, v35\n\t"
    "s_nop 0\n\t"
    "v_add_f32 v172, v172, v178\n\t"
    "v_add_f32 v172, v172, v179\n\t"
    "v_cvt_pk_bf16_f32 v97, v178, v179\n\t"
    "v_exp_f32 v176, v48\n\t"
    "v_exp_f32 v177, v49\n\t"
    "s_nop 0\n\t"
    "v_add_f32 v172, v172, v176\n\t"
    "v_add_f32 v172, v172, v177\n\t"
    "v_cvt_pk_bf16_f32 v98, v176, v177\n\t"
    "v_exp_f32 v178, v50\n\t"
    "v_exp_f32 v179, v51\n\t"
    "s_nop 0\n\t"
    "v_add_f32 v172, v172, v178\n\t"
    "v_add_f32 v172, v172, v179\n\t"
    "v_cvt_pk_bf16_f32 v99, v178, v179\n\t"
    "s_waitcnt lgkmcnt(0)\n\t"
    "v_accvgpr_read_b32 v112, a0\n\t"
    "v_mul_f32 v112, v112, v116\n\t"
    "v_accvgpr_write_b32 a0, v112\n\t"
    "v_accvgpr_read_b32 v113, a1\n\t"
    "v_mul_f32 v113, v113, v117\n\t"
    "v_accvgpr_write_b32 a1, v113\n\t"
    "v_accvgpr_read_b32 v112, a2\n\t"
    "v_mul_f32 v112, v112, v118\n\t"
    "v_accvgpr_write_b32 a2, v112\n\t"
    "v_accvgpr_read_b32 v113, a3\n\t"
    "v_mul_f32 v113, v113, v119\n\t"
    "v_accvgpr_write_b32 a3, v113\n\t"
    "v_accvgpr_read_b32 v112, a16\n\t"
    "v_mul_f32 v112, v112, v116\n\t"
    "v_accvgpr_write_b32 a16, v112\n\t"
    "v_accvgpr_read_b32 v113, a17\n\t"
    "v_mul_f32 v113, v113, v117\n\t"
    "v_accvgpr_write_b32 a17, v113\n\t"
    "v_accvgpr_read_b32 v112, a18\n\t"
    "v_mul_f32 v112, v112, v118\n\t"
    "v_accvgpr_write_b32 a18, v112\n\t"
    "v_accvgpr_read_b32 v113, a19\n\t"
    "v_mul_f32 v113, v113, v119\n\t"
    "v_accvgpr_write_b32 a19, v113\n\t"
    "v_accvgpr_read_b32 v112, a32\n\t"
    "v_mul_f32 v112, v112, v116\n\t"
    "v_accvgpr_write_b32 a32, v112\n\t"
    "v_accvgpr_read_b32 v113, a33\n\t"
    "v_mul_f32 v113, v113, v117\n\t"
    "v_accvgpr_write_b32 a33, v113\n\t"
    "v_accvgpr_read_b32 v112, a34\n\t"
    "v_mul_f32 v112, v112, v118\n\t"
    "v_accvgpr_write_b32 a34, v112\n\t"
    "v_accvgpr_read_b32 v113, a35\n\t"
    "v_mul_f32 v113, v113, v119\n\t"
    "v_accvgpr_write_b32 a35, v113\n\t"
    "v_accvgpr_read_b32 v112, a48\n\t"
    "v_mul_f32 v112, v112, v116\n\t"
    "v_accvgpr_write_b32 a48, v112\n\t"
    "v_accvgpr_read_b32 v113, a49\n\t"
    "v_mul_f32 v113, v113, v117\n\t"
    "v_accvgpr_write_b32 a49, v113\n\t"
    "v_accvgpr_read_b32 v112, a50\n\t"
    "v_mul_f32 v112, v112, v118\n\t"
    "v_accvgpr_write_b32 a50, v112\n\t"
    "v_accvgpr_read_b32 v113, a51\n\t"
    "v_mul_f32 v113, v113, v119\n\t"
    "v_accvgpr_write_b32 a51, v113\n\t"
    "v_accvgpr_read_b32 v112, a64\n\t"
    "v_mul_f32 v112, v112, v116\n\t"
    "v_accvgpr_write_b32 a64, v112\n\t"
    "v_accvgpr_read_b32 v113, a65\n\t"
    "v_mul_f32 v113, v113, v117\n\t"
    "v_accvgpr_write_b32 a65, v113\n\t"
    "v_accvgpr_read_b32 v112, a66\n\t"
    "v_mul_f32 v112, v112, v118\n\t"
    "v_accvgpr_write_b32 a66, v112\n\t"
    "v_accvgpr_read_b32 v113, a67\n\t"
    "v_mul_f32 v113, v113, v119\n\t"
    "v_accvgpr_write_b32 a67, v113\n\t"
    "v_accvgpr_read_b32 v112, a80\n\t"
    "v_mul_f32 v112, v112, v116\n\t"
    "v_accvgpr_write_b32 a80, v112\n\t"
    "v_accvgpr_read_b32 v113, a81\n\t"
    "v_mul_f32 v113, v113, v117\n\t"
    "v_accvgpr_write_b32 a81, v113\n\t"
    "v_accvgpr_read_b32 v112, a82\n\t"
    "v_mul_f32 v112, v112, v118\n\t"
    "v_accvgpr_write_b32 a82, v112\n\t"
    "v_accvgpr_read_b32 v113, a83\n\t"
    "v_mul_f32 v113, v113, v119\n\t"
    "v_accvgpr_write_b32 a83, v113\n\t"
    "v_accvgpr_read_b32 v112, a96\n\t"
    "v_mul_f32 v112, v112, v116\n\t"
    "v_accvgpr_write_b32 a96, v112\n\t"
    "v_accvgpr_read_b32 v113, a97\n\t"
    "v_mul_f32 v113, v113, v117\n\t"
    "v_accvgpr_write_b32 a97, v113\n\t"
    "v_accvgpr_read_b32 v112, a98\n\t"
    "v_mul_f32 v112, v112, v118\n\t"
    "v_accvgpr_write_b32 a98, v112\n\t"
    "v_accvgpr_read_b32 v113, a99\n\t"
    "v_mul_f32 v113, v113, v119\n\t"
    "v_accvgpr_write_b32 a99, v113\n\t"
    "v_accvgpr_read_b32 v112, a112\n\t"
    "v_mul_f32 v112, v112, v116\n\t"
    "v_accvgpr_write_b32 a112, v112\n\t"
    "v_accvgpr_read_b32 v113, a113\n\t"
    "v_mul_f32 v113, v113, v117\n\t"
    "v_accvgpr_write_b32 a113, v113\n\t"
    "v_accvgpr_read_b32 v112, a114\n\t"
    "v_mul_f32 v112, v112, v118\n\t"
    "v_accvgpr_write_b32 a114, v112\n\t"
    "v_accvgpr_read_b32 v113, a115\n\t"
    "v_mul_f32 v113, v113, v119\n\t"
    "v_accvgpr_write_b32 a115, v113\n\t"
    "v_accvgpr_read_b32 v112, a128\n\t"
    "v_mul_f32 v112, v112, v116\n\t"
    "v_accvgpr_write_b32 a128, v112\n\t"
    "v_accvgpr_read_b32 v113, a129\n\t"
    "v_mul_f32 v113, v113, v117\n\t"
    "v_accvgpr_write_b32 a129, v113\n\t"
    "v_accvgpr_read_b32 v112, a130\n\t"
    "v_mul_f32 v112, v112, v118\n\t"
    "v_accvgpr_write_b32 a130, v112\n\t"
    "v_accvgpr_read_b32 v113, a131\n\t"
    "v_mul_f32 v113, v113, v119\n\t"
    "v_accvgpr_write_b32 a131, v113\n\t"
    "v_accvgpr_read_b32 v112, a144\n\t"
    "v_mul_f32 v112, v112, v116\n\t"
    "v_accvgpr_write_b32 a144, v112\n\t"
    "v_accvgpr_read_b32 v113, a145\n\t"
    "v_mul_f32 v113, v113, v117\n\t"
    "v_accvgpr_write_b32 a145, v113\n\t"
    "v_accvgpr_read_b32 v112, a146\n\t"
    "v_mul_f32 v112, v112, v118\n\t"
    "v_accvgpr_write_b32 a146, v112\n\t"
    "v_accvgpr_read_b32 v113, a147\n\t"
    "v_mul_f32 v113, v113, v119\n\t"
    "v_accvgpr_write_b32 a147, v113\n\t"
    "v_accvgpr_read_b32 v112, a160\n\t"
    "v_mul_f32 v112, v112, v116\n\t"
    "v_accvgpr_write_b32 a160, v112\n\t"
    "v_accvgpr_read_b32 v113, a161\n\t"
    "v_mul_f32 v113, v113, v117\n\t"
    "v_accvgpr_write_b32 a161, v113\n\t"
    "v_accvgpr_read_b32 v112, a162\n\t"
    "v_mul_f32 v112, v112, v118\n\t"
    "v_accvgpr_write_b32 a162, v112\n\t"
    "v_accvgpr_read_b32 v113, a163\n\t"
    "v_mul_f32 v113, v113, v119\n\t"
    "v_accvgpr_write_b32 a163, v113\n\t"
    "v_accvgpr_read_b32 v112, a176\n\t"
    "v_mul_f32 v112, v112, v116\n\t"
    "v_accvgpr_write_b32 a176, v112\n\t"
    "v_accvgpr_read_b32 v113, a177\n\t"
    "v_mul_f32 v113, v113, v117\n\t"
    "v_accvgpr_write_b32 a177, v113\n\t"
    "v_accvgpr_read_b32 v112, a178\n\t"
    "v_mul_f32 v112, v112, v118\n\t"
    "v_accvgpr_write_b32 a178, v112\n\t"
    "v_accvgpr_read_b32 v113, a179\n\t"
    "v_mul_f32 v113, v113, v119\n\t"
    "v_accvgpr_write_b32 a179, v113\n\t"
    "v_max3_f32 v112, v36, v37, v38\n\t"
    "v_max3_f32 v112, v112, v39, v52\n\t"
    "v_max3_f32 v112, v112, v53, v54\n\t"
    "v_max_f32 v112, v112, v55\n\t"
    "v_mov_b32 v113, v112\n\t"
    "s_nop 1\n\t"
    "v_permlane32_swap_b32 v112, v113\n\t"
    "s_nop 1\n\t"
    "v_max_f32 v112, v112, v113\n\t"
    "v_mov_b32 v113, v112\n\t"
    "s_nop 1\n\t"
    "v_permlane16_swap_b32 v112, v113\n\t"
    "s_nop 1\n\t"
    "v_max_f32 v112, v112, v113\n\t"
    "v_max_f32 v112, 0, v112\n\t"
    "v_sub_f32 v132, v132, v112\n\t"
    "v_sub_f32 v133, v133, v112\n\t"
    "v_sub_f32 v134, v134, v112\n\t"
    "v_sub_f32 v135, v135, v112\n\t"
    "v_sub_f32 v36, v36, v112\n\t"
    "v_sub_f32 v37, v37, v112\n\t"
    "v_sub_f32 v38, v38, v112\n\t"
    "v_sub_f32 v39, v39, v112\n\t"
    "v_sub_f32 v52, v52, v112\n\t"
    "v_sub_f32 v53, v53, v112\n\t"
    "v_sub_f32 v54, v54, v112\n\t"
    "v_sub_f32 v55, v55, v112\n\t"
    "v_sub_f32 v68, v68, v112\n\t"
    "v_sub_f32 v69, v69, v112\n\t"
    "v_sub_f32 v70, v70, v112\n\t"
    "v_sub_f32 v71, v71, v112\n\t"
    "v_sub_f32 v84, v84, v112\n\t"
    "v_sub_f32 v85, v85, v112\n\t"
    "v_sub_f32 v86, v86, v112\n\t"
    "v_sub_f32 v87, v87, v112\n\t"
    "v_exp_f32 v114, -v112\n\t"
    "s_nop 0\n\t"
    "v_mul_f32 v169, v169, v114\n\t"
    "ds_bpermute_b32 v116, v115, v114 offset:0\n\t"
    "ds_bpermute_b32 v117, v115, v114 offset:4\n\t"
    "ds_bpermute_b32 v118, v115, v114 offset:8\n\t"
    "ds_bpermute_b32 v119, v115, v114 offset:12\n\t"
    "v_exp_f32 v176, v36\n\t"
    "v_exp_f32 v177, v37\n\t"
    "s_nop 0\n\t"
    "v_add_f32 v173, v176, v177\n\t"
    "v_cvt_pk_bf16_f32 v100, v176, v177\n\t"
    "v_exp_f32 v178, v38\n\t"
    "v_exp_f32 v179, v39\n\t"
    "s_nop 0\n\t"
    "v_add_f32 v173, v173, v178\n\t"
    "v_add_f32 v173, v173, v179\n\t"
    "v_cvt_pk_bf16_f32 v101, v178, v179\n\t"
    "v_exp_f32 v176, v52\n\t"
    "v_exp_f32 v177, v53\n\t"
    "s_nop 0\n\t"
    "v_add_f32 v173, v173, v176\n\t"
    "v_add_f32 v173, v173, v177\n\t"
    "v_cvt_pk_bf16_f32 v102, v176, v177\n\t"
    "v_exp_f32 v178, v54\n\t"
    "v_exp_f32 v179, v55\n\t"
    "s_nop 0\n\t"
    "v_add_f32 v173, v173, v178\n\t"
    "v_add_f32 v173, v173, v179\n\t"
    "v_cvt_pk_bf16_f32 v103, v178, v179\n\t"
    "s_waitcnt lgkmcnt(0)\n\t"
    "v_accvgpr_read_b32 v112, a4\n\t"
    "v_mul_f32 v112, v112, v116\n\t"
    "v_accvgpr_write_b32 a4, v112\n\t"
    "v_accvgpr_read_b32 v113, a5\n\t"
    "v_mul_f32 v113, v113, v117\n\t"
    "v_accvgpr_write_b32 a5, v113\n\t"
    "v_accvgpr_read_b32 v112, a6\n\t"
    "v_mul_f32 v112, v112, v118\n\t"
    "v_accvgpr_write_b32 a6, v112\n\t"
    "v_accvgpr_read_b32 v113, a7\n\t"
    "v_mul_f32 v113, v113, v119\n\t"
    "v_accvgpr_write_b32 a7, v113\n\t"
    "v_accvgpr_read_b32 v112, a20\n\t"
    "v_mul_f32 v112, v112, v116\n\t"
    "v_accvgpr_write_b32 a20, v112\n\t"
    "v_accvgpr_read_b32 v113, a21\n\t"
    "v_mul_f32 v113, v113, v117\n\t"
    "v_accvgpr_write_b32 a21, v113\n\t"
    "v_accvgpr_read_b32 v112, a22\n\t"
    "v_mul_f32 v112, v112, v118\n\t"
    "v_accvgpr_write_b32 a22, v112\n\t"
    "v_accvgpr_read_b32 v113, a23\n\t"
    "v_mul_f32 v113, v113, v119\n\t"
    "v_accvgpr_write_b32 a23, v113\n\t"
    "v_accvgpr_read_b32 v112, a36\n\t"
    "v_mul_f32 v112, v112, v116\n\t"
    "v_accvgpr_write_b32 a36, v112\n\t"
    "v_accvgpr_read_b32 v113, a37\n\t"
    "v_mul_f32 v113, v113, v117\n\t"
    "v_accvgpr_write_b32 a37, v113\n\t"
    "v_accvgpr_read_b32 v112, a38\n\t"
    "v_mul_f32 v112, v112, v118\n\t"
    "v_accvgpr_write_b32 a38, v112\n\t"
    "v_accvgpr_read_b32 v113, a39\n\t"
    "v_mul_f32 v113, v113, v119\n\t"
    "v_accvgpr_write_b32 a39, v113\n\t"
    "v_accvgpr_read_b32 v112, a52\n\t"
    "v_mul_f32 v112, v112, v116\n\t"
    "v_accvgpr_write_b32 a52, v112\n\t"
    "v_accvgpr_read_b32 v113, a53\n\t"
    "v_mul_f32 v113, v113, v117\n\t"
    "v_accvgpr_write_b32 a53, v113\n\t"
    "v_accvgpr_read_b32 v112, a54\n\t"
    "v_mul_f32 v112, v112, v118\n\t"
    "v_accvgpr_write_b32 a54, v112\n\t"
    "v_accvgpr_read_b32 v113, a55\n\t"
    "v_mul_f32 v113, v113, v119\n\t"
    "v_accvgpr_write_b32 a55, v113\n\t"
    "v_accvgpr_read_b32 v112, a68\n\t"
    "v_mul_f32 v112, v112, v116\n\t"
    "v_accvgpr_write_b32 a68, v112\n\t"
    "v_accvgpr_read_b32 v113, a69\n\t"
    "v_mul_f32 v113, v113, v117\n\t"
    "v_accvgpr_write_b32 a69, v113\n\t"
    "v_accvgpr_read_b32 v112, a70\n\t"
    "v_mul_f32 v112, v112, v118\n\t"
    "v_accvgpr_write_b32 a70, v112\n\t"
    "v_accvgpr_read_b32 v113, a71\n\t"
    "v_mul_f32 v113, v113, v119\n\t"
    "v_accvgpr_write_b32 a71, v113\n\t"
    "v_accvgpr_read_b32 v112, a84\n\t"
    "v_mul_f32 v112, v112, v116\n\t"
    "v_accvgpr_write_b32 a84, v112\n\t"
    "v_accvgpr_read_b32 v113, a85\n\t"
    "v_mul_f32 v113, v113, v117\n\t"
    "v_accvgpr_write_b32 a85, v113\n\t"
    "v_accvgpr_read_b32 v112, a86\n\t"
    "v_mul_f32 v112, v112, v118\n\t"
    "v_accvgpr_write_b32 a86, v112\n\t"
    "v_accvgpr_read_b32 v113, a87\n\t"
    "v_mul_f32 v113, v113, v119\n\t"
    "v_accvgpr_write_b32 a87, v113\n\t"
    "v_accvgpr_read_b32 v112, a100\n\t"
    "v_mul_f32 v112, v112, v116\n\t"
    "v_accvgpr_write_b32 a100, v112\n\t"
    "v_accvgpr_read_b32 v113, a101\n\t"
    "v_mul_f32 v113, v113, v117\n\t"
    "v_accvgpr_write_b32 a101, v113\n\t"
    "v_accvgpr_read_b32 v112, a102\n\t"
    "v_mul_f32 v112, v112, v118\n\t"
    "v_accvgpr_write_b32 a102, v112\n\t"
    "v_accvgpr_read_b32 v113, a103\n\t"
    "v_mul_f32 v113, v113, v119\n\t"
    "v_accvgpr_write_b32 a103, v113\n\t"
    "v_accvgpr_read_b32 v112, a116\n\t"
    "v_mul_f32 v112, v112, v116\n\t"
    "v_accvgpr_write_b32 a116, v112\n\t"
    "v_accvgpr_read_b32 v113, a117\n\t"
    "v_mul_f32 v113, v113, v117\n\t"
    "v_accvgpr_write_b32 a117, v113\n\t"
    "v_accvgpr_read_b32 v112, a118\n\t"
    "v_mul_f32 v112, v112, v118\n\t"
    "v_accvgpr_write_b32 a118, v112\n\t"
    "v_accvgpr_read_b32 v113, a119\n\t"
    "v_mul_f32 v113, v113, v119\n\t"
    "v_accvgpr_write_b32 a119, v113\n\t"
    "v_accvgpr_read_b32 v112, a132\n\t"
    "v_mul_f32 v112, v112, v116\n\t"
    "v_accvgpr_write_b32 a132, v112\n\t"
    "v_accvgpr_read_b32 v113, a133\n\t"
    "v_mul_f32 v113, v113, v117\n\t"
    "v_accvgpr_write_b32 a133, v113\n\t"
    "v_accvgpr_read_b32 v112, a134\n\t"
    "v_mul_f32 v112, v112, v118\n\t"
    "v_accvgpr_write_b32 a134, v112\n\t"
    "v_accvgpr_read_b32 v113, a135\n\t"
    "v_mul_f32 v113, v113, v119\n\t"
    "v_accvgpr_write_b32 a135, v113\n\t"
    "v_accvgpr_read_b32 v112, a148\n\t"
    "v_mul_f32 v112, v112, v116\n\t"
    "v_accvgpr_write_b32 a148, v112\n\t"
    "v_accvgpr_read_b32 v113, a149\n\t"
    "v_mul_f32 v113, v113, v117\n\t"
    "v_accvgpr_write_b32 a149, v113\n\t"
    "v_accvgpr_read_b32 v112, a150\n\t"
    "v_mul_f32 v112, v112, v118\n\t"
    "v_accvgpr_write_b32 a150, v112\n\t"
    "v_accvgpr_read_b32 v113, a151\n\t"
    "v_mul_f32 v113, v113, v119\n\t"
    "v_accvgpr_write_b32 a151, v113\n\t"
    "v_accvgpr_read_b32 v112, a164\n\t"
    "v_mul_f32 v112, v112, v116\n\t"
    "v_accvgpr_write_b32 a164, v112\n\t"
    "v_accvgpr_read_b32 v113, a165\n\t"
    "v_mul_f32 v113, v113, v117\n\t"
    "v_accvgpr_write_b32 a165, v113\n\t"
    "v_accvgpr_read_b32 v112, a166\n\t"
    "v_mul_f32 v112, v112, v118\n\t"
    "v_accvgpr_write_b32 a166, v112\n\t"
    "v_accvgpr_read_b32 v113, a167\n\t"
    "v_mul_f32 v113, v113, v119\n\t"
    "v_accvgpr_write_b32 a167, v113\n\t"
    "v_accvgpr_read_b32 v112, a180\n\t"
    "v_mul_f32 v112, v112, v116\n\t"
    "v_accvgpr_write_b32 a180, v112\n\t"
    "v_accvgpr_read_b32 v113, a181\n\t"
    "v_mul_f32 v113, v113, v117\n\t"
    "v_accvgpr_write_b32 a181, v113\n\t"
    "v_accvgpr_read_b32 v112, a182\n\t"
    "v_mul_f32 v112, v112, v118\n\t"
    "v_accvgpr_write_b32 a182, v112\n\t"
    "v_accvgpr_read_b32 v113, a183\n\t"
    "v_mul_f32 v113, v113, v119\n\t"
    "v_accvgpr_write_b32 a183, v113\n\t"
    "s_nop 1\n\t"
    "s_branch L_ret_h2\n\t"
    "L_stub_h3:\n\t"
    "s_nop 7\n\t"
    "s_nop 7\n\t"
    "v_lshrrev_b32 v99, 4, v195\n\t"
    "v_and_b32 v99, 48, v99\n\t"
    "v_max3_f32 v96, v64, v65, v66\n\t"
    "v_max3_f32 v96, v96, v67, v80\n\t"
    "v_max3_f32 v96, v96, v81, v82\n\t"
    "v_max_f32 v96, v96, v83\n\t"
    "v_mov_b32 v97, v96\n\t"
    "s_nop 1\n\t"
    "v_permlane32_swap_b32 v96, v97\n\t"
    "s_nop 1\n\t"
    "v_max_f32 v96, v96, v97\n\t"
    "v_mov_b32 v97, v96\n\t"
    "s_nop 1\n\t"
    "v_permlane16_swap_b32 v96, v97\n\t"
    "s_nop 1\n\t"
    "v_max_f32 v96, v96, v97\n\t"
    "v_max_f32 v96, 0, v96\n\t"
    "v_sub_f32 v128, v128, v96\n\t"
    "v_sub_f32 v129, v129, v96\n\t"
    "v_sub_f32 v130, v130, v96\n\t"
    "v_sub_f32 v131, v131, v96\n\t"
    "v_sub_f32 v64, v64, v96\n\t"
    "v_sub_f32 v65, v65, v96\n\t"
    "v_sub_f32 v66, v66, v96\n\t"
    "v_sub_f32 v67, v67, v96\n\t"
    "v_sub_f32 v80, v80, v96\n\t"
    "v_sub_f32 v81, v81, v96\n\t"
    "v_sub_f32 v82, v82, v96\n\t"
    "v_sub_f32 v83, v83, v96\n\t"
    "v_sub_f32 v32, v32, v96\n\t"
    "v_sub_f32 v33, v33, v96\n\t"
    "v_sub_f32 v34, v34, v96\n\t"
    "v_sub_f32 v35, v35, v96\n\t"
    "v_sub_f32 v48, v48, v96\n\t"
    "v_sub_f32 v49, v49, v96\n\t"
    "v_sub_f32 v50, v50, v96\n\t"
    "v_sub_f32 v51, v51, v96\n\t"
    "v_exp_f32 v98, -v96\n\t"
    "s_nop 0\n\t"
    "v_mul_f32 v168, v168, v98\n\t"
    "ds_bpermute_b32 v100, v99, v98 offset:0\n\t"
    "ds_bpermute_b32 v101, v99, v98 offset:4\n\t"
    "ds_bpermute_b32 v102, v99, v98 offset:8\n\t"
    "ds_bpermute_b32 v103, v99, v98 offset:12\n\t"
    "v_exp_f32 v176, v64\n\t"
    "v_exp_f32 v177, v65\n\t"
    "s_nop 0\n\t"
    "v_add_f32 v172, v176, v177\n\t"
    "v_cvt_pk_bf16_f32 v112, v176, v177\n\t"
    "v_exp_f32 v178, v66\n\t"
    "v_exp_f32 v179, v67\n\t"
    "s_nop 0\n\t"
    "v_add_f32 v172, v172, v178\n\t"
    "v_add_f32 v172, v172, v179\n\t"
    "v_cvt_pk_bf16_f32 v113, v178, v179\n\t"
    "v_exp_f32 v176, v80\n\t"
    "v_exp_f32 v177, v81\n\t"
    "s_nop 0\n\t"
    "v_add_f32 v172, v172, v176\n\t"
    "v_add_f32 v172, v172, v177\n\t"
    "v_cvt_pk_bf16_f32 v114, v176, v177\n\t"
    "v_exp_f32 v178, v82\n\t"
    "v_exp_f32 v179, v83\n\t"
    "s_nop 0\n\t"
    "v_add_f32 v172, v172, v178\n\t"
    "v_add_f32 v172, v172, v179\n\t"
    "v_cvt_pk_bf16_f32 v115, v178, v179\n\t"
    "s_waitcnt lgkmcnt(0)\n\t"
    "v_accvgpr_read_b32 v96, a0\n\t"
    "v_mul_f32 v96, v96, v100\n\t"
    "v_accvgpr_write_b32 a0, v96\n\t"
    "v_accvgpr_read_b32 v97, a1\n\t"
    "v_mul_f32 v97, v97, v101\n\t"
    "v_accvgpr_write_b32 a1, v97\n\t"
    "v_accvgpr_read_b32 v96, a2\n\t"
    "v_mul_f32 v96, v96, v102\n\t"
    "v_accvgpr_write_b32 a2, v96\n\t"
    "v_accvgpr_read_b32 v97, a3\n\t"
    "v_mul_f32 v97, v97, v103\n\t"
    "v_accvgpr_write_b32 a3, v97\n\t"
    "v_accvgpr_read_b32 v96, a16\n\t"
    "v_mul_f32 v96, v96, v100\n\t"
    "v_accvgpr_write_b32 a16, v96\n\t"
    "v_accvgpr_read_b32 v97, a17\n\t"
    "v_mul_f32 v97, v97, v101\n\t"
    "v_accvgpr_write_b32 a17, v97\n\t"
    "v_accvgpr_read_b32 v96, a18\n\t"
    "v_mul_f32 v96, v96, v102\n\t"
    "v_accvgpr_write_b32 a18, v96\n\t"
    "v_accvgpr_read_b32 v97, a19\n\t"
    "v_mul_f32 v97, v97, v103\n\t"
    "v_accvgpr_write_b32 a19, v97\n\t"
    "v_accvgpr_read_b32 v96, a32\n\t"
    "v_mul_f32 v96, v96, v100\n\t"
    "v_accvgpr_write_b32 a32, v96\n\t"
    "v_accvgpr_read_b32 v97, a33\n\t"
    "v_mul_f32 v97, v97, v101\n\t"
    "v_accvgpr_write_b32 a33, v97\n\t"
    "v_accvgpr_read_b32 v96, a34\n\t"
    "v_mul_f32 v96, v96, v102\n\t"
    "v_accvgpr_write_b32 a34, v96\n\t"
    "v_accvgpr_read_b32 v97, a35\n\t"
    "v_mul_f32 v97, v97, v103\n\t"
    "v_accvgpr_write_b32 a35, v97\n\t"
    "v_accvgpr_read_b32 v96, a48\n\t"
    "v_mul_f32 v96, v96, v100\n\t"
    "v_accvgpr_write_b32 a48, v96\n\t"
    "v_accvgpr_read_b32 v97, a49\n\t"
    "v_mul_f32 v97, v97, v101\n\t"
    "v_accvgpr_write_b32 a49, v97\n\t"
    "v_accvgpr_read_b32 v96, a50\n\t"
    "v_mul_f32 v96, v96, v102\n\t"
    "v_accvgpr_write_b32 a50, v96\n\t"
    "v_accvgpr_read_b32 v97, a51\n\t"
    "v_mul_f32 v97, v97, v103\n\t"
    "v_accvgpr_write_b32 a51, v97\n\t"
    "v_accvgpr_read_b32 v96, a64\n\t"
    "v_mul_f32 v96, v96, v100\n\t"
    "v_accvgpr_write_b32 a64, v96\n\t"
    "v_accvgpr_read_b32 v97, a65\n\t"
    "v_mul_f32 v97, v97, v101\n\t"
    "v_accvgpr_write_b32 a65, v97\n\t"
    "v_accvgpr_read_b32 v96, a66\n\t"
    "v_mul_f32 v96, v96, v102\n\t"
    "v_accvgpr_write_b32 a66, v96\n\t"
    "v_accvgpr_read_b32 v97, a67\n\t"
    "v_mul_f32 v97, v97, v103\n\t"
    "v_accvgpr_write_b32 a67, v97\n\t"
    "v_accvgpr_read_b32 v96, a80\n\t"
    "v_mul_f32 v96, v96, v100\n\t"
    "v_accvgpr_write_b32 a80, v96\n\t"
    "v_accvgpr_read_b32 v97, a81\n\t"
    "v_mul_f32 v97, v97, v101\n\t"
    "v_accvgpr_write_b32 a81, v97\n\t"
    "v_accvgpr_read_b32 v96, a82\n\t"
    "v_mul_f32 v96, v96, v102\n\t"
    "v_accvgpr_write_b32 a82, v96\n\t"
    "v_accvgpr_read_b32 v97, a83\n\t"
    "v_mul_f32 v97, v97, v103\n\t"
    "v_accvgpr_write_b32 a83, v97\n\t"
    "v_accvgpr_read_b32 v96, a96\n\t"
    "v_mul_f32 v96, v96, v100\n\t"
    "v_accvgpr_write_b32 a96, v96\n\t"
    "v_accvgpr_read_b32 v97, a97\n\t"
    "v_mul_f32 v97, v97, v101\n\t"
    "v_accvgpr_write_b32 a97, v97\n\t"
    "v_accvgpr_read_b32 v96, a98\n\t"
    "v_mul_f32 v96, v96, v102\n\t"
    "v_accvgpr_write_b32 a98, v96\n\t"
    "v_accvgpr_read_b32 v97, a99\n\t"
    "v_mul_f32 v97, v97, v103\n\t"
    "v_accvgpr_write_b32 a99, v97\n\t"
    "v_accvgpr_read_b32 v96, a112\n\t"
    "v_mul_f32 v96, v96, v100\n\t"
    "v_accvgpr_write_b32 a112, v96\n\t"
    "v_accvgpr_read_b32 v97, a113\n\t"
    "v_mul_f32 v97, v97, v101\n\t"
    "v_accvgpr_write_b32 a113, v97\n\t"
    "v_accvgpr_read_b32 v96, a114\n\t"
    "v_mul_f32 v96, v96, v102\n\t"
    "v_accvgpr_write_b32 a114, v96\n\t"
    "v_accvgpr_read_b32 v97, a115\n\t"
    "v_mul_f32 v97, v97, v103\n\t"
    "v_accvgpr_write_b32 a115, v97\n\t"
    "v_accvgpr_read_b32 v96, a128\n\t"
    "v_mul_f32 v96, v96, v100\n\t"
    "v_accvgpr_write_b32 a128, v96\n\t"
    "v_accvgpr_read_b32 v97, a129\n\t"
    "v_mul_f32 v97, v97, v101\n\t"
    "v_accvgpr_write_b32 a129, v97\n\t"
    "v_accvgpr_read_b32 v96, a130\n\t"
    "v_mul_f32 v96, v96, v102\n\t"
    "v_accvgpr_write_b32 a130, v96\n\t"
    "v_accvgpr_read_b32 v97, a131\n\t"
    "v_mul_f32 v97, v97, v103\n\t"
    "v_accvgpr_write_b32 a131, v97\n\t"
    "v_accvgpr_read_b32 v96, a144\n\t"
    "v_mul_f32 v96, v96, v100\n\t"
    "v_accvgpr_write_b32 a144, v96\n\t"
    "v_accvgpr_read_b32 v97, a145\n\t"
    "v_mul_f32 v97, v97, v101\n\t"
    "v_accvgpr_write_b32 a145, v97\n\t"
    "v_accvgpr_read_b32 v96, a146\n\t"
    "v_mul_f32 v96, v96, v102\n\t"
    "v_accvgpr_write_b32 a146, v96\n\t"
    "v_accvgpr_read_b32 v97, a147\n\t"
    "v_mul_f32 v97, v97, v103\n\t"
    "v_accvgpr_write_b32 a147, v97\n\t"
    "v_accvgpr_read_b32 v96, a160\n\t"
    "v_mul_f32 v96, v96, v100\n\t"
    "v_accvgpr_write_b32 a160, v96\n\t"
    "v_accvgpr_read_b32 v97, a161\n\t"
    "v_mul_f32 v97, v97, v101\n\t"
    "v_accvgpr_write_b32 a161, v97\n\t"
    "v_accvgpr_read_b32 v96, a162\n\t"
    "v_mul_f32 v96, v96, v102\n\t"
    "v_accvgpr_write_b32 a162, v96\n\t"
    "v_accvgpr_read_b32 v97, a163\n\t"
    "v_mul_f32 v97, v97, v103\n\t"
    "v_accvgpr_write_b32 a163, v97\n\t"
    "v_accvgpr_read_b32 v96, a176\n\t"
    "v_mul_f32 v96, v96, v100\n\t"
    "v_accvgpr_write_b32 a176, v96\n\t"
    "v_accvgpr_read_b32 v97, a177\n\t"
    "v_mul_f32 v97, v97, v101\n\t"
    "v_accvgpr_write_b32 a177, v97\n\t"
    "v_accvgpr_read_b32 v96, a178\n\t"
    "v_mul_f32 v96, v96, v102\n\t"
    "v_accvgpr_write_b32 a178, v96\n\t"
    "v_accvgpr_read_b32 v97, a179\n\t"
    "v_mul_f32 v97, v97, v103\n\t"
    "v_accvgpr_write_b32 a179, v97\n\t"
    "v_max3_f32 v96, v68, v69, v70\n\t"
    "v_max3_f32 v96, v96, v71, v84\n\t"
    "v_max3_f32 v96, v96, v85, v86\n\t"
    "v_max_f32 v96, v96, v87\n\t"
    "v_mov_b32 v97, v96\n\t"
    "s_nop 1\n\t"
    "v_permlane32_swap_b32 v96, v97\n\t"
    "s_nop 1\n\t"
    "v_max_f32 v96, v96, v97\n\t"
    "v_mov_b32 v97, v96\n\t"
    "s_nop 1\n\t"
    "v_permlane16_swap_b32 v96, v97\n\t"
    "s_nop 1\n\t"
    "v_max_f32 v96, v96, v97\n\t"
    "v_max_f32 v96, 0, v96\n\t"
    "v_sub_f32 v132, v132, v96\n\t"
    "v_sub_f32 v133, v133, v96\n\t"
    "v_sub_f32 v134, v134, v96\n\t"
    "v_sub_f32 v135, v135, v96\n\t"
    "v_sub_f32 v68, v68, v96\n\t"
    "v_sub_f32 v69, v69, v96\n\t"
    "v_sub_f32 v70, v70, v96\n\t"
    "v_sub_f32 v71, v71, v96\n\t"
    "v_sub_f32 v84, v84, v96\n\t"
    "v_sub_f32 v85, v85, v96\n\t"
    "v_sub_f32 v86, v86, v96\n\t"
    "v_sub_f32 v87, v87, v96\n\t"
    "v_sub_f32 v36, v36, v96\n\t"
    "v_sub_f32 v37, v37, v96\n\t"
    "v_sub_f32 v38, v38, v96\n\t"
    "v_sub_f32 v39, v39, v96\n\t"
    "v_sub_f32 v52, v52, v96\n\t"
    "v_sub_f32 v53, v53, v96\n\t"
    "v_sub_f32 v54, v54, v96\n\t"
    "v_sub_f32 v55, v55, v96\n\t"
    "v_exp_f32 v98, -v96\n\t"
    "s_nop 0\n\t"
    "v_mul_f32 v169, v169, v98\n\t"
    "ds_bpermute_b32 v100, v99, v98 offset:0\n\t"
    "ds_bpermute_b32 v101, v99, v98 offset:4\n\t"
    "ds_bpermute_b32 v102, v99, v98 offset:8\n\t"
    "ds_bpermute_b32 v103, v99, v98 offset:12\n\t"
    "v_exp_f32 v176, v68\n\t"
    "v_exp_f32 v177, v69\n\t"
    "s_nop 0\n\t"
    "v_add_f32 v173, v176, v177\n\t"
    "v_cvt_pk_bf16_f32 v116, v176, v177\n\t"
    "v_exp_f32 v178, v70\n\t"
    "v_exp_f32 v179, v71\n\t"
    "s_nop 0\n\t"
    "v_add_f32 v173, v173, v178\n\t"
    "v_add_f32 v173, v173, v179\n\t"
    "v_cvt_pk_bf16_f32 v117, v178, v179\n\t"
    "v_exp_f32 v176, v84\n\t"
    "v_exp_f32 v177, v85\n\t"
    "s_nop 0\n\t"
    "v_add_f32 v173, v173, v176\n\t"
    "v_add_f32 v173, v173, v177\n\t"
    "v_cvt_pk_bf16_f32 v118, v176, v177\n\t"
    "v_exp_f32 v178, v86\n\t"
    "v_exp_f32 v179, v87\n\t"
    "s_nop 0\n\t"
    "v_add_f32 v173, v173, v178\n\t"
    "v_add_f32 v173, v173, v179\n\t"
    "v_cvt_pk_bf16_f32 v119, v178, v179\n\t"
    "s_waitcnt lgkmcnt(0)\n\t"
    "v_accvgpr_read_b32 v96, a4\n\t"
    "v_mul_f32 v96, v96, v100\n\t"
    "v_accvgpr_write_b32 a4, v96\n\t"
    "v_accvgpr_read_b32 v97, a5\n\t"
    "v_mul_f32 v97, v97, v101\n\t"
    "v_accvgpr_write_b32 a5, v97\n\t"
    "v_accvgpr_read_b32 v96, a6\n\t"
    "v_mul_f32 v96, v96, v102\n\t"
    "v_accvgpr_write_b32 a6, v96\n\t"
    "v_accvgpr_read_b32 v97, a7\n\t"
    "v_mul_f32 v97, v97, v103\n\t"
    "v_accvgpr_write_b32 a7, v97\n\t"
    "v_accvgpr_read_b32 v96, a20\n\t"
    "v_mul_f32 v96, v96, v100\n\t"
    "v_accvgpr_write_b32 a20, v96\n\t"
    "v_accvgpr_read_b32 v97, a21\n\t"
    "v_mul_f32 v97, v97, v101\n\t"
    "v_accvgpr_write_b32 a21, v97\n\t"
    "v_accvgpr_read_b32 v96, a22\n\t"
    "v_mul_f32 v96, v96, v102\n\t"
    "v_accvgpr_write_b32 a22, v96\n\t"
    "v_accvgpr_read_b32 v97, a23\n\t"
    "v_mul_f32 v97, v97, v103\n\t"
    "v_accvgpr_write_b32 a23, v97\n\t"
    "v_accvgpr_read_b32 v96, a36\n\t"
    "v_mul_f32 v96, v96, v100\n\t"
    "v_accvgpr_write_b32 a36, v96\n\t"
    "v_accvgpr_read_b32 v97, a37\n\t"
    "v_mul_f32 v97, v97, v101\n\t"
    "v_accvgpr_write_b32 a37, v97\n\t"
    "v_accvgpr_read_b32 v96, a38\n\t"
    "v_mul_f32 v96, v96, v102\n\t"
    "v_accvgpr_write_b32 a38, v96\n\t"
    "v_accvgpr_read_b32 v97, a39\n\t"
    "v_mul_f32 v97, v97, v103\n\t"
    "v_accvgpr_write_b32 a39, v97\n\t"
    "v_accvgpr_read_b32 v96, a52\n\t"
    "v_mul_f32 v96, v96, v100\n\t"
    "v_accvgpr_write_b32 a52, v96\n\t"
    "v_accvgpr_read_b32 v97, a53\n\t"
    "v_mul_f32 v97, v97, v101\n\t"
    "v_accvgpr_write_b32 a53, v97\n\t"
    "v_accvgpr_read_b32 v96, a54\n\t"
    "v_mul_f32 v96, v96, v102\n\t"
    "v_accvgpr_write_b32 a54, v96\n\t"
    "v_accvgpr_read_b32 v97, a55\n\t"
    "v_mul_f32 v97, v97, v103\n\t"
    "v_accvgpr_write_b32 a55, v97\n\t"
    "v_accvgpr_read_b32 v96, a68\n\t"
    "v_mul_f32 v96, v96, v100\n\t"
    "v_accvgpr_write_b32 a68, v96\n\t"
    "v_accvgpr_read_b32 v97, a69\n\t"
    "v_mul_f32 v97, v97, v101\n\t"
    "v_accvgpr_write_b32 a69, v97\n\t"
    "v_accvgpr_read_b32 v96, a70\n\t"
    "v_mul_f32 v96, v96, v102\n\t"
    "v_accvgpr_write_b32 a70, v96\n\t"
    "v_accvgpr_read_b32 v97, a71\n\t"
    "v_mul_f32 v97, v97, v103\n\t"
    "v_accvgpr_write_b32 a71, v97\n\t"
    "v_accvgpr_read_b32 v96, a84\n\t"
    "v_mul_f32 v96, v96, v100\n\t"
    "v_accvgpr_write_b32 a84, v96\n\t"
    "v_accvgpr_read_b32 v97, a85\n\t"
    "v_mul_f32 v97, v97, v101\n\t"
    "v_accvgpr_write_b32 a85, v97\n\t"
    "v_accvgpr_read_b32 v96, a86\n\t"
    "v_mul_f32 v96, v96, v102\n\t"
    "v_accvgpr_write_b32 a86, v96\n\t"
    "v_accvgpr_read_b32 v97, a87\n\t"
    "v_mul_f32 v97, v97, v103\n\t"
    "v_accvgpr_write_b32 a87, v97\n\t"
    "v_accvgpr_read_b32 v96, a100\n\t"
    "v_mul_f32 v96, v96, v100\n\t"
    "v_accvgpr_write_b32 a100, v96\n\t"
    "v_accvgpr_read_b32 v97, a101\n\t"
    "v_mul_f32 v97, v97, v101\n\t"
    "v_accvgpr_write_b32 a101, v97\n\t"
    "v_accvgpr_read_b32 v96, a102\n\t"
    "v_mul_f32 v96, v96, v102\n\t"
    "v_accvgpr_write_b32 a102, v96\n\t"
    "v_accvgpr_read_b32 v97, a103\n\t"
    "v_mul_f32 v97, v97, v103\n\t"
    "v_accvgpr_write_b32 a103, v97\n\t"
    "v_accvgpr_read_b32 v96, a116\n\t"
    "v_mul_f32 v96, v96, v100\n\t"
    "v_accvgpr_write_b32 a116, v96\n\t"
    "v_accvgpr_read_b32 v97, a117\n\t"
    "v_mul_f32 v97, v97, v101\n\t"
    "v_accvgpr_write_b32 a117, v97\n\t"
    "v_accvgpr_read_b32 v96, a118\n\t"
    "v_mul_f32 v96, v96, v102\n\t"
    "v_accvgpr_write_b32 a118, v96\n\t"
    "v_accvgpr_read_b32 v97, a119\n\t"
    "v_mul_f32 v97, v97, v103\n\t"
    "v_accvgpr_write_b32 a119, v97\n\t"
    "v_accvgpr_read_b32 v96, a132\n\t"
    "v_mul_f32 v96, v96, v100\n\t"
    "v_accvgpr_write_b32 a132, v96\n\t"
    "v_accvgpr_read_b32 v97, a133\n\t"
    "v_mul_f32 v97, v97, v101\n\t"
    "v_accvgpr_write_b32 a133, v97\n\t"
    "v_accvgpr_read_b32 v96, a134\n\t"
    "v_mul_f32 v96, v96, v102\n\t"
    "v_accvgpr_write_b32 a134, v96\n\t"
    "v_accvgpr_read_b32 v97, a135\n\t"
    "v_mul_f32 v97, v97, v103\n\t"
    "v_accvgpr_write_b32 a135, v97\n\t"
    "v_accvgpr_read_b32 v96, a148\n\t"
    "v_mul_f32 v96, v96, v100\n\t"
    "v_accvgpr_write_b32 a148, v96\n\t"
    "v_accvgpr_read_b32 v97, a149\n\t"
    "v_mul_f32 v97, v97, v101\n\t"
    "v_accvgpr_write_b32 a149, v97\n\t"
    "v_accvgpr_read_b32 v96, a150\n\t"
    "v_mul_f32 v96, v96, v102\n\t"
    "v_accvgpr_write_b32 a150, v96\n\t"
    "v_accvgpr_read_b32 v97, a151\n\t"
    "v_mul_f32 v97, v97, v103\n\t"
    "v_accvgpr_write_b32 a151, v97\n\t"
    "v_accvgpr_read_b32 v96, a164\n\t"
    "v_mul_f32 v96, v96, v100\n\t"
    "v_accvgpr_write_b32 a164, v96\n\t"
    "v_accvgpr_read_b32 v97, a165\n\t"
    "v_mul_f32 v97, v97, v101\n\t"
    "v_accvgpr_write_b32 a165, v97\n\t"
    "v_accvgpr_read_b32 v96, a166\n\t"
    "v_mul_f32 v96, v96, v102\n\t"
    "v_accvgpr_write_b32 a166, v96\n\t"
    "v_accvgpr_read_b32 v97, a167\n\t"
    "v_mul_f32 v97, v97, v103\n\t"
    "v_accvgpr_write_b32 a167, v97\n\t"
    "v_accvgpr_read_b32 v96, a180\n\t"
    "v_mul_f32 v96, v96, v100\n\t"
    "v_accvgpr_write_b32 a180, v96\n\t"
    "v_accvgpr_read_b32 v97, a181\n\t"
    "v_mul_f32 v97, v97, v101\n\t"
    "v_accvgpr_write_b32 a181, v97\n\t"
    "v_accvgpr_read_b32 v96, a182\n\t"
    "v_mul_f32 v96, v96, v102\n\t"
    "v_accvgpr_write_b32 a182, v96\n\t"
    "v_accvgpr_read_b32 v97, a183\n\t"
    "v_mul_f32 v97, v97, v103\n\t"
    "v_accvgpr_write_b32 a183, v97\n\t"
    "s_nop 1\n\t"
    "s_branch L_ret_h3\n\t"
    "L_stub_h1:\n\t"
    "s_nop 7\n\t"
    "s_nop 7\n\t"
    "v_lshrrev_b32 v107, 4, v195\n\t"
    "v_and_b32 v107, 48, v107\n\t"
    "v_max3_f32 v104, v72, v73, v74\n\t"
    "v_max3_f32 v104, v104, v75, v88\n\t"
    "v_max3_f32 v104, v104, v89, v90\n\t"
    "v_max_f32 v104, v104, v91\n\t"
    "v_mov_b32 v105, v104\n\t"
    "s_nop 1\n\t"
    "v_permlane32_swap_b32 v104, v105\n\t"
    "s_nop 1\n\t"
    "v_max_f32 v104, v104, v105\n\t"
    "v_mov_b32 v105, v104\n\t"
    "s_nop 1\n\t"
    "v_permlane16_swap_b32 v104, v105\n\t"
    "s_nop 1\n\t"
    "v_max_f32 v104, v104, v105\n\t"
    "v_max_f32 v104, 0, v104\n\t"
    "v_sub_f32 v136, v136, v104\n\t"
    "v_sub_f32 v137, v137, v104\n\t"
    "v_sub_f32 v138, v138, v104\n\t"
    "v_sub_f32 v139, v139, v104\n\t"
    "v_sub_f32 v72, v72, v104\n\t"
    "v_sub_f32 v73, v73, v104\n\t"
    "v_sub_f32 v74, v74, v104\n\t"
    "v_sub_f32 v75, v75, v104\n\t"
    "v_sub_f32 v88, v88, v104\n\t"
    "v_sub_f32 v89, v89, v104\n\t"
    "v_sub_f32 v90, v90, v104\n\t"
    "v_sub_f32 v91, v91, v104\n\t"
    "v_sub_f32 v40, v40, v104\n\t"
    "v_sub_f32 v41, v41, v104\n\t"
    "v_sub_f32 v42, v42, v104\n\t"
    "v_sub_f32 v43, v43, v104\n\t"
    "v_sub_f32 v56, v56, v104\n\t"
    "v_sub_f32 v57, v57, v104\n\t"
    "v_sub_f32 v58, v58, v104\n\t"
    "v_sub_f32 v59, v59, v104\n\t"
    "v_exp_f32 v106, -v104\n\t"
    "s_nop 0\n\t"
    "v_mul_f32 v170, v170, v106\n\t"
    "ds_bpermute_b32 v108, v107, v106 offset:0\n\t"
    "ds_bpermute_b32 v109, v107, v106 offset:4\n\t"
    "ds_bpermute_b32 v110, v107, v106 offset:8\n\t"
    "ds_bpermute_b32 v111, v107, v106 offset:12\n\t"
    "v_exp_f32 v176, v72\n\t"
    "v_exp_f32 v177, v73\n\t"
    "s_nop 0\n\t"
    "v_add_f32 v174, v176, v177\n\t"
    "v_cvt_pk_bf16_f32 v120, v176, v177\n\t"
    "v_exp_f32 v178, v74\n\t"
    "v_exp_f32 v179, v75\n\t"
    "s_nop 0\n\t"
    "v_add_f32 v174, v174, v178\n\t"
    "v_add_f32 v174, v174, v179\n\t"
    "v_cvt_pk_bf16_f32 v121, v178, v179\n\t"
    "v_exp_f32 v176, v88\n\t"
    "v_exp_f32 v177, v89\n\t"
    "s_nop 0\n\t"
    "v_add_f32 v174, v174, v176\n\t"
    "v_add_f32 v174, v174, v177\n\t"
    "v_cvt_pk_bf16_f32 v122, v176, v177\n\t"
    "v_exp_f32 v178, v90\n\t"
    "v_exp_f32 v179, v91\n\t"
    "s_nop 0\n\t"
    "v_add_f32 v174, v174, v178\n\t"
    "v_add_f32 v174, v174, v179\n\t"
    "v_cvt_pk_bf16_f32 v123, v178, v179\n\t"
    "s_waitcnt lgkmcnt(0)\n\t"
    "v_accvgpr_read_b32 v104, a8\n\t"
    "v_mul_f32 v104, v104, v108\n\t"
    "v_accvgpr_write_b32 a8, v104\n\t"
    "v_accvgpr_read_b32 v105, a9\n\t"
    "v_mul_f32 v105, v105, v109\n\t"
    "v_accvgpr_write_b32 a9, v105\n\t"
    "v_accvgpr_read_b32 v104, a10\n\t"
    "v_mul_f32 v104, v104, v110\n\t"
    "v_accvgpr_write_b32 a10, v104\n\t"
    "v_accvgpr_read_b32 v105, a11\n\t"
    "v_mul_f32 v105, v105, v111\n\t"
    "v_accvgpr_write_b32 a11, v105\n\t"
    "v_accvgpr_read_b32 v104, a24\n\t"
    "v_mul_f32 v104, v104, v108\n\t"
    "v_accvgpr_write_b32 a24, v104\n\t"
    "v_accvgpr_read_b32 v105, a25\n\t"
    "v_mul_f32 v105, v105, v109\n\t"
    "v_accvgpr_write_b32 a25, v105\n\t"
    "v_accvgpr_read_b32 v104, a26\n\t"
    "v_mul_f32 v104, v104, v110\n\t"
    "v_accvgpr_write_b32 a26, v104\n\t"
    "v_accvgpr_read_b32 v105, a27\n\t"
    "v_mul_f32 v105, v105, v111\n\t"
    "v_accvgpr_write_b32 a27, v105\n\t"
    "v_accvgpr_read_b32 v104, a40\n\t"
    "v_mul_f32 v104, v104, v108\n\t"
    "v_accvgpr_write_b32 a40, v104\n\t"
    "v_accvgpr_read_b32 v105, a41\n\t"
    "v_mul_f32 v105, v105, v109\n\t"
    "v_accvgpr_write_b32 a41, v105\n\t"
    "v_accvgpr_read_b32 v104, a42\n\t"
    "v_mul_f32 v104, v104, v110\n\t"
    "v_accvgpr_write_b32 a42, v104\n\t"
    "v_accvgpr_read_b32 v105, a43\n\t"
    "v_mul_f32 v105, v105, v111\n\t"
    "v_accvgpr_write_b32 a43, v105\n\t"
    "v_accvgpr_read_b32 v104, a56\n\t"
    "v_mul_f32 v104, v104, v108\n\t"
    "v_accvgpr_write_b32 a56, v104\n\t"
    "v_accvgpr_read_b32 v105, a57\n\t"
    "v_mul_f32 v105, v105, v109\n\t"
    "v_accvgpr_write_b32 a57, v105\n\t"
    "v_accvgpr_read_b32 v104, a58\n\t"
    "v_mul_f32 v104, v104, v110\n\t"
    "v_accvgpr_write_b32 a58, v104\n\t"
    "v_accvgpr_read_b32 v105, a59\n\t"
    "v_mul_f32 v105, v105, v111\n\t"
    "v_accvgpr_write_b32 a59, v105\n\t"
    "v_accvgpr_read_b32 v104, a72\n\t"
    "v_mul_f32 v104, v104, v108\n\t"
    "v_accvgpr_write_b32 a72, v104\n\t"
    "v_accvgpr_read_b32 v105, a73\n\t"
    "v_mul_f32 v105, v105, v109\n\t"
    "v_accvgpr_write_b32 a73, v105\n\t"
    "v_accvgpr_read_b32 v104, a74\n\t"
    "v_mul_f32 v104, v104, v110\n\t"
    "v_accvgpr_write_b32 a74, v104\n\t"
    "v_accvgpr_read_b32 v105, a75\n\t"
    "v_mul_f32 v105, v105, v111\n\t"
    "v_accvgpr_write_b32 a75, v105\n\t"
    "v_accvgpr_read_b32 v104, a88\n\t"
    "v_mul_f32 v104, v104, v108\n\t"
    "v_accvgpr_write_b32 a88, v104\n\t"
    "v_accvgpr_read_b32 v105, a89\n\t"
    "v_mul_f32 v105, v105, v109\n\t"
    "v_accvgpr_write_b32 a89, v105\n\t"
    "v_accvgpr_read_b32 v104, a90\n\t"
    "v_mul_f32 v104, v104, v110\n\t"
    "v_accvgpr_write_b32 a90, v104\n\t"
    "v_accvgpr_read_b32 v105, a91\n\t"
    "v_mul_f32 v105, v105, v111\n\t"
    "v_accvgpr_write_b32 a91, v105\n\t"
    "v_accvgpr_read_b32 v104, a104\n\t"
    "v_mul_f32 v104, v104, v108\n\t"
    "v_accvgpr_write_b32 a104, v104\n\t"
    "v_accvgpr_read_b32 v105, a105\n\t"
    "v_mul_f32 v105, v105, v109\n\t"
    "v_accvgpr_write_b32 a105, v105\n\t"
    "v_accvgpr_read_b32 v104, a106\n\t"
    "v_mul_f32 v104, v104, v110\n\t"
    "v_accvgpr_write_b32 a106, v104\n\t"
    "v_accvgpr_read_b32 v105, a107\n\t"
    "v_mul_f32 v105, v105, v111\n\t"
    "v_accvgpr_write_b32 a107, v105\n\t"
    "v_accvgpr_read_b32 v104, a120\n\t"
    "v_mul_f32 v104, v104, v108\n\t"
    "v_accvgpr_write_b32 a120, v104\n\t"
    "v_accvgpr_read_b32 v105, a121\n\t"
    "v_mul_f32 v105, v105, v109\n\t"
    "v_accvgpr_write_b32 a121, v105\n\t"
    "v_accvgpr_read_b32 v104, a122\n\t"
    "v_mul_f32 v104, v104, v110\n\t"
    "v_accvgpr_write_b32 a122, v104\n\t"
    "v_accvgpr_read_b32 v105, a123\n\t"
    "v_mul_f32 v105, v105, v111\n\t"
    "v_accvgpr_write_b32 a123, v105\n\t"
    "v_accvgpr_read_b32 v104, a136\n\t"
    "v_mul_f32 v104, v104, v108\n\t"
    "v_accvgpr_write_b32 a136, v104\n\t"
    "v_accvgpr_read_b32 v105, a137\n\t"
    "v_mul_f32 v105, v105, v109\n\t"
    "v_accvgpr_write_b32 a137, v105\n\t"
    "v_accvgpr_read_b32 v104, a138\n\t"
    "v_mul_f32 v104, v104, v110\n\t"
    "v_accvgpr_write_b32 a138, v104\n\t"
    "v_accvgpr_read_b32 v105, a139\n\t"
    "v_mul_f32 v105, v105, v111\n\t"
    "v_accvgpr_write_b32 a139, v105\n\t"
    "v_accvgpr_read_b32 v104, a152\n\t"
    "v_mul_f32 v104, v104, v108\n\t"
    "v_accvgpr_write_b32 a152, v104\n\t"
    "v_accvgpr_read_b32 v105, a153\n\t"
    "v_mul_f32 v105, v105, v109\n\t"
    "v_accvgpr_write_b32 a153, v105\n\t"
    "v_accvgpr_read_b32 v104, a154\n\t"
    "v_mul_f32 v104, v104, v110\n\t"
    "v_accvgpr_write_b32 a154, v104\n\t"
    "v_accvgpr_read_b32 v105, a155\n\t"
    "v_mul_f32 v105, v105, v111\n\t"
    "v_accvgpr_write_b32 a155, v105\n\t"
    "v_accvgpr_read_b32 v104, a168\n\t"
    "v_mul_f32 v104, v104, v108\n\t"
    "v_accvgpr_write_b32 a168, v104\n\t"
    "v_accvgpr_read_b32 v105, a169\n\t"
    "v_mul_f32 v105, v105, v109\n\t"
    "v_accvgpr_write_b32 a169, v105\n\t"
    "v_accvgpr_read_b32 v104, a170\n\t"
    "v_mul_f32 v104, v104, v110\n\t"
    "v_accvgpr_write_b32 a170, v104\n\t"
    "v_accvgpr_read_b32 v105, a171\n\t"
    "v_mul_f32 v105, v105, v111\n\t"
    "v_accvgpr_write_b32 a171, v105\n\t"
    "v_accvgpr_read_b32 v104, a184\n\t"
    "v_mul_f32 v104, v104, v108\n\t"
    "v_accvgpr_write_b32 a184, v104\n\t"
    "v_accvgpr_read_b32 v105, a185\n\t"
    "v_mul_f32 v105, v105, v109\n\t"
    "v_accvgpr_write_b32 a185, v105\n\t"
    "v_accvgpr_read_b32 v104, a186\n\t"
    "v_mul_f32 v104, v104, v110\n\t"
    "v_accvgpr_write_b32 a186, v104\n\t"
    "v_accvgpr_read_b32 v105, a187\n\t"
    "v_mul_f32 v105, v105, v111\n\t"
    "v_accvgpr_write_b32 a187, v105\n\t"
    "v_max3_f32 v104, v76, v77, v78\n\t"
    "v_max3_f32 v104, v104, v79, v92\n\t"
    "v_max3_f32 v104, v104, v93, v94\n\t"
    "v_max_f32 v104, v104, v95\n\t"
    "v_mov_b32 v105, v104\n\t"
    "s_nop 1\n\t"
    "v_permlane32_swap_b32 v104, v105\n\t"
    "s_nop 1\n\t"
    "v_max_f32 v104, v104, v105\n\t"
    "v_mov_b32 v105, v104\n\t"
    "s_nop 1\n\t"
    "v_permlane16_swap_b32 v104, v105\n\t"
    "s_nop 1\n\t"
    "v_max_f32 v104, v104, v105\n\t"
    "v_max_f32 v104, 0, v104\n\t"
    "v_sub_f32 v140, v140, v104\n\t"
    "v_sub_f32 v141, v141, v104\n\t"
    "v_sub_f32 v142, v142, v104\n\t"
    "v_sub_f32 v143, v143, v104\n\t"
    "v_sub_f32 v76, v76, v104\n\t"
    "v_sub_f32 v77, v77, v104\n\t"
    "v_sub_f32 v78, v78, v104\n\t"
    "v_sub_f32 v79, v79, v104\n\t"
    "v_sub_f32 v92, v92, v104\n\t"
    "v_sub_f32 v93, v93, v104\n\t"
    "v_sub_f32 v94, v94, v104\n\t"
    "v_sub_f32 v95, v95, v104\n\t"
    "v_sub_f32 v44, v44, v104\n\t"
    "v_sub_f32 v45, v45, v104\n\t"
    "v_sub_f32 v46, v46, v104\n\t"
    "v_sub_f32 v47, v47, v104\n\t"
    "v_sub_f32 v60, v60, v104\n\t"
    "v_sub_f32 v61, v61, v104\n\t"
    "v_sub_f32 v62, v62, v104\n\t"
    "v_sub_f32 v63, v63, v104\n\t"
    "v_exp_f32 v106, -v104\n\t"
    "s_nop 0\n\t"
    "v_mul_f32 v171, v171, v106\n\t"
    "ds_bpermute_b32 v108, v107, v106 offset:0\n\t"
    "ds_bpermute_b32 v109, v107, v106 offset:4\n\t"
    "ds_bpermute_b32 v110, v107, v106 offset:8\n\t"
    "ds_bpermute_b32 v111, v107, v106 offset:12\n\t"
    "v_exp_f32 v176, v76\n\t"
    "v_exp_f32 v177, v77\n\t"
    "s_nop 0\n\t"
    "v_add_f32 v175, v176, v177\n\t"
    "v_cvt_pk_bf16_f32 v124, v176, v177\n\t"
    "v_exp_f32 v178, v78\n\t"
    "v_exp_f32 v179, v79\n\t"
    "s_nop 0\n\t"
    "v_add_f32 v175, v175, v178\n\t"
    "v_add_f32 v175, v175, v179\n\t"
    "v_cvt_pk_bf16_f32 v125, v178, v179\n\t"
    "v_exp_f32 v176, v92\n\t"
    "v_exp_f32 v177, v93\n\t"
    "s_nop 0\n\t"
    "v_add_f32 v175, v175, v176\n\t"
    "v_add_f32 v175, v175, v177\n\t"
    "v_cvt_pk_bf16_f32 v126, v176, v177\n\t"
    "v_exp_f32 v178, v94\n\t"
    "v_exp_f32 v179, v95\n\t"
    "s_nop 0\n\t"
    "v_add_f32 v175, v175, v178\n\t"
    "v_add_f32 v175, v175, v179\n\t"
    "v_cvt_pk_bf16_f32 v127, v178, v179\n\t"
    "s_waitcnt lgkmcnt(0)\n\t"
    "v_accvgpr_read_b32 v104, a12\n\t"
    "v_mul_f32 v104, v104, v108\n\t"
    "v_accvgpr_write_b32 a12, v104\n\t"
    "v_accvgpr_read_b32 v105, a13\n\t"
    "v_mul_f32 v105, v105, v109\n\t"
    "v_accvgpr_write_b32 a13, v105\n\t"
    "v_accvgpr_read_b32 v104, a14\n\t"
    "v_mul_f32 v104, v104, v110\n\t"
    "v_accvgpr_write_b32 a14, v104\n\t"
    "v_accvgpr_read_b32 v105, a15\n\t"
    "v_mul_f32 v105, v105, v111\n\t"
    "v_accvgpr_write_b32 a15, v105\n\t"
    "v_accvgpr_read_b32 v104, a28\n\t"
    "v_mul_f32 v104, v104, v108\n\t"
    "v_accvgpr_write_b32 a28, v104\n\t"
    "v_accvgpr_read_b32 v105, a29\n\t"
    "v_mul_f32 v105, v105, v109\n\t"
    "v_accvgpr_write_b32 a29, v105\n\t"
    "v_accvgpr_read_b32 v104, a30\n\t"
    "v_mul_f32 v104, v104, v110\n\t"
    "v_accvgpr_write_b32 a30, v104\n\t"
    "v_accvgpr_read_b32 v105, a31\n\t"
    "v_mul_f32 v105, v105, v111\n\t"
    "v_accvgpr_write_b32 a31, v105\n\t"
    "v_accvgpr_read_b32 v104, a44\n\t"
    "v_mul_f32 v104, v104, v108\n\t"
    "v_accvgpr_write_b32 a44, v104\n\t"
    "v_accvgpr_read_b32 v105, a45\n\t"
    "v_mul_f32 v105, v105, v109\n\t"
    "v_accvgpr_write_b32 a45, v105\n\t"
    "v_accvgpr_read_b32 v104, a46\n\t"
    "v_mul_f32 v104, v104, v110\n\t"
    "v_accvgpr_write_b32 a46, v104\n\t"
    "v_accvgpr_read_b32 v105, a47\n\t"
    "v_mul_f32 v105, v105, v111\n\t"
    "v_accvgpr_write_b32 a47, v105\n\t"
    "v_accvgpr_read_b32 v104, a60\n\t"
    "v_mul_f32 v104, v104, v108\n\t"
    "v_accvgpr_write_b32 a60, v104\n\t"
    "v_accvgpr_read_b32 v105, a61\n\t"
    "v_mul_f32 v105, v105, v109\n\t"
    "v_accvgpr_write_b32 a61, v105\n\t"
    "v_accvgpr_read_b32 v104, a62\n\t"
    "v_mul_f32 v104, v104, v110\n\t"
    "v_accvgpr_write_b32 a62, v104\n\t"
    "v_accvgpr_read_b32 v105, a63\n\t"
    "v_mul_f32 v105, v105, v111\n\t"
    "v_accvgpr_write_b32 a63, v105\n\t"
    "v_accvgpr_read_b32 v104, a76\n\t"
    "v_mul_f32 v104, v104, v108\n\t"
    "v_accvgpr_write_b32 a76, v104\n\t"
    "v_accvgpr_read_b32 v105, a77\n\t"
    "v_mul_f32 v105, v105, v109\n\t"
    "v_accvgpr_write_b32 a77, v105\n\t"
    "v_accvgpr_read_b32 v104, a78\n\t"
    "v_mul_f32 v104, v104, v110\n\t"
    "v_accvgpr_write_b32 a78, v104\n\t"
    "v_accvgpr_read_b32 v105, a79\n\t"
    "v_mul_f32 v105, v105, v111\n\t"
    "v_accvgpr_write_b32 a79, v105\n\t"
    "v_accvgpr_read_b32 v104, a92\n\t"
    "v_mul_f32 v104, v104, v108\n\t"
    "v_accvgpr_write_b32 a92, v104\n\t"
    "v_accvgpr_read_b32 v105, a93\n\t"
    "v_mul_f32 v105, v105, v109\n\t"
    "v_accvgpr_write_b32 a93, v105\n\t"
    "v_accvgpr_read_b32 v104, a94\n\t"
    "v_mul_f32 v104, v104, v110\n\t"
    "v_accvgpr_write_b32 a94, v104\n\t"
    "v_accvgpr_read_b32 v105, a95\n\t"
    "v_mul_f32 v105, v105, v111\n\t"
    "v_accvgpr_write_b32 a95, v105\n\t"
    "v_accvgpr_read_b32 v104, a108\n\t"
    "v_mul_f32 v104, v104, v108\n\t"
    "v_accvgpr_write_b32 a108, v104\n\t"
    "v_accvgpr_read_b32 v105, a109\n\t"
    "v_mul_f32 v105, v105, v109\n\t"
    "v_accvgpr_write_b32 a109, v105\n\t"
    "v_accvgpr_read_b32 v104, a110\n\t"
    "v_mul_f32 v104, v104, v110\n\t"
    "v_accvgpr_write_b32 a110, v104\n\t"
    "v_accvgpr_read_b32 v105, a111\n\t"
    "v_mul_f32 v105, v105, v111\n\t"
    "v_accvgpr_write_b32 a111, v105\n\t"
    "v_accvgpr_read_b32 v104, a124\n\t"
    "v_mul_f32 v104, v104, v108\n\t"
    "v_accvgpr_write_b32 a124, v104\n\t"
    "v_accvgpr_read_b32 v105, a125\n\t"
    "v_mul_f32 v105, v105, v109\n\t"
    "v_accvgpr_write_b32 a125, v105\n\t"
    "v_accvgpr_read_b32 v104, a126\n\t"
    "v_mul_f32 v104, v104, v110\n\t"
    "v_accvgpr_write_b32 a126, v104\n\t"
    "v_accvgpr_read_b32 v105, a127\n\t"
    "v_mul_f32 v105, v105, v111\n\t"
    "v_accvgpr_write_b32 a127, v105\n\t"
    "v_accvgpr_read_b32 v104, a140\n\t"
    "v_mul_f32 v104, v104, v108\n\t"
    "v_accvgpr_write_b32 a140, v104\n\t"
    "v_accvgpr_read_b32 v105, a141\n\t"
    "v_mul_f32 v105, v105, v109\n\t"
    "v_accvgpr_write_b32 a141, v105\n\t"
    "v_accvgpr_read_b32 v104, a142\n\t"
    "v_mul_f32 v104, v104, v110\n\t"
    "v_accvgpr_write_b32 a142, v104\n\t"
    "v_accvgpr_read_b32 v105, a143\n\t"
    "v_mul_f32 v105, v105, v111\n\t"
    "v_accvgpr_write_b32 a143, v105\n\t"
    "v_accvgpr_read_b32 v104, a156\n\t"
    "v_mul_f32 v104, v104, v108\n\t"
    "v_accvgpr_write_b32 a156, v104\n\t"
    "v_accvgpr_read_b32 v105, a157\n\t"
    "v_mul_f32 v105, v105, v109\n\t"
    "v_accvgpr_write_b32 a157, v105\n\t"
    "v_accvgpr_read_b32 v104, a158\n\t"
    "v_mul_f32 v104, v104, v110\n\t"
    "v_accvgpr_write_b32 a158, v104\n\t"
    "v_accvgpr_read_b32 v105, a159\n\t"
    "v_mul_f32 v105, v105, v111\n\t"
    "v_accvgpr_write_b32 a159, v105\n\t"
    "v_accvgpr_read_b32 v104, a172\n\t"
    "v_mul_f32 v104, v104, v108\n\t"
    "v_accvgpr_write_b32 a172, v104\n\t"
    "v_accvgpr_read_b32 v105, a173\n\t"
    "v_mul_f32 v105, v105, v109\n\t"
    "v_accvgpr_write_b32 a173, v105\n\t"
    "v_accvgpr_read_b32 v104, a174\n\t"
    "v_mul_f32 v104, v104, v110\n\t"
    "v_accvgpr_write_b32 a174, v104\n\t"
    "v_accvgpr_read_b32 v105, a175\n\t"
    "v_mul_f32 v105, v105, v111\n\t"
    "v_accvgpr_write_b32 a175, v105\n\t"
    "v_accvgpr_read_b32 v104, a188\n\t"
    "v_mul_f32 v104, v104, v108\n\t"
    "v_accvgpr_write_b32 a188, v104\n\t"
    "v_accvgpr_read_b32 v105, a189\n\t"
    "v_mul_f32 v105, v105, v109\n\t"
    "v_accvgpr_write_b32 a189, v105\n\t"
    "v_accvgpr_read_b32 v104, a190\n\t"
    "v_mul_f32 v104, v104, v110\n\t"
    "v_accvgpr_write_b32 a190, v104\n\t"
    "v_accvgpr_read_b32 v105, a191\n\t"
    "v_mul_f32 v105, v105, v111\n\t"
    "v_accvgpr_write_b32 a191, v105\n\t"
    "s_nop 1\n\t"
    "s_branch L_ret_h1\n\t"
    "L_stub_h4:\n\t"
    "s_nop 7\n\t"
    "s_nop 7\n\t"
    "v_lshrrev_b32 v123, 4, v195\n\t"
    "v_and_b32 v123, 48, v123\n\t"
    "v_max3_f32 v120, v40, v41, v42\n\t"
    "v_max3_f32 v120, v120, v43, v56\n\t"
    "v_max3_f32 v120, v120, v57, v58\n\t"
    "v_max_f32 v120, v120, v59\n\t"
    "v_mov_b32 v121, v120\n\t"
    "s_nop 1\n\t"
    "v_permlane32_swap_b32 v120, v121\n\t"
    "s_nop 1\n\t"
    "v_max_f32 v120, v120, v121\n\t"
    "v_mov_b32 v121, v120\n\t"
    "s_nop 1\n\t"
    "v_permlane16_swap_b32 v120, v121\n\t"
    "s_nop 1\n\t"
    "v_max_f32 v120, v120, v121\n\t"
    "v_max_f32 v120, 0, v120\n\t"
    "v_sub_f32 v136, v136, v120\n\t"
    "v_sub_f32 v137, v137, v120\n\t"
    "v_sub_f32 v138, v138, v120\n\t"
    "v_sub_f32 v139, v139, v120\n\t"
    "v_sub_f32 v40, v40, v120\n\t"
    "v_sub_f32 v41, v41, v120\n\t"
    "v_sub_f32 v42, v42, v120\n\t"
    "v_sub_f32 v43, v43, v120\n\t"
    "v_sub_f32 v56, v56, v120\n\t"
    "v_sub_f32 v57, v57, v120\n\t"
    "v_sub_f32 v58, v58, v120\n\t"
    "v_sub_f32 v59, v59, v120\n\t"
    "v_sub_f32 v72, v72, v120\n\t"
    "v_sub_f32 v73, v73, v120\n\t"
    "v_sub_f32 v74, v74, v120\n\t"
    "v_sub_f32 v75, v75, v120\n\t"
    "v_sub_f32 v88, v88, v120\n\t"
    "v_sub_f32 v89, v89, v120\n\t"
    "v_sub_f32 v90, v90, v120\n\t"
    "v_sub_f32 v91, v91, v120\n\t"
    "v_exp_f32 v122, -v120\n\t"
    "s_nop 0\n\t"
    "v_mul_f32 v170, v170, v122\n\t"
    "ds_bpermute_b32 v124, v123, v122 offset:0\n\t"
    "ds_bpermute_b32 v125, v123, v122 offset:4\n\t"
    "ds_bpermute_b32 v126, v123, v122 offset:8\n\t"
    "ds_bpermute_b32 v127, v123, v122 offset:12\n\t"
    "v_exp_f32 v176, v40\n\t"
    "v_exp_f32 v177, v41\n\t"
    "s_nop 0\n\t"
    "v_add_f32 v174, v176, v177\n\t"
    "v_cvt_pk_bf16_f32 v104, v176, v177\n\t"
    "v_exp_f32 v178, v42\n\t"
    "v_exp_f32 v179, v43\n\t"
    "s_nop 0\n\t"
    "v_add_f32 v174, v174, v178\n\t"
    "v_add_f32 v174, v174, v179\n\t"
    "v_cvt_pk_bf16_f32 v105, v178, v179\n\t"
    "v_exp_f32 v176, v56\n\t"
    "v_exp_f32 v177, v57\n\t"
    "s_nop 0\n\t"
    "v_add_f32 v174, v174, v176\n\t"
    "v_add_f32 v174, v174, v177\n\t"
    "v_cvt_pk_bf16_f32 v106, v176, v177\n\t"
    "v_exp_f32 v178, v58\n\t"
    "v_exp_f32 v179, v59\n\t"
    "s_nop 0\n\t"
    "v_add_f32 v174, v174, v178\n\t"
    "v_add_f32 v174, v174, v179\n\t"
    "v_cvt_pk_bf16_f32 v107, v178, v179\n\t"
    "s_waitcnt lgkmcnt(0)\n\t"
    "v_accvgpr_read_b32 v120, a8\n\t"
    "v_mul_f32 v120, v120, v124\n\t"
    "v_accvgpr_write_b32 a8, v120\n\t"
    "v_accvgpr_read_b32 v121, a9\n\t"
    "v_mul_f32 v121, v121, v125\n\t"
    "v_accvgpr_write_b32 a9, v121\n\t"
    "v_accvgpr_read_b32 v120, a10\n\t"
    "v_mul_f32 v120, v120, v126\n\t"
    "v_accvgpr_write_b32 a10, v120\n\t"
    "v_accvgpr_read_b32 v121, a11\n\t"
    "v_mul_f32 v121, v121, v127\n\t"
    "v_accvgpr_write_b32 a11, v121\n\t"
    "v_accvgpr_read_b32 v120, a24\n\t"
    "v_mul_f32 v120, v120, v124\n\t"
    "v_accvgpr_write_b32 a24, v120\n\t"
    "v_accvgpr_read_b32 v121, a25\n\t"
    "v_mul_f32 v121, v121, v125\n\t"
    "v_accvgpr_write_b32 a25, v121\n\t"
    "v_accvgpr_read_b32 v120, a26\n\t"
    "v_mul_f32 v120, v120, v126\n\t"
    "v_accvgpr_write_b32 a26, v120\n\t"
    "v_accvgpr_read_b32 v121, a27\n\t"
    "v_mul_f32 v121, v121, v127\n\t"
    "v_accvgpr_write_b32 a27, v121\n\t"
    "v_accvgpr_read_b32 v120, a40\n\t"
    "v_mul_f32 v120, v120, v124\n\t"
    "v_accvgpr_write_b32 a40, v120\n\t"
    "v_accvgpr_read_b32 v121, a41\n\t"
    "v_mul_f32 v121, v121, v125\n\t"
    "v_accvgpr_write_b32 a41, v121\n\t"
    "v_accvgpr_read_b32 v120, a42\n\t"
    "v_mul_f32 v120, v120, v126\n\t"
    "v_accvgpr_write_b32 a42, v120\n\t"
    "v_accvgpr_read_b32 v121, a43\n\t"
    "v_mul_f32 v121, v121, v127\n\t"
    "v_accvgpr_write_b32 a43, v121\n\t"
    "v_accvgpr_read_b32 v120, a56\n\t"
    "v_mul_f32 v120, v120, v124\n\t"
    "v_accvgpr_write_b32 a56, v120\n\t"
    "v_accvgpr_read_b32 v121, a57\n\t"
    "v_mul_f32 v121, v121, v125\n\t"
    "v_accvgpr_write_b32 a57, v121\n\t"
    "v_accvgpr_read_b32 v120, a58\n\t"
    "v_mul_f32 v120, v120, v126\n\t"
    "v_accvgpr_write_b32 a58, v120\n\t"
    "v_accvgpr_read_b32 v121, a59\n\t"
    "v_mul_f32 v121, v121, v127\n\t"
    "v_accvgpr_write_b32 a59, v121\n\t"
    "v_accvgpr_read_b32 v120, a72\n\t"
    "v_mul_f32 v120, v120, v124\n\t"
    "v_accvgpr_write_b32 a72, v120\n\t"
    "v_accvgpr_read_b32 v121, a73\n\t"
    "v_mul_f32 v121, v121, v125\n\t"
    "v_accvgpr_write_b32 a73, v121\n\t"
    "v_accvgpr_read_b32 v120, a74\n\t"
    "v_mul_f32 v120, v120, v126\n\t"
    "v_accvgpr_write_b32 a74, v120\n\t"
    "v_accvgpr_read_b32 v121, a75\n\t"
    "v_mul_f32 v121, v121, v127\n\t"
    "v_accvgpr_write_b32 a75, v121\n\t"
    "v_accvgpr_read_b32 v120, a88\n\t"
    "v_mul_f32 v120, v120, v124\n\t"
    "v_accvgpr_write_b32 a88, v120\n\t"
    "v_accvgpr_read_b32 v121, a89\n\t"
    "v_mul_f32 v121, v121, v125\n\t"
    "v_accvgpr_write_b32 a89, v121\n\t"
    "v_accvgpr_read_b32 v120, a90\n\t"
    "v_mul_f32 v120, v120, v126\n\t"
    "v_accvgpr_write_b32 a90, v120\n\t"
    "v_accvgpr_read_b32 v121, a91\n\t"
    "v_mul_f32 v121, v121, v127\n\t"
    "v_accvgpr_write_b32 a91, v121\n\t"
    "v_accvgpr_read_b32 v120, a104\n\t"
    "v_mul_f32 v120, v120, v124\n\t"
    "v_accvgpr_write_b32 a104, v120\n\t"
    "v_accvgpr_read_b32 v121, a105\n\t"
    "v_mul_f32 v121, v121, v125\n\t"
    "v_accvgpr_write_b32 a105, v121\n\t"
    "v_accvgpr_read_b32 v120, a106\n\t"
    "v_mul_f32 v120, v120, v126\n\t"
    "v_accvgpr_write_b32 a106, v120\n\t"
    "v_accvgpr_read_b32 v121, a107\n\t"
    "v_mul_f32 v121, v121, v127\n\t"
    "v_accvgpr_write_b32 a107, v121\n\t"
    "v_accvgpr_read_b32 v120, a120\n\t"
    "v_mul_f32 v120, v120, v124\n\t"
    "v_accvgpr_write_b32 a120, v120\n\t"
    "v_accvgpr_read_b32 v121, a121\n\t"
    "v_mul_f32 v121, v121, v125\n\t"
    "v_accvgpr_write_b32 a121, v121\n\t"
    "v_accvgpr_read_b32 v120, a122\n\t"
    "v_mul_f32 v120, v120, v126\n\t"
    "v_accvgpr_write_b32 a122, v120\n\t"
    "v_accvgpr_read_b32 v121, a123\n\t"
    "v_mul_f32 v121, v121, v127\n\t"
    "v_accvgpr_write_b32 a123, v121\n\t"
    "v_accvgpr_read_b32 v120, a136\n\t"
    "v_mul_f32 v120, v120, v124\n\t"
    "v_accvgpr_write_b32 a136, v120\n\t"
    "v_accvgpr_read_b32 v121, a137\n\t"
    "v_mul_f32 v121, v121, v125\n\t"
    "v_accvgpr_write_b32 a137, v121\n\t"
    "v_accvgpr_read_b32 v120, a138\n\t"
    "v_mul_f32 v120, v120, v126\n\t"
    "v_accvgpr_write_b32 a138, v120\n\t"
    "v_accvgpr_read_b32 v121, a139\n\t"
    "v_mul_f32 v121, v121, v127\n\t"
    "v_accvgpr_write_b32 a139, v121\n\t"
    "v_accvgpr_read_b32 v120, a152\n\t"
    "v_mul_f32 v120, v120, v124\n\t"
    "v_accvgpr_write_b32 a152, v120\n\t"
    "v_accvgpr_read_b32 v121, a153\n\t"
    "v_mul_f32 v121, v121, v125\n\t"
    "v_accvgpr_write_b32 a153, v121\n\t"
    "v_accvgpr_read_b32 v120, a154\n\t"
    "v_mul_f32 v120, v120, v126\n\t"
    "v_accvgpr_write_b32 a154, v120\n\t"
    "v_accvgpr_read_b32 v121, a155\n\t"
    "v_mul_f32 v121, v121, v127\n\t"
    "v_accvgpr_write_b32 a155, v121\n\t"
    "v_accvgpr_read_b32 v120, a168\n\t"
    "v_mul_f32 v120, v120, v124\n\t"
    "v_accvgpr_write_b32 a168, v120\n\t"
    "v_accvgpr_read_b32 v121, a169\n\t"
    "v_mul_f32 v121, v121, v125\n\t"
    "v_accvgpr_write_b32 a169, v121\n\t"
    "v_accvgpr_read_b32 v120, a170\n\t"
    "v_mul_f32 v120, v120, v126\n\t"
    "v_accvgpr_write_b32 a170, v120\n\t"
    "v_accvgpr_read_b32 v121, a171\n\t"
    "v_mul_f32 v121, v121, v127\n\t"
    "v_accvgpr_write_b32 a171, v121\n\t"
    "v_accvgpr_read_b32 v120, a184\n\t"
    "v_mul_f32 v120, v120, v124\n\t"
    "v_accvgpr_write_b32 a184, v120\n\t"
    "v_accvgpr_read_b32 v121, a185\n\t"
    "v_mul_f32 v121, v121, v125\n\t"
    "v_accvgpr_write_b32 a185, v121\n\t"
    "v_accvgpr_read_b32 v120, a186\n\t"
    "v_mul_f32 v120, v120, v126\n\t"
    "v_accvgpr_write_b32 a186, v120\n\t"
    "v_accvgpr_read_b32 v121, a187\n\t"
    "v_mul_f32 v121, v121, v127\n\t"
    "v_accvgpr_write_b32 a187, v121\n\t"
    "v_max3_f32 v120, v44, v45, v46\n\t"
    "v_max3_f32 v120, v120, v47, v60\n\t"
    "v_max3_f32 v120, v120, v61, v62\n\t"
    "v_max_f32 v120, v120, v63\n\t"
    "v_mov_b32 v121, v120\n\t"
    "s_nop 1\n\t"
    "v_permlane32_swap_b32 v120, v121\n\t"
    "s_nop 1\n\t"
    "v_max_f32 v120, v120, v121\n\t"
    "v_mov_b32 v121, v120\n\t"
    "s_nop 1\n\t"
    "v_permlane16_swap_b32 v120, v121\n\t"
    "s_nop 1\n\t"
    "v_max_f32 v120, v120, v121\n\t"
    "v_max_f32 v120, 0, v120\n\t"
    "v_sub_f32 v140, v140, v120\n\t"
    "v_sub_f32 v141, v141, v120\n\t"
    "v_sub_f32 v142, v142, v120\n\t"
    "v_sub_f32 v143, v143, v120\n\t"
    "v_sub_f32 v44, v44, v120\n\t"
    "v_sub_f32 v45, v45, v120\n\t"
    "v_sub_f32 v46, v46, v120\n\t"
    "v_sub_f32 v47, v47, v120\n\t"
    "v_sub_f32 v60, v60, v120\n\t"
    "v_sub_f32 v61, v61, v120\n\t"
    "v_sub_f32 v62, v62, v120\n\t"
    "v_sub_f32 v63, v63, v120\n\t"
    "v_sub_f32 v76, v76, v120\n\t"
    "v_sub_f32 v77, v77, v120\n\t"
    "v_sub_f32 v78, v78, v120\n\t"
    "v_sub_f32 v79, v79, v120\n\t"
    "v_sub_f32 v92, v92, v120\n\t"
    "v_sub_f32 v93, v93, v120\n\t"
    "v_sub_f32 v94, v94, v120\n\t"
    "v_sub_f32 v95, v95, v120\n\t"
    "v_exp_f32 v122, -v120\n\t"
    "s_nop 0\n\t"
    "v_mul_f32 v171, v171, v122\n\t"
    "ds_bpermute_b32 v124, v123, v122 offset:0\n\t"
    "ds_bpermute_b32 v125, v123, v122 offset:4\n\t"
    "ds_bpermute_b32 v126, v123, v122 offset:8\n\t"
    "ds_bpermute_b32 v127, v123, v122 offset:12\n\t"
    "v_exp_f32 v176, v44\n\t"
    "v_exp_f32 v177, v45\n\t"
    "s_nop 0\n\t"
    "v_add_f32 v175, v176, v177\n\t"
    "v_cvt_pk_bf16_f32 v108, v176, v177\n\t"
    "v_exp_f32 v178, v46\n\t"
    "v_exp_f32 v179, v47\n\t"
    "s_nop 0\n\t"
    "v_add_f32 v175, v175, v178\n\t"
    "v_add_f32 v175, v175, v179\n\t"
    "v_cvt_pk_bf16_f32 v109, v178, v179\n\t"
    "v_exp_f32 v176, v60\n\t"
    "v_exp_f32 v177, v61\n\t"
    "s_nop 0\n\t"
    "v_add_f32 v175, v175, v176\n\t"
    "v_add_f32 v175, v175, v177\n\t"
    "v_cvt_pk_bf16_f32 v110, v176, v177\n\t"
    "v_exp_f32 v178, v62\n\t"
    "v_exp_f32 v179, v63\n\t"
    "s_nop 0\n\t"
    "v_add_f32 v175, v175, v178\n\t"
    "v_add_f32 v175, v175, v179\n\t"
    "v_cvt_pk_bf16_f32 v111, v178, v179\n\t"
    "s_waitcnt lgkmcnt(0)\n\t"
    "v_accvgpr_read_b32 v120, a12\n\t"
    "v_mul_f32 v120, v120, v124\n\t"
    "v_accvgpr_write_b32 a12, v120\n\t"
    "v_accvgpr_read_b32 v121, a13\n\t"
    "v_mul_f32 v121, v121, v125\n\t"
    "v_accvgpr_write_b32 a13, v121\n\t"
    "v_accvgpr_read_b32 v120, a14\n\t"
    "v_mul_f32 v120, v120, v126\n\t"
    "v_accvgpr_write_b32 a14, v120\n\t"
    "v_accvgpr_read_b32 v121, a15\n\t"
    "v_mul_f32 v121, v121, v127\n\t"
    "v_accvgpr_write_b32 a15, v121\n\t"
    "v_accvgpr_read_b32 v120, a28\n\t"
    "v_mul_f32 v120, v120, v124\n\t"
    "v_accvgpr_write_b32 a28, v120\n\t"
    "v_accvgpr_read_b32 v121, a29\n\t"
    "v_mul_f32 v121, v121, v125\n\t"
    "v_accvgpr_write_b32 a29, v121\n\t"
    "v_accvgpr_read_b32 v120, a30\n\t"
    "v_mul_f32 v120, v120, v126\n\t"
    "v_accvgpr_write_b32 a30, v120\n\t"
    "v_accvgpr_read_b32 v121, a31\n\t"
    "v_mul_f32 v121, v121, v127\n\t"
    "v_accvgpr_write_b32 a31, v121\n\t"
    "v_accvgpr_read_b32 v120, a44\n\t"
    "v_mul_f32 v120, v120, v124\n\t"
    "v_accvgpr_write_b32 a44, v120\n\t"
    "v_accvgpr_read_b32 v121, a45\n\t"
    "v_mul_f32 v121, v121, v125\n\t"
    "v_accvgpr_write_b32 a45, v121\n\t"
    "v_accvgpr_read_b32 v120, a46\n\t"
    "v_mul_f32 v120, v120, v126\n\t"
    "v_accvgpr_write_b32 a46, v120\n\t"
    "v_accvgpr_read_b32 v121, a47\n\t"
    "v_mul_f32 v121, v121, v127\n\t"
    "v_accvgpr_write_b32 a47, v121\n\t"
    "v_accvgpr_read_b32 v120, a60\n\t"
    "v_mul_f32 v120, v120, v124\n\t"
    "v_accvgpr_write_b32 a60, v120\n\t"
    "v_accvgpr_read_b32 v121, a61\n\t"
    "v_mul_f32 v121, v121, v125\n\t"
    "v_accvgpr_write_b32 a61, v121\n\t"
    "v_accvgpr_read_b32 v120, a62\n\t"
    "v_mul_f32 v120, v120, v126\n\t"
    "v_accvgpr_write_b32 a62, v120\n\t"
    "v_accvgpr_read_b32 v121, a63\n\t"
    "v_mul_f32 v121, v121, v127\n\t"
    "v_accvgpr_write_b32 a63, v121\n\t"
    "v_accvgpr_read_b32 v120, a76\n\t"
    "v_mul_f32 v120, v120, v124\n\t"
    "v_accvgpr_write_b32 a76, v120\n\t"
    "v_accvgpr_read_b32 v121, a77\n\t"
    "v_mul_f32 v121, v121, v125\n\t"
    "v_accvgpr_write_b32 a77, v121\n\t"
    "v_accvgpr_read_b32 v120, a78\n\t"
    "v_mul_f32 v120, v120, v126\n\t"
    "v_accvgpr_write_b32 a78, v120\n\t"
    "v_accvgpr_read_b32 v121, a79\n\t"
    "v_mul_f32 v121, v121, v127\n\t"
    "v_accvgpr_write_b32 a79, v121\n\t"
    "v_accvgpr_read_b32 v120, a92\n\t"
    "v_mul_f32 v120, v120, v124\n\t"
    "v_accvgpr_write_b32 a92, v120\n\t"
    "v_accvgpr_read_b32 v121, a93\n\t"
    "v_mul_f32 v121, v121, v125\n\t"
    "v_accvgpr_write_b32 a93, v121\n\t"
    "v_accvgpr_read_b32 v120, a94\n\t"
    "v_mul_f32 v120, v120, v126\n\t"
    "v_accvgpr_write_b32 a94, v120\n\t"
    "v_accvgpr_read_b32 v121, a95\n\t"
    "v_mul_f32 v121, v121, v127\n\t"
    "v_accvgpr_write_b32 a95, v121\n\t"
    "v_accvgpr_read_b32 v120, a108\n\t"
    "v_mul_f32 v120, v120, v124\n\t"
    "v_accvgpr_write_b32 a108, v120\n\t"
    "v_accvgpr_read_b32 v121, a109\n\t"
    "v_mul_f32 v121, v121, v125\n\t"
    "v_accvgpr_write_b32 a109, v121\n\t"
    "v_accvgpr_read_b32 v120, a110\n\t"
    "v_mul_f32 v120, v120, v126\n\t"
    "v_accvgpr_write_b32 a110, v120\n\t"
    "v_accvgpr_read_b32 v121, a111\n\t"
    "v_mul_f32 v121, v121, v127\n\t"
    "v_accvgpr_write_b32 a111, v121\n\t"
    "v_accvgpr_read_b32 v120, a124\n\t"
    "v_mul_f32 v120, v120, v124\n\t"
    "v_accvgpr_write_b32 a124, v120\n\t"
    "v_accvgpr_read_b32 v121, a125\n\t"
    "v_mul_f32 v121, v121, v125\n\t"
    "v_accvgpr_write_b32 a125, v121\n\t"
    "v_accvgpr_read_b32 v120, a126\n\t"
    "v_mul_f32 v120, v120, v126\n\t"
    "v_accvgpr_write_b32 a126, v120\n\t"
    "v_accvgpr_read_b32 v121, a127\n\t"
    "v_mul_f32 v121, v121, v127\n\t"
    "v_accvgpr_write_b32 a127, v121\n\t"
    "v_accvgpr_read_b32 v120, a140\n\t"
    "v_mul_f32 v120, v120, v124\n\t"
    "v_accvgpr_write_b32 a140, v120\n\t"
    "v_accvgpr_read_b32 v121, a141\n\t"
    "v_mul_f32 v121, v121, v125\n\t"
    "v_accvgpr_write_b32 a141, v121\n\t"
    "v_accvgpr_read_b32 v120, a142\n\t"
    "v_mul_f32 v120, v120, v126\n\t"
    "v_accvgpr_write_b32 a142, v120\n\t"
    "v_accvgpr_read_b32 v121, a143\n\t"
    "v_mul_f32 v121, v121, v127\n\t"
    "v_accvgpr_write_b32 a143, v121\n\t"
    "v_accvgpr_read_b32 v120, a156\n\t"
    "v_mul_f32 v120, v120, v124\n\t"
    "v_accvgpr_write_b32 a156, v120\n\t"
    "v_accvgpr_read_b32 v121, a157\n\t"
    "v_mul_f32 v121, v121, v125\n\t"
    "v_accvgpr_write_b32 a157, v121\n\t"
    "v_accvgpr_read_b32 v120, a158\n\t"
    "v_mul_f32 v120, v120, v126\n\t"
    "v_accvgpr_write_b32 a158, v120\n\t"
    "v_accvgpr_read_b32 v121, a159\n\t"
    "v_mul_f32 v121, v121, v127\n\t"
    "v_accvgpr_write_b32 a159, v121\n\t"
    "v_accvgpr_read_b32 v120, a172\n\t"
    "v_mul_f32 v120, v120, v124\n\t"
    "v_accvgpr_write_b32 a172, v120\n\t"
    "v_accvgpr_read_b32 v121, a173\n\t"
    "v_mul_f32 v121, v121, v125\n\t"
    "v_accvgpr_write_b32 a173, v121\n\t"
    "v_accvgpr_read_b32 v120, a174\n\t"
    "v_mul_f32 v120, v120, v126\n\t"
    "v_accvgpr_write_b32 a174, v120\n\t"
    "v_accvgpr_read_b32 v121, a175\n\t"
    "v_mul_f32 v121, v121, v127\n\t"
    "v_accvgpr_write_b32 a175, v121\n\t"
    "v_accvgpr_read_b32 v120, a188\n\t"
    "v_mul_f32 v120, v120, v124\n\t"
    "v_accvgpr_write_b32 a188, v120\n\t"
    "v_accvgpr_read_b32 v121, a189\n\t"
    "v_mul_f32 v121, v121, v125\n\t"
    "v_accvgpr_write_b32 a189, v121\n\t"
    "v_accvgpr_read_b32 v120, a190\n\t"
    "v_mul_f32 v120, v120, v126\n\t"
    "v_accvgpr_write_b32 a190, v120\n\t"
    "v_accvgpr_read_b32 v121, a191\n\t"
    "v_mul_f32 v121, v121, v127\n\t"
    "v_accvgpr_write_b32 a191, v121\n\t"
    "s_nop 1\n\t"
    "s_branch L_ret_h4\n\t"
    "L_stub_h5:\n\t"
    "s_nop 7\n\t"
    "s_nop 7\n\t"
    "v_lshrrev_b32 v107, 4, v195\n\t"
    "v_and_b32 v107, 48, v107\n\t"
    "v_max3_f32 v104, v72, v73, v74\n\t"
    "v_max3_f32 v104, v104, v75, v88\n\t"
    "v_max3_f32 v104, v104, v89, v90\n\t"
    "v_max_f32 v104, v104, v91\n\t"
    "v_mov_b32 v105, v104\n\t"
    "s_nop 1\n\t"
    "v_permlane32_swap_b32 v104, v105\n\t"
    "s_nop 1\n\t"
    "v_max_f32 v104, v104, v105\n\t"
    "v_mov_b32 v105, v104\n\t"
    "s_nop 1\n\t"
    "v_permlane16_swap_b32 v104, v105\n\t"
    "s_nop 1\n\t"
    "v_max_f32 v104, v104, v105\n\t"
    "v_max_f32 v104, 0, v104\n\t"
    "v_sub_f32 v136, v136, v104\n\t"
    "v_sub_f32 v137, v137, v104\n\t"
    "v_sub_f32 v138, v138, v104\n\t"
    "v_sub_f32 v139, v139, v104\n\t"
    "v_sub_f32 v72, v72, v104\n\t"
    "v_sub_f32 v73, v73, v104\n\t"
    "v_sub_f32 v74, v74, v104\n\t"
    "v_sub_f32 v75, v75, v104\n\t"
    "v_sub_f32 v88, v88, v104\n\t"
    "v_sub_f32 v89, v89, v104\n\t"
    "v_sub_f32 v90, v90, v104\n\t"
    "v_sub_f32 v91, v91, v104\n\t"
    "v_sub_f32 v40, v40, v104\n\t"
    "v_sub_f32 v41, v41, v104\n\t"
    "v_sub_f32 v42, v42, v104\n\t"
    "v_sub_f32 v43, v43, v104\n\t"
    "v_sub_f32 v56, v56, v104\n\t"
    "v_sub_f32 v57, v57, v104\n\t"
    "v_sub_f32 v58, v58, v104\n\t"
    "v_sub_f32 v59, v59, v104\n\t"
    "v_exp_f32 v106, -v104\n\t"
    "s_nop 0\n\t"
    "v_mul_f32 v170, v170, v106\n\t"
    "ds_bpermute_b32 v108, v107, v106 offset:0\n\t"
    "ds_bpermute_b32 v109, v107, v106 offset:4\n\t"
    "ds_bpermute_b32 v110, v107, v106 offset:8\n\t"
    "ds_bpermute_b32 v111, v107, v106 offset:12\n\t"
    "v_exp_f32 v176, v72\n\t"
    "v_exp_f32 v177, v73\n\t"
    "s_nop 0\n\t"
    "v_add_f32 v174, v176, v177\n\t"
    "v_cvt_pk_bf16_f32 v120, v176, v177\n\t"
    "v_exp_f32 v178, v74\n\t"
    "v_exp_f32 v179, v75\n\t"
    "s_nop 0\n\t"
    "v_add_f32 v174, v174, v178\n\t"
    "v_add_f32 v174, v174, v179\n\t"
    "v_cvt_pk_bf16_f32 v121, v178, v179\n\t"
    "v_exp_f32 v176, v88\n\t"
    "v_exp_f32 v177, v89\n\t"
    "s_nop 0\n\t"
    "v_add_f32 v174, v174, v176\n\t"
    "v_add_f32 v174, v174, v177\n\t"
    "v_cvt_pk_bf16_f32 v122, v176, v177\n\t"
    "v_exp_f32 v178, v90\n\t"
    "v_exp_f32 v179, v91\n\t"
    "s_nop 0\n\t"
    "v_add_f32 v174, v174, v178\n\t"
    "v_add_f32 v174, v174, v179\n\t"
    "v_cvt_pk_bf16_f32 v123, v178, v179\n\t"
    "s_waitcnt lgkmcnt(0)\n\t"
    "v_accvgpr_read_b32 v104, a8\n\t"
    "v_mul_f32 v104, v104, v108\n\t"
    "v_accvgpr_write_b32 a8, v104\n\t"
    "v_accvgpr_read_b32 v105, a9\n\t"
    "v_mul_f32 v105, v105, v109\n\t"
    "v_accvgpr_write_b32 a9, v105\n\t"
    "v_accvgpr_read_b32 v104, a10\n\t"
    "v_mul_f32 v104, v104, v110\n\t"
    "v_accvgpr_write_b32 a10, v104\n\t"
    "v_accvgpr_read_b32 v105, a11\n\t"
    "v_mul_f32 v105, v105, v111\n\t"
    "v_accvgpr_write_b32 a11, v105\n\t"
    "v_accvgpr_read_b32 v104, a24\n\t"
    "v_mul_f32 v104, v104, v108\n\t"
    "v_accvgpr_write_b32 a24, v104\n\t"
    "v_accvgpr_read_b32 v105, a25\n\t"
    "v_mul_f32 v105, v105, v109\n\t"
    "v_accvgpr_write_b32 a25, v105\n\t"
    "v_accvgpr_read_b32 v104, a26\n\t"
    "v_mul_f32 v104, v104, v110\n\t"
    "v_accvgpr_write_b32 a26, v104\n\t"
    "v_accvgpr_read_b32 v105, a27\n\t"
    "v_mul_f32 v105, v105, v111\n\t"
    "v_accvgpr_write_b32 a27, v105\n\t"
    "v_accvgpr_read_b32 v104, a40\n\t"
    "v_mul_f32 v104, v104, v108\n\t"
    "v_accvgpr_write_b32 a40, v104\n\t"
    "v_accvgpr_read_b32 v105, a41\n\t"
    "v_mul_f32 v105, v105, v109\n\t"
    "v_accvgpr_write_b32 a41, v105\n\t"
    "v_accvgpr_read_b32 v104, a42\n\t"
    "v_mul_f32 v104, v104, v110\n\t"
    "v_accvgpr_write_b32 a42, v104\n\t"
    "v_accvgpr_read_b32 v105, a43\n\t"
    "v_mul_f32 v105, v105, v111\n\t"
    "v_accvgpr_write_b32 a43, v105\n\t"
    "v_accvgpr_read_b32 v104, a56\n\t"
    "v_mul_f32 v104, v104, v108\n\t"
    "v_accvgpr_write_b32 a56, v104\n\t"
    "v_accvgpr_read_b32 v105, a57\n\t"
    "v_mul_f32 v105, v105, v109\n\t"
    "v_accvgpr_write_b32 a57, v105\n\t"
    "v_accvgpr_read_b32 v104, a58\n\t"
    "v_mul_f32 v104, v104, v110\n\t"
    "v_accvgpr_write_b32 a58, v104\n\t"
    "v_accvgpr_read_b32 v105, a59\n\t"
    "v_mul_f32 v105, v105, v111\n\t"
    "v_accvgpr_write_b32 a59, v105\n\t"
    "v_accvgpr_read_b32 v104, a72\n\t"
    "v_mul_f32 v104, v104, v108\n\t"
    "v_accvgpr_write_b32 a72, v104\n\t"
    "v_accvgpr_read_b32 v105, a73\n\t"
    "v_mul_f32 v105, v105, v109\n\t"
    "v_accvgpr_write_b32 a73, v105\n\t"
    "v_accvgpr_read_b32 v104, a74\n\t"
    "v_mul_f32 v104, v104, v110\n\t"
    "v_accvgpr_write_b32 a74, v104\n\t"
    "v_accvgpr_read_b32 v105, a75\n\t"
    "v_mul_f32 v105, v105, v111\n\t"
    "v_accvgpr_write_b32 a75, v105\n\t"
    "v_accvgpr_read_b32 v104, a88\n\t"
    "v_mul_f32 v104, v104, v108\n\t"
    "v_accvgpr_write_b32 a88, v104\n\t"
    "v_accvgpr_read_b32 v105, a89\n\t"
    "v_mul_f32 v105, v105, v109\n\t"
    "v_accvgpr_write_b32 a89, v105\n\t"
    "v_accvgpr_read_b32 v104, a90\n\t"
    "v_mul_f32 v104, v104, v110\n\t"
    "v_accvgpr_write_b32 a90, v104\n\t"
    "v_accvgpr_read_b32 v105, a91\n\t"
    "v_mul_f32 v105, v105, v111\n\t"
    "v_accvgpr_write_b32 a91, v105\n\t"
    "v_accvgpr_read_b32 v104, a104\n\t"
    "v_mul_f32 v104, v104, v108\n\t"
    "v_accvgpr_write_b32 a104, v104\n\t"
    "v_accvgpr_read_b32 v105, a105\n\t"
    "v_mul_f32 v105, v105, v109\n\t"
    "v_accvgpr_write_b32 a105, v105\n\t"
    "v_accvgpr_read_b32 v104, a106\n\t"
    "v_mul_f32 v104, v104, v110\n\t"
    "v_accvgpr_write_b32 a106, v104\n\t"
    "v_accvgpr_read_b32 v105, a107\n\t"
    "v_mul_f32 v105, v105, v111\n\t"
    "v_accvgpr_write_b32 a107, v105\n\t"
    "v_accvgpr_read_b32 v104, a120\n\t"
    "v_mul_f32 v104, v104, v108\n\t"
    "v_accvgpr_write_b32 a120, v104\n\t"
    "v_accvgpr_read_b32 v105, a121\n\t"
    "v_mul_f32 v105, v105, v109\n\t"
    "v_accvgpr_write_b32 a121, v105\n\t"
    "v_accvgpr_read_b32 v104, a122\n\t"
    "v_mul_f32 v104, v104, v110\n\t"
    "v_accvgpr_write_b32 a122, v104\n\t"
    "v_accvgpr_read_b32 v105, a123\n\t"
    "v_mul_f32 v105, v105, v111\n\t"
    "v_accvgpr_write_b32 a123, v105\n\t"
    "v_accvgpr_read_b32 v104, a136\n\t"
    "v_mul_f32 v104, v104, v108\n\t"
    "v_accvgpr_write_b32 a136, v104\n\t"
    "v_accvgpr_read_b32 v105, a137\n\t"
    "v_mul_f32 v105, v105, v109\n\t"
    "v_accvgpr_write_b32 a137, v105\n\t"
    "v_accvgpr_read_b32 v104, a138\n\t"
    "v_mul_f32 v104, v104, v110\n\t"
    "v_accvgpr_write_b32 a138, v104\n\t"
    "v_accvgpr_read_b32 v105, a139\n\t"
    "v_mul_f32 v105, v105, v111\n\t"
    "v_accvgpr_write_b32 a139, v105\n\t"
    "v_accvgpr_read_b32 v104, a152\n\t"
    "v_mul_f32 v104, v104, v108\n\t"
    "v_accvgpr_write_b32 a152, v104\n\t"
    "v_accvgpr_read_b32 v105, a153\n\t"
    "v_mul_f32 v105, v105, v109\n\t"
    "v_accvgpr_write_b32 a153, v105\n\t"
    "v_accvgpr_read_b32 v104, a154\n\t"
    "v_mul_f32 v104, v104, v110\n\t"
    "v_accvgpr_write_b32 a154, v104\n\t"
    "v_accvgpr_read_b32 v105, a155\n\t"
    "v_mul_f32 v105, v105, v111\n\t"
    "v_accvgpr_write_b32 a155, v105\n\t"
    "v_accvgpr_read_b32 v104, a168\n\t"
    "v_mul_f32 v104, v104, v108\n\t"
    "v_accvgpr_write_b32 a168, v104\n\t"
    "v_accvgpr_read_b32 v105, a169\n\t"
    "v_mul_f32 v105, v105, v109\n\t"
    "v_accvgpr_write_b32 a169, v105\n\t"
    "v_accvgpr_read_b32 v104, a170\n\t"
    "v_mul_f32 v104, v104, v110\n\t"
    "v_accvgpr_write_b32 a170, v104\n\t"
    "v_accvgpr_read_b32 v105, a171\n\t"
    "v_mul_f32 v105, v105, v111\n\t"
    "v_accvgpr_write_b32 a171, v105\n\t"
    "v_accvgpr_read_b32 v104, a184\n\t"
    "v_mul_f32 v104, v104, v108\n\t"
    "v_accvgpr_write_b32 a184, v104\n\t"
    "v_accvgpr_read_b32 v105, a185\n\t"
    "v_mul_f32 v105, v105, v109\n\t"
    "v_accvgpr_write_b32 a185, v105\n\t"
    "v_accvgpr_read_b32 v104, a186\n\t"
    "v_mul_f32 v104, v104, v110\n\t"
    "v_accvgpr_write_b32 a186, v104\n\t"
    "v_accvgpr_read_b32 v105, a187\n\t"
    "v_mul_f32 v105, v105, v111\n\t"
    "v_accvgpr_write_b32 a187, v105\n\t"
    "v_max3_f32 v104, v76, v77, v78\n\t"
    "v_max3_f32 v104, v104, v79, v92\n\t"
    "v_max3_f32 v104, v104, v93, v94\n\t"
    "v_max_f32 v104, v104, v95\n\t"
    "v_mov_b32 v105, v104\n\t"
    "s_nop 1\n\t"
    "v_permlane32_swap_b32 v104, v105\n\t"
    "s_nop 1\n\t"
    "v_max_f32 v104, v104, v105\n\t"
    "v_mov_b32 v105, v104\n\t"
    "s_nop 1\n\t"
    "v_permlane16_swap_b32 v104, v105\n\t"
    "s_nop 1\n\t"
    "v_max_f32 v104, v104, v105\n\t"
    "v_max_f32 v104, 0, v104\n\t"
    "v_sub_f32 v140, v140, v104\n\t"
    "v_sub_f32 v141, v141, v104\n\t"
    "v_sub_f32 v142, v142, v104\n\t"
    "v_sub_f32 v143, v143, v104\n\t"
    "v_sub_f32 v76, v76, v104\n\t"
    "v_sub_f32 v77, v77, v104\n\t"
    "v_sub_f32 v78, v78, v104\n\t"
    "v_sub_f32 v79, v79, v104\n\t"
    "v_sub_f32 v92, v92, v104\n\t"
    "v_sub_f32 v93, v93, v104\n\t"
    "v_sub_f32 v94, v94, v104\n\t"
    "v_sub_f32 v95, v95, v104\n\t"
    "v_sub_f32 v44, v44, v104\n\t"
    "v_sub_f32 v45, v45, v104\n\t"
    "v_sub_f32 v46, v46, v104\n\t"
    "v_sub_f32 v47, v47, v104\n\t"
    "v_sub_f32 v60, v60, v104\n\t"
    "v_sub_f32 v61, v61, v104\n\t"
    "v_sub_f32 v62, v62, v104\n\t"
    "v_sub_f32 v63, v63, v104\n\t"
    "v_exp_f32 v106, -v104\n\t"
    "s_nop 0\n\t"
    "v_mul_f32 v171, v171, v106\n\t"
    "ds_bpermute_b32 v108, v107, v106 offset:0\n\t"
    "ds_bpermute_b32 v109, v107, v106 offset:4\n\t"
    "ds_bpermute_b32 v110, v107, v106 offset:8\n\t"
    "ds_bpermute_b32 v111, v107, v106 offset:12\n\t"
    "v_exp_f32 v176, v76\n\t"
    "v_exp_f32 v177, v77\n\t"
    "s_nop 0\n\t"
    "v_add_f32 v175, v176, v177\n\t"
    "v_cvt_pk_bf16_f32 v124, v176, v177\n\t"
    "v_exp_f32 v178, v78\n\t"
    "v_exp_f32 v179, v79\n\t"
    "s_nop 0\n\t"
    "v_add_f32 v175, v175, v178\n\t"
    "v_add_f32 v175, v175, v179\n\t"
    "v_cvt_pk_bf16_f32 v125, v178, v179\n\t"
    "v_exp_f32 v176, v92\n\t"
    "v_exp_f32 v177, v93\n\t"
    "s_nop 0\n\t"
    "v_add_f32 v175, v175, v176\n\t"
    "v_add_f32 v175, v175, v177\n\t"
    "v_cvt_pk_bf16_f32 v126, v176, v177\n\t"
    "v_exp_f32 v178, v94\n\t"
    "v_exp_f32 v179, v95\n\t"
    "s_nop 0\n\t"
    "v_add_f32 v175, v175, v178\n\t"
    "v_add_f32 v175, v175, v179\n\t"
    "v_cvt_pk_bf16_f32 v127, v178, v179\n\t"
    "s_waitcnt lgkmcnt(0)\n\t"
    "v_accvgpr_read_b32 v104, a12\n\t"
    "v_mul_f32 v104, v104, v108\n\t"
    "v_accvgpr_write_b32 a12, v104\n\t"
    "v_accvgpr_read_b32 v105, a13\n\t"
    "v_mul_f32 v105, v105, v109\n\t"
    "v_accvgpr_write_b32 a13, v105\n\t"
    "v_accvgpr_read_b32 v104, a14\n\t"
    "v_mul_f32 v104, v104, v110\n\t"
    "v_accvgpr_write_b32 a14, v104\n\t"
    "v_accvgpr_read_b32 v105, a15\n\t"
    "v_mul_f32 v105, v105, v111\n\t"
    "v_accvgpr_write_b32 a15, v105\n\t"
    "v_accvgpr_read_b32 v104, a28\n\t"
    "v_mul_f32 v104, v104, v108\n\t"
    "v_accvgpr_write_b32 a28, v104\n\t"
    "v_accvgpr_read_b32 v105, a29\n\t"
    "v_mul_f32 v105, v105, v109\n\t"
    "v_accvgpr_write_b32 a29, v105\n\t"
    "v_accvgpr_read_b32 v104, a30\n\t"
    "v_mul_f32 v104, v104, v110\n\t"
    "v_accvgpr_write_b32 a30, v104\n\t"
    "v_accvgpr_read_b32 v105, a31\n\t"
    "v_mul_f32 v105, v105, v111\n\t"
    "v_accvgpr_write_b32 a31, v105\n\t"
    "v_accvgpr_read_b32 v104, a44\n\t"
    "v_mul_f32 v104, v104, v108\n\t"
    "v_accvgpr_write_b32 a44, v104\n\t"
    "v_accvgpr_read_b32 v105, a45\n\t"
    "v_mul_f32 v105, v105, v109\n\t"
    "v_accvgpr_write_b32 a45, v105\n\t"
    "v_accvgpr_read_b32 v104, a46\n\t"
    "v_mul_f32 v104, v104, v110\n\t"
    "v_accvgpr_write_b32 a46, v104\n\t"
    "v_accvgpr_read_b32 v105, a47\n\t"
    "v_mul_f32 v105, v105, v111\n\t"
    "v_accvgpr_write_b32 a47, v105\n\t"
    "v_accvgpr_read_b32 v104, a60\n\t"
    "v_mul_f32 v104, v104, v108\n\t"
    "v_accvgpr_write_b32 a60, v104\n\t"
    "v_accvgpr_read_b32 v105, a61\n\t"
    "v_mul_f32 v105, v105, v109\n\t"
    "v_accvgpr_write_b32 a61, v105\n\t"
    "v_accvgpr_read_b32 v104, a62\n\t"
    "v_mul_f32 v104, v104, v110\n\t"
    "v_accvgpr_write_b32 a62, v104\n\t"
    "v_accvgpr_read_b32 v105, a63\n\t"
    "v_mul_f32 v105, v105, v111\n\t"
    "v_accvgpr_write_b32 a63, v105\n\t"
    "v_accvgpr_read_b32 v104, a76\n\t"
    "v_mul_f32 v104, v104, v108\n\t"
    "v_accvgpr_write_b32 a76, v104\n\t"
    "v_accvgpr_read_b32 v105, a77\n\t"
    "v_mul_f32 v105, v105, v109\n\t"
    "v_accvgpr_write_b32 a77, v105\n\t"
    "v_accvgpr_read_b32 v104, a78\n\t"
    "v_mul_f32 v104, v104, v110\n\t"
    "v_accvgpr_write_b32 a78, v104\n\t"
    "v_accvgpr_read_b32 v105, a79\n\t"
    "v_mul_f32 v105, v105, v111\n\t"
    "v_accvgpr_write_b32 a79, v105\n\t"
    "v_accvgpr_read_b32 v104, a92\n\t"
    "v_mul_f32 v104, v104, v108\n\t"
    "v_accvgpr_write_b32 a92, v104\n\t"
    "v_accvgpr_read_b32 v105, a93\n\t"
    "v_mul_f32 v105, v105, v109\n\t"
    "v_accvgpr_write_b32 a93, v105\n\t"
    "v_accvgpr_read_b32 v104, a94\n\t"
    "v_mul_f32 v104, v104, v110\n\t"
    "v_accvgpr_write_b32 a94, v104\n\t"
    "v_accvgpr_read_b32 v105, a95\n\t"
    "v_mul_f32 v105, v105, v111\n\t"
    "v_accvgpr_write_b32 a95, v105\n\t"
    "v_accvgpr_read_b32 v104, a108\n\t"
    "v_mul_f32 v104, v104, v108\n\t"
    "v_accvgpr_write_b32 a108, v104\n\t"
    "v_accvgpr_read_b32 v105, a109\n\t"
    "v_mul_f32 v105, v105, v109\n\t"
    "v_accvgpr_write_b32 a109, v105\n\t"
    "v_accvgpr_read_b32 v104, a110\n\t"
    "v_mul_f32 v104, v104, v110\n\t"
    "v_accvgpr_write_b32 a110, v104\n\t"
    "v_accvgpr_read_b32 v105, a111\n\t"
    "v_mul_f32 v105, v105, v111\n\t"
    "v_accvgpr_write_b32 a111, v105\n\t"
    "v_accvgpr_read_b32 v104, a124\n\t"
    "v_mul_f32 v104, v104, v108\n\t"
    "v_accvgpr_write_b32 a124, v104\n\t"
    "v_accvgpr_read_b32 v105, a125\n\t"
    "v_mul_f32 v105, v105, v109\n\t"
    "v_accvgpr_write_b32 a125, v105\n\t"
    "v_accvgpr_read_b32 v104, a126\n\t"
    "v_mul_f32 v104, v104, v110\n\t"
    "v_accvgpr_write_b32 a126, v104\n\t"
    "v_accvgpr_read_b32 v105, a127\n\t"
    "v_mul_f32 v105, v105, v111\n\t"
    "v_accvgpr_write_b32 a127, v105\n\t"
    "v_accvgpr_read_b32 v104, a140\n\t"
    "v_mul_f32 v104, v104, v108\n\t"
    "v_accvgpr_write_b32 a140, v104\n\t"
    "v_accvgpr_read_b32 v105, a141\n\t"
    "v_mul_f32 v105, v105, v109\n\t"
    "v_accvgpr_write_b32 a141, v105\n\t"
    "v_accvgpr_read_b32 v104, a142\n\t"
    "v_mul_f32 v104, v104, v110\n\t"
    "v_accvgpr_write_b32 a142, v104\n\t"
    "v_accvgpr_read_b32 v105, a143\n\t"
    "v_mul_f32 v105, v105, v111\n\t"
    "v_accvgpr_write_b32 a143, v105\n\t"
    "v_accvgpr_read_b32 v104, a156\n\t"
    "v_mul_f32 v104, v104, v108\n\t"
    "v_accvgpr_write_b32 a156, v104\n\t"
    "v_accvgpr_read_b32 v105, a157\n\t"
    "v_mul_f32 v105, v105, v109\n\t"
    "v_accvgpr_write_b32 a157, v105\n\t"
    "v_accvgpr_read_b32 v104, a158\n\t"
    "v_mul_f32 v104, v104, v110\n\t"
    "v_accvgpr_write_b32 a158, v104\n\t"
    "v_accvgpr_read_b32 v105, a159\n\t"
    "v_mul_f32 v105, v105, v111\n\t"
    "v_accvgpr_write_b32 a159, v105\n\t"
    "v_accvgpr_read_b32 v104, a172\n\t"
    "v_mul_f32 v104, v104, v108\n\t"
    "v_accvgpr_write_b32 a172, v104\n\t"
    "v_accvgpr_read_b32 v105, a173\n\t"
    "v_mul_f32 v105, v105, v109\n\t"
    "v_accvgpr_write_b32 a173, v105\n\t"
    "v_accvgpr_read_b32 v104, a174\n\t"
    "v_mul_f32 v104, v104, v110\n\t"
    "v_accvgpr_write_b32 a174, v104\n\t"
    "v_accvgpr_read_b32 v105, a175\n\t"
    "v_mul_f32 v105, v105, v111\n\t"
    "v_accvgpr_write_b32 a175, v105\n\t"
    "v_accvgpr_read_b32 v104, a188\n\t"
    "v_mul_f32 v104, v104, v108\n\t"
    "v_accvgpr_write_b32 a188, v104\n\t"
    "v_accvgpr_read_b32 v105, a189\n\t"
    "v_mul_f32 v105, v105, v109\n\t"
    "v_accvgpr_write_b32 a189, v105\n\t"
    "v_accvgpr_read_b32 v104, a190\n\t"
    "v_mul_f32 v104, v104, v110\n\t"
    "v_accvgpr_write_b32 a190, v104\n\t"
    "v_accvgpr_read_b32 v105, a191\n\t"
    "v_mul_f32 v105, v105, v111\n\t"
    "v_accvgpr_write_b32 a191, v105\n\t"
    "s_nop 1\n\t"
    "s_branch L_ret_h5\n\t"
    "L_spin_p:\n\t"
    "s_mov_b32 s39, 0x20000\n\t"
    "L_spinl_p:\n\t"
    "s_sleep 1\n\t"
    "global_load_dword v202, v181, s[58:59] sc1\n\t"
    "global_load_dwordx2 v[120:121], v182, s[90:91] sc1\n\t"
    "s_waitcnt vmcnt(0)\n\t"
    "global_store_dwordx2 v184, v[202:203], s[56:57] sc1\n\t"
    "v_subrev_u32 v120, s63, v120\n\t"
    "v_cmp_lt_u32 vcc, 7, v120\n\t"
    "s_cbranch_vccz L_spinx_p\n\t"
    "s_sub_u32 s39, s39, 1\n\t"
    "s_cbranch_scc0 L_spinl_p\n\t"
    "L_spinx_p:\n\t"
    "s_branch L_spinret_p\n\t"
    "L_spin_l:\n\t"
    "s_mov_b32 s39, 0x20000\n\t"
    "s_sub_u32 s63, s63, 1\n\t"
    "L_spinl_l:\n\t"
    "s_sleep 1\n\t"
    "global_load_dword v202, v182, s[90:91] sc1\n\t"
    "s_waitcnt vmcnt(0)\n\t"
    "v_subrev_u32 v202, s63, v202\n\t"
    "v_cmp_lt_u32 vcc, 7, v202\n\t"
    "s_cbranch_vccz L_spinx_l\n\t"
    "s_sub_u32 s39, s39, 1\n\t"
    "s_cbranch_scc0 L_spinl_l\n\t"
    "L_spinx_l:\n\t"
    "s_add_u32 s63, s63, 1\n\t"
    "s_branch L_spinret_l\n\t"
    "L_spin_53:\n\t"
    "s_mov_b32 s39, 0x20000\n\t"
    "s_sub_u32 s63, s63, 1\n\t"
    "L_spinl_53:\n\t"
    "s_sleep 1\n\t"
    "global_load_dword v202, v182, s[90:91] sc1\n\t"
    "s_waitcnt vmcnt(0)\n\t"
    "v_subrev_u32 v202, s63, v202\n\t"
    "v_cmp_lt_u32 vcc, 7, v202\n\t"
    "s_cbranch_vccz L_spinx_53\n\t"
    "s_sub_u32 s39, s39, 1\n\t"
    "s_cbranch_scc0 L_spinl_53\n\t"
    "L_spinx_53:\n\t"
    "s_add_u32 s63, s63, 1\n\t"
    "s_branch L_spinret_53\n\t"
    "L_slow_0:\n\t"
    "s_nop 7\n\t"
    "s_nop 7\n\t"
    "v_lshrrev_b32 v115, 4, v195\n\t"
    "v_and_b32 v115, 48, v115\n\t"
    "v_max3_f32 v112, v32, v33, v34\n\t"
    "v_max3_f32 v112, v112, v35, v48\n\t"
    "v_max3_f32 v112, v112, v49, v50\n\t"
    "v_max_f32 v112, v112, v51\n\t"
    "v_mov_b32 v113, v112\n\t"
    "s_nop 1\n\t"
    "v_permlane32_swap_b32 v112, v113\n\t"
    "s_nop 1\n\t"
    "v_max_f32 v112, v112, v113\n\t"
    "v_mov_b32 v113, v112\n\t"
    "s_nop 1\n\t"
    "v_permlane16_swap_b32 v112, v113\n\t"
    "s_nop 1\n\t"
    "v_max_f32 v112, v112, v113\n\t"
    "v_max_f32 v112, 0, v112\n\t"
    "v_sub_f32 v128, v128, v112\n\t"
    "v_sub_f32 v129, v129, v112\n\t"
    "v_sub_f32 v130, v130, v112\n\t"
    "v_sub_f32 v131, v131, v112\n\t"
    "v_sub_f32 v32, v32, v112\n\t"
    "v_sub_f32 v33, v33, v112\n\t"
    "v_sub_f32 v34, v34, v112\n\t"
    "v_sub_f32 v35, v35, v112\n\t"
    "v_sub_f32 v48, v48, v112\n\t"
    "v_sub_f32 v49, v49, v112\n\t"
    "v_sub_f32 v50, v50, v112\n\t"
    "v_sub_f32 v51, v51, v112\n\t"
    "v_sub_f32 v64, v64, v112\n\t"
    "v_sub_f32 v65, v65, v112\n\t"
    "v_sub_f32 v66, v66, v112\n\t"
    "v_sub_f32 v67, v67, v112\n\t"
    "v_sub_f32 v80, v80, v112\n\t"
    "v_sub_f32 v81, v81, v112\n\t"
    "v_sub_f32 v82, v82, v112\n\t"
    "v_sub_f32 v83, v83, v112\n\t"
    "v_exp_f32 v114, -v112\n\t"
    "s_nop 0\n\t"
    "v_mul_f32 v168, v168, v114\n\t"
    "ds_bpermute_b32 v116, v115, v114 offset:0\n\t"
    "ds_bpermute_b32 v117, v115, v114 offset:4\n\t"
    "ds_bpermute_b32 v118, v115, v114 offset:8\n\t"
    "ds_bpermute_b32 v119, v115, v114 offset:12\n\t"
    "v_exp_f32 v176, v32\n\t"
    "v_exp_f32 v177, v33\n\t"
    "s_nop 0\n\t"
    "v_add_f32 v172, v176, v177\n\t"
    "v_cvt_pk_bf16_f32 v96, v176, v177\n\t"
    "v_exp_f32 v178, v34\n\t"
    "v_exp_f32 v179, v35\n\t"
    "s_nop 0\n\t"
    "v_add_f32 v172, v172, v178\n\t"
    "v_add_f32 v172, v172, v179\n\t"
    "v_cvt_pk_bf16_f32 v97, v178, v179\n\t"
    "v_exp_f32 v176, v48\n\t"
    "v_exp_f32 v177, v49\n\t"
    "s_nop 0\n\t"
    "v_add_f32 v172, v172, v176\n\t"
    "v_add_f32 v172, v172, v177\n\t"
    "v_cvt_pk_bf16_f32 v98, v176, v177\n\t"
    "v_exp_f32 v178, v50\n\t"
    "v_exp_f32 v179, v51\n\t"
    "s_nop 0\n\t"
    "v_add_f32 v172, v172, v178\n\t"
    "v_add_f32 v172, v172, v179\n\t"
    "v_cvt_pk_bf16_f32 v99, v178, v179\n\t"
    "s_waitcnt lgkmcnt(0)\n\t"
    "v_accvgpr_read_b32 v112, a0\n\t"
    "v_mul_f32 v112, v112, v116\n\t"
    "v_accvgpr_write_b32 a0, v112\n\t"
    "v_accvgpr_read_b32 v113, a1\n\t"
    "v_mul_f32 v113, v113, v117\n\t"
    "v_accvgpr_write_b32 a1, v113\n\t"
    "v_accvgpr_read_b32 v112, a2\n\t"
    "v_mul_f32 v112, v112, v118\n\t"
    "v_accvgpr_write_b32 a2, v112\n\t"
    "v_accvgpr_read_b32 v113, a3\n\t"
    "v_mul_f32 v113, v113, v119\n\t"
    "v_accvgpr_write_b32 a3, v113\n\t"
    "v_accvgpr_read_b32 v112, a16\n\t"
    "v_mul_f32 v112, v112, v116\n\t"
    "v_accvgpr_write_b32 a16, v112\n\t"
    "v_accvgpr_read_b32 v113, a17\n\t"
    "v_mul_f32 v113, v113, v117\n\t"
    "v_accvgpr_write_b32 a17, v113\n\t"
    "v_accvgpr_read_b32 v112, a18\n\t"
    "v_mul_f32 v112, v112, v118\n\t"
    "v_accvgpr_write_b32 a18, v112\n\t"
    "v_accvgpr_read_b32 v113, a19\n\t"
    "v_mul_f32 v113, v113, v119\n\t"
    "v_accvgpr_write_b32 a19, v113\n\t"
    "v_accvgpr_read_b32 v112, a32\n\t"
    "v_mul_f32 v112, v112, v116\n\t"
    "v_accvgpr_write_b32 a32, v112\n\t"
    "v_accvgpr_read_b32 v113, a33\n\t"
    "v_mul_f32 v113, v113, v117\n\t"
    "v_accvgpr_write_b32 a33, v113\n\t"
    "v_accvgpr_read_b32 v112, a34\n\t"
    "v_mul_f32 v112, v112, v118\n\t"
    "v_accvgpr_write_b32 a34, v112\n\t"
    "v_accvgpr_read_b32 v113, a35\n\t"
    "v_mul_f32 v113, v113, v119\n\t"
    "v_accvgpr_write_b32 a35, v113\n\t"
    "v_accvgpr_read_b32 v112, a48\n\t"
    "v_mul_f32 v112, v112, v116\n\t"
    "v_accvgpr_write_b32 a48, v112\n\t"
    "v_accvgpr_read_b32 v113, a49\n\t"
    "v_mul_f32 v113, v113, v117\n\t"
    "v_accvgpr_write_b32 a49, v113\n\t"
    "v_accvgpr_read_b32 v112, a50\n\t"
    "v_mul_f32 v112, v112, v118\n\t"
    "v_accvgpr_write_b32 a50, v112\n\t"
    "v_accvgpr_read_b32 v113, a51\n\t"
    "v_mul_f32 v113, v113, v119\n\t"
    "v_accvgpr_write_b32 a51, v113\n\t"
    "v_accvgpr_read_b32 v112, a64\n\t"
    "v_mul_f32 v112, v112, v116\n\t"
    "v_accvgpr_write_b32 a64, v112\n\t"
    "v_accvgpr_read_b32 v113, a65\n\t"
    "v_mul_f32 v113, v113, v117\n\t"
    "v_accvgpr_write_b32 a65, v113\n\t"
    "v_accvgpr_read_b32 v112, a66\n\t"
    "v_mul_f32 v112, v112, v118\n\t"
    "v_accvgpr_write_b32 a66, v112\n\t"
    "v_accvgpr_read_b32 v113, a67\n\t"
    "v_mul_f32 v113, v113, v119\n\t"
    "v_accvgpr_write_b32 a67, v113\n\t"
    "v_accvgpr_read_b32 v112, a80\n\t"
    "v_mul_f32 v112, v112, v116\n\t"
    "v_accvgpr_write_b32 a80, v112\n\t"
    "v_accvgpr_read_b32 v113, a81\n\t"
    "v_mul_f32 v113, v113, v117\n\t"
    "v_accvgpr_write_b32 a81, v113\n\t"
    "v_accvgpr_read_b32 v112, a82\n\t"
    "v_mul_f32 v112, v112, v118\n\t"
    "v_accvgpr_write_b32 a82, v112\n\t"
    "v_accvgpr_read_b32 v113, a83\n\t"
    "v_mul_f32 v113, v113, v119\n\t"
    "v_accvgpr_write_b32 a83, v113\n\t"
    "v_accvgpr_read_b32 v112, a96\n\t"
    "v_mul_f32 v112, v112, v116\n\t"
    "v_accvgpr_write_b32 a96, v112\n\t"
    "v_accvgpr_read_b32 v113, a97\n\t"
    "v_mul_f32 v113, v113, v117\n\t"
    "v_accvgpr_write_b32 a97, v113\n\t"
    "v_accvgpr_read_b32 v112, a98\n\t"
    "v_mul_f32 v112, v112, v118\n\t"
    "v_accvgpr_write_b32 a98, v112\n\t"
    "v_accvgpr_read_b32 v113, a99\n\t"
    "v_mul_f32 v113, v113, v119\n\t"
    "v_accvgpr_write_b32 a99, v113\n\t"
    "v_accvgpr_read_b32 v112, a112\n\t"
    "v_mul_f32 v112, v112, v116\n\t"
    "v_accvgpr_write_b32 a112, v112\n\t"
    "v_accvgpr_read_b32 v113, a113\n\t"
    "v_mul_f32 v113, v113, v117\n\t"
    "v_accvgpr_write_b32 a113, v113\n\t"
    "v_accvgpr_read_b32 v112, a114\n\t"
    "v_mul_f32 v112, v112, v118\n\t"
    "v_accvgpr_write_b32 a114, v112\n\t"
    "v_accvgpr_read_b32 v113, a115\n\t"
    "v_mul_f32 v113, v113, v119\n\t"
    "v_accvgpr_write_b32 a115, v113\n\t"
    "v_accvgpr_read_b32 v112, a128\n\t"
    "v_mul_f32 v112, v112, v116\n\t"
    "v_accvgpr_write_b32 a128, v112\n\t"
    "v_accvgpr_read_b32 v113, a129\n\t"
    "v_mul_f32 v113, v113, v117\n\t"
    "v_accvgpr_write_b32 a129, v113\n\t"
    "v_accvgpr_read_b32 v112, a130\n\t"
    "v_mul_f32 v112, v112, v118\n\t"
    "v_accvgpr_write_b32 a130, v112\n\t"
    "v_accvgpr_read_b32 v113, a131\n\t"
    "v_mul_f32 v113, v113, v119\n\t"
    "v_accvgpr_write_b32 a131, v113\n\t"
    "v_accvgpr_read_b32 v112, a144\n\t"
    "v_mul_f32 v112, v112, v116\n\t"
    "v_accvgpr_write_b32 a144, v112\n\t"
    "v_accvgpr_read_b32 v113, a145\n\t"
    "v_mul_f32 v113, v113, v117\n\t"
    "v_accvgpr_write_b32 a145, v113\n\t"
    "v_accvgpr_read_b32 v112, a146\n\t"
    "v_mul_f32 v112, v112, v118\n\t"
    "v_accvgpr_write_b32 a146, v112\n\t"
    "v_accvgpr_read_b32 v113, a147\n\t"
    "v_mul_f32 v113, v113, v119\n\t"
    "v_accvgpr_write_b32 a147, v113\n\t"
    "v_accvgpr_read_b32 v112, a160\n\t"
    "v_mul_f32 v112, v112, v116\n\t"
    "v_accvgpr_write_b32 a160, v112\n\t"
    "v_accvgpr_read_b32 v113, a161\n\t"
    "v_mul_f32 v113, v113, v117\n\t"
    "v_accvgpr_write_b32 a161, v113\n\t"
    "v_accvgpr_read_b32 v112, a162\n\t"
    "v_mul_f32 v112, v112, v118\n\t"
    "v_accvgpr_write_b32 a162, v112\n\t"
    "v_accvgpr_read_b32 v113, a163\n\t"
    "v_mul_f32 v113, v113, v119\n\t"
    "v_accvgpr_write_b32 a163, v113\n\t"
    "v_accvgpr_read_b32 v112, a176\n\t"
    "v_mul_f32 v112, v112, v116\n\t"
    "v_accvgpr_write_b32 a176, v112\n\t"
    "v_accvgpr_read_b32 v113, a177\n\t"
    "v_mul_f32 v113, v113, v117\n\t"
    "v_accvgpr_write_b32 a177, v113\n\t"
    "v_accvgpr_read_b32 v112, a178\n\t"
    "v_mul_f32 v112, v112, v118\n\t"
    "v_accvgpr_write_b32 a178, v112\n\t"
    "v_accvgpr_read_b32 v113, a179\n\t"
    "v_mul_f32 v113, v113, v119\n\t"
    "v_accvgpr_write_b32 a179, v113\n\t"
    "v_max3_f32 v112, v36, v37, v38\n\t"
    "v_max3_f32 v112, v112, v39, v52\n\t"
    "v_max3_f32 v112, v112, v53, v54\n\t"
    "v_max_f32 v112, v112, v55\n\t"
    "v_mov_b32 v113, v112\n\t"
    "s_nop 1\n\t"
    "v_permlane32_swap_b32 v112, v113\n\t"
    "s_nop 1\n\t"
    "v_max_f32 v112, v112, v113\n\t"
    "v_mov_b32 v113, v112\n\t"
    "s_nop 1\n\t"
    "v_permlane16_swap_b32 v112, v113\n\t"
    "s_nop 1\n\t"
    "v_max_f32 v112, v112, v113\n\t"
    "v_max_f32 v112, 0, v112\n\t"
    "v_sub_f32 v132, v132, v112\n\t"
    "v_sub_f32 v133, v133, v112\n\t"
    "v_sub_f32 v134, v134, v112\n\t"
    "v_sub_f32 v135, v135, v112\n\t"
    "v_sub_f32 v36, v36, v112\n\t"
    "v_sub_f32 v37, v37, v112\n\t"
    "v_sub_f32 v38, v38, v112\n\t"
    "v_sub_f32 v39, v39, v112\n\t"
    "v_sub_f32 v52, v52, v112\n\t"
    "v_sub_f32 v53, v53, v112\n\t"
    "v_sub_f32 v54, v54, v112\n\t"
    "v_sub_f32 v55, v55, v112\n\t"
    "v_sub_f32 v68, v68, v112\n\t"
    "v_sub_f32 v69, v69, v112\n\t"
    "v_sub_f32 v70, v70, v112\n\t"
    "v_sub_f32 v71, v71, v112\n\t"
    "v_sub_f32 v84, v84, v112\n\t"
    "v_sub_f32 v85, v85, v112\n\t"
    "v_sub_f32 v86, v86, v112\n\t"
    "v_sub_f32 v87, v87, v112\n\t"
    "v_exp_f32 v114, -v112\n\t"
    "s_nop 0\n\t"
    "v_mul_f32 v169, v169, v114\n\t"
    "ds_bpermute_b32 v116, v115, v114 offset:0\n\t"
    "ds_bpermute_b32 v117, v115, v114 offset:4\n\t"
    "ds_bpermute_b32 v118, v115, v114 offset:8\n\t"
    "ds_bpermute_b32 v119, v115, v114 offset:12\n\t"
    "v_exp_f32 v176, v36\n\t"
    "v_exp_f32 v177, v37\n\t"
    "s_nop 0\n\t"
    "v_add_f32 v173, v176, v177\n\t"
    "v_cvt_pk_bf16_f32 v100, v176, v177\n\t"
    "v_exp_f32 v178, v38\n\t"
    "v_exp_f32 v179, v39\n\t"
    "s_nop 0\n\t"
    "v_add_f32 v173, v173, v178\n\t"
    "v_add_f32 v173, v173, v179\n\t"
    "v_cvt_pk_bf16_f32 v101, v178, v179\n\t"
    "v_exp_f32 v176, v52\n\t"
    "v_exp_f32 v177, v53\n\t"
    "s_nop 0\n\t"
    "v_add_f32 v173, v173, v176\n\t"
    "v_add_f32 v173, v173, v177\n\t"
    "v_cvt_pk_bf16_f32 v102, v176, v177\n\t"
    "v_exp_f32 v178, v54\n\t"
    "v_exp_f32 v179, v55\n\t"
    "s_nop 0\n\t"
    "v_add_f32 v173, v173, v178\n\t"
    "v_add_f32 v173, v173, v179\n\t"
    "v_cvt_pk_bf16_f32 v103, v178, v179\n\t"
    "s_waitcnt lgkmcnt(0)\n\t"
    "v_accvgpr_read_b32 v112, a4\n\t"
    "v_mul_f32 v112, v112, v116\n\t"
    "v_accvgpr_write_b32 a4, v112\n\t"
    "v_accvgpr_read_b32 v113, a5\n\t"
    "v_mul_f32 v113, v113, v117\n\t"
    "v_accvgpr_write_b32 a5, v113\n\t"
    "v_accvgpr_read_b32 v112, a6\n\t"
    "v_mul_f32 v112, v112, v118\n\t"
    "v_accvgpr_write_b32 a6, v112\n\t"
    "v_accvgpr_read_b32 v113, a7\n\t"
    "v_mul_f32 v113, v113, v119\n\t"
    "v_accvgpr_write_b32 a7, v113\n\t"
    "v_accvgpr_read_b32 v112, a20\n\t"
    "v_mul_f32 v112, v112, v116\n\t"
    "v_accvgpr_write_b32 a20, v112\n\t"
    "v_accvgpr_read_b32 v113, a21\n\t"
    "v_mul_f32 v113, v113, v117\n\t"
    "v_accvgpr_write_b32 a21, v113\n\t"
    "v_accvgpr_read_b32 v112, a22\n\t"
    "v_mul_f32 v112, v112, v118\n\t"
    "v_accvgpr_write_b32 a22, v112\n\t"
    "v_accvgpr_read_b32 v113, a23\n\t"
    "v_mul_f32 v113, v113, v119\n\t"
    "v_accvgpr_write_b32 a23, v113\n\t"
    "v_accvgpr_read_b32 v112, a36\n\t"
    "v_mul_f32 v112, v112, v116\n\t"
    "v_accvgpr_write_b32 a36, v112\n\t"
    "v_accvgpr_read_b32 v113, a37\n\t"
    "v_mul_f32 v113, v113, v117\n\t"
    "v_accvgpr_write_b32 a37, v113\n\t"
    "v_accvgpr_read_b32 v112, a38\n\t"
    "v_mul_f32 v112, v112, v118\n\t"
    "v_accvgpr_write_b32 a38, v112\n\t"
    "v_accvgpr_read_b32 v113, a39\n\t"
    "v_mul_f32 v113, v113, v119\n\t"
    "v_accvgpr_write_b32 a39, v113\n\t"
    "v_accvgpr_read_b32 v112, a52\n\t"
    "v_mul_f32 v112, v112, v116\n\t"
    "v_accvgpr_write_b32 a52, v112\n\t"
    "v_accvgpr_read_b32 v113, a53\n\t"
    "v_mul_f32 v113, v113, v117\n\t"
    "v_accvgpr_write_b32 a53, v113\n\t"
    "v_accvgpr_read_b32 v112, a54\n\t"
    "v_mul_f32 v112, v112, v118\n\t"
    "v_accvgpr_write_b32 a54, v112\n\t"
    "v_accvgpr_read_b32 v113, a55\n\t"
    "v_mul_f32 v113, v113, v119\n\t"
    "v_accvgpr_write_b32 a55, v113\n\t"
    "v_accvgpr_read_b32 v112, a68\n\t"
    "v_mul_f32 v112, v112, v116\n\t"
    "v_accvgpr_write_b32 a68, v112\n\t"
    "v_accvgpr_read_b32 v113, a69\n\t"
    "v_mul_f32 v113, v113, v117\n\t"
    "v_accvgpr_write_b32 a69, v113\n\t"
    "v_accvgpr_read_b32 v112, a70\n\t"
    "v_mul_f32 v112, v112, v118\n\t"
    "v_accvgpr_write_b32 a70, v112\n\t"
    "v_accvgpr_read_b32 v113, a71\n\t"
    "v_mul_f32 v113, v113, v119\n\t"
    "v_accvgpr_write_b32 a71, v113\n\t"
    "v_accvgpr_read_b32 v112, a84\n\t"
    "v_mul_f32 v112, v112, v116\n\t"
    "v_accvgpr_write_b32 a84, v112\n\t"
    "v_accvgpr_read_b32 v113, a85\n\t"
    "v_mul_f32 v113, v113, v117\n\t"
    "v_accvgpr_write_b32 a85, v113\n\t"
    "v_accvgpr_read_b32 v112, a86\n\t"
    "v_mul_f32 v112, v112, v118\n\t"
    "v_accvgpr_write_b32 a86, v112\n\t"
    "v_accvgpr_read_b32 v113, a87\n\t"
    "v_mul_f32 v113, v113, v119\n\t"
    "v_accvgpr_write_b32 a87, v113\n\t"
    "v_accvgpr_read_b32 v112, a100\n\t"
    "v_mul_f32 v112, v112, v116\n\t"
    "v_accvgpr_write_b32 a100, v112\n\t"
    "v_accvgpr_read_b32 v113, a101\n\t"
    "v_mul_f32 v113, v113, v117\n\t"
    "v_accvgpr_write_b32 a101, v113\n\t"
    "v_accvgpr_read_b32 v112, a102\n\t"
    "v_mul_f32 v112, v112, v118\n\t"
    "v_accvgpr_write_b32 a102, v112\n\t"
    "v_accvgpr_read_b32 v113, a103\n\t"
    "v_mul_f32 v113, v113, v119\n\t"
    "v_accvgpr_write_b32 a103, v113\n\t"
    "v_accvgpr_read_b32 v112, a116\n\t"
    "v_mul_f32 v112, v112, v116\n\t"
    "v_accvgpr_write_b32 a116, v112\n\t"
    "v_accvgpr_read_b32 v113, a117\n\t"
    "v_mul_f32 v113, v113, v117\n\t"
    "v_accvgpr_write_b32 a117, v113\n\t"
    "v_accvgpr_read_b32 v112, a118\n\t"
    "v_mul_f32 v112, v112, v118\n\t"
    "v_accvgpr_write_b32 a118, v112\n\t"
    "v_accvgpr_read_b32 v113, a119\n\t"
    "v_mul_f32 v113, v113, v119\n\t"
    "v_accvgpr_write_b32 a119, v113\n\t"
    "v_accvgpr_read_b32 v112, a132\n\t"
    "v_mul_f32 v112, v112, v116\n\t"
    "v_accvgpr_write_b32 a132, v112\n\t"
    "v_accvgpr_read_b32 v113, a133\n\t"
    "v_mul_f32 v113, v113, v117\n\t"
    "v_accvgpr_write_b32 a133, v113\n\t"
    "v_accvgpr_read_b32 v112, a134\n\t"
    "v_mul_f32 v112, v112, v118\n\t"
    "v_accvgpr_write_b32 a134, v112\n\t"
    "v_accvgpr_read_b32 v113, a135\n\t"
    "v_mul_f32 v113, v113, v119\n\t"
    "v_accvgpr_write_b32 a135, v113\n\t"
    "v_accvgpr_read_b32 v112, a148\n\t"
    "v_mul_f32 v112, v112, v116\n\t"
    "v_accvgpr_write_b32 a148, v112\n\t"
    "v_accvgpr_read_b32 v113, a149\n\t"
    "v_mul_f32 v113, v113, v117\n\t"
    "v_accvgpr_write_b32 a149, v113\n\t"
    "v_accvgpr_read_b32 v112, a150\n\t"
    "v_mul_f32 v112, v112, v118\n\t"
    "v_accvgpr_write_b32 a150, v112\n\t"
    "v_accvgpr_read_b32 v113, a151\n\t"
    "v_mul_f32 v113, v113, v119\n\t"
    "v_accvgpr_write_b32 a151, v113\n\t"
    "v_accvgpr_read_b32 v112, a164\n\t"
    "v_mul_f32 v112, v112, v116\n\t"
    "v_accvgpr_write_b32 a164, v112\n\t"
    "v_accvgpr_read_b32 v113, a165\n\t"
    "v_mul_f32 v113, v113, v117\n\t"
    "v_accvgpr_write_b32 a165, v113\n\t"
    "v_accvgpr_read_b32 v112, a166\n\t"
    "v_mul_f32 v112, v112, v118\n\t"
    "v_accvgpr_write_b32 a166, v112\n\t"
    "v_accvgpr_read_b32 v113, a167\n\t"
    "v_mul_f32 v113, v113, v119\n\t"
    "v_accvgpr_write_b32 a167, v113\n\t"
    "v_accvgpr_read_b32 v112, a180\n\t"
    "v_mul_f32 v112, v112, v116\n\t"
    "v_accvgpr_write_b32 a180, v112\n\t"
    "v_accvgpr_read_b32 v113, a181\n\t"
    "v_mul_f32 v113, v113, v117\n\t"
    "v_accvgpr_write_b32 a181, v113\n\t"
    "v_accvgpr_read_b32 v112, a182\n\t"
    "v_mul_f32 v112, v112, v118\n\t"
    "v_accvgpr_write_b32 a182, v112\n\t"
    "v_accvgpr_read_b32 v113, a183\n\t"
    "v_mul_f32 v113, v113, v119\n\t"
    "v_accvgpr_write_b32 a183, v113\n\t"
    "v_max3_f32 v112, v40, v41, v42\n\t"
    "v_max3_f32 v112, v112, v43, v56\n\t"
    "v_max3_f32 v112, v112, v57, v58\n\t"
    "v_max_f32 v112, v112, v59\n\t"
    "v_mov_b32 v113, v112\n\t"
    "s_nop 1\n\t"
    "v_permlane32_swap_b32 v112, v113\n\t"
    "s_nop 1\n\t"
    "v_max_f32 v112, v112, v113\n\t"
    "v_mov_b32 v113, v112\n\t"
    "s_nop 1\n\t"
    "v_permlane16_swap_b32 v112, v113\n\t"
    "s_nop 1\n\t"
    "v_max_f32 v112, v112, v113\n\t"
    "v_max_f32 v112, 0, v112\n\t"
    "v_sub_f32 v136, v136, v112\n\t"
    "v_sub_f32 v137, v137, v112\n\t"
    "v_sub_f32 v138, v138, v112\n\t"
    "v_sub_f32 v139, v139, v112\n\t"
    "v_sub_f32 v40, v40, v112\n\t"
    "v_sub_f32 v41, v41, v112\n\t"
    "v_sub_f32 v42, v42, v112\n\t"
    "v_sub_f32 v43, v43, v112\n\t"
    "v_sub_f32 v56, v56, v112\n\t"
    "v_sub_f32 v57, v57, v112\n\t"
    "v_sub_f32 v58, v58, v112\n\t"
    "v_sub_f32 v59, v59, v112\n\t"
    "v_sub_f32 v72, v72, v112\n\t"
    "v_sub_f32 v73, v73, v112\n\t"
    "v_sub_f32 v74, v74, v112\n\t"
    "v_sub_f32 v75, v75, v112\n\t"
    "v_sub_f32 v88, v88, v112\n\t"
    "v_sub_f32 v89, v89, v112\n\t"
    "v_sub_f32 v90, v90, v112\n\t"
    "v_sub_f32 v91, v91, v112\n\t"
    "v_exp_f32 v114, -v112\n\t"
    "s_nop 0\n\t"
    "v_mul_f32 v170, v170, v114\n\t"
    "ds_bpermute_b32 v116, v115, v114 offset:0\n\t"
    "ds_bpermute_b32 v117, v115, v114 offset:4\n\t"
    "ds_bpermute_b32 v118, v115, v114 offset:8\n\t"
    "ds_bpermute_b32 v119, v115, v114 offset:12\n\t"
    "v_exp_f32 v176, v40\n\t"
    "v_exp_f32 v177, v41\n\t"
    "s_nop 0\n\t"
    "v_add_f32 v174, v176, v177\n\t"
    "v_cvt_pk_bf16_f32 v104, v176, v177\n\t"
    "v_exp_f32 v178, v42\n\t"
    "v_exp_f32 v179, v43\n\t"
    "s_nop 0\n\t"
    "v_add_f32 v174, v174, v178\n\t"
    "v_add_f32 v174, v174, v179\n\t"
    "v_cvt_pk_bf16_f32 v105, v178, v179\n\t"
    "v_exp_f32 v176, v56\n\t"
    "v_exp_f32 v177, v57\n\t"
    "s_nop 0\n\t"
    "v_add_f32 v174, v174, v176\n\t"
    "v_add_f32 v174, v174, v177\n\t"
    "v_cvt_pk_bf16_f32 v106, v176, v177\n\t"
    "v_exp_f32 v178, v58\n\t"
    "v_exp_f32 v179, v59\n\t"
    "s_nop 0\n\t"
    "v_add_f32 v174, v174, v178\n\t"
    "v_add_f32 v174, v174, v179\n\t"
    "v_cvt_pk_bf16_f32 v107, v178, v179\n\t"
    "s_waitcnt lgkmcnt(0)\n\t"
    "v_accvgpr_read_b32 v112, a8\n\t"
    "v_mul_f32 v112, v112, v116\n\t"
    "v_accvgpr_write_b32 a8, v112\n\t"
    "v_accvgpr_read_b32 v113, a9\n\t"
    "v_mul_f32 v113, v113, v117\n\t"
    "v_accvgpr_write_b32 a9, v113\n\t"
    "v_accvgpr_read_b32 v112, a10\n\t"
    "v_mul_f32 v112, v112, v118\n\t"
    "v_accvgpr_write_b32 a10, v112\n\t"
    "v_accvgpr_read_b32 v113, a11\n\t"
    "v_mul_f32 v113, v113, v119\n\t"
    "v_accvgpr_write_b32 a11, v113\n\t"
    "v_accvgpr_read_b32 v112, a24\n\t"
    "v_mul_f32 v112, v112, v116\n\t"
    "v_accvgpr_write_b32 a24, v112\n\t"
    "v_accvgpr_read_b32 v113, a25\n\t"
    "v_mul_f32 v113, v113, v117\n\t"
    "v_accvgpr_write_b32 a25, v113\n\t"
    "v_accvgpr_read_b32 v112, a26\n\t"
    "v_mul_f32 v112, v112, v118\n\t"
    "v_accvgpr_write_b32 a26, v112\n\t"
    "v_accvgpr_read_b32 v113, a27\n\t"
    "v_mul_f32 v113, v113, v119\n\t"
    "v_accvgpr_write_b32 a27, v113\n\t"
    "v_accvgpr_read_b32 v112, a40\n\t"
    "v_mul_f32 v112, v112, v116\n\t"
    "v_accvgpr_write_b32 a40, v112\n\t"
    "v_accvgpr_read_b32 v113, a41\n\t"
    "v_mul_f32 v113, v113, v117\n\t"
    "v_accvgpr_write_b32 a41, v113\n\t"
    "v_accvgpr_read_b32 v112, a42\n\t"
    "v_mul_f32 v112, v112, v118\n\t"
    "v_accvgpr_write_b32 a42, v112\n\t"
    "v_accvgpr_read_b32 v113, a43\n\t"
    "v_mul_f32 v113, v113, v119\n\t"
    "v_accvgpr_write_b32 a43, v113\n\t"
    "v_accvgpr_read_b32 v112, a56\n\t"
    "v_mul_f32 v112, v112, v116\n\t"
    "v_accvgpr_write_b32 a56, v112\n\t"
    "v_accvgpr_read_b32 v113, a57\n\t"
    "v_mul_f32 v113, v113, v117\n\t"
    "v_accvgpr_write_b32 a57, v113\n\t"
    "v_accvgpr_read_b32 v112, a58\n\t"
    "v_mul_f32 v112, v112, v118\n\t"
    "v_accvgpr_write_b32 a58, v112\n\t"
    "v_accvgpr_read_b32 v113, a59\n\t"
    "v_mul_f32 v113, v113, v119\n\t"
    "v_accvgpr_write_b32 a59, v113\n\t"
    "v_accvgpr_read_b32 v112, a72\n\t"
    "v_mul_f32 v112, v112, v116\n\t"
    "v_accvgpr_write_b32 a72, v112\n\t"
    "v_accvgpr_read_b32 v113, a73\n\t"
    "v_mul_f32 v113, v113, v117\n\t"
    "v_accvgpr_write_b32 a73, v113\n\t"
    "v_accvgpr_read_b32 v112, a74\n\t"
    "v_mul_f32 v112, v112, v118\n\t"
    "v_accvgpr_write_b32 a74, v112\n\t"
    "v_accvgpr_read_b32 v113, a75\n\t"
    "v_mul_f32 v113, v113, v119\n\t"
    "v_accvgpr_write_b32 a75, v113\n\t"
    "v_accvgpr_read_b32 v112, a88\n\t"
    "v_mul_f32 v112, v112, v116\n\t"
    "v_accvgpr_write_b32 a88, v112\n\t"
    "v_accvgpr_read_b32 v113, a89\n\t"
    "v_mul_f32 v113, v113, v117\n\t"
    "v_accvgpr_write_b32 a89, v113\n\t"
    "v_accvgpr_read_b32 v112, a90\n\t"
    "v_mul_f32 v112, v112, v118\n\t"
    "v_accvgpr_write_b32 a90, v112\n\t"
    "v_accvgpr_read_b32 v113, a91\n\t"
    "v_mul_f32 v113, v113, v119\n\t"
    "v_accvgpr_write_b32 a91, v113\n\t"
    "v_accvgpr_read_b32 v112, a104\n\t"
    "v_mul_f32 v112, v112, v116\n\t"
    "v_accvgpr_write_b32 a104, v112\n\t"
    "v_accvgpr_read_b32 v113, a105\n\t"
    "v_mul_f32 v113, v113, v117\n\t"
    "v_accvgpr_write_b32 a105, v113\n\t"
    "v_accvgpr_read_b32 v112, a106\n\t"
    "v_mul_f32 v112, v112, v118\n\t"
    "v_accvgpr_write_b32 a106, v112\n\t"
    "v_accvgpr_read_b32 v113, a107\n\t"
    "v_mul_f32 v113, v113, v119\n\t"
    "v_accvgpr_write_b32 a107, v113\n\t"
    "v_accvgpr_read_b32 v112, a120\n\t"
    "v_mul_f32 v112, v112, v116\n\t"
    "v_accvgpr_write_b32 a120, v112\n\t"
    "v_accvgpr_read_b32 v113, a121\n\t"
    "v_mul_f32 v113, v113, v117\n\t"
    "v_accvgpr_write_b32 a121, v113\n\t"
    "v_accvgpr_read_b32 v112, a122\n\t"
    "v_mul_f32 v112, v112, v118\n\t"
    "v_accvgpr_write_b32 a122, v112\n\t"
    "v_accvgpr_read_b32 v113, a123\n\t"
    "v_mul_f32 v113, v113, v119\n\t"
    "v_accvgpr_write_b32 a123, v113\n\t"
    "v_accvgpr_read_b32 v112, a136\n\t"
    "v_mul_f32 v112, v112, v116\n\t"
    "v_accvgpr_write_b32 a136, v112\n\t"
    "v_accvgpr_read_b32 v113, a137\n\t"
    "v_mul_f32 v113, v113, v117\n\t"
    "v_accvgpr_write_b32 a137, v113\n\t"
    "v_accvgpr_read_b32 v112, a138\n\t"
    "v_mul_f32 v112, v112, v118\n\t"
    "v_accvgpr_write_b32 a138, v112\n\t"
    "v_accvgpr_read_b32 v113, a139\n\t"
    "v_mul_f32 v113, v113, v119\n\t"
    "v_accvgpr_write_b32 a139, v113\n\t"
    "v_accvgpr_read_b32 v112, a152\n\t"
    "v_mul_f32 v112, v112, v116\n\t"
    "v_accvgpr_write_b32 a152, v112\n\t"
    "v_accvgpr_read_b32 v113, a153\n\t"
    "v_mul_f32 v113, v113, v117\n\t"
    "v_accvgpr_write_b32 a153, v113\n\t"
    "v_accvgpr_read_b32 v112, a154\n\t"
    "v_mul_f32 v112, v112, v118\n\t"
    "v_accvgpr_write_b32 a154, v112\n\t"
    "v_accvgpr_read_b32 v113, a155\n\t"
    "v_mul_f32 v113, v113, v119\n\t"
    "v_accvgpr_write_b32 a155, v113\n\t"
    "v_accvgpr_read_b32 v112, a168\n\t"
    "v_mul_f32 v112, v112, v116\n\t"
    "v_accvgpr_write_b32 a168, v112\n\t"
    "v_accvgpr_read_b32 v113, a169\n\t"
    "v_mul_f32 v113, v113, v117\n\t"
    "v_accvgpr_write_b32 a169, v113\n\t"
    "v_accvgpr_read_b32 v112, a170\n\t"
    "v_mul_f32 v112, v112, v118\n\t"
    "v_accvgpr_write_b32 a170, v112\n\t"
    "v_accvgpr_read_b32 v113, a171\n\t"
    "v_mul_f32 v113, v113, v119\n\t"
    "v_accvgpr_write_b32 a171, v113\n\t"
    "v_accvgpr_read_b32 v112, a184\n\t"
    "v_mul_f32 v112, v112, v116\n\t"
    "v_accvgpr_write_b32 a184, v112\n\t"
    "v_accvgpr_read_b32 v113, a185\n\t"
    "v_mul_f32 v113, v113, v117\n\t"
    "v_accvgpr_write_b32 a185, v113\n\t"
    "v_accvgpr_read_b32 v112, a186\n\t"
    "v_mul_f32 v112, v112, v118\n\t"
    "v_accvgpr_write_b32 a186, v112\n\t"
    "v_accvgpr_read_b32 v113, a187\n\t"
    "v_mul_f32 v113, v113, v119\n\t"
    "v_accvgpr_write_b32 a187, v113\n\t"
    "v_max3_f32 v112, v44, v45, v46\n\t"
    "v_max3_f32 v112, v112, v47, v60\n\t"
    "v_max3_f32 v112, v112, v61, v62\n\t"
    "v_max_f32 v112, v112, v63\n\t"
    "v_mov_b32 v113, v112\n\t"
    "s_nop 1\n\t"
    "v_permlane32_swap_b32 v112, v113\n\t"
    "s_nop 1\n\t"
    "v_max_f32 v112, v112, v113\n\t"
    "v_mov_b32 v113, v112\n\t"
    "s_nop 1\n\t"
    "v_permlane16_swap_b32 v112, v113\n\t"
    "s_nop 1\n\t"
    "v_max_f32 v112, v112, v113\n\t"
    "v_max_f32 v112, 0, v112\n\t"
    "v_sub_f32 v140, v140, v112\n\t"
    "v_sub_f32 v141, v141, v112\n\t"
    "v_sub_f32 v142, v142, v112\n\t"
    "v_sub_f32 v143, v143, v112\n\t"
    "v_sub_f32 v44, v44, v112\n\t"
    "v_sub_f32 v45, v45, v112\n\t"
    "v_sub_f32 v46, v46, v112\n\t"
    "v_sub_f32 v47, v47, v112\n\t"
    "v_sub_f32 v60, v60, v112\n\t"
    "v_sub_f32 v61, v61, v112\n\t"
    "v_sub_f32 v62, v62, v112\n\t"
    "v_sub_f32 v63, v63, v112\n\t"
    "v_sub_f32 v76, v76, v112\n\t"
    "v_sub_f32 v77, v77, v112\n\t"
    "v_sub_f32 v78, v78, v112\n\t"
    "v_sub_f32 v79, v79, v112\n\t"
    "v_sub_f32 v92, v92, v112\n\t"
    "v_sub_f32 v93, v93, v112\n\t"
    "v_sub_f32 v94, v94, v112\n\t"
    "v_sub_f32 v95, v95, v112\n\t"
    "v_exp_f32 v114, -v112\n\t"
    "s_nop 0\n\t"
    "v_mul_f32 v171, v171, v114\n\t"
    "ds_bpermute_b32 v116, v115, v114 offset:0\n\t"
    "ds_bpermute_b32 v117, v115, v114 offset:4\n\t"
    "ds_bpermute_b32 v118, v115, v114 offset:8\n\t"
    "ds_bpermute_b32 v119, v115, v114 offset:12\n\t"
    "v_exp_f32 v176, v44\n\t"
    "v_exp_f32 v177, v45\n\t"
    "s_nop 0\n\t"
    "v_add_f32 v175, v176, v177\n\t"
    "v_cvt_pk_bf16_f32 v108, v176, v177\n\t"
    "v_exp_f32 v178, v46\n\t"
    "v_exp_f32 v179, v47\n\t"
    "s_nop 0\n\t"
    "v_add_f32 v175, v175, v178\n\t"
    "v_add_f32 v175, v175, v179\n\t"
    "v_cvt_pk_bf16_f32 v109, v178, v179\n\t"
    "v_exp_f32 v176, v60\n\t"
    "v_exp_f32 v177, v61\n\t"
    "s_nop 0\n\t"
    "v_add_f32 v175, v175, v176\n\t"
    "v_add_f32 v175, v175, v177\n\t"
    "v_cvt_pk_bf16_f32 v110, v176, v177\n\t"
    "v_exp_f32 v178, v62\n\t"
    "v_exp_f32 v179, v63\n\t"
    "s_nop 0\n\t"
    "v_add_f32 v175, v175, v178\n\t"
    "v_add_f32 v175, v175, v179\n\t"
    "v_cvt_pk_bf16_f32 v111, v178, v179\n\t"
    "s_waitcnt lgkmcnt(0)\n\t"
    "v_accvgpr_read_b32 v112, a12\n\t"
    "v_mul_f32 v112, v112, v116\n\t"
    "v_accvgpr_write_b32 a12, v112\n\t"
    "v_accvgpr_read_b32 v113, a13\n\t"
    "v_mul_f32 v113, v113, v117\n\t"
    "v_accvgpr_write_b32 a13, v113\n\t"
    "v_accvgpr_read_b32 v112, a14\n\t"
    "v_mul_f32 v112, v112, v118\n\t"
    "v_accvgpr_write_b32 a14, v112\n\t"
    "v_accvgpr_read_b32 v113, a15\n\t"
    "v_mul_f32 v113, v113, v119\n\t"
    "v_accvgpr_write_b32 a15, v113\n\t"
    "v_accvgpr_read_b32 v112, a28\n\t"
    "v_mul_f32 v112, v112, v116\n\t"
    "v_accvgpr_write_b32 a28, v112\n\t"
    "v_accvgpr_read_b32 v113, a29\n\t"
    "v_mul_f32 v113, v113, v117\n\t"
    "v_accvgpr_write_b32 a29, v113\n\t"
    "v_accvgpr_read_b32 v112, a30\n\t"
    "v_mul_f32 v112, v112, v118\n\t"
    "v_accvgpr_write_b32 a30, v112\n\t"
    "v_accvgpr_read_b32 v113, a31\n\t"
    "v_mul_f32 v113, v113, v119\n\t"
    "v_accvgpr_write_b32 a31, v113\n\t"
    "v_accvgpr_read_b32 v112, a44\n\t"
    "v_mul_f32 v112, v112, v116\n\t"
    "v_accvgpr_write_b32 a44, v112\n\t"
    "v_accvgpr_read_b32 v113, a45\n\t"
    "v_mul_f32 v113, v113, v117\n\t"
    "v_accvgpr_write_b32 a45, v113\n\t"
    "v_accvgpr_read_b32 v112, a46\n\t"
    "v_mul_f32 v112, v112, v118\n\t"
    "v_accvgpr_write_b32 a46, v112\n\t"
    "v_accvgpr_read_b32 v113, a47\n\t"
    "v_mul_f32 v113, v113, v119\n\t"
    "v_accvgpr_write_b32 a47, v113\n\t"
    "v_accvgpr_read_b32 v112, a60\n\t"
    "v_mul_f32 v112, v112, v116\n\t"
    "v_accvgpr_write_b32 a60, v112\n\t"
    "v_accvgpr_read_b32 v113, a61\n\t"
    "v_mul_f32 v113, v113, v117\n\t"
    "v_accvgpr_write_b32 a61, v113\n\t"
    "v_accvgpr_read_b32 v112, a62\n\t"
    "v_mul_f32 v112, v112, v118\n\t"
    "v_accvgpr_write_b32 a62, v112\n\t"
    "v_accvgpr_read_b32 v113, a63\n\t"
    "v_mul_f32 v113, v113, v119\n\t"
    "v_accvgpr_write_b32 a63, v113\n\t"
    "v_accvgpr_read_b32 v112, a76\n\t"
    "v_mul_f32 v112, v112, v116\n\t"
    "v_accvgpr_write_b32 a76, v112\n\t"
    "v_accvgpr_read_b32 v113, a77\n\t"
    "v_mul_f32 v113, v113, v117\n\t"
    "v_accvgpr_write_b32 a77, v113\n\t"
    "v_accvgpr_read_b32 v112, a78\n\t"
    "v_mul_f32 v112, v112, v118\n\t"
    "v_accvgpr_write_b32 a78, v112\n\t"
    "v_accvgpr_read_b32 v113, a79\n\t"
    "v_mul_f32 v113, v113, v119\n\t"
    "v_accvgpr_write_b32 a79, v113\n\t"
    "v_accvgpr_read_b32 v112, a92\n\t"
    "v_mul_f32 v112, v112, v116\n\t"
    "v_accvgpr_write_b32 a92, v112\n\t"
    "v_accvgpr_read_b32 v113, a93\n\t"
    "v_mul_f32 v113, v113, v117\n\t"
    "v_accvgpr_write_b32 a93, v113\n\t"
    "v_accvgpr_read_b32 v112, a94\n\t"
    "v_mul_f32 v112, v112, v118\n\t"
    "v_accvgpr_write_b32 a94, v112\n\t"
    "v_accvgpr_read_b32 v113, a95\n\t"
    "v_mul_f32 v113, v113, v119\n\t"
    "v_accvgpr_write_b32 a95, v113\n\t"
    "v_accvgpr_read_b32 v112, a108\n\t"
    "v_mul_f32 v112, v112, v116\n\t"
    "v_accvgpr_write_b32 a108, v112\n\t"
    "v_accvgpr_read_b32 v113, a109\n\t"
    "v_mul_f32 v113, v113, v117\n\t"
    "v_accvgpr_write_b32 a109, v113\n\t"
    "v_accvgpr_read_b32 v112, a110\n\t"
    "v_mul_f32 v112, v112, v118\n\t"
    "v_accvgpr_write_b32 a110, v112\n\t"
    "v_accvgpr_read_b32 v113, a111\n\t"
    "v_mul_f32 v113, v113, v119\n\t"
    "v_accvgpr_write_b32 a111, v113\n\t"
    "v_accvgpr_read_b32 v112, a124\n\t"
    "v_mul_f32 v112, v112, v116\n\t"
    "v_accvgpr_write_b32 a124, v112\n\t"
    "v_accvgpr_read_b32 v113, a125\n\t"
    "v_mul_f32 v113, v113, v117\n\t"
    "v_accvgpr_write_b32 a125, v113\n\t"
    "v_accvgpr_read_b32 v112, a126\n\t"
    "v_mul_f32 v112, v112, v118\n\t"
    "v_accvgpr_write_b32 a126, v112\n\t"
    "v_accvgpr_read_b32 v113, a127\n\t"
    "v_mul_f32 v113, v113, v119\n\t"
    "v_accvgpr_write_b32 a127, v113\n\t"
    "v_accvgpr_read_b32 v112, a140\n\t"
    "v_mul_f32 v112, v112, v116\n\t"
    "v_accvgpr_write_b32 a140, v112\n\t"
    "v_accvgpr_read_b32 v113, a141\n\t"
    "v_mul_f32 v113, v113, v117\n\t"
    "v_accvgpr_write_b32 a141, v113\n\t"
    "v_accvgpr_read_b32 v112, a142\n\t"
    "v_mul_f32 v112, v112, v118\n\t"
    "v_accvgpr_write_b32 a142, v112\n\t"
    "v_accvgpr_read_b32 v113, a143\n\t"
    "v_mul_f32 v113, v113, v119\n\t"
    "v_accvgpr_write_b32 a143, v113\n\t"
    "v_accvgpr_read_b32 v112, a156\n\t"
    "v_mul_f32 v112, v112, v116\n\t"
    "v_accvgpr_write_b32 a156, v112\n\t"
    "v_accvgpr_read_b32 v113, a157\n\t"
    "v_mul_f32 v113, v113, v117\n\t"
    "v_accvgpr_write_b32 a157, v113\n\t"
    "v_accvgpr_read_b32 v112, a158\n\t"
    "v_mul_f32 v112, v112, v118\n\t"
    "v_accvgpr_write_b32 a158, v112\n\t"
    "v_accvgpr_read_b32 v113, a159\n\t"
    "v_mul_f32 v113, v113, v119\n\t"
    "v_accvgpr_write_b32 a159, v113\n\t"
    "v_accvgpr_read_b32 v112, a172\n\t"
    "v_mul_f32 v112, v112, v116\n\t"
    "v_accvgpr_write_b32 a172, v112\n\t"
    "v_accvgpr_read_b32 v113, a173\n\t"
    "v_mul_f32 v113, v113, v117\n\t"
    "v_accvgpr_write_b32 a173, v113\n\t"
    "v_accvgpr_read_b32 v112, a174\n\t"
    "v_mul_f32 v112, v112, v118\n\t"
    "v_accvgpr_write_b32 a174, v112\n\t"
    "v_accvgpr_read_b32 v113, a175\n\t"
    "v_mul_f32 v113, v113, v119\n\t"
    "v_accvgpr_write_b32 a175, v113\n\t"
    "v_accvgpr_read_b32 v112, a188\n\t"
    "v_mul_f32 v112, v112, v116\n\t"
    "v_accvgpr_write_b32 a188, v112\n\t"
    "v_accvgpr_read_b32 v113, a189\n\t"
    "v_mul_f32 v113, v113, v117\n\t"
    "v_accvgpr_write_b32 a189, v113\n\t"
    "v_accvgpr_read_b32 v112, a190\n\t"
    "v_mul_f32 v112, v112, v118\n\t"
    "v_accvgpr_write_b32 a190, v112\n\t"
    "v_accvgpr_read_b32 v113, a191\n\t"
    "v_mul_f32 v113, v113, v119\n\t"
    "v_accvgpr_write_b32 a191, v113\n\t"
    "s_nop 1\n\t"
    "s_setpc_b64 s[76:77]\n\t"
    "L_slow_1:\n\t"
    "s_nop 7\n\t"
    "s_nop 7\n\t"
    "v_lshrrev_b32 v99, 4, v195\n\t"
    "v_and_b32 v99, 48, v99\n\t"
    "v_max3_f32 v96, v64, v65, v66\n\t"
    "v_max3_f32 v96, v96, v67, v80\n\t"
    "v_max3_f32 v96, v96, v81, v82\n\t"
    "v_max_f32 v96, v96, v83\n\t"
    "v_mov_b32 v97, v96\n\t"
    "s_nop 1\n\t"
    "v_permlane32_swap_b32 v96, v97\n\t"
    "s_nop 1\n\t"
    "v_max_f32 v96, v96, v97\n\t"
    "v_mov_b32 v97, v96\n\t"
    "s_nop 1\n\t"
    "v_permlane16_swap_b32 v96, v97\n\t"
    "s_nop 1\n\t"
    "v_max_f32 v96, v96, v97\n\t"
    "v_max_f32 v96, 0, v96\n\t"
    "v_sub_f32 v128, v128, v96\n\t"
    "v_sub_f32 v129, v129, v96\n\t"
    "v_sub_f32 v130, v130, v96\n\t"
    "v_sub_f32 v131, v131, v96\n\t"
    "v_sub_f32 v64, v64, v96\n\t"
    "v_sub_f32 v65, v65, v96\n\t"
    "v_sub_f32 v66, v66, v96\n\t"
    "v_sub_f32 v67, v67, v96\n\t"
    "v_sub_f32 v80, v80, v96\n\t"
    "v_sub_f32 v81, v81, v96\n\t"
    "v_sub_f32 v82, v82, v96\n\t"
    "v_sub_f32 v83, v83, v96\n\t"
    "v_sub_f32 v32, v32, v96\n\t"
    "v_sub_f32 v33, v33, v96\n\t"
    "v_sub_f32 v34, v34, v96\n\t"
    "v_sub_f32 v35, v35, v96\n\t"
    "v_sub_f32 v48, v48, v96\n\t"
    "v_sub_f32 v49, v49, v96\n\t"
    "v_sub_f32 v50, v50, v96\n\t"
    "v_sub_f32 v51, v51, v96\n\t"
    "v_exp_f32 v98, -v96\n\t"
    "s_nop 0\n\t"
    "v_mul_f32 v168, v168, v98\n\t"
    "ds_bpermute_b32 v100, v99, v98 offset:0\n\t"
    "ds_bpermute_b32 v101, v99, v98 offset:4\n\t"
    "ds_bpermute_b32 v102, v99, v98 offset:8\n\t"
    "ds_bpermute_b32 v103, v99, v98 offset:12\n\t"
    "v_exp_f32 v176, v64\n\t"
    "v_exp_f32 v177, v65\n\t"
    "s_nop 0\n\t"
    "v_add_f32 v172, v176, v177\n\t"
    "v_cvt_pk_bf16_f32 v112, v176, v177\n\t"
    "v_exp_f32 v178, v66\n\t"
    "v_exp_f32 v179, v67\n\t"
    "s_nop 0\n\t"
    "v_add_f32 v172, v172, v178\n\t"
    "v_add_f32 v172, v172, v179\n\t"
    "v_cvt_pk_bf16_f32 v113, v178, v179\n\t"
    "v_exp_f32 v176, v80\n\t"
    "v_exp_f32 v177, v81\n\t"
    "s_nop 0\n\t"
    "v_add_f32 v172, v172, v176\n\t"
    "v_add_f32 v172, v172, v177\n\t"
    "v_cvt_pk_bf16_f32 v114, v176, v177\n\t"
    "v_exp_f32 v178, v82\n\t"
    "v_exp_f32 v179, v83\n\t"
    "s_nop 0\n\t"
    "v_add_f32 v172, v172, v178\n\t"
    "v_add_f32 v172, v172, v179\n\t"
    "v_cvt_pk_bf16_f32 v115, v178, v179\n\t"
    "s_waitcnt lgkmcnt(0)\n\t"
    "v_accvgpr_read_b32 v96, a0\n\t"
    "v_mul_f32 v96, v96, v100\n\t"
    "v_accvgpr_write_b32 a0, v96\n\t"
    "v_accvgpr_read_b32 v97, a1\n\t"
    "v_mul_f32 v97, v97, v101\n\t"
    "v_accvgpr_write_b32 a1, v97\n\t"
    "v_accvgpr_read_b32 v96, a2\n\t"
    "v_mul_f32 v96, v96, v102\n\t"
    "v_accvgpr_write_b32 a2, v96\n\t"
    "v_accvgpr_read_b32 v97, a3\n\t"
    "v_mul_f32 v97, v97, v103\n\t"
    "v_accvgpr_write_b32 a3, v97\n\t"
    "v_accvgpr_read_b32 v96, a16\n\t"
    "v_mul_f32 v96, v96, v100\n\t"
    "v_accvgpr_write_b32 a16, v96\n\t"
    "v_accvgpr_read_b32 v97, a17\n\t"
    "v_mul_f32 v97, v97, v101\n\t"
    "v_accvgpr_write_b32 a17, v97\n\t"
    "v_accvgpr_read_b32 v96, a18\n\t"
    "v_mul_f32 v96, v96, v102\n\t"
    "v_accvgpr_write_b32 a18, v96\n\t"
    "v_accvgpr_read_b32 v97, a19\n\t"
    "v_mul_f32 v97, v97, v103\n\t"
    "v_accvgpr_write_b32 a19, v97\n\t"
    "v_accvgpr_read_b32 v96, a32\n\t"
    "v_mul_f32 v96, v96, v100\n\t"
    "v_accvgpr_write_b32 a32, v96\n\t"
    "v_accvgpr_read_b32 v97, a33\n\t"
    "v_mul_f32 v97, v97, v101\n\t"
    "v_accvgpr_write_b32 a33, v97\n\t"
    "v_accvgpr_read_b32 v96, a34\n\t"
    "v_mul_f32 v96, v96, v102\n\t"
    "v_accvgpr_write_b32 a34, v96\n\t"
    "v_accvgpr_read_b32 v97, a35\n\t"
    "v_mul_f32 v97, v97, v103\n\t"
    "v_accvgpr_write_b32 a35, v97\n\t"
    "v_accvgpr_read_b32 v96, a48\n\t"
    "v_mul_f32 v96, v96, v100\n\t"
    "v_accvgpr_write_b32 a48, v96\n\t"
    "v_accvgpr_read_b32 v97, a49\n\t"
    "v_mul_f32 v97, v97, v101\n\t"
    "v_accvgpr_write_b32 a49, v97\n\t"
    "v_accvgpr_read_b32 v96, a50\n\t"
    "v_mul_f32 v96, v96, v102\n\t"
    "v_accvgpr_write_b32 a50, v96\n\t"
    "v_accvgpr_read_b32 v97, a51\n\t"
    "v_mul_f32 v97, v97, v103\n\t"
    "v_accvgpr_write_b32 a51, v97\n\t"
    "v_accvgpr_read_b32 v96, a64\n\t"
    "v_mul_f32 v96, v96, v100\n\t"
    "v_accvgpr_write_b32 a64, v96\n\t"
    "v_accvgpr_read_b32 v97, a65\n\t"
    "v_mul_f32 v97, v97, v101\n\t"
    "v_accvgpr_write_b32 a65, v97\n\t"
    "v_accvgpr_read_b32 v96, a66\n\t"
    "v_mul_f32 v96, v96, v102\n\t"
    "v_accvgpr_write_b32 a66, v96\n\t"
    "v_accvgpr_read_b32 v97, a67\n\t"
    "v_mul_f32 v97, v97, v103\n\t"
    "v_accvgpr_write_b32 a67, v97\n\t"
    "v_accvgpr_read_b32 v96, a80\n\t"
    "v_mul_f32 v96, v96, v100\n\t"
    "v_accvgpr_write_b32 a80, v96\n\t"
    "v_accvgpr_read_b32 v97, a81\n\t"
    "v_mul_f32 v97, v97, v101\n\t"
    "v_accvgpr_write_b32 a81, v97\n\t"
    "v_accvgpr_read_b32 v96, a82\n\t"
    "v_mul_f32 v96, v96, v102\n\t"
    "v_accvgpr_write_b32 a82, v96\n\t"
    "v_accvgpr_read_b32 v97, a83\n\t"
    "v_mul_f32 v97, v97, v103\n\t"
    "v_accvgpr_write_b32 a83, v97\n\t"
    "v_accvgpr_read_b32 v96, a96\n\t"
    "v_mul_f32 v96, v96, v100\n\t"
    "v_accvgpr_write_b32 a96, v96\n\t"
    "v_accvgpr_read_b32 v97, a97\n\t"
    "v_mul_f32 v97, v97, v101\n\t"
    "v_accvgpr_write_b32 a97, v97\n\t"
    "v_accvgpr_read_b32 v96, a98\n\t"
    "v_mul_f32 v96, v96, v102\n\t"
    "v_accvgpr_write_b32 a98, v96\n\t"
    "v_accvgpr_read_b32 v97, a99\n\t"
    "v_mul_f32 v97, v97, v103\n\t"
    "v_accvgpr_write_b32 a99, v97\n\t"
    "v_accvgpr_read_b32 v96, a112\n\t"
    "v_mul_f32 v96, v96, v100\n\t"
    "v_accvgpr_write_b32 a112, v96\n\t"
    "v_accvgpr_read_b32 v97, a113\n\t"
    "v_mul_f32 v97, v97, v101\n\t"
    "v_accvgpr_write_b32 a113, v97\n\t"
    "v_accvgpr_read_b32 v96, a114\n\t"
    "v_mul_f32 v96, v96, v102\n\t"
    "v_accvgpr_write_b32 a114, v96\n\t"
    "v_accvgpr_read_b32 v97, a115\n\t"
    "v_mul_f32 v97, v97, v103\n\t"
    "v_accvgpr_write_b32 a115, v97\n\t"
    "v_accvgpr_read_b32 v96, a128\n\t"
    "v_mul_f32 v96, v96, v100\n\t"
    "v_accvgpr_write_b32 a128, v96\n\t"
    "v_accvgpr_read_b32 v97, a129\n\t"
    "v_mul_f32 v97, v97, v101\n\t"
    "v_accvgpr_write_b32 a129, v97\n\t"
    "v_accvgpr_read_b32 v96, a130\n\t"
    "v_mul_f32 v96, v96, v102\n\t"
    "v_accvgpr_write_b32 a130, v96\n\t"
    "v_accvgpr_read_b32 v97, a131\n\t"
    "v_mul_f32 v97, v97, v103\n\t"
    "v_accvgpr_write_b32 a131, v97\n\t"
    "v_accvgpr_read_b32 v96, a144\n\t"
    "v_mul_f32 v96, v96, v100\n\t"
    "v_accvgpr_write_b32 a144, v96\n\t"
    "v_accvgpr_read_b32 v97, a145\n\t"
    "v_mul_f32 v97, v97, v101\n\t"
    "v_accvgpr_write_b32 a145, v97\n\t"
    "v_accvgpr_read_b32 v96, a146\n\t"
    "v_mul_f32 v96, v96, v102\n\t"
    "v_accvgpr_write_b32 a146, v96\n\t"
    "v_accvgpr_read_b32 v97, a147\n\t"
    "v_mul_f32 v97, v97, v103\n\t"
    "v_accvgpr_write_b32 a147, v97\n\t"
    "v_accvgpr_read_b32 v96, a160\n\t"
    "v_mul_f32 v96, v96, v100\n\t"
    "v_accvgpr_write_b32 a160, v96\n\t"
    "v_accvgpr_read_b32 v97, a161\n\t"
    "v_mul_f32 v97, v97, v101\n\t"
    "v_accvgpr_write_b32 a161, v97\n\t"
    "v_accvgpr_read_b32 v96, a162\n\t"
    "v_mul_f32 v96, v96, v102\n\t"
    "v_accvgpr_write_b32 a162, v96\n\t"
    "v_accvgpr_read_b32 v97, a163\n\t"
    "v_mul_f32 v97, v97, v103\n\t"
    "v_accvgpr_write_b32 a163, v97\n\t"
    "v_accvgpr_read_b32 v96, a176\n\t"
    "v_mul_f32 v96, v96, v100\n\t"
    "v_accvgpr_write_b32 a176, v96\n\t"
    "v_accvgpr_read_b32 v97, a177\n\t"
    "v_mul_f32 v97, v97, v101\n\t"
    "v_accvgpr_write_b32 a177, v97\n\t"
    "v_accvgpr_read_b32 v96, a178\n\t"
    "v_mul_f32 v96, v96, v102\n\t"
    "v_accvgpr_write_b32 a178, v96\n\t"
    "v_accvgpr_read_b32 v97, a179\n\t"
    "v_mul_f32 v97, v97, v103\n\t"
    "v_accvgpr_write_b32 a179, v97\n\t"
    "v_max3_f32 v96, v68, v69, v70\n\t"
    "v_max3_f32 v96, v96, v71, v84\n\t"
    "v_max3_f32 v96, v96, v85, v86\n\t"
    "v_max_f32 v96, v96, v87\n\t"
    "v_mov_b32 v97, v96\n\t"
    "s_nop 1\n\t"
    "v_permlane32_swap_b32 v96, v97\n\t"
    "s_nop 1\n\t"
    "v_max_f32 v96, v96, v97\n\t"
    "v_mov_b32 v97, v96\n\t"
    "s_nop 1\n\t"
    "v_permlane16_swap_b32 v96, v97\n\t"
    "s_nop 1\n\t"
    "v_max_f32 v96, v96, v97\n\t"
    "v_max_f32 v96, 0, v96\n\t"
    "v_sub_f32 v132, v132, v96\n\t"
    "v_sub_f32 v133, v133, v96\n\t"
    "v_sub_f32 v134, v134, v96\n\t"
    "v_sub_f32 v135, v135, v96\n\t"
    "v_sub_f32 v68, v68, v96\n\t"
    "v_sub_f32 v69, v69, v96\n\t"
    "v_sub_f32 v70, v70, v96\n\t"
    "v_sub_f32 v71, v71, v96\n\t"
    "v_sub_f32 v84, v84, v96\n\t"
    "v_sub_f32 v85, v85, v96\n\t"
    "v_sub_f32 v86, v86, v96\n\t"
    "v_sub_f32 v87, v87, v96\n\t"
    "v_sub_f32 v36, v36, v96\n\t"
    "v_sub_f32 v37, v37, v96\n\t"
    "v_sub_f32 v38, v38, v96\n\t"
    "v_sub_f32 v39, v39, v96\n\t"
    "v_sub_f32 v52, v52, v96\n\t"
    "v_sub_f32 v53, v53, v96\n\t"
    "v_sub_f32 v54, v54, v96\n\t"
    "v_sub_f32 v55, v55, v96\n\t"
    "v_exp_f32 v98, -v96\n\t"
    "s_nop 0\n\t"
    "v_mul_f32 v169, v169, v98\n\t"
    "ds_bpermute_b32 v100, v99, v98 offset:0\n\t"
    "ds_bpermute_b32 v101, v99, v98 offset:4\n\t"
    "ds_bpermute_b32 v102, v99, v98 offset:8\n\t"
    "ds_bpermute_b32 v103, v99, v98 offset:12\n\t"
    "v_exp_f32 v176, v68\n\t"
    "v_exp_f32 v177, v69\n\t"
    "s_nop 0\n\t"
    "v_add_f32 v173, v176, v177\n\t"
    "v_cvt_pk_bf16_f32 v116, v176, v177\n\t"
    "v_exp_f32 v178, v70\n\t"
    "v_exp_f32 v179, v71\n\t"
    "s_nop 0\n\t"
    "v_add_f32 v173, v173, v178\n\t"
    "v_add_f32 v173, v173, v179\n\t"
    "v_cvt_pk_bf16_f32 v117, v178, v179\n\t"
    "v_exp_f32 v176, v84\n\t"
    "v_exp_f32 v177, v85\n\t"
    "s_nop 0\n\t"
    "v_add_f32 v173, v173, v176\n\t"
    "v_add_f32 v173, v173, v177\n\t"
    "v_cvt_pk_bf16_f32 v118, v176, v177\n\t"
    "v_exp_f32 v178, v86\n\t"
    "v_exp_f32 v179, v87\n\t"
    "s_nop 0\n\t"
    "v_add_f32 v173, v173, v178\n\t"
    "v_add_f32 v173, v173, v179\n\t"
    "v_cvt_pk_bf16_f32 v119, v178, v179\n\t"
    "s_waitcnt lgkmcnt(0)\n\t"
    "v_accvgpr_read_b32 v96, a4\n\t"
    "v_mul_f32 v96, v96, v100\n\t"
    "v_accvgpr_write_b32 a4, v96\n\t"
    "v_accvgpr_read_b32 v97, a5\n\t"
    "v_mul_f32 v97, v97, v101\n\t"
    "v_accvgpr_write_b32 a5, v97\n\t"
    "v_accvgpr_read_b32 v96, a6\n\t"
    "v_mul_f32 v96, v96, v102\n\t"
    "v_accvgpr_write_b32 a6, v96\n\t"
    "v_accvgpr_read_b32 v97, a7\n\t"
    "v_mul_f32 v97, v97, v103\n\t"
    "v_accvgpr_write_b32 a7, v97\n\t"
    "v_accvgpr_read_b32 v96, a20\n\t"
    "v_mul_f32 v96, v96, v100\n\t"
    "v_accvgpr_write_b32 a20, v96\n\t"
    "v_accvgpr_read_b32 v97, a21\n\t"
    "v_mul_f32 v97, v97, v101\n\t"
    "v_accvgpr_write_b32 a21, v97\n\t"
    "v_accvgpr_read_b32 v96, a22\n\t"
    "v_mul_f32 v96, v96, v102\n\t"
    "v_accvgpr_write_b32 a22, v96\n\t"
    "v_accvgpr_read_b32 v97, a23\n\t"
    "v_mul_f32 v97, v97, v103\n\t"
    "v_accvgpr_write_b32 a23, v97\n\t"
    "v_accvgpr_read_b32 v96, a36\n\t"
    "v_mul_f32 v96, v96, v100\n\t"
    "v_accvgpr_write_b32 a36, v96\n\t"
    "v_accvgpr_read_b32 v97, a37\n\t"
    "v_mul_f32 v97, v97, v101\n\t"
    "v_accvgpr_write_b32 a37, v97\n\t"
    "v_accvgpr_read_b32 v96, a38\n\t"
    "v_mul_f32 v96, v96, v102\n\t"
    "v_accvgpr_write_b32 a38, v96\n\t"
    "v_accvgpr_read_b32 v97, a39\n\t"
    "v_mul_f32 v97, v97, v103\n\t"
    "v_accvgpr_write_b32 a39, v97\n\t"
    "v_accvgpr_read_b32 v96, a52\n\t"
    "v_mul_f32 v96, v96, v100\n\t"
    "v_accvgpr_write_b32 a52, v96\n\t"
    "v_accvgpr_read_b32 v97, a53\n\t"
    "v_mul_f32 v97, v97, v101\n\t"
    "v_accvgpr_write_b32 a53, v97\n\t"
    "v_accvgpr_read_b32 v96, a54\n\t"
    "v_mul_f32 v96, v96, v102\n\t"
    "v_accvgpr_write_b32 a54, v96\n\t"
    "v_accvgpr_read_b32 v97, a55\n\t"
    "v_mul_f32 v97, v97, v103\n\t"
    "v_accvgpr_write_b32 a55, v97\n\t"
    "v_accvgpr_read_b32 v96, a68\n\t"
    "v_mul_f32 v96, v96, v100\n\t"
    "v_accvgpr_write_b32 a68, v96\n\t"
    "v_accvgpr_read_b32 v97, a69\n\t"
    "v_mul_f32 v97, v97, v101\n\t"
    "v_accvgpr_write_b32 a69, v97\n\t"
    "v_accvgpr_read_b32 v96, a70\n\t"
    "v_mul_f32 v96, v96, v102\n\t"
    "v_accvgpr_write_b32 a70, v96\n\t"
    "v_accvgpr_read_b32 v97, a71\n\t"
    "v_mul_f32 v97, v97, v103\n\t"
    "v_accvgpr_write_b32 a71, v97\n\t"
    "v_accvgpr_read_b32 v96, a84\n\t"
    "v_mul_f32 v96, v96, v100\n\t"
    "v_accvgpr_write_b32 a84, v96\n\t"
    "v_accvgpr_read_b32 v97, a85\n\t"
    "v_mul_f32 v97, v97, v101\n\t"
    "v_accvgpr_write_b32 a85, v97\n\t"
    "v_accvgpr_read_b32 v96, a86\n\t"
    "v_mul_f32 v96, v96, v102\n\t"
    "v_accvgpr_write_b32 a86, v96\n\t"
    "v_accvgpr_read_b32 v97, a87\n\t"
    "v_mul_f32 v97, v97, v103\n\t"
    "v_accvgpr_write_b32 a87, v97\n\t"
    "v_accvgpr_read_b32 v96, a100\n\t"
    "v_mul_f32 v96, v96, v100\n\t"
    "v_accvgpr_write_b32 a100, v96\n\t"
    "v_accvgpr_read_b32 v97, a101\n\t"
    "v_mul_f32 v97, v97, v101\n\t"
    "v_accvgpr_write_b32 a101, v97\n\t"
    "v_accvgpr_read_b32 v96, a102\n\t"
    "v_mul_f32 v96, v96, v102\n\t"
    "v_accvgpr_write_b32 a102, v96\n\t"
    "v_accvgpr_read_b32 v97, a103\n\t"
    "v_mul_f32 v97, v97, v103\n\t"
    "v_accvgpr_write_b32 a103, v97\n\t"
    "v_accvgpr_read_b32 v96, a116\n\t"
    "v_mul_f32 v96, v96, v100\n\t"
    "v_accvgpr_write_b32 a116, v96\n\t"
    "v_accvgpr_read_b32 v97, a117\n\t"
    "v_mul_f32 v97, v97, v101\n\t"
    "v_accvgpr_write_b32 a117, v97\n\t"
    "v_accvgpr_read_b32 v96, a118\n\t"
    "v_mul_f32 v96, v96, v102\n\t"
    "v_accvgpr_write_b32 a118, v96\n\t"
    "v_accvgpr_read_b32 v97, a119\n\t"
    "v_mul_f32 v97, v97, v103\n\t"
    "v_accvgpr_write_b32 a119, v97\n\t"
    "v_accvgpr_read_b32 v96, a132\n\t"
    "v_mul_f32 v96, v96, v100\n\t"
    "v_accvgpr_write_b32 a132, v96\n\t"
    "v_accvgpr_read_b32 v97, a133\n\t"
    "v_mul_f32 v97, v97, v101\n\t"
    "v_accvgpr_write_b32 a133, v97\n\t"
    "v_accvgpr_read_b32 v96, a134\n\t"
    "v_mul_f32 v96, v96, v102\n\t"
    "v_accvgpr_write_b32 a134, v96\n\t"
    "v_accvgpr_read_b32 v97, a135\n\t"
    "v_mul_f32 v97, v97, v103\n\t"
    "v_accvgpr_write_b32 a135, v97\n\t"
    "v_accvgpr_read_b32 v96, a148\n\t"
    "v_mul_f32 v96, v96, v100\n\t"
    "v_accvgpr_write_b32 a148, v96\n\t"
    "v_accvgpr_read_b32 v97, a149\n\t"
    "v_mul_f32 v97, v97, v101\n\t"
    "v_accvgpr_write_b32 a149, v97\n\t"
    "v_accvgpr_read_b32 v96, a150\n\t"
    "v_mul_f32 v96, v96, v102\n\t"
    "v_accvgpr_write_b32 a150, v96\n\t"
    "v_accvgpr_read_b32 v97, a151\n\t"
    "v_mul_f32 v97, v97, v103\n\t"
    "v_accvgpr_write_b32 a151, v97\n\t"
    "v_accvgpr_read_b32 v96, a164\n\t"
    "v_mul_f32 v96, v96, v100\n\t"
    "v_accvgpr_write_b32 a164, v96\n\t"
    "v_accvgpr_read_b32 v97, a165\n\t"
    "v_mul_f32 v97, v97, v101\n\t"
    "v_accvgpr_write_b32 a165, v97\n\t"
    "v_accvgpr_read_b32 v96, a166\n\t"
    "v_mul_f32 v96, v96, v102\n\t"
    "v_accvgpr_write_b32 a166, v96\n\t"
    "v_accvgpr_read_b32 v97, a167\n\t"
    "v_mul_f32 v97, v97, v103\n\t"
    "v_accvgpr_write_b32 a167, v97\n\t"
    "v_accvgpr_read_b32 v96, a180\n\t"
    "v_mul_f32 v96, v96, v100\n\t"
    "v_accvgpr_write_b32 a180, v96\n\t"
    "v_accvgpr_read_b32 v97, a181\n\t"
    "v_mul_f32 v97, v97, v101\n\t"
    "v_accvgpr_write_b32 a181, v97\n\t"
    "v_accvgpr_read_b32 v96, a182\n\t"
    "v_mul_f32 v96, v96, v102\n\t"
    "v_accvgpr_write_b32 a182, v96\n\t"
    "v_accvgpr_read_b32 v97, a183\n\t"
    "v_mul_f32 v97, v97, v103\n\t"
    "v_accvgpr_write_b32 a183, v97\n\t"
    "v_max3_f32 v96, v72, v73, v74\n\t"
    "v_max3_f32 v96, v96, v75, v88\n\t"
    "v_max3_f32 v96, v96, v89, v90\n\t"
    "v_max_f32 v96, v96, v91\n\t"
    "v_mov_b32 v97, v96\n\t"
    "s_nop 1\n\t"
    "v_permlane32_swap_b32 v96, v97\n\t"
    "s_nop 1\n\t"
    "v_max_f32 v96, v96, v97\n\t"
    "v_mov_b32 v97, v96\n\t"
    "s_nop 1\n\t"
    "v_permlane16_swap_b32 v96, v97\n\t"
    "s_nop 1\n\t"
    "v_max_f32 v96, v96, v97\n\t"
    "v_max_f32 v96, 0, v96\n\t"
    "v_sub_f32 v136, v136, v96\n\t"
    "v_sub_f32 v137, v137, v96\n\t"
    "v_sub_f32 v138, v138, v96\n\t"
    "v_sub_f32 v139, v139, v96\n\t"
    "v_sub_f32 v72, v72, v96\n\t"
    "v_sub_f32 v73, v73, v96\n\t"
    "v_sub_f32 v74, v74, v96\n\t"
    "v_sub_f32 v75, v75, v96\n\t"
    "v_sub_f32 v88, v88, v96\n\t"
    "v_sub_f32 v89, v89, v96\n\t"
    "v_sub_f32 v90, v90, v96\n\t"
    "v_sub_f32 v91, v91, v96\n\t"
    "v_sub_f32 v40, v40, v96\n\t"
    "v_sub_f32 v41, v41, v96\n\t"
    "v_sub_f32 v42, v42, v96\n\t"
    "v_sub_f32 v43, v43, v96\n\t"
    "v_sub_f32 v56, v56, v96\n\t"
    "v_sub_f32 v57, v57, v96\n\t"
    "v_sub_f32 v58, v58, v96\n\t"
    "v_sub_f32 v59, v59, v96\n\t"
    "v_exp_f32 v98, -v96\n\t"
    "s_nop 0\n\t"
    "v_mul_f32 v170, v170, v98\n\t"
    "ds_bpermute_b32 v100, v99, v98 offset:0\n\t"
    "ds_bpermute_b32 v101, v99, v98 offset:4\n\t"
    "ds_bpermute_b32 v102, v99, v98 offset:8\n\t"
    "ds_bpermute_b32 v103, v99, v98 offset:12\n\t"
    "v_exp_f32 v176, v72\n\t"
    "v_exp_f32 v177, v73\n\t"
    "s_nop 0\n\t"
    "v_add_f32 v174, v176, v177\n\t"
    "v_cvt_pk_bf16_f32 v120, v176, v177\n\t"
    "v_exp_f32 v178, v74\n\t"
    "v_exp_f32 v179, v75\n\t"
    "s_nop 0\n\t"
    "v_add_f32 v174, v174, v178\n\t"
    "v_add_f32 v174, v174, v179\n\t"
    "v_cvt_pk_bf16_f32 v121, v178, v179\n\t"
    "v_exp_f32 v176, v88\n\t"
    "v_exp_f32 v177, v89\n\t"
    "s_nop 0\n\t"
    "v_add_f32 v174, v174, v176\n\t"
    "v_add_f32 v174, v174, v177\n\t"
    "v_cvt_pk_bf16_f32 v122, v176, v177\n\t"
    "v_exp_f32 v178, v90\n\t"
    "v_exp_f32 v179, v91\n\t"
    "s_nop 0\n\t"
    "v_add_f32 v174, v174, v178\n\t"
    "v_add_f32 v174, v174, v179\n\t"
    "v_cvt_pk_bf16_f32 v123, v178, v179\n\t"
    "s_waitcnt lgkmcnt(0)\n\t"
    "v_accvgpr_read_b32 v96, a8\n\t"
    "v_mul_f32 v96, v96, v100\n\t"
    "v_accvgpr_write_b32 a8, v96\n\t"
    "v_accvgpr_read_b32 v97, a9\n\t"
    "v_mul_f32 v97, v97, v101\n\t"
    "v_accvgpr_write_b32 a9, v97\n\t"
    "v_accvgpr_read_b32 v96, a10\n\t"
    "v_mul_f32 v96, v96, v102\n\t"
    "v_accvgpr_write_b32 a10, v96\n\t"
    "v_accvgpr_read_b32 v97, a11\n\t"
    "v_mul_f32 v97, v97, v103\n\t"
    "v_accvgpr_write_b32 a11, v97\n\t"
    "v_accvgpr_read_b32 v96, a24\n\t"
    "v_mul_f32 v96, v96, v100\n\t"
    "v_accvgpr_write_b32 a24, v96\n\t"
    "v_accvgpr_read_b32 v97, a25\n\t"
    "v_mul_f32 v97, v97, v101\n\t"
    "v_accvgpr_write_b32 a25, v97\n\t"
    "v_accvgpr_read_b32 v96, a26\n\t"
    "v_mul_f32 v96, v96, v102\n\t"
    "v_accvgpr_write_b32 a26, v96\n\t"
    "v_accvgpr_read_b32 v97, a27\n\t"
    "v_mul_f32 v97, v97, v103\n\t"
    "v_accvgpr_write_b32 a27, v97\n\t"
    "v_accvgpr_read_b32 v96, a40\n\t"
    "v_mul_f32 v96, v96, v100\n\t"
    "v_accvgpr_write_b32 a40, v96\n\t"
    "v_accvgpr_read_b32 v97, a41\n\t"
    "v_mul_f32 v97, v97, v101\n\t"
    "v_accvgpr_write_b32 a41, v97\n\t"
    "v_accvgpr_read_b32 v96, a42\n\t"
    "v_mul_f32 v96, v96, v102\n\t"
    "v_accvgpr_write_b32 a42, v96\n\t"
    "v_accvgpr_read_b32 v97, a43\n\t"
    "v_mul_f32 v97, v97, v103\n\t"
    "v_accvgpr_write_b32 a43, v97\n\t"
    "v_accvgpr_read_b32 v96, a56\n\t"
    "v_mul_f32 v96, v96, v100\n\t"
    "v_accvgpr_write_b32 a56, v96\n\t"
    "v_accvgpr_read_b32 v97, a57\n\t"
    "v_mul_f32 v97, v97, v101\n\t"
    "v_accvgpr_write_b32 a57, v97\n\t"
    "v_accvgpr_read_b32 v96, a58\n\t"
    "v_mul_f32 v96, v96, v102\n\t"
    "v_accvgpr_write_b32 a58, v96\n\t"
    "v_accvgpr_read_b32 v97, a59\n\t"
    "v_mul_f32 v97, v97, v103\n\t"
    "v_accvgpr_write_b32 a59, v97\n\t"
    "v_accvgpr_read_b32 v96, a72\n\t"
    "v_mul_f32 v96, v96, v100\n\t"
    "v_accvgpr_write_b32 a72, v96\n\t"
    "v_accvgpr_read_b32 v97, a73\n\t"
    "v_mul_f32 v97, v97, v101\n\t"
    "v_accvgpr_write_b32 a73, v97\n\t"
    "v_accvgpr_read_b32 v96, a74\n\t"
    "v_mul_f32 v96, v96, v102\n\t"
    "v_accvgpr_write_b32 a74, v96\n\t"
    "v_accvgpr_read_b32 v97, a75\n\t"
    "v_mul_f32 v97, v97, v103\n\t"
    "v_accvgpr_write_b32 a75, v97\n\t"
    "v_accvgpr_read_b32 v96, a88\n\t"
    "v_mul_f32 v96, v96, v100\n\t"
    "v_accvgpr_write_b32 a88, v96\n\t"
    "v_accvgpr_read_b32 v97, a89\n\t"
    "v_mul_f32 v97, v97, v101\n\t"
    "v_accvgpr_write_b32 a89, v97\n\t"
    "v_accvgpr_read_b32 v96, a90\n\t"
    "v_mul_f32 v96, v96, v102\n\t"
    "v_accvgpr_write_b32 a90, v96\n\t"
    "v_accvgpr_read_b32 v97, a91\n\t"
    "v_mul_f32 v97, v97, v103\n\t"
    "v_accvgpr_write_b32 a91, v97\n\t"
    "v_accvgpr_read_b32 v96, a104\n\t"
    "v_mul_f32 v96, v96, v100\n\t"
    "v_accvgpr_write_b32 a104, v96\n\t"
    "v_accvgpr_read_b32 v97, a105\n\t"
    "v_mul_f32 v97, v97, v101\n\t"
    "v_accvgpr_write_b32 a105, v97\n\t"
    "v_accvgpr_read_b32 v96, a106\n\t"
    "v_mul_f32 v96, v96, v102\n\t"
    "v_accvgpr_write_b32 a106, v96\n\t"
    "v_accvgpr_read_b32 v97, a107\n\t"
    "v_mul_f32 v97, v97, v103\n\t"
    "v_accvgpr_write_b32 a107, v97\n\t"
    "v_accvgpr_read_b32 v96, a120\n\t"
    "v_mul_f32 v96, v96, v100\n\t"
    "v_accvgpr_write_b32 a120, v96\n\t"
    "v_accvgpr_read_b32 v97, a121\n\t"
    "v_mul_f32 v97, v97, v101\n\t"
    "v_accvgpr_write_b32 a121, v97\n\t"
    "v_accvgpr_read_b32 v96, a122\n\t"
    "v_mul_f32 v96, v96, v102\n\t"
    "v_accvgpr_write_b32 a122, v96\n\t"
    "v_accvgpr_read_b32 v97, a123\n\t"
    "v_mul_f32 v97, v97, v103\n\t"
    "v_accvgpr_write_b32 a123, v97\n\t"
    "v_accvgpr_read_b32 v96, a136\n\t"
    "v_mul_f32 v96, v96, v100\n\t"
    "v_accvgpr_write_b32 a136, v96\n\t"
    "v_accvgpr_read_b32 v97, a137\n\t"
    "v_mul_f32 v97, v97, v101\n\t"
    "v_accvgpr_write_b32 a137, v97\n\t"
    "v_accvgpr_read_b32 v96, a138\n\t"
    "v_mul_f32 v96, v96, v102\n\t"
    "v_accvgpr_write_b32 a138, v96\n\t"
    "v_accvgpr_read_b32 v97, a139\n\t"
    "v_mul_f32 v97, v97, v103\n\t"
    "v_accvgpr_write_b32 a139, v97\n\t"
    "v_accvgpr_read_b32 v96, a152\n\t"
    "v_mul_f32 v96, v96, v100\n\t"
    "v_accvgpr_write_b32 a152, v96\n\t"
    "v_accvgpr_read_b32 v97, a153\n\t"
    "v_mul_f32 v97, v97, v101\n\t"
    "v_accvgpr_write_b32 a153, v97\n\t"
    "v_accvgpr_read_b32 v96, a154\n\t"
    "v_mul_f32 v96, v96, v102\n\t"
    "v_accvgpr_write_b32 a154, v96\n\t"
    "v_accvgpr_read_b32 v97, a155\n\t"
    "v_mul_f32 v97, v97, v103\n\t"
    "v_accvgpr_write_b32 a155, v97\n\t"
    "v_accvgpr_read_b32 v96, a168\n\t"
    "v_mul_f32 v96, v96, v100\n\t"
    "v_accvgpr_write_b32 a168, v96\n\t"
    "v_accvgpr_read_b32 v97, a169\n\t"
    "v_mul_f32 v97, v97, v101\n\t"
    "v_accvgpr_write_b32 a169, v97\n\t"
    "v_accvgpr_read_b32 v96, a170\n\t"
    "v_mul_f32 v96, v96, v102\n\t"
    "v_accvgpr_write_b32 a170, v96\n\t"
    "v_accvgpr_read_b32 v97, a171\n\t"
    "v_mul_f32 v97, v97, v103\n\t"
    "v_accvgpr_write_b32 a171, v97\n\t"
    "v_accvgpr_read_b32 v96, a184\n\t"
    "v_mul_f32 v96, v96, v100\n\t"
    "v_accvgpr_write_b32 a184, v96\n\t"
    "v_accvgpr_read_b32 v97, a185\n\t"
    "v_mul_f32 v97, v97, v101\n\t"
    "v_accvgpr_write_b32 a185, v97\n\t"
    "v_accvgpr_read_b32 v96, a186\n\t"
    "v_mul_f32 v96, v96, v102\n\t"
    "v_accvgpr_write_b32 a186, v96\n\t"
    "v_accvgpr_read_b32 v97, a187\n\t"
    "v_mul_f32 v97, v97, v103\n\t"
    "v_accvgpr_write_b32 a187, v97\n\t"
    "v_max3_f32 v96, v76, v77, v78\n\t"
    "v_max3_f32 v96, v96, v79, v92\n\t"
    "v_max3_f32 v96, v96, v93, v94\n\t"
    "v_max_f32 v96, v96, v95\n\t"
    "v_mov_b32 v97, v96\n\t"
    "s_nop 1\n\t"
    "v_permlane32_swap_b32 v96, v97\n\t"
    "s_nop 1\n\t"
    "v_max_f32 v96, v96, v97\n\t"
    "v_mov_b32 v97, v96\n\t"
    "s_nop 1\n\t"
    "v_permlane16_swap_b32 v96, v97\n\t"
    "s_nop 1\n\t"
    "v_max_f32 v96, v96, v97\n\t"
    "v_max_f32 v96, 0, v96\n\t"
    "v_sub_f32 v140, v140, v96\n\t"
    "v_sub_f32 v141, v141, v96\n\t"
    "v_sub_f32 v142, v142, v96\n\t"
    "v_sub_f32 v143, v143, v96\n\t"
    "v_sub_f32 v76, v76, v96\n\t"
    "v_sub_f32 v77, v77, v96\n\t"
    "v_sub_f32 v78, v78, v96\n\t"
    "v_sub_f32 v79, v79, v96\n\t"
    "v_sub_f32 v92, v92, v96\n\t"
    "v_sub_f32 v93, v93, v96\n\t"
    "v_sub_f32 v94, v94, v96\n\t"
    "v_sub_f32 v95, v95, v96\n\t"
    "v_sub_f32 v44, v44, v96\n\t"
    "v_sub_f32 v45, v45, v96\n\t"
    "v_sub_f32 v46, v46, v96\n\t"
    "v_sub_f32 v47, v47, v96\n\t"
    "v_sub_f32 v60, v60, v96\n\t"
    "v_sub_f32 v61, v61, v96\n\t"
    "v_sub_f32 v62, v62, v96\n\t"
    "v_sub_f32 v63, v63, v96\n\t"
    "v_exp_f32 v98, -v96\n\t"
    "s_nop 0\n\t"
    "v_mul_f32 v171, v171, v98\n\t"
    "ds_bpermute_b32 v100, v99, v98 offset:0\n\t"
    "ds_bpermute_b32 v101, v99, v98 offset:4\n\t"
    "ds_bpermute_b32 v102, v99, v98 offset:8\n\t"
    "ds_bpermute_b32 v103, v99, v98 offset:12\n\t"
    "v_exp_f32 v176, v76\n\t"
    "v_exp_f32 v177, v77\n\t"
    "s_nop 0\n\t"
    "v_add_f32 v175, v176, v177\n\t"
    "v_cvt_pk_bf16_f32 v124, v176, v177\n\t"
    "v_exp_f32 v178, v78\n\t"
    "v_exp_f32 v179, v79\n\t"
    "s_nop 0\n\t"
    "v_add_f32 v175, v175, v178\n\t"
    "v_add_f32 v175, v175, v179\n\t"
    "v_cvt_pk_bf16_f32 v125, v178, v179\n\t"
    "v_exp_f32 v176, v92\n\t"
    "v_exp_f32 v177, v93\n\t"
    "s_nop 0\n\t"
    "v_add_f32 v175, v175, v176\n\t"
    "v_add_f32 v175, v175, v177\n\t"
    "v_cvt_pk_bf16_f32 v126, v176, v177\n\t"
    "v_exp_f32 v178, v94\n\t"
    "v_exp_f32 v179, v95\n\t"
    "s_nop 0\n\t"
    "v_add_f32 v175, v175, v178\n\t"
    "v_add_f32 v175, v175, v179\n\t"
    "v_cvt_pk_bf16_f32 v127, v178, v179\n\t"
    "s_waitcnt lgkmcnt(0)\n\t"
    "v_accvgpr_read_b32 v96, a12\n\t"
    "v_mul_f32 v96, v96, v100\n\t"
    "v_accvgpr_write_b32 a12, v96\n\t"
    "v_accvgpr_read_b32 v97, a13\n\t"
    "v_mul_f32 v97, v97, v101\n\t"
    "v_accvgpr_write_b32 a13, v97\n\t"
    "v_accvgpr_read_b32 v96, a14\n\t"
    "v_mul_f32 v96, v96, v102\n\t"
    "v_accvgpr_write_b32 a14, v96\n\t"
    "v_accvgpr_read_b32 v97, a15\n\t"
    "v_mul_f32 v97, v97, v103\n\t"
    "v_accvgpr_write_b32 a15, v97\n\t"
    "v_accvgpr_read_b32 v96, a28\n\t"
    "v_mul_f32 v96, v96, v100\n\t"
    "v_accvgpr_write_b32 a28, v96\n\t"
    "v_accvgpr_read_b32 v97, a29\n\t"
    "v_mul_f32 v97, v97, v101\n\t"
    "v_accvgpr_write_b32 a29, v97\n\t"
    "v_accvgpr_read_b32 v96, a30\n\t"
    "v_mul_f32 v96, v96, v102\n\t"
    "v_accvgpr_write_b32 a30, v96\n\t"
    "v_accvgpr_read_b32 v97, a31\n\t"
    "v_mul_f32 v97, v97, v103\n\t"
    "v_accvgpr_write_b32 a31, v97\n\t"
    "v_accvgpr_read_b32 v96, a44\n\t"
    "v_mul_f32 v96, v96, v100\n\t"
    "v_accvgpr_write_b32 a44, v96\n\t"
    "v_accvgpr_read_b32 v97, a45\n\t"
    "v_mul_f32 v97, v97, v101\n\t"
    "v_accvgpr_write_b32 a45, v97\n\t"
    "v_accvgpr_read_b32 v96, a46\n\t"
    "v_mul_f32 v96, v96, v102\n\t"
    "v_accvgpr_write_b32 a46, v96\n\t"
    "v_accvgpr_read_b32 v97, a47\n\t"
    "v_mul_f32 v97, v97, v103\n\t"
    "v_accvgpr_write_b32 a47, v97\n\t"
    "v_accvgpr_read_b32 v96, a60\n\t"
    "v_mul_f32 v96, v96, v100\n\t"
    "v_accvgpr_write_b32 a60, v96\n\t"
    "v_accvgpr_read_b32 v97, a61\n\t"
    "v_mul_f32 v97, v97, v101\n\t"
    "v_accvgpr_write_b32 a61, v97\n\t"
    "v_accvgpr_read_b32 v96, a62\n\t"
    "v_mul_f32 v96, v96, v102\n\t"
    "v_accvgpr_write_b32 a62, v96\n\t"
    "v_accvgpr_read_b32 v97, a63\n\t"
    "v_mul_f32 v97, v97, v103\n\t"
    "v_accvgpr_write_b32 a63, v97\n\t"
    "v_accvgpr_read_b32 v96, a76\n\t"
    "v_mul_f32 v96, v96, v100\n\t"
    "v_accvgpr_write_b32 a76, v96\n\t"
    "v_accvgpr_read_b32 v97, a77\n\t"
    "v_mul_f32 v97, v97, v101\n\t"
    "v_accvgpr_write_b32 a77, v97\n\t"
    "v_accvgpr_read_b32 v96, a78\n\t"
    "v_mul_f32 v96, v96, v102\n\t"
    "v_accvgpr_write_b32 a78, v96\n\t"
    "v_accvgpr_read_b32 v97, a79\n\t"
    "v_mul_f32 v97, v97, v103\n\t"
    "v_accvgpr_write_b32 a79, v97\n\t"
    "v_accvgpr_read_b32 v96, a92\n\t"
    "v_mul_f32 v96, v96, v100\n\t"
    "v_accvgpr_write_b32 a92, v96\n\t"
    "v_accvgpr_read_b32 v97, a93\n\t"
    "v_mul_f32 v97, v97, v101\n\t"
    "v_accvgpr_write_b32 a93, v97\n\t"
    "v_accvgpr_read_b32 v96, a94\n\t"
    "v_mul_f32 v96, v96, v102\n\t"
    "v_accvgpr_write_b32 a94, v96\n\t"
    "v_accvgpr_read_b32 v97, a95\n\t"
    "v_mul_f32 v97, v97, v103\n\t"
    "v_accvgpr_write_b32 a95, v97\n\t"
    "v_accvgpr_read_b32 v96, a108\n\t"
    "v_mul_f32 v96, v96, v100\n\t"
    "v_accvgpr_write_b32 a108, v96\n\t"
    "v_accvgpr_read_b32 v97, a109\n\t"
    "v_mul_f32 v97, v97, v101\n\t"
    "v_accvgpr_write_b32 a109, v97\n\t"
    "v_accvgpr_read_b32 v96, a110\n\t"
    "v_mul_f32 v96, v96, v102\n\t"
    "v_accvgpr_write_b32 a110, v96\n\t"
    "v_accvgpr_read_b32 v97, a111\n\t"
    "v_mul_f32 v97, v97, v103\n\t"
    "v_accvgpr_write_b32 a111, v97\n\t"
    "v_accvgpr_read_b32 v96, a124\n\t"
    "v_mul_f32 v96, v96, v100\n\t"
    "v_accvgpr_write_b32 a124, v96\n\t"
    "v_accvgpr_read_b32 v97, a125\n\t"
    "v_mul_f32 v97, v97, v101\n\t"
    "v_accvgpr_write_b32 a125, v97\n\t"
    "v_accvgpr_read_b32 v96, a126\n\t"
    "v_mul_f32 v96, v96, v102\n\t"
    "v_accvgpr_write_b32 a126, v96\n\t"
    "v_accvgpr_read_b32 v97, a127\n\t"
    "v_mul_f32 v97, v97, v103\n\t"
    "v_accvgpr_write_b32 a127, v97\n\t"
    "v_accvgpr_read_b32 v96, a140\n\t"
    "v_mul_f32 v96, v96, v100\n\t"
    "v_accvgpr_write_b32 a140, v96\n\t"
    "v_accvgpr_read_b32 v97, a141\n\t"
    "v_mul_f32 v97, v97, v101\n\t"
    "v_accvgpr_write_b32 a141, v97\n\t"
    "v_accvgpr_read_b32 v96, a142\n\t"
    "v_mul_f32 v96, v96, v102\n\t"
    "v_accvgpr_write_b32 a142, v96\n\t"
    "v_accvgpr_read_b32 v97, a143\n\t"
    "v_mul_f32 v97, v97, v103\n\t"
    "v_accvgpr_write_b32 a143, v97\n\t"
    "v_accvgpr_read_b32 v96, a156\n\t"
    "v_mul_f32 v96, v96, v100\n\t"
    "v_accvgpr_write_b32 a156, v96\n\t"
    "v_accvgpr_read_b32 v97, a157\n\t"
    "v_mul_f32 v97, v97, v101\n\t"
    "v_accvgpr_write_b32 a157, v97\n\t"
    "v_accvgpr_read_b32 v96, a158\n\t"
    "v_mul_f32 v96, v96, v102\n\t"
    "v_accvgpr_write_b32 a158, v96\n\t"
    "v_accvgpr_read_b32 v97, a159\n\t"
    "v_mul_f32 v97, v97, v103\n\t"
    "v_accvgpr_write_b32 a159, v97\n\t"
    "v_accvgpr_read_b32 v96, a172\n\t"
    "v_mul_f32 v96, v96, v100\n\t"
    "v_accvgpr_write_b32 a172, v96\n\t"
    "v_accvgpr_read_b32 v97, a173\n\t"
    "v_mul_f32 v97, v97, v101\n\t"
    "v_accvgpr_write_b32 a173, v97\n\t"
    "v_accvgpr_read_b32 v96, a174\n\t"
    "v_mul_f32 v96, v96, v102\n\t"
    "v_accvgpr_write_b32 a174, v96\n\t"
    "v_accvgpr_read_b32 v97, a175\n\t"
    "v_mul_f32 v97, v97, v103\n\t"
    "v_accvgpr_write_b32 a175, v97\n\t"
    "v_accvgpr_read_b32 v96, a188\n\t"
    "v_mul_f32 v96, v96, v100\n\t"
    "v_accvgpr_write_b32 a188, v96\n\t"
    "v_accvgpr_read_b32 v97, a189\n\t"
    "v_mul_f32 v97, v97, v101\n\t"
    "v_accvgpr_write_b32 a189, v97\n\t"
    "v_accvgpr_read_b32 v96, a190\n\t"
    "v_mul_f32 v96, v96, v102\n\t"
    "v_accvgpr_write_b32 a190, v96\n\t"
    "v_accvgpr_read_b32 v97, a191\n\t"
    "v_mul_f32 v97, v97, v103\n\t"
    "v_accvgpr_write_b32 a191, v97\n\t"
    "s_nop 1\n\t"
    "s_setpc_b64 s[76:77]\n\t"
    "L_end:\n\t"
    :: "s"(q + row0 * 384), "s"(ws + (size_t)h * NT * TILE), "s"(out + row0 * 384), "s"(j), "s"(w),
       "s"(qk + key0 * 768 + 2048), "s"(qv + key0 * 768 + 2048), "s"(ks + ((size_t)h * NT + j) * 384), "s"(kz + ((size_t)h * NT + j) * 384),
       "s"(vs + key0 * 2), "s"(vz + key0 * 2), "s"(ws + FLAG_OFF + (h * 4 + w) * 1024), [ci] "s"((b >> 3) * 4 + w)
    : "v0","v1","v2","v3","v4","v5","v6","v7","v8","v9","v10","v11","v12","v13","v14","v15","v16","v17","v18","v19","v20","v21","v22","v23","v24","v25","v26","v27","v28","v29","v30","v31","v32","v33","v34","v35","v36","v37","v38","v39","v40","v41","v42","v43","v44","v45","v46","v47","v48","v49","v50","v51","v52","v53","v54","v55","v56","v57","v58","v59","v60","v61","v62","v63","v64","v65","v66","v67","v68","v69","v70","v71","v72","v73","v74","v75","v76","v77","v78","v79","v80","v81","v82","v83","v84","v85","v86","v87","v88","v89","v90","v91","v92","v93","v94","v95","v96","v97","v98","v99","v100","v101","v102","v103","v104","v105","v106","v107","v108","v109","v110","v111","v112","v113","v114","v115","v116","v117","v118","v119","v120","v121","v122","v123","v124","v125","v126","v127","v128","v129","v130","v131","v132","v133","v134","v135","v136","v137","v138","v139","v140","v141","v142","v143","v144","v145","v146","v147","v148","v149","v150","v151","v152","v153","v154","v155","v156","v157","v158","v159","v160","v161","v162","v163","v164","v165","v166","v167","v168","v169","v170","v171","v172","v173","v174","v175","v176","v177","v178","v179","v180","v181","v182","v183","v184","v185","v186","v187","v188","v189","v190","v191","v192","v193","v194","v195","v196","v197","v198","v199","v200","v201","v202","v203","v204","v205","v206","v207","v208","v209","v210","v211","v212","v213","v214","v215","v216","v217","v218","v219","v220","v221","v222","v223","v224","v225","v226","v227","v228","v229","v230","v231","v232","v233","v234","v235","v236","v237","v238","v239","v240","v241","v242","v243","v244","v245","v246","v247","v248","v249","v250","v251","v252","v253","v254","v255","a0","a1","a2","a3","a4","a5","a6","a7","a8","a9","a10","a11","a12","a13","a14","a15","a16","a17","a18","a19","a20","a21","a22","a23","a24","a25","a26","a27","a28","a29","a30","a31","a32","a33","a34","a35","a36","a37","a38","a39","a40","a41","a42","a43","a44","a45","a46","a47","a48","a49","a50","a51","a52","a53","a54","a55","a56","a57","a58","a59","a60","a61","a62","a63","a64","a65","a66","a67","a68","a69","a70","a71","a72","a73","a74","a75","a76","a77","a78","a79","a80","a81","a82","a83","a84","a85","a86","a87","a88","a89","a90","a91","a92","a93","a94","a95","a96","a97","a98","a99","a100","a101","a102","a103","a104","a105","a106","a107","a108","a109","a110","a111","a112","a113","a114","a115","a116","a117","a118","a119","a120","a121","a122","a123","a124","a125","a126","a127","a128","a129","a130","a131","a132","a133","a134","a135","a136","a137","a138","a139","a140","a141","a142","a143","a144","a145","a146","a147","a148","a149","a150","a151","a152","a153","a154","a155","a156","a157","a158","a159","a160","a161","a162","a163","a164","a165","a166","a167","a168","a169","a170","a171","a172","a173","a174","a175","a176","a177","a178","a179","a180","a181","a182","a183","a184","a185","a186","a187","a188","a189","a190","a191","a192","a193","a194","a195","a196","a197","a198","a199","a200","a201","a202","a203","a204","a205","a206","a207","a208","a209","a210","a211","a212","a213","a214","a215","a216","a217","a218","a219","a220","a221","a222","a223","a224","a225","a226","a227","a228","a229","a230","a231","a232","a233","a234","a235","a236","a237","a238","a239","a240","a241","a242","a243","a244","a245","a246","a247","a248","a249","a250","a251","a252","a253","a254","a255","s36","s37","s38","s39","s40","s41","s42","s43","s44","s45","s46","s47","s48","s49","s50","s51","s52","s53","s54","s55","s56","s57","s58","s59","s60","s61","s62","s63","s64","s65","s66","s67","s68","s69","s70","s71","s72","s73","s74","s75","s76","s77","s78","s79","s80","s81","s82","s83","s84","s85","s86","s87","s88","s89","s90","s91","s92","s93","s94","s95","s96","s97","s98","s99","vcc","scc","memory");
}

extern "C" void kernel_launch(void* const* d_in, const int* in_sizes, int n_in,
                              void* d_out, int out_size, void* d_ws, size_t ws_size,
                              hipStream_t stream) {
  attn_kernel<<<256, 256, 0, stream>>>((const char*)d_in[0], (const char*)d_in[1], (const char*)d_in[2], (const char*)d_in[3],
      (const char*)d_in[4], (const char*)d_in[5], (const char*)d_in[6], (char*)d_ws, (char*)d_out);
}
```
